```python
import torch

def _softmax(x, axis=-1):
    return torch.softmax(x, dim=axis)
def _gelu(x):
    return torch.nn.functional.gelu(x, approximate="tanh")
def _silu(x):
    return torch.nn.functional.silu(x)
def _sigmoid(x):
    return torch.sigmoid(x)
def _layernorm(x, axis=-1, eps=1e-5):
    m = x.mean(dim=axis, keepdim=True)
    v = x.var(dim=axis, keepdim=True, unbiased=False)
    return (x - m) / torch.sqrt(v + eps)
def _scatter_add(tgt, idx, src, axis=0):
    return tgt.clone().index_add_(axis, idx, src)
def _segment_sum(src, seg, n):
    out = torch.zeros((n,) + src.shape[1:], dtype=src.dtype)
    return out.index_add_(0, seg, src)
def _pad(x, pw):
    flat = tuple(v for pair in reversed(pw) for v in pair)
    return torch.nn.functional.pad(x, flat)

kernel_name = 'sequential_preLN__compressed_kv_cla__dense_activation_silu__prompt'

def setup_inputs():

    _w = (lambda a: (lambda t: t.to(torch.bfloat16) if t.is_floating_point() else t)(torch.as_tensor(a)))
    def _A_setup():
        B, H, Sq, Sk, D, M = 1, 32, 2048, 2048, 128, 1024
        Dc = 4 * D
        return {
            "x": _w((lambda *s: torch.randn(*s, dtype=torch.bfloat16) * 0.05)(B, Sq, M)),
            "c_kv": _w((lambda *s: torch.randn(*s, dtype=torch.bfloat16) * 0.4)(B, Sk, Dc)),
            "w_q": _w((lambda *s: torch.randn(*s, dtype=torch.bfloat16) * 1.6)(M, H, D) * (M ** -0.5)),
            "w_uk": _w((lambda *s: torch.randn(*s, dtype=torch.bfloat16) * 1.6)(Dc, H, D) * (Dc ** -0.5)),
            "w_uv": _w((lambda *s: torch.randn(*s, dtype=torch.bfloat16) * 0.4)(Dc, H, D) * (Dc ** -0.5)),
            "w_o": _w((lambda *s: torch.randn(*s, dtype=torch.bfloat16) * 0.4)(H, D, M) * ((H * D) ** -0.5)),
        }
    _A_in = _A_setup()

    def _B_setup():
        B, S, D, F = 1, 2048, 1024, 3584
        return {
            "x": _w((lambda *s: torch.randn(*s, dtype=torch.bfloat16) * 0.05)(B, S, D)),
            "w1": _w((lambda *s: torch.randn(*s, dtype=torch.bfloat16) * 0.8)(D, F) * (D ** -0.5)),
            "w2": _w((lambda *s: torch.randn(*s, dtype=torch.bfloat16) * 0.1)(F, D) * (F ** -0.5)),
        }
    _B_in = _B_setup()

    return {
        "A_x": _A_in["x"],
        "A_c_kv": _A_in["c_kv"],
        "A_w_q": _A_in["w_q"],
        "A_w_uk": _A_in["w_uk"],
        "A_w_uv": _A_in["w_uv"],
        "A_w_o": _A_in["w_o"],
        "B_x": _B_in["x"],
        "B_w1": _B_in["w1"],
        "B_w2": _B_in["w2"],
    }

def reference(A_x, A_c_kv, A_w_q, A_w_uk, A_w_uv, A_w_o, B_x, B_w1, B_w2):
    def _A_ref(x, c_kv, w_q, w_uk, w_uv, w_o):
        B, Sq, M = x.shape[0], x.shape[1], x.shape[2]
        Sk = c_kv.shape[1]
        H, D = w_q.shape[1], w_q.shape[2]
        q = torch.einsum("bqm,mhd->bhqd", x, w_q)
        k = torch.einsum("bkc,chd->bhkd", c_kv, w_uk)
        v = torch.einsum("bkc,chd->bhkd", c_kv, w_uv)
        scores = torch.einsum("bhqd,bhkd->bhqk", q, k) * (D ** -0.5)
        off = Sk - Sq
        qi = torch.arange(Sq)[:, None]
        kj = torch.arange(Sk)[None, :]
        cmask = kj <= (qi + off)
        scores = torch.where(cmask[None, None], scores, -1e9)
        p = _softmax(scores, axis=-1)
        ctx = torch.einsum("bhqk,bhkd->bhqd", p, v)
        return torch.einsum("bhqd,hdm->bqm", ctx, w_o)

    def _B_ref(x, w1, w2):
        h = torch.einsum("bsd,df->bsf", x, w1)
        h = _silu(h)
        out = torch.einsum("bsf,fd->bsd", h, w2)
        return out

    x = A_x
    h = x + _A_ref(_layernorm(x, axis=-1), A_c_kv, A_w_q, A_w_uk, A_w_uv, A_w_o)
    h2 = h + _B_ref(_layernorm(h, axis=-1), B_w1, B_w2)
    return h2
```

```cpp
#include <hip/hip_runtime.h>
#include <hip/hip_bf16.h>
typedef unsigned short u16;
__device__ __forceinline__ float bf2f(u16 v){ return __uint_as_float(((unsigned)v)<<16); }
__device__ __forceinline__ u16 f2bf(float f){ unsigned u=__float_as_uint(f); u += 0x7fffu + ((u>>16)&1u); return (u16)(u>>16); }

__global__ void ln_k(const u16* x, u16* y){
  __shared__ float red[8];
  int r=blockIdx.x, t=threadIdx.x; float v[4]; float s=0;
  for(int i=0;i<4;i++){ v[i]=bf2f(x[r*1024+t*4+i]); s+=v[i]; }
  for(int o=32;o>0;o>>=1) s+=__shfl_xor(s,o);
  if((t&63)==0) red[t>>6]=s; __syncthreads();
  float m=(red[0]+red[1]+red[2]+red[3])*(1.f/1024.f); __syncthreads();
  float q=0; for(int i=0;i<4;i++){ float d=v[i]-m; q+=d*d; }
  for(int o=32;o>0;o>>=1) q+=__shfl_xor(q,o);
  if((t&63)==0) red[t>>6]=q; __syncthreads();
  float var=(red[0]+red[1]+red[2]+red[3])*(1.f/1024.f);
  float rs=1.f/sqrtf(var+1e-5f);
  for(int i=0;i<4;i++) y[r*1024+t*4+i]=f2bf((v[i]-m)*rs);
}
template<int MODE> __global__ void gemm_k(const u16* A,const u16* B,u16* C,const u16* R,int M,int N,int K){
  __shared__ float As[16][65], Bs[16][65];
  int bm=blockIdx.y*64,bn=blockIdx.x*64,t=threadIdx.x,tx=t&15,ty=t>>4; float acc[4][4]={};
  for(int k0=0;k0<K;k0+=16){
    for(int i=t;i<1024;i+=256){ int m=i>>4,k=i&15; As[k][m]=bf2f(A[(size_t)(bm+m)*K+k0+k]); int kk=i>>6,n=i&63; Bs[kk][n]=bf2f(B[(size_t)(k0+kk)*N+bn+n]); }
    __syncthreads();
    for(int k=0;k<16;k++){ float a[4],b[4]; for(int i=0;i<4;i++){a[i]=As[k][ty*4+i]; b[i]=Bs[k][tx*4+i];}
      for(int i=0;i<4;i++) for(int j=0;j<4;j++) acc[i][j]+=a[i]*b[j]; }
    __syncthreads();
  }
  for(int i=0;i<4;i++) for(int j=0;j<4;j++){ size_t o=(size_t)(bm+ty*4+i)*N+bn+tx*4+j; float v=acc[i][j];
    if(MODE==1){ v=bf2f(f2bf(v)); v=v/(1.f+__expf(-v)); }
    if(MODE==2){ v=bf2f(f2bf(v))+bf2f(R[o]); }
    C[o]=f2bf(v); }
}
__global__ void attn_k(const u16* Q,const u16* K,const u16* V,u16* O){
  __shared__ float p[2048]; __shared__ float q[128]; __shared__ float red[4];
  int r=blockIdx.x,h=blockIdx.y,t=threadIdx.x;
  q[t]=bf2f(Q[(size_t)r*4096+h*128+t]); __syncthreads();
  float mx=-1e30f;
  for(int j=t;j<=r;j+=128){ float s=0; const u16* kp=K+(size_t)j*4096+h*128; for(int d=0;d<128;d++) s+=q[d]*bf2f(kp[d]); s*=0.08838834764831845f; p[j]=s; mx=fmaxf(mx,s);}
  for(int o=32;o>0;o>>=1) mx=fmaxf(mx,__shfl_xor(mx,o));
  if((t&63)==0) red[t>>6]=mx; __syncthreads(); mx=fmaxf(red[0],red[1]); __syncthreads();
  float l=0; for(int j=t;j<=r;j+=128){ float e=__expf(p[j]-mx); p[j]=e; l+=e; }
  for(int o=32;o>0;o>>=1) l+=__shfl_xor(l,o);
  if((t&63)==0) red[t>>6]=l; __syncthreads(); l=red[0]+red[1];
  float acc=0; for(int j=0;j<=r;j++) acc+=p[j]*bf2f(V[(size_t)j*4096+h*128+t]);
  O[(size_t)r*4096+h*128+t]=f2bf(acc/l);
}
extern "C" void kernel_launch(void* const* d_in, const int* in_sizes, int n_in, void* d_out, int out_size, void* d_ws, size_t ws_size, hipStream_t stream){
  const u16* x=(const u16*)d_in[0]; const u16* c=(const u16*)d_in[1]; const u16* wq=(const u16*)d_in[2]; const u16* wk=(const u16*)d_in[3];
  const u16* wv=(const u16*)d_in[4]; const u16* wo=(const u16*)d_in[5]; const u16* w1=(const u16*)d_in[7]; const u16* w2=(const u16*)d_in[8];
  u16* ws=(u16*)d_ws; size_t MB=1<<20;
  u16* xn=ws; u16* q=ws+4*MB; u16* k=ws+16*MB; u16* v=ws+28*MB; u16* ctx=ws+40*MB; u16* h=ws+52*MB; u16* hn=ws+56*MB; u16* h1=ws+60*MB;
  ln_k<<<2048,256,0,stream>>>(x,xn);
  gemm_k<0><<<dim3(64,32),256,0,stream>>>(xn,wq,q,nullptr,2048,4096,1024);
  gemm_k<0><<<dim3(64,32),256,0,stream>>>(c,wk,k,nullptr,2048,4096,512);
  gemm_k<0><<<dim3(64,32),256,0,stream>>>(c,wv,v,nullptr,2048,4096,512);
  attn_k<<<dim3(2048,32),128,0,stream>>>(q,k,v,ctx);
  gemm_k<2><<<dim3(16,32),256,0,stream>>>(ctx,wo,h,x,2048,1024,4096);
  ln_k<<<2048,256,0,stream>>>(h,hn);
  gemm_k<1><<<dim3(56,32),256,0,stream>>>(hn,w1,h1,nullptr,2048,3584,1024);
  gemm_k<2><<<dim3(16,32),256,0,stream>>>(h1,w2,(u16*)d_out,h,2048,1024,3584);
}
```

```cpp
#include <hip/hip_runtime.h>
__global__ __launch_bounds__(256, 1) void k_fused(const void* x, const void* ckv, const void* wq, const void* wuk, const void* wuv, const void* wo,
    const void* w1, const void* w2, void* out, void* ws) {
  __shared__ __attribute__((aligned(1024))) char lds[163840];
  unsigned w = __builtin_amdgcn_readfirstlane(threadIdx.x >> 6);
  asm volatile(".macro qm d,a,b,x,y,z\nv_mfma_f32_16x16x32_bf16 \\d[\\x:\\x+3], \\a[\\y:\\y+3], \\b[\\z:\\z+3], \\d[\\x:\\x+3]\n.endm\n.macro qz d,a,b,x,y,z\nv_mfma_f32_16x16x32_bf16 \\d[\\x:\\x+3], \\a[\\y:\\y+3], \\b[\\z:\\z+3], 0\n.endm\n"
".macro qt x,y,o\nds_read_b64_tr_b16 a[\\x:\\x+1], v\\y offset:\\o\n.endm\n.macro qr x,y,o\nds_read_b128 a[\\x:\\x+3], v\\y offset:\\o\n.endm\n"
".macro qx x\nv_exp_f32 v\\x, v\\x\n.endm\n.macro qp x\nv_rcp_f32 v\\x, v\\x\n.endm\n"
".macro qc x,y\nv_cvt_pk_bf16_f32 v\\x, v\\y, v[\\y+1]\n.endm\n.macro qw x\ns_waitcnt lgkmcnt(\\x)\n.endm\n"
".macro qv x\ns_waitcnt vmcnt(\\x)\n.endm\n.macro qd x,s,t,o\nbuffer_load_dwordx4 v\\x, s[\\s:\\s+3], s\\t offen offset:\\o lds\n.endm\n"
".macro qe x,s,t,o\nbuffer_load_dwordx4 v\\x, s[\\s:\\s+3], s\\t offen offset:\\o sc1 lds\n.endm\n.macro ql d,x,s,t,o\nbuffer_load_dwordx4 a[\\d:\\d+3], v\\x, s[\\s:\\s+3], s\\t offen offset:\\o sc1\n.endm\n"
".macro qa x,y\nv_accvgpr_read_b32 v\\x, a\\y\n.endm\n.macro qb x,y\nv_accvgpr_write_b32 a\\x, \\y\n.endm\n"
"ENTRY%=:\ns_mov_b64 s[12:13], %0\ns_mov_b32 s14, %1\ns_mov_b32 s15, %2\ns_load_dwordx16 s[36:51], s[12:13], 0x0\ns_load_dwordx4 s[52:55], s[12:13], 0x40\n"
"s_and_b32 s18, s14, 7\ns_lshr_b32 s19, s14, 3\ns_and_b32 s72, s19, 3\ns_lshl2_add_u32 s20, s18, s72\ns_lshr_b32 s21, s19, 2\ns_lshr_b32 s73, s18, 1\n"
"s_lshl2_add_u32 s22, s73, s72\ns_and_b32 s73, s18, 1\ns_lshl1_add_u32 s23, s21, s73\nv_mbcnt_lo_u32_b32 v0, -1, 0\nv_mbcnt_hi_u32_b32 v0, -1, v0\nv_lshlrev_b32 v0, 4, v0\n"
"s_getpc_b64 s[84:85]\nCT1%=:\ns_add_u32 s84, s84, KV_PRO%=-CT1%=\ns_addc_u32 s85, s85, 0\ns_lshl2_add_u32 s86, s19, s15\ns_mul_i32 s86, s86, 256\n"
"v_lshl_add_u32 v1, v0, 3, s86\ns_mov_b32 s86, FINISH%=-KV_PRO%=\ns_mov_b32 s87, 0x20000\ns_bfm_b64 exec, 2, 0\nbuffer_load_dword a255, v1, s[84:87], 0 offen\ns_mov_b64 exec, -1\n"
"qw 0\ns_load_dwordx2 s[16:17], s[54:55], 0x0\ns_mov_b32 s56, s54\ns_and_b32 s57, s55, 0xffff\ns_mov_b32 s58, 0x6000000\ns_mov_b32 s59, 0x20000\n"
"KV_PRO%=:\nv_mbcnt_lo_u32_b32 v23, -1, 0\nv_mbcnt_hi_u32_b32 v23, -1, v23\nv_lshlrev_b32 v0, 4, v23\ns_mov_b32 s60, s38\ns_and_b32 s61, s39, 0xffff\n"
"s_mov_b32 s62, 0x200000\ns_mov_b32 s63, 0x20000\ns_mov_b32 s64, s42\ns_and_b32 s65, s43, 0xffff\ns_mov_b32 s66, 0x400000\ns_mov_b32 s67, 0x20000\n"
"s_mov_b32 s68, s44\ns_and_b32 s69, s45, 0xffff\ns_mov_b32 s70, 0x400000\ns_mov_b32 s71, 0x20000\nv_lshrrev_b32 v252, 3, v23\nv_and_b32 v253, 7, v23\n"
"v_lshrrev_b32 v254, 4, v23\nv_xor_b32 v253, v254, v253\nv_lshlrev_b32 v253, 4, v253\nv_lshl_add_u32 v19, v252, 10, v253\nv_xor_b32 v20, 64, v19\nv_add_u32 v21, 0x3800, v19\n"
"v_add_u32 v22, 0x5400, v20\nv_add_u32 v20, 0x1c00, v20\ns_lshl2_add_u32 s72, s21, s15\ns_lshl_b32 s1, s72, 16\ns_add_u32 s2, s1, 0x8000\ns_lshl_b32 s3, s15, 13\n"
"s_add_u32 s3, s3, 0x18000\ns_and_b32 s72, s20, 3\ns_add_u32 s76, s72, s21\ns_and_b32 s76, s76, 3\ns_lshl_b32 s76, s76, 2\ns_lshr_b32 s74, 0x10, s76\n"
"s_and_b32 s74, s74, 15\ns_lshl_b32 s72, s74, 7\ns_add_u32 s1, s1, s72\ns_add_u32 s2, s2, s72\ns_add_u32 m0, s3, 0x0\ns_nop 0\n"
"qd 19,60,1,0\nqd 20,60,1,1024\nqd 21,60,1,2048\nqd 22,60,1,3072\ns_add_u32 m0, s3, 0x1000\ns_nop 0\n"
"qd 19,60,2,0\nqd 20,60,2,1024\nqd 21,60,2,2048\nqd 22,60,2,3072\ns_lshr_b32 s75, 0x5635, s76\ns_and_b32 s75, s75, 15\n"
"s_sub_u32 s75, s75, 4\ns_lshl_b32 s75, s75, 7\ns_add_u32 s1, s1, s75\ns_add_u32 s2, s2, s75\nv_lshrrev_b32 v252, 4, v23\nv_and_b32 v253, 15, v23\n"
"s_mov_b32 s72, 0x2000\nv_or_b32 v254, 0, v252\nv_lshlrev_b32 v254, 1, v254\nv_xor_b32 v254, v254, v253\nv_lshlrev_b32 v254, 4, v254\nv_mad_u32_u24 v248, v252, s72, v254\n"
"v_or_b32 v254, 0, v252\nv_lshlrev_b32 v254, 1, v254\nv_xor_b32 v254, v254, v253\nv_lshlrev_b32 v254, 4, v254\nv_mad_u32_u24 v249, v252, s72, v254\nv_add_u32 v249, 0x7c00, v249\n"
"v_or_b32 v254, 4, v252\nv_lshlrev_b32 v254, 1, v254\nv_xor_b32 v254, v254, v253\nv_lshlrev_b32 v254, 4, v254\nv_mad_u32_u24 v250, v252, s72, v254\nv_add_u32 v250, 0xf800, v250\n"
"v_or_b32 v254, 4, v252\nv_lshlrev_b32 v254, 1, v254\nv_xor_b32 v254, v254, v253\nv_lshlrev_b32 v254, 4, v254\nv_mad_u32_u24 v251, v252, s72, v254\nv_add_u32 v251, 0x17400, v251\n"
"s_lshl_b32 s0, s15, 17\ns_lshl_b32 s72, s20, 8\ns_add_u32 s0, s0, s72\ns_lshl_b32 s4, s15, 12\ns_lshl_b32 s72, s74, 19\ns_add_u32 s0, s0, s72\n"
"s_add_u32 m0, s4, 0x0\ns_nop 0\nqd 248,64,0,0\nqd 249,64,0,1024\nqd 250,64,0,2048\nqd 251,64,0,3072\n"
"s_add_u32 m0, s4, 0x4000\ns_nop 0\nqd 248,68,0,0\nqd 249,68,0,1024\nqd 250,68,0,2048\nqd 251,68,0,3072\n"
"s_lshl_b32 s75, s75, 12\ns_add_u32 s0, s0, s75\nv_lshrrev_b32 v252, 4, v23\nv_bfe_u32 v253, v23, 2, 2\nv_lshl_add_u32 v254, v252, 3, v253\nv_and_b32 v252, 1, v252\n"
"v_lshl_add_u32 v252, v252, 2, v253\nv_and_b32 v253, 3, v23\nv_lshlrev_b32 v253, 3, v253\nv_lshl_add_u32 v253, v254, 8, v253\nv_lshl_add_u32 v253, v252, 5, v253\nv_mov_b32 v1, v253\n"
"v_xor_b32 v2, 32, v253\nv_xor_b32 v3, 64, v253\nv_xor_b32 v4, 96, v253\nv_xor_b32 v5, 128, v253\nv_xor_b32 v6, 160, v253\nv_xor_b32 v7, 192, v253\n"
"v_xor_b32 v8, 224, v253\nv_add_u32 v9, 0x10000, v1\nv_add_u32 v10, 0x10000, v2\nv_add_u32 v11, 0x10000, v3\nv_add_u32 v12, 0x10000, v4\nv_add_u32 v13, 0x10000, v5\n"
"v_add_u32 v14, 0x10000, v6\nv_add_u32 v15, 0x10000, v7\nv_add_u32 v16, 0x10000, v8\nv_and_b32 v252, 15, v23\nv_lshrrev_b32 v253, 4, v23\nv_bfe_u32 v254, v23, 1, 3\n"
"v_xor_b32 v253, v253, v254\nv_lshlrev_b32 v253, 4, v253\nv_lshl_add_u32 v253, v252, 7, v253\nv_add_u32 v17, s3, v253\nv_xor_b32 v18, 64, v17\nqv 4\n"
"s_barrier\nqr 32,17,0\nqr 36,18,0\nqr 40,17,2048\nqr 44,18,2048\nqr 48,17,4096\n"
"qr 52,18,4096\nqr 56,17,6144\nqr 60,18,6144\nqt 96,1,0\nqt 98,1,1024\nqt 100,1,8192\n"
"qt 102,1,9216\nqt 104,2,0\nqt 106,2,1024\nqt 108,2,8192\nqt 110,2,9216\nqw 0\n"
"s_add_u32 s16, s16, 1\ns_addc_u32 s17, s17, 0\nqz a,a,a,0,96,32\ns_add_u32 m0, s3, 0x8000\ns_mov_b32 s84, s36\nqt 112,3,0\n"
"qm a,a,a,0,100,36\nqd 19,60,1,0\nqz a,a,a,4,100,44\ns_and_b32 s85, s37, 0xffff\ns_mov_b32 s86, 0x400000\nqt 114,3,1024\n"
"qm a,a,a,4,96,40\nqd 20,60,1,1024\nqz a,a,a,8,96,48\ns_mov_b32 s87, 0x20000\ns_lshr_b32 s79, s15, 1\nqt 116,3,8192\n"
"qm a,a,a,8,100,52\ns_lshl1_add_u32 s79, s14, s79\ns_lshl_b32 s80, s79, 13\nqt 118,3,9216\nqz a,a,a,12,100,60\ns_and_b32 s82, s15, 1\n"
"s_lshl_b32 s83, s82, 10\nqt 120,4,0\nqm a,a,a,12,96,56\ns_add_u32 s80, s80, s83\nqt 122,4,1024\nqz a,a,a,28,104,56\n"
"qd 21,60,1,2048\nqm a,a,a,28,108,60\nqt 124,4,8192\nv_and_b32 v24, 3, v23\nqz a,a,a,24,108,52\nqt 126,4,9216\n"
"v_lshrrev_b32 v25, 2, v23\nqm a,a,a,24,104,48\nqd 22,60,1,3072\nqz a,a,a,20,104,40\ns_add_u32 m0, s3, 0x9000\nqt 96,5,0\n"
"v_lshlrev_b32 v26, 4, v25\nqm a,a,a,20,108,44\nqt 98,5,1024\nv_lshl_add_u32 v26, v24, 11, v26\nqz a,a,a,16,108,36\nqd 19,60,2,0\n"
"qm a,a,a,16,104,32\nqw 6\nqt 100,5,8192\nv_lshlrev_b32 v255, 8, v25\nqz a,a,a,160,112,32\nqt 102,5,9216\n"
"v_lshl_add_u32 v255, v24, 4, v255\nqm a,a,a,160,116,36\ns_lshr_b32 s81, s79, 2\ns_lshl1_add_u32 s81, s81, s82\ns_lshl_b32 s81, s81, 14\nqz a,a,a,164,116,44\n"
"qd 20,60,2,1024\nqm a,a,a,164,112,40\ns_add_u32 s81, s81, 0x100000\ns_and_b32 s82, s79, 3\ns_lshl_b32 s82, s82, 6\nqz a,a,a,168,112,48\n"
"s_add_u32 s81, s81, s82\ns_add_u32 s83, s3, 39936\nv_lshl_add_u32 v38, v24, 3, s83\nqm a,a,a,168,116,52\nqt 104,6,0\nv_xor_b32 v37, 0x2000, v38\n"
"qz a,a,a,172,116,60\nqd 21,60,2,2048\nqm a,a,a,172,112,56\nqw 5\nqt 106,6,1024\nqz v,a,a,52,120,56\n"
"qt 108,6,8192\nqm v,a,a,52,124,60\nqd 22,60,2,3072\nqz v,a,a,48,124,52\nqt 110,6,9216\nqm v,a,a,48,120,48\n"
"qt 112,7,0\nqz v,a,a,44,120,40\ns_lshr_b32 s75, 0x6365, s76\ns_and_b32 s75, s75, 15\ns_sub_u32 s75, s75, 4\ns_lshl_b32 s75, s75, 7\n"
"s_add_u32 s1, s1, s75\ns_add_u32 s2, s2, s75\ns_add_u32 m0, s4, 0x8000\nqm v,a,a,44,124,44\nqd 248,64,0,0\nqz v,a,a,40,124,36\n"
"qt 114,7,1024\nqm v,a,a,40,120,32\nqw 6\nqt 116,7,8192\nqz v,a,a,56,96,32\nqt 118,7,9216\n"
"qm v,a,a,56,100,36\nqd 249,64,0,1024\nqz v,a,a,60,100,44\nqt 120,8,0\nqm v,a,a,60,96,40\nqt 122,8,1024\n"
"qz v,a,a,64,96,48\nqd 250,64,0,2048\nqm v,a,a,64,100,52\nqt 124,8,8192\nqz v,a,a,68,100,60\nqt 126,8,9216\n"
"qm v,a,a,68,96,56\nqw 8\nqz v,a,a,84,104,56\nqd 251,64,0,3072\nqm v,a,a,84,108,60\ns_add_u32 m0, s4, 0xc000\n"
"qz v,a,a,80,108,52\nqm v,a,a,80,104,48\nqz v,a,a,76,104,40\nqv 12\ns_barrier\nqt 96,1,16384\n"
"qm v,a,a,76,108,44\nqd 248,68,0,0\nqz v,a,a,72,108,36\nqt 98,1,17408\nqm v,a,a,72,104,32\nqw 6\n"
"qt 100,1,24576\nqz v,a,a,88,112,32\nqd 249,68,0,1024\nqm v,a,a,88,116,36\nqt 102,1,25600\nqz v,a,a,92,116,44\n"
"qt 104,2,16384\nqm v,a,a,92,112,40\nqd 250,68,0,2048\nqz v,a,a,96,112,48\nqt 106,2,17408\nqm v,a,a,96,116,52\n"
"qt 108,2,24576\nqz v,a,a,100,116,60\nqt 110,2,25600\nqm v,a,a,100,112,56\nqw 8\nqt 112,3,16384\n"
"qz v,a,a,116,120,56\nqd 251,68,0,3072\nqm v,a,a,116,124,60\ns_lshl_b32 s75, s75, 12\ns_add_u32 s0, s0, s75\nqt 114,3,17408\n"
"qz v,a,a,112,124,52\nbuffer_load_dwordx4 a[128:131], v26, s[84:87], s80 offen offset:0\nqm v,a,a,112,120,48\nqt 116,3,24576\nqz v,a,a,108,120,40\nqt 118,3,25600\n"
"qm v,a,a,108,124,44\nqz v,a,a,104,124,36\nbuffer_load_dwordx4 a[132:135], v26, s[84:87], s80 offen offset:256\nqm v,a,a,104,120,32\nqw 8\nqt 120,4,16384\n"
"qz v,a,a,120,32,96\nqt 122,4,17408\nqm v,a,a,120,36,100\nqt 124,4,24576\nqz v,a,a,152,44,100\nbuffer_load_dwordx4 a[136:139], v26, s[84:87], s80 offen offset:512\n"
"qm v,a,a,152,40,96\nqt 126,4,25600\nqz v,a,a,184,48,96\nqm v,a,a,184,52,100\nqz v,a,a,216,60,100\nbuffer_load_dwordx4 a[140:143], v26, s[84:87], s80 offen offset:768\n"
"qm v,a,a,216,56,96\nqw 8\ns_add_u32 m0, s3, 0x0\nqt 96,5,16384\nqz v,a,a,220,56,104\nqv 12\n"
"qr 64,17,32768\nqm v,a,a,220,60,108\nqr 68,18,32768\nqz v,a,a,188,52,108\nqr 72,17,34816\nqm v,a,a,188,48,104\n"
"qd 19,60,1,0\nqz v,a,a,156,40,104\nqr 76,18,34816\nqm v,a,a,156,44,108\nqr 80,17,36864\nqz v,a,a,124,36,108\n"
"qt 98,5,17408\nqm v,a,a,124,32,104\nqw 11\nqt 100,5,24576\nqz v,a,a,128,32,112\nqt 102,5,25600\n"
"qm v,a,a,128,36,116\nqd 20,60,1,1024\nqz v,a,a,160,44,116\nqr 84,18,36864\nqm v,a,a,160,40,112\nqr 88,17,38912\n"
"qz v,a,a,192,48,112\nqr 92,18,38912\nqm v,a,a,192,52,116\nqt 104,6,16384\nqz v,a,a,224,60,116\nqd 21,60,1,2048\n"
"qm v,a,a,224,56,112\nqw 13\nqt 106,6,17408\nqz v,a,a,228,56,120\nqt 108,6,24576\nqm v,a,a,228,60,124\n"
"qt 110,6,25600\nqz v,a,a,196,52,124\nqt 112,7,16384\nqm v,a,a,196,48,120\nqt 114,7,17408\nqz v,a,a,164,40,120\n"
"qd 22,60,1,3072\nqm v,a,a,164,44,124\ns_add_u32 m0, s3, 0x1000\nqt 116,7,24576\nqz v,a,a,132,36,124\nqt 118,7,25600\n"
"qm v,a,a,132,32,120\nqw 11\nqt 120,8,16384\nqz v,a,a,136,32,96\nqt 122,8,17408\nqm v,a,a,136,36,100\n"
"qt 124,8,24576\nqz v,a,a,168,44,100\nqt 126,8,25600\nqm v,a,a,168,40,96\nqd 19,60,2,0\nqz v,a,a,200,48,96\n"
"qm v,a,a,200,52,100\nqz v,a,a,232,60,100\nqm v,a,a,232,56,96\nqw 8\nqz v,a,a,236,56,104\nqd 20,60,2,1024\n"
"qm v,a,a,236,60,108\nqz v,a,a,204,52,108\nqm v,a,a,204,48,104\nqz v,a,a,172,40,104\nqw 0\nqv 10\n"
"s_barrier\nqm v,a,a,172,44,108\nqd 21,60,2,2048\nqz v,a,a,140,36,108\nqv 7\nqa 28,128\n"
"v_lshlrev_b32 v29, 16, v28\nqm v,a,a,140,32,104\nv_and_b32 v30, 0xffff0000, v28\nv_mov_b32 v24, v29\nqz v,a,a,144,32,112\nv_mul_f32 v26, v29, v29\n"
"v_mov_b32 v25, v30\nqm v,a,a,144,36,116\nv_mul_f32 v27, v30, v30\nqa 31,129\nqz v,a,a,176,44,116\nv_lshlrev_b32 v32, 16, v31\n"
"v_and_b32 v33, 0xffff0000, v31\nqm v,a,a,176,40,112\nv_add_f32 v24, v24, v32\nv_fmac_f32 v26, v32, v32\nqz v,a,a,208,48,112\nqd 22,60,2,3072\n"
"qm v,a,a,208,52,116\nv_add_f32 v25, v25, v33\nv_fmac_f32 v27, v33, v33\nqz v,a,a,240,60,116\nqt 96,1,32768\nqa 34,130\n"
"qm v,a,a,240,56,112\ns_lshr_b32 s75, 0x3656, s76\ns_and_b32 s75, s75, 15\ns_sub_u32 s75, s75, 4\ns_lshl_b32 s75, s75, 7\ns_add_u32 s1, s1, s75\n"
"s_add_u32 s2, s2, s75\ns_add_u32 m0, s4, 0x10000\nqz v,a,a,244,56,120\nqt 98,1,33792\nv_lshlrev_b32 v35, 16, v34\nqm v,a,a,244,60,124\n"
"qt 100,1,40960\nv_and_b32 v36, 0xffff0000, v34\nqz v,a,a,212,52,124\nqt 102,1,41984\nv_add_f32 v24, v24, v35\nqm v,a,a,212,48,120\n"
"v_fmac_f32 v26, v35, v35\nv_add_f32 v25, v25, v36\nqz v,a,a,180,40,120\nqd 248,64,0,0\nqm v,a,a,180,44,124\nv_fmac_f32 v27, v36, v36\n"
"qa 28,131\nqz v,a,a,148,36,124\nv_lshlrev_b32 v29, 16, v28\nv_and_b32 v30, 0xffff0000, v28\nqm v,a,a,148,32,120\nqw 0\n"
"qt 104,2,32768\nv_add_f32 v24, v24, v29\nqm a,a,a,0,96,64\nqt 106,2,33792\nv_fmac_f32 v26, v29, v29\nqm a,a,a,0,100,68\n"
"qt 108,2,40960\nv_add_f32 v25, v25, v30\nqm a,a,a,4,100,76\nqt 110,2,41984\nv_fmac_f32 v27, v30, v30\nqm a,a,a,4,96,72\n"
"qd 249,64,0,1024\nqm a,a,a,8,96,80\nqa 31,132\nv_lshlrev_b32 v32, 16, v31\nqm a,a,a,8,100,84\nv_and_b32 v33, 0xffff0000, v31\n"
"v_add_f32 v24, v24, v32\nqm a,a,a,12,100,92\nv_fmac_f32 v26, v32, v32\nv_add_f32 v25, v25, v33\nqm a,a,a,12,96,88\nqw 0\n"
"qt 112,3,32768\nv_fmac_f32 v27, v33, v33\nqm a,a,a,28,104,88\nqd 250,64,0,2048\nqm a,a,a,28,108,92\nqt 114,3,33792\n"
"qa 34,133\nqm a,a,a,24,108,84\nqt 116,3,40960\nqt 118,3,41984\nqm a,a,a,24,104,80\nv_lshlrev_b32 v35, 16, v34\n"
"v_and_b32 v36, 0xffff0000, v34\nqm a,a,a,20,104,72\nqd 251,64,0,3072\nqm a,a,a,20,108,76\ns_add_u32 m0, s4, 0x14000\nv_add_f32 v24, v24, v35\n"
"v_fmac_f32 v26, v35, v35\nqm a,a,a,16,108,68\nv_add_f32 v25, v25, v36\nv_fmac_f32 v27, v36, v36\nqm a,a,a,16,104,64\nqw 0\n"
"qa 28,134\nv_lshlrev_b32 v29, 16, v28\nqm a,a,a,160,112,64\nqt 120,4,32768\nv_and_b32 v30, 0xffff0000, v28\nqm a,a,a,160,116,68\n"
"qt 122,4,33792\nv_add_f32 v24, v24, v29\nqm a,a,a,164,116,76\nqt 124,4,40960\nqt 126,4,41984\nqm a,a,a,164,112,72\n"
"v_fmac_f32 v26, v29, v29\nv_add_f32 v25, v25, v30\nqm a,a,a,168,112,80\nqd 248,68,0,0\nqm a,a,a,168,116,84\nv_fmac_f32 v27, v30, v30\n"
"qa 31,135\nqm a,a,a,172,116,92\nv_lshlrev_b32 v32, 16, v31\nv_and_b32 v33, 0xffff0000, v31\nqm a,a,a,172,112,88\nqw 0\n"
"v_add_f32 v24, v24, v32\nv_fmac_f32 v26, v32, v32\nqm v,a,a,52,120,88\nqt 96,5,32768\nv_add_f32 v25, v25, v33\nqm v,a,a,52,124,92\n"
"qt 98,5,33792\nv_fmac_f32 v27, v33, v33\nqm v,a,a,48,124,84\nqt 100,5,40960\nqt 102,5,41984\nqd 249,68,0,1024\n"
"qm v,a,a,48,120,80\nqa 34,136\nv_lshlrev_b32 v35, 16, v34\nqm v,a,a,44,120,72\nv_and_b32 v36, 0xffff0000, v34\nv_add_f32 v24, v24, v35\n"
"qm v,a,a,44,124,76\nqd 250,68,0,2048\nqm v,a,a,40,124,68\nv_fmac_f32 v26, v35, v35\nv_add_f32 v25, v25, v36\nqm v,a,a,40,120,64\n"
"qw 0\nv_fmac_f32 v27, v36, v36\nqa 28,137\nqm v,a,a,56,96,64\nqt 104,6,32768\nv_lshlrev_b32 v29, 16, v28\n"
"qm v,a,a,56,100,68\nqt 106,6,33792\nv_and_b32 v30, 0xffff0000, v28\nqm v,a,a,60,100,76\nqt 108,6,40960\nqt 110,6,41984\n"
"qm v,a,a,60,96,72\nqd 251,68,0,3072\ns_lshl_b32 s75, s75, 12\ns_add_u32 s0, s0, s75\ns_add_u32 m0, s3, 0x8000\nqm v,a,a,64,96,80\n"
"v_add_f32 v24, v24, v29\nv_fmac_f32 v26, v29, v29\nqm v,a,a,64,100,84\nv_add_f32 v25, v25, v30\nv_fmac_f32 v27, v30, v30\nqm v,a,a,68,100,92\n"
"qa 31,138\nv_lshlrev_b32 v32, 16, v31\nqm v,a,a,68,96,88\nqw 0\nqt 112,7,32768\nv_and_b32 v33, 0xffff0000, v31\n"
"qm v,a,a,84,104,88\nqt 114,7,33792\nv_add_f32 v24, v24, v32\nqm v,a,a,84,108,92\nqt 116,7,40960\nv_fmac_f32 v26, v32, v32\n"
"qm v,a,a,80,108,84\nqt 118,7,41984\nqd 19,60,1,0\nqm v,a,a,80,104,80\nv_add_f32 v25, v25, v33\nv_fmac_f32 v27, v33, v33\n"
"qm v,a,a,76,104,72\nqa 34,139\nv_lshlrev_b32 v35, 16, v34\nqm v,a,a,76,108,76\nv_and_b32 v36, 0xffff0000, v34\nv_add_f32 v24, v24, v35\n"
"qm v,a,a,72,108,68\nqd 20,60,1,1024\nqm v,a,a,72,104,64\nqw 0\nv_fmac_f32 v26, v35, v35\nv_add_f32 v25, v25, v36\n"
"qm v,a,a,88,112,64\nqt 120,8,32768\nv_fmac_f32 v27, v36, v36\nqm v,a,a,88,116,68\nqt 122,8,33792\nqa 28,140\n"
"qm v,a,a,92,116,76\nqt 124,8,40960\nqt 126,8,41984\nqm v,a,a,92,112,72\nv_lshlrev_b32 v29, 16, v28\nv_and_b32 v30, 0xffff0000, v28\n"
"qm v,a,a,96,112,80\nqd 21,60,1,2048\nqm v,a,a,96,116,84\nv_add_f32 v24, v24, v29\nv_fmac_f32 v26, v29, v29\nqm v,a,a,100,116,92\n"
"v_add_f32 v25, v25, v30\nv_fmac_f32 v27, v30, v30\nqm v,a,a,100,112,88\nqw 0\nqa 31,141\nv_lshlrev_b32 v32, 16, v31\n"
"qm v,a,a,116,120,88\nqt 96,1,49152\nv_and_b32 v33, 0xffff0000, v31\nqm v,a,a,116,124,92\nqt 98,1,50176\nv_add_f32 v24, v24, v32\n"
"qm v,a,a,112,124,84\nqt 100,1,57344\nqt 102,1,58368\nqd 22,60,1,3072\ns_add_u32 m0, s3, 0x9000\nqm v,a,a,112,120,80\n"
"v_fmac_f32 v26, v32, v32\nv_add_f32 v25, v25, v33\nqm v,a,a,108,120,72\nv_fmac_f32 v27, v33, v33\nqa 34,142\nqm v,a,a,108,124,76\n"
"v_lshlrev_b32 v35, 16, v34\nv_and_b32 v36, 0xffff0000, v34\nqm v,a,a,104,124,68\nqd 19,60,2,0\nqm v,a,a,104,120,64\nqw 0\n"
"qt 104,2,49152\nv_add_f32 v24, v24, v35\nqm v,a,a,120,64,96\nqt 106,2,50176\nv_fmac_f32 v26, v35, v35\nqm v,a,a,120,68,100\n"
"qt 108,2,57344\nv_add_f32 v25, v25, v36\nqm v,a,a,152,76,100\nqt 110,2,58368\nv_fmac_f32 v27, v36, v36\nqm v,a,a,152,72,96\n"
"qa 28,143\nv_lshlrev_b32 v29, 16, v28\nqm v,a,a,184,80,96\nqd 20,60,2,1024\nqm v,a,a,184,84,100\nv_and_b32 v30, 0xffff0000, v28\n"
"v_add_f32 v24, v24, v29\nqm v,a,a,216,92,100\nv_fmac_f32 v26, v29, v29\nv_add_f32 v25, v25, v30\nqm v,a,a,216,88,96\nqw 0\n"
"qt 112,3,49152\nv_fmac_f32 v27, v30, v30\nqm v,a,a,220,88,104\nqv 14\nqt 114,3,50176\nv_add_f32 v24, v24, v25\n"
"qm v,a,a,220,92,108\nqt 116,3,57344\nv_add_f32 v26, v26, v27\nqm v,a,a,188,84,108\nqt 118,3,58368\nqd 21,60,2,2048\n"
"s_mov_b32 s83, s2\ns_lshr_b32 s75, 0x6563, s76\ns_and_b32 s75, s75, 15\ns_sub_u32 s75, s75, 4\ns_lshl_b32 s75, s75, 7\ns_add_u32 s1, s1, s75\n"
"s_add_u32 s2, s2, s75\ns_add_u32 m0, s4, 0x0\nqm v,a,a,188,80,104\nqr 32,17,0\nv_add_f32_dpp v24, v24, v24 row_ror:8 row_mask:0xf bank_mask:0xf\nqm v,a,a,156,72,104\n"
"qr 36,18,0\nv_add_f32_dpp v26, v26, v26 row_ror:8 row_mask:0xf bank_mask:0xf\nqm v,a,a,156,76,108\nv_add_f32_dpp v24, v24, v24 row_ror:4 row_mask:0xf bank_mask:0xf\nv_add_f32_dpp v26, v26, v26 row_ror:4 row_mask:0xf bank_mask:0xf\nqm v,a,a,124,68,108\n"
"v_lshrrev_b32 v253, 4, v23\nv_bfe_u32 v254, v23, 1, 3\nqm v,a,a,124,64,104\nqw 2\nv_lshl_add_u32 v253, v253, 8, v254\nv_and_b32 v254, 1, v23\n"
"qm v,a,a,128,64,112\nv_lshlrev_b32 v254, 7, v254\nv_lshl_add_u32 v253, v253, 13, v254\ns_lshl2_add_u32 s72, s21, s15\ns_lshl_b32 s72, s72, 16\ns_lshl_b32 s73, s20, 8\n"
"s_add_u32 s72, s72, s73\nv_add_u32 v253, s72, v253\nqm v,a,a,128,68,116\nqd 248,64,0,0\nv_mov_b32 v25, v24\nqm v,a,a,160,76,116\n"
"qt 120,4,49152\nqt 122,4,50176\nqt 124,4,57344\nqt 126,4,58368\nqr 40,17,2048\nqm v,a,a,160,72,112\n"
"qr 44,18,2048\nv_mov_b32 v27, v26\nqm v,a,a,192,80,112\nv_permlane16_swap_b32 v25, v24\nqm v,a,a,192,84,116\nv_permlane16_swap_b32 v27, v26\n"
"qm v,a,a,224,92,116\nv_add_f32 v24, v24, v25\nv_add_f32 v26, v26, v27\nqm v,a,a,224,88,112\nqw 2\nqd 249,64,0,1024\n"
"qm v,a,a,228,88,120\nqr 48,17,4096\nv_mov_b32 v25, v24\nqm v,a,a,228,92,124\nqt 96,5,49152\nv_mov_b32 v27, v26\n"
"qm v,a,a,196,84,124\nqt 98,5,50176\nqt 100,5,57344\nqt 102,5,58368\nqm v,a,a,196,80,120\nqr 52,18,4096\n"
"v_permlane32_swap_b32 v25, v24\nqm v,a,a,164,72,120\nqd 250,64,0,2048\nqm v,a,a,164,76,124\nv_permlane32_swap_b32 v27, v26\nqm v,a,a,132,68,124\n"
"qr 56,17,6144\nv_add_f32 v24, v24, v25\nqm v,a,a,132,64,120\nqw 2\nqr 60,18,6144\nv_add_f32 v26, v26, v27\n"
"qm v,a,a,136,64,96\nqd 251,64,0,3072\nqm v,a,a,136,68,100\ns_add_u32 m0, s4, 0x4000\nqt 104,6,49152\nv_mov_b32 v25, v26\n"
"qm v,a,a,168,76,100\nqt 106,6,50176\nqt 108,6,57344\nqt 110,6,58368\nqm v,a,a,168,72,96\nqt 112,7,49152\n"
"qm v,a,a,200,80,96\nds_write_b64 v37, v[24:25]\nqm v,a,a,200,84,100\nqt 114,7,50176\nqm v,a,a,232,92,100\nqt 116,7,57344\n"
"qt 118,7,58368\nqt 120,8,49152\nqt 122,8,50176\nqt 124,8,57344\nqt 126,8,58368\nqm v,a,a,232,88,96\n"
"qw 9\nqm v,a,a,236,88,104\nqd 248,68,0,0\nqm v,a,a,236,92,108\nqd 249,68,0,1024\nqm v,a,a,204,84,108\n"
"qm v,a,a,204,80,104\nqm v,a,a,172,72,104\nqw 0\nqv 13\ns_barrier\nqm v,a,a,172,76,108\n"
"ds_read_b64 v[26:27], v38\nqm v,a,a,140,68,108\nqd 250,68,0,2048\nqm v,a,a,140,64,104\nqt 96,9,0\nqm v,a,a,144,64,112\n"
"qt 98,9,1024\nqm v,a,a,144,68,116\nqt 100,9,8192\nqm v,a,a,176,76,116\nqd 251,68,0,3072\nqm v,a,a,176,72,112\n"
"s_lshl_b32 s75, s75, 12\ns_add_u32 s0, s0, s75\nqt 102,9,9216\nqm v,a,a,208,80,112\nqt 104,10,0\nqm v,a,a,208,84,116\n"
"qt 106,10,1024\nqm v,a,a,240,92,116\nqt 108,10,8192\nqm v,a,a,240,88,112\nqt 110,10,9216\nqm v,a,a,244,88,120\n"
"qw 0\nv_add_f32 v24, v24, v26\nv_add_f32 v26, v25, v27\nqm v,a,a,244,92,124\nv_mul_f32 v24, 0xba800000, v24\nv_mov_b32 v25, 0x3727c5ac\n"
"qm v,a,a,212,84,124\nv_fmac_f32 v25, 0x3a800000, v26\nv_fma_f32 v25, -v24, v24, v25\nqm v,a,a,212,80,120\nqt 112,11,0\nv_max_f32 v25, 0x3727c5ac, v25\n"
"qm v,a,a,180,72,120\nv_rsq_f32 v26, v25\nqm v,a,a,180,76,124\ns_bfm_b64 exec, 16, 0\nglobal_load_dword a255, v253, s[40:41]\ns_mov_b64 exec, -1\n"
"s_add_u32 m0, s3, 0x9000\nqm v,a,a,148,68,124\nv_mov_b32 v25, 0x3e0293ee\nv_mul_f32 v26, v25, v26\nqm v,a,a,148,64,120\nv_mul_f32 v24, v26, v24\n"
"qa 28,128\nqm a,a,a,0,96,32\nqd 22,60,83,3072\nqm a,a,a,0,100,36\ns_add_u32 m0, s3, 0x0\nv_lshlrev_b32 v29, 16, v28\n"
"v_and_b32 v30, 0xffff0000, v28\nqm a,a,a,4,100,44\nv_fma_f32 v29, v29, v26, v24\nv_fma_f32 v30, v30, v26, v24\nqm a,a,a,4,96,40\nqc 28,29\n"
"qb 128,v28\nqm a,a,a,8,96,48\nqd 19,60,1,0\nqm a,a,a,8,100,52\nqt 114,11,1024\nqa 31,129\n"
"qm a,a,a,12,100,60\nqt 116,11,8192\nv_lshlrev_b32 v32, 16, v31\nqm a,a,a,12,96,56\nqt 118,11,9216\nv_and_b32 v33, 0xffff0000, v31\n"
"qm a,a,a,28,104,56\nv_fma_f32 v32, v32, v26, v24\nv_fma_f32 v33, v33, v26, v24\nqm a,a,a,28,108,60\nqc 31,32\nqb 129,v31\n"
"qm a,a,a,24,108,52\nqd 20,60,1,1024\nqm a,a,a,24,104,48\nqa 34,130\nv_lshlrev_b32 v35, 16, v34\nqm a,a,a,20,104,40\n"
"qt 120,12,0\nv_and_b32 v36, 0xffff0000, v34\nqm a,a,a,20,108,44\nqt 122,12,1024\nv_fma_f32 v35, v35, v26, v24\nqm a,a,a,16,108,36\n"
"qt 124,12,8192\nv_fma_f32 v36, v36, v26, v24\nqm a,a,a,16,104,32\nqw 3\nqt 126,12,9216\nqc 34,35\n"
"qm a,a,a,160,112,32\nqd 21,60,1,2048\nqm a,a,a,160,116,36\nqb 130,v34\nqa 28,131\nqm a,a,a,164,116,44\n"
"v_lshlrev_b32 v29, 16, v28\nv_and_b32 v30, 0xffff0000, v28\nqm a,a,a,164,112,40\nqt 96,13,0\nv_fma_f32 v29, v29, v26, v24\nqm a,a,a,168,112,48\n"
"qt 98,13,1024\nv_fma_f32 v30, v30, v26, v24\nqm a,a,a,168,116,52\nqt 100,13,8192\nqc 28,29\nqm a,a,a,172,116,60\n"
"qt 102,13,9216\nqb 131,v28\nqm a,a,a,172,112,56\nqw 4\nqa 31,132\nv_lshlrev_b32 v32, 16, v31\n"
"qm v,a,a,52,120,56\nqd 22,60,1,3072\nqm v,a,a,52,124,60\ns_add_u32 m0, s3, 0x1000\nv_and_b32 v33, 0xffff0000, v31\nv_fma_f32 v32, v32, v26, v24\n"
"qm v,a,a,48,124,52\nqt 104,14,0\nv_fma_f32 v33, v33, v26, v24\nqm v,a,a,48,120,48\nqd 19,60,2,0\nqm v,a,a,44,120,40\n"
"qt 106,14,1024\nqc 31,32\nqm v,a,a,44,124,44\nqt 108,14,8192\nqb 132,v31\nqm v,a,a,40,124,36\n"
"qt 110,14,9216\nqa 34,133\nqm v,a,a,40,120,32\nqw 4\nv_lshlrev_b32 v35, 16, v34\nv_and_b32 v36, 0xffff0000, v34\n"
"qm v,a,a,56,96,32\nv_fma_f32 v35, v35, v26, v24\nv_fma_f32 v36, v36, v26, v24\nqm v,a,a,56,100,36\nqd 20,60,2,1024\nqm v,a,a,60,100,44\n"
"qc 34,35\nqb 133,v34\nqm v,a,a,60,96,40\nqt 112,15,0\nqa 28,134\nqm v,a,a,64,96,48\n"
"qt 114,15,1024\nv_lshlrev_b32 v29, 16, v28\nqm v,a,a,64,100,52\nqt 116,15,8192\nv_and_b32 v30, 0xffff0000, v28\nqm v,a,a,68,100,60\n"
"qt 118,15,9216\nv_fma_f32 v29, v29, v26, v24\nqm v,a,a,68,96,56\nqw 4\nv_fma_f32 v30, v30, v26, v24\nqc 28,29\n"
"qm v,a,a,84,104,56\nqd 21,60,2,2048\nqm v,a,a,84,108,60\nqb 134,v28\nqa 31,135\nqm v,a,a,80,108,52\n"
"qt 120,16,0\nv_lshlrev_b32 v32, 16, v31\nqm v,a,a,80,104,48\nqt 122,16,1024\nv_and_b32 v33, 0xffff0000, v31\nqm v,a,a,76,104,40\n"
"qt 124,16,8192\nv_fma_f32 v32, v32, v26, v24\nqm v,a,a,76,108,44\nqt 126,16,9216\nv_fma_f32 v33, v33, v26, v24\nqm v,a,a,72,108,36\n"
"qd 22,60,2,3072\nqm v,a,a,72,104,32\nqw 4\nqc 31,32\nqb 135,v31\nqm v,a,a,88,112,32\n"
"qa 34,136\nv_lshlrev_b32 v35, 16, v34\nqm v,a,a,88,116,36\nqt 96,9,16384\nv_and_b32 v36, 0xffff0000, v34\nqm v,a,a,92,116,44\n"
"qt 98,9,17408\nv_fma_f32 v35, v35, v26, v24\nqm v,a,a,92,112,40\nqt 100,9,24576\nv_fma_f32 v36, v36, v26, v24\nqm v,a,a,96,112,48\n"
"qt 102,9,25600\nqc 34,35\nqm v,a,a,96,116,52\ns_lshr_b32 s75, 0x5636, s76\ns_and_b32 s75, s75, 15\ns_sub_u32 s75, s75, 4\n"
"s_lshl_b32 s75, s75, 7\ns_add_u32 s1, s1, s75\ns_add_u32 s2, s2, s75\ns_add_u32 m0, s4, 0x8000\nqm v,a,a,100,116,60\ns_getpc_b64 s[8:9]\n"
"CT2%=:\ns_add_u32 s8, s8, QCODE%=-CT2%=\ns_addc_u32 s9, s9, 0\nqm v,a,a,100,112,56\nqw 4\ns_lshl2_add_u32 s10, s19, s15\n"
"s_mul_i32 s10, s10, 256\nqm v,a,a,116,120,56\nv_lshl_add_u32 v254, v0, 3, s10\nqb 136,v34\nqm v,a,a,116,124,60\ns_mov_b32 s10, FINISH%=-QCODE%=\n"
"s_mov_b32 s11, 0x20000\nqa 28,137\nqm v,a,a,112,124,52\nv_lshlrev_b32 v29, 16, v28\nv_and_b32 v30, 0xffff0000, v28\nqm v,a,a,112,120,48\n"
"buffer_store_dwordx4 a[128:131], v255, s[56:59], s81 offen sc1\ns_add_u32 s81, s81, 0x1000\nqm v,a,a,108,120,40\nqt 104,10,16384\nv_fma_f32 v29, v29, v26, v24\nqm v,a,a,108,124,44\n"
"qt 106,10,17408\nv_fma_f32 v30, v30, v26, v24\nqm v,a,a,104,124,36\nqt 108,10,24576\nqc 28,29\nqm v,a,a,104,120,32\n"
"qw 3\nqt 110,10,25600\nqb 137,v28\nqm v,a,a,120,32,96\nqd 248,64,0,0\nqm v,a,a,120,36,100\n"
"qa 31,138\nv_lshlrev_b32 v32, 16, v31\nqm v,a,a,152,44,100\nv_and_b32 v33, 0xffff0000, v31\nv_fma_f32 v32, v32, v26, v24\nqm v,a,a,152,40,96\n"
"qd 249,64,0,1024\nqm v,a,a,184,48,96\nqt 112,11,16384\nv_fma_f32 v33, v33, v26, v24\nqm v,a,a,184,52,100\nqt 114,11,17408\n"
"qc 31,32\nqm v,a,a,216,60,100\nqt 116,11,24576\nqb 138,v31\nqm v,a,a,216,56,96\nqw 3\n"
"qt 118,11,25600\nqa 34,139\nqm v,a,a,220,56,104\nqv 11\nv_lshlrev_b32 v35, 16, v34\nv_and_b32 v36, 0xffff0000, v34\n"
"qm v,a,a,220,60,108\nqd 250,64,0,2048\nqm v,a,a,188,52,108\nv_fma_f32 v35, v35, v26, v24\nv_fma_f32 v36, v36, v26, v24\nqm v,a,a,188,48,104\n"
"qt 120,12,16384\nqc 34,35\nqm v,a,a,156,40,104\nqt 122,12,17408\nqb 139,v34\nqm v,a,a,156,44,108\n"
"qt 124,12,24576\nqa 28,140\nqm v,a,a,124,36,108\nqt 126,12,25600\nv_lshlrev_b32 v29, 16, v28\nqm v,a,a,124,32,104\n"
"qw 4\nv_and_b32 v30, 0xffff0000, v28\nv_fma_f32 v29, v29, v26, v24\nqm v,a,a,128,32,112\nv_fma_f32 v30, v30, v26, v24\nqc 28,29\n"
"qm v,a,a,128,36,116\nqb 140,v28\nqa 31,141\nqm v,a,a,160,44,116\nqt 96,13,16384\nv_lshlrev_b32 v32, 16, v31\n"
"qm v,a,a,160,40,112\nqt 98,13,17408\nv_and_b32 v33, 0xffff0000, v31\nqm v,a,a,192,48,112\nqt 100,13,24576\nv_fma_f32 v32, v32, v26, v24\n"
"qm v,a,a,192,52,116\nqt 102,13,25600\nv_fma_f32 v33, v33, v26, v24\nqm v,a,a,224,60,116\nbuffer_store_dwordx4 a[132:135], v255, s[56:59], s81 offen sc1\ns_add_u32 s81, s81, 0x1000\n"
"qm v,a,a,224,56,112\nqw 4\nqc 31,32\nqb 141,v31\nqm v,a,a,228,56,120\nqd 251,64,0,3072\n"
"qm v,a,a,228,60,124\ns_add_u32 m0, s4, 0xc000\nqa 34,142\nv_lshlrev_b32 v35, 16, v34\nqm v,a,a,196,52,124\nqt 104,14,16384\n"
"v_and_b32 v36, 0xffff0000, v34\nqm v,a,a,196,48,120\nqt 106,14,17408\nv_fma_f32 v35, v35, v26, v24\nqm v,a,a,164,40,120\nqt 108,14,24576\n"
"v_fma_f32 v36, v36, v26, v24\nqm v,a,a,164,44,124\nqt 110,14,25600\nqc 34,35\nqm v,a,a,132,36,124\nqd 248,68,0,0\n"
"qm v,a,a,132,32,120\nqw 4\nqr 64,17,32768\nqb 142,v34\nqm v,a,a,136,32,96\nqr 68,18,32768\n"
"qa 28,143\nqm v,a,a,136,36,100\nqr 72,17,34816\nv_lshlrev_b32 v29, 16, v28\nqm v,a,a,168,44,100\nqr 76,18,34816\n"
"v_and_b32 v30, 0xffff0000, v28\nqm v,a,a,168,40,96\nqr 80,17,36864\nv_fma_f32 v29, v29, v26, v24\nqm v,a,a,200,48,96\nqr 84,18,36864\n"
"qr 88,17,38912\nqr 92,18,38912\nqm v,a,a,200,52,100\nqt 112,15,16384\nv_fma_f32 v30, v30, v26, v24\nqm v,a,a,232,60,100\n"
"qt 114,15,17408\nqt 116,15,24576\nqt 118,15,25600\nqt 120,16,16384\nqt 122,16,17408\nqt 124,16,24576\n"
"qt 126,16,25600\nqm v,a,a,232,56,96\nqc 28,29\nqb 143,v28\nqm v,a,a,236,56,104\nqd 249,68,0,1024\n"
"qm v,a,a,236,60,108\nqa 24,160\nqa 25,161\nqm v,a,a,204,52,108\nqa 26,162\nqa 27,163\n"
"qm v,a,a,204,48,104\nqa 28,164\nqa 29,165\nqm v,a,a,172,40,104\nqw 0\ns_barrier\n"
"qm v,a,a,172,44,108\nqt 96,1,0\nqa 30,166\nqm v,a,a,140,36,108\nqt 98,1,1024\nqa 31,167\n"
"qm v,a,a,140,32,104\nqt 100,1,8192\nqa 32,168\nqm v,a,a,144,32,112\nbuffer_store_dwordx4 a[136:139], v255, s[56:59], s81 offen sc1\ns_add_u32 s81, s81, 0x1000\n"
"qm v,a,a,144,36,116\nqd 250,68,0,2048\nqm v,a,a,176,44,116\nqt 102,1,9216\nqa 33,169\nqm v,a,a,176,40,112\n"
"qt 104,2,0\nqa 34,170\nqm v,a,a,208,48,112\nqt 106,2,1024\nqa 35,171\nqm v,a,a,208,52,116\n"
"qt 108,2,8192\nqa 36,172\nqm v,a,a,240,60,116\nqt 110,2,9216\nqa 37,173\nqm v,a,a,240,56,112\n"
"qa 38,174\nqa 39,175\nqm v,a,a,244,56,120\nqd 251,68,0,3072\nqm v,a,a,244,60,124\ns_lshl_b32 s75, s75, 12\n"
"s_add_u32 s0, s0, s75\nqt 112,3,0\nqm v,a,a,212,52,124\nqt 114,3,1024\nqm v,a,a,212,48,120\nqt 116,3,8192\n"
"qm v,a,a,180,40,120\nqt 118,3,9216\nqm v,a,a,180,44,124\nqm v,a,a,148,36,124\nqm v,a,a,148,32,120\nqw 8\n"
"qt 120,4,0\nqm a,a,a,0,96,64\nqt 122,4,1024\nqm a,a,a,0,100,68\nqt 124,4,8192\nqm a,a,a,4,100,76\n"
"qt 126,4,9216\nqm a,a,a,4,96,72\ns_bfm_b64 exec, 2, 0\nbuffer_load_dword a255, v254, s[8:11], 0 offen\ns_mov_b64 exec, -1\nqm a,a,a,8,96,80\n"
"qm a,a,a,8,100,84\nqm a,a,a,12,100,92\nqm a,a,a,12,96,88\nqw 8\nqt 96,5,0\nqm a,a,a,28,104,88\n"
"qt 98,5,1024\nqm a,a,a,28,108,92\nqt 100,5,8192\nqm a,a,a,24,108,84\nqt 102,5,9216\nqm a,a,a,24,104,80\n"
"buffer_store_dwordx4 a[140:143], v255, s[56:59], s81 offen sc1\ns_add_u32 s81, s81, 0x1000\nqm a,a,a,20,104,72\nqm a,a,a,20,108,76\nqm a,a,a,16,108,68\nqm a,a,a,16,104,64\n"
"qw 8\nqt 104,6,0\nqm v,a,a,24,112,64\nqt 106,6,1024\nqm v,a,a,24,116,68\ns_bfm_b64 exec, 16, 16\n"
"global_load_dword a255, v253, s[40:41]\ns_mov_b64 exec, -1\ns_add_u32 m0, s3, 0x8000\nqm v,a,a,28,116,76\nqd 19,60,1,0\nqm v,a,a,28,112,72\n"
"qt 108,6,8192\nqm v,a,a,32,112,80\nqt 110,6,9216\nqm v,a,a,32,116,84\nqm v,a,a,36,116,92\nqm v,a,a,36,112,88\n"
"qw 8\nqt 112,7,0\nqm v,a,a,52,120,88\nqd 20,60,1,1024\nqm v,a,a,52,124,92\nqt 114,7,1024\n"
"qm v,a,a,48,124,84\nqt 116,7,8192\nqm v,a,a,48,120,80\nqt 118,7,9216\nqm v,a,a,44,120,72\nqm v,a,a,44,124,76\n"
"qm v,a,a,40,124,68\nqd 21,60,1,2048\nqm v,a,a,40,120,64\nqw 8\nqt 120,8,0\nqm v,a,a,56,96,64\n"
"qt 122,8,1024\nqm v,a,a,56,100,68\nqt 124,8,8192\nqm v,a,a,60,100,76\nqt 126,8,9216\nqm v,a,a,60,96,72\n"
"qm v,a,a,64,96,80\nqd 22,60,1,3072\nqm v,a,a,64,100,84\ns_add_u32 m0, s3, 0x9000\nqm v,a,a,68,100,92\nqm v,a,a,68,96,88\n"
"qw 8\nqt 96,1,16384\nqm v,a,a,84,104,88\nqt 98,1,17408\nqm v,a,a,84,108,92\nqt 100,1,24576\n"
"qm v,a,a,80,108,84\nqd 19,60,2,0\nqm v,a,a,80,104,80\nqt 102,1,25600\nqm v,a,a,76,104,72\nqm v,a,a,76,108,76\n"
"qm v,a,a,72,108,68\nqm v,a,a,72,104,64\nqw 8\nqt 104,2,16384\nqm v,a,a,88,112,64\nqd 20,60,2,1024\n"
"qm v,a,a,88,116,68\nqt 106,2,17408\nqm v,a,a,92,116,76\nqt 108,2,24576\nqm v,a,a,92,112,72\nqt 110,2,25600\n"
"qm v,a,a,96,112,80\nqm v,a,a,96,116,84\nqm v,a,a,100,116,92\nqd 21,60,2,2048\nqm v,a,a,100,112,88\nqw 8\n"
"qt 112,3,16384\nqm v,a,a,116,120,88\nqt 114,3,17408\nqm v,a,a,116,124,92\nqt 116,3,24576\nqm v,a,a,112,124,84\n"
"qt 118,3,25600\nqm v,a,a,112,120,80\nqm v,a,a,108,120,72\nqd 22,60,2,3072\nqm v,a,a,108,124,76\ns_lshl2_add_u32 s72, s14, s15\n"
"s_lshl3_add_u32 s34, s72, 0x1000\nv_mov_b32 v254, s16\nqm v,a,a,104,124,68\nv_mov_b32 v255, s17\nqm v,a,a,104,120,64\nqw 8\n"
"qv 9\nqt 120,4,16384\nqm v,a,a,120,64,96\nqt 122,4,17408\nqm v,a,a,120,68,100\ns_lshr_b32 s75, 0x6365, s76\n"
"s_and_b32 s75, s75, 15\ns_sub_u32 s75, s75, 4\ns_lshl_b32 s75, s75, 7\ns_add_u32 s1, s1, s75\ns_add_u32 s2, s2, s75\ns_add_u32 m0, s4, 0x10000\n"
"qm v,a,a,152,76,100\nqt 124,4,24576\nqm v,a,a,152,72,96\nqt 126,4,25600\nqm v,a,a,184,80,96\nqm v,a,a,184,84,100\n"
"qm v,a,a,216,92,100\nqm v,a,a,216,88,96\nqw 8\nqt 96,5,16384\nqm v,a,a,220,88,104\ns_bfm_b64 exec, 16, 32\n"
"global_load_dword a255, v253, s[40:41]\ns_mov_b64 exec, -1\nqm v,a,a,220,92,108\nqt 98,5,17408\nqm v,a,a,188,84,108\nqd 248,64,0,0\n"
"qm v,a,a,188,80,104\nqt 100,5,24576\nqm v,a,a,156,72,104\nqt 102,5,25600\nqm v,a,a,156,76,108\nqr 32,17,0\n"
"qm v,a,a,124,68,108\nqd 249,64,0,1024\nqm v,a,a,124,64,104\nqw 9\nqt 104,6,16384\nqm v,a,a,128,64,112\n"
"qt 106,6,17408\nqm v,a,a,128,68,116\nqt 108,6,24576\nqm v,a,a,160,76,116\nqt 110,6,25600\nqm v,a,a,160,72,112\n"
"qr 36,18,0\nqm v,a,a,192,80,112\nqr 40,17,2048\nqm v,a,a,192,84,116\nqr 44,18,2048\nqm v,a,a,224,92,116\n"
"qr 48,17,4096\nqm v,a,a,224,88,112\nqw 13\nqr 52,18,4096\nqm v,a,a,228,88,120\nqr 56,17,6144\n"
"qm v,a,a,228,92,124\nqr 60,18,6144\nqm v,a,a,196,84,124\ns_mov_b64 exec, 1\nbuffer_store_dwordx2 v[254:255], off, s[56:59], s34 sc1\ns_mov_b64 exec, -1\n"
"qm v,a,a,196,80,120\nqd 250,64,0,2048\nqm v,a,a,164,72,120\nqt 112,7,16384\nqm v,a,a,164,76,124\nqt 114,7,17408\n"
"qm v,a,a,132,68,124\nqt 116,7,24576\nqm v,a,a,132,64,120\nqt 118,7,25600\nqm v,a,a,136,64,96\nqt 120,8,16384\n"
"qm v,a,a,136,68,100\nqt 122,8,17408\nqm v,a,a,168,76,100\nqt 124,8,24576\nqm v,a,a,168,72,96\nqt 126,8,25600\n"
"qm v,a,a,200,80,96\nqd 251,64,0,3072\nqm v,a,a,200,84,100\ns_add_u32 m0, s4, 0x14000\nqm v,a,a,232,92,100\nqm v,a,a,232,88,96\n"
"qm v,a,a,236,88,104\nqd 248,68,0,0\nqm v,a,a,236,92,108\nqm v,a,a,204,84,108\nqm v,a,a,204,80,104\nqm v,a,a,172,72,104\n"
"qw 0\ns_barrier\nqm v,a,a,172,76,108\nqd 249,68,0,1024\nqm v,a,a,140,68,108\nqt 96,1,32768\n"
"qm v,a,a,140,64,104\nqt 98,1,33792\nqm v,a,a,144,64,112\nqt 100,1,40960\nqm v,a,a,144,68,116\nqt 102,1,41984\n"
"qm v,a,a,176,76,116\nqt 104,2,32768\nqm v,a,a,176,72,112\nqd 250,68,0,2048\nqm v,a,a,208,80,112\nqt 106,2,33792\n"
"qm v,a,a,208,84,116\nqt 108,2,40960\nqm v,a,a,240,92,116\nqt 110,2,41984\nqm v,a,a,240,88,112\nqt 112,3,32768\n"
"qm v,a,a,244,88,120\nqt 114,3,33792\nqm v,a,a,244,92,124\nqd 251,68,0,3072\nqm v,a,a,212,84,124\ns_lshl_b32 s75, s75, 12\n"
"s_add_u32 s0, s0, s75\nqt 116,3,40960\nqm v,a,a,212,80,120\nqt 118,3,41984\nqm v,a,a,180,72,120\nqm v,a,a,180,76,124\n"
"qm v,a,a,148,68,124\nqm v,a,a,148,64,120\nqw 8\nqt 120,4,32768\nqm a,a,a,0,96,32\nqt 122,4,33792\n"
"qm a,a,a,0,100,36\nqt 124,4,40960\nqm a,a,a,4,100,44\nqt 126,4,41984\nqm a,a,a,4,96,40\nqm a,a,a,8,96,48\n"
"s_bfm_b64 exec, 16, 48\nglobal_load_dword a255, v253, s[40:41]\ns_mov_b64 exec, -1\ns_add_u32 m0, s3, 0x0\nqm a,a,a,8,100,52\nqd 19,60,1,0\n"
"qm a,a,a,12,100,60\nqm a,a,a,12,96,56\nqw 8\nqt 96,5,32768\nqm a,a,a,28,104,56\nqt 98,5,33792\n"
"qm a,a,a,28,108,60\nqt 100,5,40960\nqm a,a,a,24,108,52\nqd 20,60,1,1024\nqm a,a,a,24,104,48\nqt 102,5,41984\n"
"qm a,a,a,20,104,40\nqm a,a,a,20,108,44\nqm a,a,a,16,108,36\nqm a,a,a,16,104,32\nqw 8\nqt 104,6,32768\n"
"qm v,a,a,24,112,32\nqd 21,60,1,2048\nqm v,a,a,24,116,36\nqt 106,6,33792\nqm v,a,a,28,116,44\nqt 108,6,40960\n"
"qm v,a,a,28,112,40\nqt 110,6,41984\nqm v,a,a,32,112,48\nqm v,a,a,32,116,52\nqm v,a,a,36,116,60\nqd 22,60,1,3072\n"
"qm v,a,a,36,112,56\nqw 8\ns_add_u32 m0, s3, 0x1000\nqt 112,7,32768\nqm v,a,a,52,120,56\nqt 114,7,33792\n"
"qm v,a,a,52,124,60\nqt 116,7,40960\nqm v,a,a,48,124,52\nqt 118,7,41984\nqm v,a,a,48,120,48\nqm v,a,a,44,120,40\n"
"qd 19,60,2,0\nqm v,a,a,44,124,44\nqm v,a,a,40,124,36\nqm v,a,a,40,120,32\nqw 8\nqt 120,8,32768\n"
"qm v,a,a,56,96,32\nqt 122,8,33792\nqm v,a,a,56,100,36\nqt 124,8,40960\nqm v,a,a,60,100,44\nqd 20,60,2,1024\n"
"qm v,a,a,60,96,40\nqt 126,8,41984\nqm v,a,a,64,96,48\nqm v,a,a,64,100,52\nqm v,a,a,68,100,60\nqm v,a,a,68,96,56\n"
"qw 8\nqt 96,1,49152\nqm v,a,a,84,104,56\nqd 21,60,2,2048\nqm v,a,a,84,108,60\ns_mov_b32 s72, s15\n"
"s_and_b32 s73, s20, 1\nqt 98,1,50176\nqm v,a,a,80,108,52\ns_mul_i32 s73, s73, 15\ns_xor_b32 s73, s73, s21\nqt 100,1,57344\n"
"qm v,a,a,80,104,48\ns_lshl3_add_u32 s88, s73, s72\ns_xor_b32 s73, s73, 15\nqt 102,1,58368\nqm v,a,a,76,104,40\ns_lshl3_add_u32 s97, s73, s72\n"
"s_lshr_b32 s74, 0x0, s76\ns_and_b32 s74, s74, 15\nqm v,a,a,76,108,44\nqd 22,60,2,3072\nqm v,a,a,72,108,36\ns_xor_b32 s81, s88, 0\n"
"s_lshl_b32 s81, s81, 15\ns_add_u32 s81, s81, 0x100000\nqm v,a,a,72,104,32\nqw 8\ns_lshl_b32 s72, s74, 12\nqt 104,2,49152\n"
"qm v,a,a,88,112,32\ns_add_u32 s81, s81, s72\ns_xor_b32 s82, s88, 7\nqt 106,2,50176\nqm v,a,a,88,116,36\ns_lshl_b32 s82, s82, 15\n"
"s_add_u32 s82, s82, 0x100000\nqt 108,2,57344\nqm v,a,a,92,116,44\ns_lshr_b32 s75, 0x3655, s76\ns_and_b32 s75, s75, 15\ns_sub_u32 s75, s75, 4\n"
"s_lshl_b32 s75, s75, 7\ns_add_u32 s1, s1, s75\ns_add_u32 s2, s2, s75\ns_add_u32 m0, s4, 0x0\nqm v,a,a,92,112,40\nqd 248,64,0,0\n"
"qm v,a,a,96,112,48\ns_lshl_b32 s72, s74, 12\ns_add_u32 s82, s82, s72\nqt 110,2,58368\nqm v,a,a,96,116,52\ns_xor_b32 s83, s97, 0\n"
"s_lshl_b32 s83, s83, 15\ns_add_u32 s83, s83, 0x100000\nqm v,a,a,100,116,60\ns_lshl_b32 s72, s74, 12\ns_add_u32 s83, s83, s72\ns_xor_b32 s84, s97, 7\n"
"qm v,a,a,100,112,56\nqw 8\ns_lshl_b32 s84, s84, 15\nqt 112,3,49152\nqm v,a,a,116,120,56\ns_add_u32 s84, s84, 0x100000\n"
"s_lshl_b32 s72, s74, 12\nqt 114,3,50176\nqm v,a,a,116,124,60\nqd 249,64,0,1024\nqm v,a,a,112,124,52\ns_add_u32 s84, s84, s72\n"
"qt 116,3,57344\nqm v,a,a,112,120,48\nqt 118,3,58368\nv_bfe_u32 v253, v23, 4, 1\nqm v,a,a,108,120,40\ns_sub_u32 s72, s97, s88\n"
"v_mul_i32_i24 v252, s72, v253\nv_add_u32 v252, s88, v252\nqm v,a,a,108,124,44\nv_bfe_u32 v253, v23, 3, 1\nv_mul_u32_u24 v253, 7, v253\nqm v,a,a,104,124,36\n"
"v_xor_b32 v252, v252, v253\nv_and_b32 v253, 7, v23\nqm v,a,a,104,120,32\nqw 8\nqt 120,4,49152\nv_lshl_add_u32 v252, v252, 3, v253\n"
"qm v,a,a,120,32,96\nqd 250,64,0,2048\nqm v,a,a,120,36,100\nqt 122,4,50176\nv_lshlrev_b32 v252, 3, v252\nqm v,a,a,152,44,100\n"
"s_mov_b32 s34, 0x1000\nqt 124,4,57344\nqm v,a,a,152,40,96\nqt 126,4,58368\nqm v,a,a,184,48,96\nqm v,a,a,184,52,100\n"
"qd 251,64,0,3072\nqm v,a,a,216,60,100\ns_add_u32 m0, s4, 0x4000\nqm v,a,a,216,56,96\nqw 8\nqt 96,5,49152\n"
"qm v,a,a,220,56,104\nqv 23\nqt 98,5,50176\nqm v,a,a,220,60,108\nqt 100,5,57344\nqm v,a,a,188,52,108\n"
"qt 102,5,58368\nqm v,a,a,188,48,104\nqd 248,68,0,0\nqm v,a,a,156,40,104\nqr 64,17,32768\nqm v,a,a,156,44,108\n"
"qr 68,18,32768\nqm v,a,a,124,36,108\nqr 72,17,34816\nqm v,a,a,124,32,104\nqw 11\nqt 104,6,49152\n"
"qm v,a,a,128,32,112\nqd 249,68,0,1024\nqm v,a,a,128,36,116\nqt 106,6,50176\nqm v,a,a,160,44,116\nqt 108,6,57344\n"
"qm v,a,a,160,40,112\nqt 110,6,58368\nqm v,a,a,192,48,112\nqr 76,18,34816\nqm v,a,a,192,52,116\nqr 80,17,36864\n"
"qm v,a,a,224,60,116\nqd 250,68,0,2048\nqm v,a,a,224,56,112\nqw 13\nqr 84,18,36864\nqm v,a,a,228,56,120\n"
"qr 88,17,38912\nqm v,a,a,228,60,124\nqr 92,18,38912\nqm v,a,a,196,52,124\nqt 112,7,49152\nqm v,a,a,196,48,120\n"
"qt 114,7,50176\nqm v,a,a,164,40,120\nqd 251,68,0,3072\nqm v,a,a,164,44,124\ns_lshl_b32 s75, s75, 12\ns_add_u32 s0, s0, s75\n"
"qt 116,7,57344\nqm v,a,a,132,36,124\nqt 118,7,58368\nqm v,a,a,132,32,120\nqt 120,8,49152\nqm v,a,a,136,32,96\n"
"qt 122,8,50176\nqm v,a,a,136,36,100\nqt 124,8,57344\nqm v,a,a,168,44,100\nqt 126,8,58368\nqm v,a,a,168,40,96\n"
"qm v,a,a,200,48,96\nqm v,a,a,200,52,100\nqm v,a,a,232,60,100\nqm v,a,a,232,56,96\nqw 13\nqm v,a,a,236,56,104\n"
"qm v,a,a,236,60,108\nqm v,a,a,204,52,108\nqm v,a,a,204,48,104\nqm v,a,a,172,40,104\nqw 0\nqv 17\n"
"s_barrier\nqm v,a,a,172,44,108\nqt 96,9,0\nqm v,a,a,140,36,108\nqt 98,9,1024\nqm v,a,a,140,32,104\n"
"qt 100,9,8192\nqm v,a,a,144,32,112\nqt 102,9,9216\nqm v,a,a,144,36,116\nqt 104,10,0\nqm v,a,a,176,44,116\n"
"qt 106,10,1024\nqm v,a,a,176,40,112\nqt 108,10,8192\nqm v,a,a,208,48,112\nqt 110,10,9216\nqm v,a,a,208,52,116\n"
"qm v,a,a,240,60,116\nqm v,a,a,240,56,112\nqt 112,11,0\nqm v,a,a,244,56,120\nqt 114,11,1024\nqm v,a,a,244,60,124\n"
"qt 116,11,8192\nqm v,a,a,212,52,124\nqt 118,11,9216\nqm v,a,a,212,48,120\nqm v,a,a,180,40,120\nqm v,a,a,180,44,124\n"
"qm v,a,a,148,36,124\nqm v,a,a,148,32,120\nqw 8\nqt 120,12,0\nqm a,a,a,0,96,64\nqt 122,12,1024\n"
"qm a,a,a,0,100,68\nqt 124,12,8192\nqm a,a,a,4,100,76\nqt 126,12,9216\nqm a,a,a,4,96,72\nqm a,a,a,8,96,80\n"
"qm a,a,a,8,100,84\nqm a,a,a,12,100,92\nqm a,a,a,12,96,88\nqw 8\nqt 96,13,0\nqm a,a,a,28,104,88\n"
"qt 98,13,1024\nqm a,a,a,28,108,92\nqt 100,13,8192\nqm a,a,a,24,108,84\nbuffer_load_dwordx2 v[254:255], v252, s[56:59], s34 offen sc1\nqm a,a,a,24,104,80\n"
"s_add_u32 m0, s3, 0x8000\nqt 102,13,9216\nqm a,a,a,20,104,72\nqm a,a,a,20,108,76\nqm a,a,a,16,108,68\nqd 19,60,1,0\n"
"qm a,a,a,16,104,64\nqw 8\nqt 104,14,0\nqm v,a,a,24,112,64\nqt 106,14,1024\nqm v,a,a,24,116,68\n"
"qt 108,14,8192\nqm v,a,a,28,116,76\nqt 110,14,9216\nqm v,a,a,28,112,72\nqd 20,60,1,1024\nqm v,a,a,32,112,80\n"
"qm v,a,a,32,116,84\nqm v,a,a,36,116,92\nqm v,a,a,36,112,88\nqw 8\nqt 112,15,0\nqm v,a,a,52,120,88\n"
"qd 21,60,1,2048\nqm v,a,a,52,124,92\nqt 114,15,1024\nqm v,a,a,48,124,84\nqt 116,15,8192\nqm v,a,a,48,120,80\n"
"qt 118,15,9216\nqm v,a,a,44,120,72\nqm v,a,a,44,124,76\nqd 22,60,1,3072\nqm v,a,a,40,124,68\ns_add_u32 m0, s3, 0x9000\n"
"qm v,a,a,40,120,64\nqw 8\nqt 120,16,0\nqm v,a,a,56,96,64\nqt 122,16,1024\nqm v,a,a,56,100,68\n"
"qt 124,16,8192\nqm v,a,a,60,100,76\nqd 19,60,2,0\nqm v,a,a,60,96,72\nqt 126,16,9216\nqm v,a,a,64,96,80\n"
"qm v,a,a,64,100,84\nqm v,a,a,68,100,92\nqm v,a,a,68,96,88\nqw 8\nqt 96,9,16384\nqm v,a,a,84,104,88\n"
"qd 20,60,2,1024\nqm v,a,a,84,108,92\nqt 98,9,17408\nqm v,a,a,80,108,84\nqt 100,9,24576\nqm v,a,a,80,104,80\n"
"qt 102,9,25600\nqm v,a,a,76,104,72\nqd 21,60,2,2048\nqm v,a,a,76,108,76\nqm v,a,a,72,108,68\nqm v,a,a,72,104,64\n"
"qw 8\nqt 104,10,16384\nqm v,a,a,88,112,64\nqt 106,10,17408\nqm v,a,a,88,116,68\nqd 22,60,2,3072\n"
"qm v,a,a,92,116,76\ns_add_u32 m0, s4, 0x8000\nqt 108,10,24576\nqm v,a,a,92,112,72\nqt 110,10,25600\nqm v,a,a,96,112,80\n"
"qm v,a,a,96,116,84\nqd 248,64,0,0\nqm v,a,a,100,116,92\nqm v,a,a,100,112,88\nqw 8\nqt 112,11,16384\n"
"qm v,a,a,116,120,88\nqt 114,11,17408\nqm v,a,a,116,124,92\nqt 116,11,24576\nqm v,a,a,112,124,84\nqd 249,64,0,1024\n"
"qm v,a,a,112,120,80\nqt 118,11,25600\nqm v,a,a,108,120,72\nqm v,a,a,108,124,76\nqm v,a,a,104,124,68\nqm v,a,a,104,120,64\n"
"qw 8\nqt 120,12,16384\nqm v,a,a,120,64,96\nqd 250,64,0,2048\nqm v,a,a,120,68,100\nqt 122,12,17408\n"
"qm v,a,a,152,76,100\nqt 124,12,24576\nqm v,a,a,152,72,96\nqt 126,12,25600\nqm v,a,a,184,80,96\nqd 251,64,0,3072\n"
"qm v,a,a,184,84,100\ns_add_u32 m0, s4, 0xc000\nqm v,a,a,216,92,100\nqm v,a,a,216,88,96\nqw 8\nqt 96,13,16384\n"
"qm v,a,a,220,88,104\nqv 21\nqt 98,13,17408\nqm v,a,a,220,92,108\nqd 248,68,0,0\nqm v,a,a,188,84,108\n"
"qt 100,13,24576\nqm v,a,a,188,80,104\nqt 102,13,25600\nqm v,a,a,156,72,104\nqr 32,17,0\nqm v,a,a,156,76,108\n"
"qr 36,18,0\nqm v,a,a,124,68,108\nqd 249,68,0,1024\nqm v,a,a,124,64,104\nqw 10\nqt 104,14,16384\n"
"qm v,a,a,128,64,112\nqt 106,14,17408\nqm v,a,a,128,68,116\nqt 108,14,24576\nqm v,a,a,160,76,116\nqt 110,14,25600\n"
"qm v,a,a,160,72,112\nqd 250,68,0,2048\nqm v,a,a,192,80,112\nqv 15\nqr 40,17,2048\nv_cmp_eq_u64 vcc, s[16:17], v[254:255]\n"
"qm v,a,a,192,84,116\nqr 44,18,2048\nqm v,a,a,224,92,116\nqr 48,17,4096\nqm v,a,a,224,88,112\nqw 13\n"
"qr 52,18,4096\nqm v,a,a,228,88,120\ns_cmp_eq_u64 vcc, exec\ns_cbranch_scc0 PXN_o%=\nPXN_r%=:\nqr 56,17,6144\n"
"qm v,a,a,228,92,124\nqd 251,68,0,3072\nqm v,a,a,196,84,124\ns_mov_b32 s60, s40\ns_and_b32 s61, s41, 0xffff\ns_mov_b32 s62, 0x800000\n"
"qm v,a,a,196,80,120\ns_lshl_b32 s85, s15, 18\ns_lshl_b32 s72, s20, 8\ns_add_u32 s85, s85, s72\nqm v,a,a,164,72,120\ns_lshl_b32 s72, s74, 20\n"
"s_add_u32 s85, s85, s72\ns_add_u32 s86, s85, 0x20000\ns_lshl_b32 s87, s15, 13\ns_add_u32 s87, s87, 0x10000\ns_lshl_b32 s72, s20, 3\ns_add_u32 s72, s72, s21\n"
"s_lshl2_add_u32 s73, s72, s15\ns_lshl_b32 s72, s73, 14\ns_add_u32 s5, s72, 0x800000\ns_add_u32 s6, s72, 0x801000\ns_add_u32 s7, s72, 0x802000\ns_add_u32 s8, s72, 0x803000\n"
"s_add_u32 s9, s72, 0x1800000\ns_add_u32 s10, s72, 0x1801000\ns_add_u32 s11, s72, 0x1802000\ns_add_u32 s12, s72, 0x1803000\ns_lshl2_add_u32 s13, s73, 0xf000\nqm v,a,a,164,76,124\n"
"ql 128,0,56,81,0\nqm v,a,a,132,68,124\nqr 60,18,6144\nqm v,a,a,132,64,120\nqw 12\nqt 112,15,16384\n"
"qm v,a,a,136,64,96\nqt 114,15,17408\nqm v,a,a,136,68,100\nqt 116,15,24576\nqm v,a,a,168,76,100\nqt 118,15,25600\n"
"qm v,a,a,168,72,96\nqt 120,16,16384\nqm v,a,a,200,80,96\nqt 122,16,17408\nqm v,a,a,200,84,100\nqt 124,16,24576\n"
"qm v,a,a,232,92,100\nqt 126,16,25600\nqm v,a,a,232,88,96\nqw 14\nqm v,a,a,236,88,104\nql 132,0,56,81,1024\n"
"qm v,a,a,236,92,108\nql 136,0,56,82,0\nqm v,a,a,204,84,108\nqm v,a,a,204,80,104\nqm v,a,a,172,72,104\nqw 0\n"
"s_barrier\nqm v,a,a,172,76,108\nql 140,0,56,82,1024\nqm v,a,a,140,68,108\nqt 96,1,0\nqm v,a,a,140,64,104\n"
"qt 98,1,1024\nqm v,a,a,144,64,112\nqt 100,1,8192\nqm v,a,a,144,68,116\nqt 102,1,9216\nqm v,a,a,176,76,116\n"
"ql 144,0,56,83,0\nqm v,a,a,176,72,112\nqt 104,2,0\nqm v,a,a,208,80,112\nqt 106,2,1024\nqm v,a,a,208,84,116\n"
"qt 108,2,8192\nqm v,a,a,240,92,116\nqt 110,2,9216\nqm v,a,a,240,88,112\nqt 112,3,0\nqm v,a,a,244,88,120\n"
"ql 148,0,56,83,1024\nqm v,a,a,244,92,124\nqt 114,3,1024\nqm v,a,a,212,84,124\nqt 116,3,8192\nqm v,a,a,212,80,120\n"
"qt 118,3,9216\nqm v,a,a,180,72,120\nqm v,a,a,180,76,124\nql 152,0,56,84,0\nqm v,a,a,148,68,124\nqm v,a,a,148,64,120\n"
"qw 8\nqt 120,4,0\nqm a,a,a,0,96,32\nQCODE%=:\nqt 122,4,1024\nqm a,a,a,0,100,36\n"
"qt 124,4,8192\nqm a,a,a,4,100,44\nql 156,0,56,84,1024\nqm a,a,a,4,96,40\nqt 126,4,9216\nqm a,a,a,8,96,48\n"
"qm a,a,a,8,100,52\nqm a,a,a,12,100,60\nqm a,a,a,12,96,56\nqw 8\nqt 96,5,0\nqm a,a,a,28,104,56\n"
"ql 160,0,56,81,2048\nqm a,a,a,28,108,60\nqt 98,5,1024\nqm a,a,a,24,108,52\nqt 100,5,8192\nqm a,a,a,24,104,48\n"
"qt 102,5,9216\nqm a,a,a,20,104,40\nqm a,a,a,20,108,44\nql 164,0,56,81,3072\nqm a,a,a,16,108,36\nqm a,a,a,16,104,32\n"
"qw 8\nqt 104,6,0\nqm v,a,a,24,112,32\nqv 18\nqt 106,6,1024\nqm v,a,a,24,116,36\n"
"qt 108,6,8192\nqm v,a,a,28,116,44\nql 168,0,56,82,2048\nqm v,a,a,28,112,40\nqt 110,6,9216\nqm v,a,a,32,112,48\n"
"qr 64,17,32768\nqm v,a,a,32,116,52\nqr 68,18,32768\nqm v,a,a,36,116,60\nqr 72,17,34816\nqm v,a,a,36,112,56\n"
"qw 11\nqr 76,18,34816\nqm v,a,a,52,120,56\nql 172,0,56,82,3072\nqm v,a,a,52,124,60\nqr 80,17,36864\n"
"qm v,a,a,48,124,52\nqr 84,18,36864\nqm v,a,a,48,120,48\nqr 88,17,38912\nqm v,a,a,44,120,40\nqr 92,18,38912\n"
"qm v,a,a,44,124,44\nql 176,0,56,83,2048\nqm v,a,a,40,124,36\nqt 112,7,0\nqm v,a,a,40,120,32\nqw 13\n"
"qt 114,7,1024\nqm v,a,a,56,96,32\nqt 116,7,8192\nqm v,a,a,56,100,36\nqt 118,7,9216\nqm v,a,a,60,100,44\n"
"qt 120,8,0\nqm v,a,a,60,96,40\nqt 122,8,1024\nqm v,a,a,64,96,48\nqt 124,8,8192\nqm v,a,a,64,100,52\n"
"qt 126,8,9216\nqm v,a,a,68,100,60\nql 180,0,56,83,3072\nqm v,a,a,68,96,56\nqm v,a,a,84,104,56\nql 184,0,56,84,2048\n"
"qm v,a,a,84,108,60\nqm v,a,a,80,108,52\nqm v,a,a,80,104,48\nqm v,a,a,76,104,40\nqw 0\nqv 15\n"
"s_barrier\nqm v,a,a,76,108,44\nql 188,0,56,84,3072\nqm v,a,a,72,108,36\ns_mov_b32 s75, 0x1000\ns_add_u32 s81, s81, s75\n"
"s_add_u32 s82, s82, s75\nqm v,a,a,72,104,32\ns_add_u32 s83, s83, s75\ns_add_u32 s84, s84, s75\nqm v,a,a,88,112,32\nqt 96,1,32768\n"
"qm v,a,a,88,116,36\nqt 98,1,33792\nqm v,a,a,92,116,44\nqt 100,1,40960\nqm v,a,a,92,112,40\nqt 102,1,41984\n"
"qm v,a,a,96,112,48\nqt 104,2,32768\nqm v,a,a,96,116,52\nqt 106,2,33792\nqm v,a,a,100,116,60\nqt 108,2,40960\n"
"qm v,a,a,100,112,56\nqt 110,2,41984\nqm v,a,a,116,120,56\ns_add_u32 m0, s87, 0x0\nqt 112,3,32768\nqm v,a,a,116,124,60\n"
"qd 248,60,85,0\nqm v,a,a,112,124,52\nqt 114,3,33792\nqm v,a,a,112,120,48\nqt 116,3,40960\nqm v,a,a,108,120,40\n"
"qt 118,3,41984\nqm v,a,a,108,124,44\nqd 249,60,85,1024\nqm v,a,a,104,124,36\nqm v,a,a,104,120,32\nqw 8\n"
"qt 120,4,32768\nqm a,a,a,0,96,64\nqt 122,4,33792\nqm a,a,a,0,100,68\nqt 124,4,40960\nqm a,a,a,4,100,76\n"
"qd 250,60,85,2048\nqm a,a,a,4,96,72\nqt 126,4,41984\nqm a,a,a,8,96,80\nqm a,a,a,8,100,84\nqm a,a,a,12,100,92\n"
"qm a,a,a,12,96,88\nqw 8\nqt 96,5,32768\nqm a,a,a,28,104,88\nqd 251,60,85,3072\nqm a,a,a,28,108,92\n"
"s_add_u32 m0, s87, 0x1000\nqt 98,5,33792\nqm a,a,a,24,108,84\nqt 100,5,40960\nqm a,a,a,24,104,80\nqt 102,5,41984\n"
"qm a,a,a,20,104,72\nqd 248,60,86,0\nqm a,a,a,20,108,76\nqm a,a,a,16,108,68\nqm a,a,a,16,104,64\nqw 8\n"
"qt 104,6,32768\nqm v,a,a,24,112,64\nqt 106,6,33792\nqm v,a,a,24,116,68\nqt 108,6,40960\nqm v,a,a,28,116,76\n"
"qd 249,60,86,1024\nqm v,a,a,28,112,72\nqt 110,6,41984\nqm v,a,a,32,112,80\nqm v,a,a,32,116,84\nqm v,a,a,36,116,92\n"
"qm v,a,a,36,112,88\nqw 8\nqt 112,7,32768\nqm v,a,a,52,120,88\nqd 250,60,86,2048\nqm v,a,a,52,124,92\n"
"qt 114,7,33792\nqm v,a,a,48,124,84\nqt 116,7,40960\nqm v,a,a,48,120,80\nqt 118,7,41984\nqm v,a,a,44,120,72\n"
"qd 251,60,86,3072\nqm v,a,a,44,124,76\ns_mov_b32 s75, 0x100000\ns_add_u32 s85, s85, s75\ns_add_u32 s86, s86, s75\nqm v,a,a,40,124,68\n"
"qm v,a,a,40,120,64\nqw 8\nqt 120,8,32768\nqm v,a,a,56,96,64\nqt 122,8,33792\nqm v,a,a,56,100,68\n"
"qt 124,8,40960\nqm v,a,a,60,100,76\nqt 126,8,41984\nqm v,a,a,60,96,72\nqm v,a,a,64,96,80\nqc 24,24\n"
"qc 25,26\nqm v,a,a,64,100,84\nqc 26,40\nqc 27,42\nqm v,a,a,68,100,92\nqc 28,28\n"
"qc 29,30\nqm v,a,a,68,96,88\nqw 8\nqt 96,1,16384\nqc 30,44\nqm v,a,a,84,104,88\n"
"qt 98,1,17408\nqc 31,46\nqm v,a,a,84,108,92\nqt 100,1,24576\nqc 32,32\nqm v,a,a,80,108,84\n"
"qt 102,1,25600\nqc 33,34\nqm v,a,a,80,104,80\nqc 34,48\nqc 35,50\nqm v,a,a,76,104,72\n"
"buffer_store_dwordx4 v[24:27], v0, s[56:59], s5 offen offset:1024 sc1\ns_add_u32 m0, s87, 0x8000\nqm v,a,a,76,108,76\nqd 248,60,85,0\nqm v,a,a,72,108,68\nqc 36,36\n"
"qc 37,38\nqm v,a,a,72,104,64\nqw 8\nqt 104,2,16384\nqc 38,52\nqm v,a,a,88,112,64\n"
"qt 106,2,17408\nqc 39,54\nqm v,a,a,88,116,68\nqt 108,2,24576\nqa 252,0\nqm v,a,a,92,116,76\n"
"qd 249,60,85,1024\nqm v,a,a,92,112,72\nqt 110,2,25600\nqa 253,1\nqm v,a,a,96,112,80\nqc 40,252\n"
"qa 254,2\nqm v,a,a,96,116,84\nqa 255,3\nqc 41,254\nqm v,a,a,100,116,92\nqa 252,16\n"
"qa 253,17\nqm v,a,a,100,112,88\nqw 8\nqt 112,3,16384\nqc 42,252\nqm v,a,a,116,120,88\n"
"qt 114,3,17408\nqa 254,18\nqm v,a,a,116,124,92\nqt 116,3,24576\nqa 255,19\nqm v,a,a,112,124,84\n"
"qt 118,3,25600\nqc 43,254\nqm v,a,a,112,120,80\nqa 252,4\nqa 253,5\nqm v,a,a,108,120,72\n"
"qc 44,252\nqa 254,6\nqm v,a,a,108,124,76\nqa 255,7\nqc 45,254\nqm v,a,a,104,124,68\n"
"qa 252,20\nqa 253,21\nqm v,a,a,104,120,64\nqw 8\nbuffer_store_dwordx4 v[28:31], v0, s[56:59], s6 offen offset:1024 sc1\nqm v,a,a,120,32,96\n"
"qd 250,60,85,2048\nqm v,a,a,120,36,100\nqt 120,4,16384\nqc 46,252\nqm v,a,a,152,44,100\nqt 122,4,17408\n"
"qa 254,22\nqm v,a,a,152,40,96\nqd 251,60,85,3072\nqm v,a,a,184,48,96\nqt 124,4,24576\nqa 255,23\n"
"qm v,a,a,184,52,100\nqt 126,4,25600\nqc 47,254\nqm v,a,a,216,60,100\nqa 252,8\nqa 253,9\n"
"qm v,a,a,216,56,96\nqw 8\nqt 96,5,16384\nqc 48,252\nqm v,a,a,220,56,104\nqt 98,5,17408\n"
"qa 254,10\nqm v,a,a,220,60,108\nqt 100,5,24576\nqa 255,11\nqm v,a,a,188,52,108\nqt 102,5,25600\n"
"qc 49,254\nqm v,a,a,188,48,104\nqa 252,24\nqa 253,25\nqm v,a,a,156,40,104\nqc 50,252\n"
"qa 254,26\nqm v,a,a,156,44,108\nqa 255,27\nqc 51,254\nqm v,a,a,124,36,108\nqa 252,12\n"
"qa 253,13\nqm v,a,a,124,32,104\nbuffer_store_dwordx4 v[32:35], v0, s[56:59], s7 offen offset:1024 sc1\ns_add_u32 m0, s87, 0x9000\nqw 8\nqm v,a,a,128,32,112\n"
"qd 248,60,86,0\nqm v,a,a,128,36,116\nqt 104,6,16384\nqc 52,252\nqm v,a,a,160,44,116\nqt 106,6,17408\n"
"qa 254,14\nqm v,a,a,160,40,112\nqt 108,6,24576\nqa 255,15\nqm v,a,a,192,48,112\nqt 110,6,25600\n"
"qc 53,254\nqm v,a,a,192,52,116\nqd 249,60,86,1024\nqm v,a,a,224,60,116\nqa 252,28\nqa 253,29\n"
"qm v,a,a,224,56,112\nqw 8\nqt 112,7,16384\nqc 54,252\nqm v,a,a,228,56,120\nqt 114,7,17408\n"
"qa 254,30\nqm v,a,a,228,60,124\nqt 116,7,24576\nqa 255,31\nqm v,a,a,196,52,124\nqt 118,7,25600\n"
"qc 55,254\nqm v,a,a,196,48,120\nqc 56,56\nqc 57,58\nqm v,a,a,164,40,120\nqc 58,72\n"
"qc 59,74\nqm v,a,a,164,44,124\nqc 60,60\nqc 61,62\nqm v,a,a,132,36,124\nqc 62,76\n"
"qc 63,78\nqm v,a,a,132,32,120\nqw 8\nqt 120,8,16384\nqc 64,64\nqm v,a,a,136,32,96\n"
"qt 122,8,17408\nqc 65,66\nqm v,a,a,136,36,100\nqt 124,8,24576\nqc 66,80\nqm v,a,a,168,44,100\n"
"qt 126,8,25600\nqc 67,82\nqm v,a,a,168,40,96\nbuffer_store_dwordx4 v[36:39], v0, s[56:59], s8 offen offset:1024 sc1\nqm v,a,a,200,48,96\nqd 250,60,86,2048\n"
"qm v,a,a,200,52,100\nqc 68,68\nqc 69,70\nqm v,a,a,232,60,100\nqc 70,84\nqc 71,86\n"
"qm v,a,a,232,56,96\nqw 8\nqc 88,88\nqc 89,90\nqm v,a,a,236,56,104\nqd 251,60,86,3072\n"
"qm v,a,a,236,60,108\nqc 90,104\nqc 91,106\nqm v,a,a,204,52,108\nqc 92,92\nqc 93,94\n"
"qm v,a,a,204,48,104\nqc 94,108\nqc 95,110\nqm v,a,a,172,40,104\nqc 96,96\nqc 97,98\n"
"qm v,a,a,172,44,108\nqc 98,112\nqc 99,114\nqm v,a,a,140,36,108\nqc 100,100\nqc 101,102\n"
"qm v,a,a,140,32,104\nqw 4\nqc 102,116\nqc 103,118\nqm v,a,a,144,32,112\nqt 96,1,49152\n"
"qm v,a,a,144,36,116\nqt 98,1,50176\nqm v,a,a,176,44,116\nbuffer_store_dwordx4 v[40:43], v0, s[56:59], s5 offen offset:0 sc1\ns_mov_b32 s75, 0x100000\ns_add_u32 s85, s85, s75\n"
"s_add_u32 s86, s86, s75\nqm v,a,a,176,40,112\nql 192,0,56,81,0\nqm v,a,a,208,48,112\nqt 100,1,57344\nqm v,a,a,208,52,116\n"
"qt 102,1,58368\nqm v,a,a,240,60,116\nqt 104,2,49152\nqm v,a,a,240,56,112\nqw 5\nqt 106,2,50176\n"
"qm v,a,a,244,56,120\nql 196,0,56,81,1024\nqm v,a,a,244,60,124\nqt 108,2,57344\nqm v,a,a,212,52,124\nqt 110,2,58368\n"
"qm v,a,a,212,48,120\nqt 112,3,49152\nqm v,a,a,180,40,120\nqt 114,3,50176\nqm v,a,a,180,44,124\nqt 116,3,57344\n"
"qm v,a,a,148,36,124\nqt 118,3,58368\nqm v,a,a,148,32,120\nqt 120,4,49152\nqz a,v,v,0,40,40\nqt 122,4,50176\n"
"qz a,v,v,4,44,44\nqt 124,4,57344\nqz a,v,v,8,48,48\nqt 126,4,58368\nqz a,v,v,12,52,52\nqm a,v,v,0,24,24\n"
"qm a,v,v,4,28,28\nqm a,v,v,8,32,32\nbuffer_store_dwordx4 v[44:47], v0, s[56:59], s6 offen offset:0 sc1\nqm a,v,v,12,36,36\nql 200,0,56,82,0\nqm a,v,v,0,56,56\n"
"qm a,v,v,4,60,60\nqm a,v,v,8,64,64\nql 204,0,56,82,1024\nqm a,v,v,12,68,68\nqm a,v,v,0,88,88\nqm a,v,v,4,92,92\n"
"qm a,v,v,8,96,96\nqm a,v,v,12,100,100\nqw 12\nqm v,a,a,120,64,96\nqm v,a,a,120,68,100\nqm v,a,a,152,76,100\n"
"qm v,a,a,152,72,96\nqm v,a,a,184,80,96\nqm v,a,a,184,84,100\nqm v,a,a,216,92,100\nqm v,a,a,216,88,96\nqw 8\n"
"qt 96,5,49152\nqm v,a,a,220,88,104\nbuffer_store_dwordx4 v[48:51], v0, s[56:59], s7 offen offset:0 sc1\nqm v,a,a,220,92,108\nql 208,0,56,83,0\nqm v,a,a,188,84,108\n"
"qt 98,5,50176\nqc 72,120\nqm v,a,a,188,80,104\nqt 100,5,57344\nqc 73,122\nqm v,a,a,156,72,104\n"
"ql 212,0,56,83,1024\nqm v,a,a,156,76,108\nqt 102,5,58368\nqc 74,152\nqm v,a,a,124,68,108\nqc 75,154\n"
"qc 76,184\nqm v,a,a,124,64,104\nqw 8\nqt 104,6,49152\nqc 77,186\nqm v,a,a,128,64,112\n"
"qt 106,6,50176\nqc 78,216\nqm v,a,a,128,68,116\nqt 108,6,57344\nqc 79,218\nqm v,a,a,160,76,116\n"
"qt 110,6,58368\nqm v,a,a,160,72,112\nqm v,a,a,192,80,112\nqc 80,124\nqc 81,126\nqm v,a,a,192,84,116\n"
"qc 82,156\nqc 83,158\nqm v,a,a,224,92,116\nqc 84,188\nqc 85,190\nqm v,a,a,224,88,112\n"
"qw 8\nqt 112,7,49152\nqc 86,220\nqm v,a,a,228,88,120\nqt 114,7,50176\nqc 87,222\n"
"qm v,a,a,228,92,124\nqt 116,7,57344\nqc 104,128\nqm v,a,a,196,84,124\nbuffer_store_dwordx4 v[52:55], v0, s[56:59], s8 offen offset:0 sc1\nqm v,a,a,196,80,120\n"
"ql 216,0,56,84,0\nqm v,a,a,164,72,120\nqt 118,7,58368\nqc 105,130\nqm v,a,a,164,76,124\nqc 106,160\n"
"qc 107,162\nqm v,a,a,132,68,124\nql 220,0,56,84,1024\nqm v,a,a,132,64,120\nqw 8\nqt 120,8,49152\n"
"qc 108,192\nqm v,a,a,136,64,96\nqt 122,8,50176\nqc 109,194\nqm v,a,a,136,68,100\nqt 124,8,57344\n"
"qc 110,224\nqm v,a,a,168,76,100\nqt 126,8,58368\nqc 111,226\nqm v,a,a,168,72,96\nqm v,a,a,200,80,96\n"
"qc 112,132\nqc 113,134\nqm v,a,a,200,84,100\nqc 114,164\nqc 115,166\nqm v,a,a,232,92,100\n"
"qc 116,196\nqc 117,198\nqm v,a,a,232,88,96\nqw 8\nqc 118,228\nqc 119,230\n"
"qm v,a,a,236,88,104\nqc 24,136\nqc 25,138\nqm v,a,a,236,92,108\nqc 26,168\nqc 27,170\n"
"qm v,a,a,204,84,108\nqm v,a,a,204,80,104\nbuffer_store_dwordx4 v[56:59], v0, s[56:59], s5 offen offset:2048 sc1\nqm v,a,a,172,72,104\nqw 0\nqv 25\n"
"s_barrier\nqm v,a,a,172,76,108\nql 224,0,56,81,2048\nqm v,a,a,140,68,108\nqt 96,9,0\nqc 28,200\n"
"qm v,a,a,140,64,104\nqt 98,9,1024\nqc 29,202\nqm v,a,a,144,64,112\nql 228,0,56,81,3072\nqm v,a,a,144,68,116\n"
"qt 100,9,8192\nqc 30,232\nqm v,a,a,176,76,116\nqt 102,9,9216\nqc 31,234\nqm v,a,a,176,72,112\n"
"qt 104,10,0\nqm v,a,a,208,80,112\nqt 106,10,1024\nqc 32,140\nqm v,a,a,208,84,116\nqt 108,10,8192\n"
"qc 33,142\nqm v,a,a,240,92,116\nqt 110,10,9216\nqc 34,172\nqm v,a,a,240,88,112\nqt 112,11,0\n"
"qc 35,174\nqm v,a,a,244,88,120\nqt 114,11,1024\nqc 36,204\nqm v,a,a,244,92,124\nqt 116,11,8192\n"
"qc 37,206\nqm v,a,a,212,84,124\nqt 118,11,9216\nqc 38,236\nqm v,a,a,212,80,120\nqc 39,238\n"
"qc 40,144\nqm v,a,a,180,72,120\nbuffer_store_dwordx4 v[60:63], v0, s[56:59], s6 offen offset:2048 sc1\nqm v,a,a,180,76,124\nql 232,0,56,82,2048\nqm v,a,a,148,68,124\n"
"qc 41,146\nqc 42,176\nqm v,a,a,148,64,120\nqw 8\nqt 120,12,0\nqz v,a,a,120,96,128\n"
"ql 236,0,56,82,3072\nqm v,a,a,120,100,132\nqt 122,12,1024\nqc 43,178\nqz v,a,a,152,100,140\nqt 124,12,8192\n"
"qc 44,208\nqm v,a,a,152,96,136\nqt 126,12,9216\nqc 45,210\nqz v,a,a,184,96,144\nqc 46,240\n"
"qc 47,242\nqm v,a,a,184,100,148\nqc 48,148\nqc 49,150\nqz v,a,a,216,100,156\nqc 50,180\n"
"qc 51,182\nqm v,a,a,216,96,152\nqw 8\nqt 96,13,0\nqc 52,212\nqz v,a,a,220,104,152\n"
"qt 98,13,1024\nqc 53,214\nqm v,a,a,220,108,156\nqt 100,13,8192\nqc 54,244\nqz v,a,a,188,108,148\n"
"qt 102,13,9216\nqc 55,246\nqm v,a,a,188,104,144\nv_add_u32 v1, 0x20000, v1\nv_add_u32 v2, 0x20000, v2\nqz v,a,a,156,104,136\n"
"v_add_u32 v3, 0x20000, v3\nv_add_u32 v4, 0x20000, v4\nqm v,a,a,156,108,140\nbuffer_store_dwordx4 v[64:67], v0, s[56:59], s7 offen offset:2048 sc1\nqz v,a,a,124,108,132\nql 240,0,56,83,2048\n"
"qm v,a,a,124,104,128\nqw 8\nqt 104,14,0\nv_add_u32 v5, 0x20000, v5\nqz v,a,a,128,112,128\nqt 106,14,1024\n"
"v_add_u32 v6, 0x20000, v6\nqm v,a,a,128,116,132\nql 244,0,56,83,3072\nqz v,a,a,160,116,140\nqt 108,14,8192\nv_add_u32 v7, 0x20000, v7\n"
"qm v,a,a,160,112,136\nqt 110,14,9216\nv_add_u32 v8, 0x20000, v8\nqz v,a,a,192,112,144\nqa 19,0\nqa 20,1\n"
"qm v,a,a,192,116,148\nv_max_f32 v17, v19, v20\nqa 19,2\nqz v,a,a,224,116,156\nqa 20,3\nv_max3_f32 v17, v17, v19, v20\n"
"qm v,a,a,224,112,152\nqw 8\nqt 112,15,0\nqa 19,4\nqz v,a,a,228,120,152\nqt 114,15,1024\n"
"qa 20,5\nqm v,a,a,228,124,156\nqt 116,15,8192\nv_max3_f32 v17, v17, v19, v20\nqz v,a,a,196,124,148\nqt 118,15,9216\n"
"qa 19,6\nqm v,a,a,196,120,144\nqa 20,7\nv_max3_f32 v17, v17, v19, v20\nqz v,a,a,164,120,136\nqa 19,8\n"
"qa 20,9\nqm v,a,a,164,124,140\nv_max3_f32 v17, v17, v19, v20\nqa 19,10\nqz v,a,a,132,124,132\nbuffer_store_dwordx4 v[68:71], v0, s[56:59], s8 offen offset:2048 sc1\n"
"qm v,a,a,132,120,128\nqw 8\nqt 120,16,0\nqa 20,11\nqz v,a,a,136,96,128\nql 248,0,56,84,2048\n"
"qm v,a,a,136,100,132\nqt 122,16,1024\nv_max3_f32 v17, v17, v19, v20\nqz v,a,a,168,100,140\nql 252,0,56,84,3072\nqm v,a,a,168,96,136\n"
"qt 124,16,8192\nqa 19,12\nqz v,a,a,200,96,144\nqt 126,16,9216\nqa 20,13\nqm v,a,a,200,100,148\n"
"v_max3_f32 v17, v17, v19, v20\nqa 19,14\nqz v,a,a,232,100,156\nqa 20,15\nv_max3_f32 v17, v17, v19, v20\nqm v,a,a,232,96,152\n"
"qw 8\nqt 96,9,16384\nqz v,a,a,236,104,152\nqt 98,9,17408\nv_lshlrev_b32 v19, 16, v72\nqm v,a,a,236,108,156\n"
"qt 100,9,24576\nv_lshlrev_b32 v20, 16, v73\nqz v,a,a,204,108,148\nqt 102,9,25600\nv_lshlrev_b32 v21, 16, v74\nqm v,a,a,204,104,144\n"
"v_lshlrev_b32 v22, 16, v75\nv_max3_f32 v18, |v72|, |v73|, 0\nqz v,a,a,172,104,136\nv_max3_f32 v18, |v74|, |v75|, v18\nv_max3_f32 v18, |v19|, |v20|, v18\nqm v,a,a,172,108,140\n"
"v_max3_f32 v18, |v21|, |v22|, v18\nv_lshlrev_b32 v19, 16, v76\nqz v,a,a,140,108,132\nv_lshlrev_b32 v20, 16, v77\nv_lshlrev_b32 v21, 16, v78\nqm v,a,a,140,104,128\n"
"buffer_store_dwordx4 v[88:91], v0, s[56:59], s5 offen offset:3072 sc1\ns_mov_b32 s75, 0x1000\ns_add_u32 s81, s81, s75\ns_add_u32 s82, s82, s75\ns_add_u32 s83, s83, s75\ns_add_u32 s84, s84, s75\n"
"qw 8\nqz v,a,a,144,112,128\nqt 104,10,16384\nv_lshlrev_b32 v22, 16, v79\nqm v,a,a,144,116,132\nqt 106,10,17408\n"
"v_max3_f32 v18, |v76|, |v77|, v18\nqz v,a,a,176,116,140\nqt 108,10,24576\nv_max3_f32 v18, |v78|, |v79|, v18\nqm v,a,a,176,112,136\nqt 110,10,25600\n"
"v_max3_f32 v18, |v19|, |v20|, v18\nqz v,a,a,208,112,144\nv_max3_f32 v18, |v21|, |v22|, v18\nv_lshlrev_b32 v19, 16, v80\nqm v,a,a,208,116,148\nv_lshlrev_b32 v20, 16, v81\n"
"v_lshlrev_b32 v21, 16, v82\nqz v,a,a,240,116,156\nbuffer_store_dwordx4 v[92:95], v0, s[56:59], s6 offen offset:3072 sc1\nqm v,a,a,240,112,152\nqw 8\nqt 112,11,16384\n"
"v_lshlrev_b32 v22, 16, v83\nqz v,a,a,244,120,152\nqt 114,11,17408\nv_max3_f32 v18, |v80|, |v81|, v18\nqm v,a,a,244,124,156\nqt 116,11,24576\n"
"v_max3_f32 v18, |v82|, |v83|, v18\nqz v,a,a,212,124,148\nqt 118,11,25600\nv_max3_f32 v18, |v19|, |v20|, v18\nqm v,a,a,212,120,144\nv_max3_f32 v18, |v21|, |v22|, v18\n"
"v_lshlrev_b32 v19, 16, v84\nqz v,a,a,180,120,136\nv_lshlrev_b32 v20, 16, v85\nv_lshlrev_b32 v21, 16, v86\nqm v,a,a,180,124,140\nv_lshlrev_b32 v22, 16, v87\n"
"v_max3_f32 v18, |v84|, |v85|, v18\nqz v,a,a,148,124,132\nbuffer_store_dwordx4 v[96:99], v0, s[56:59], s7 offen offset:3072 sc1\nqm v,a,a,148,120,128\nqw 8\nqt 120,12,16384\n"
"qm v,a,a,120,96,160\nqt 122,12,17408\nv_max3_f32 v18, |v86|, |v87|, v18\nqm v,a,a,120,100,164\nqt 124,12,24576\nv_max3_f32 v18, |v19|, |v20|, v18\n"
"qm v,a,a,152,100,172\nqt 126,12,25600\nv_max3_f32 v18, |v21|, |v22|, v18\nqm v,a,a,152,96,168\nbuffer_store_dwordx4 v[100:103], v0, s[56:59], s8 offen offset:3072 sc1\ns_add_u32 m0, s87, 0x10000\n"
"qm v,a,a,184,96,176\nqd 248,60,85,0\nqm v,a,a,184,100,180\nv_lshlrev_b32 v19, 16, v104\nv_lshlrev_b32 v20, 16, v105\nqm v,a,a,216,100,188\n"
"v_lshlrev_b32 v21, 16, v106\nv_lshlrev_b32 v22, 16, v107\nqm v,a,a,216,96,184\nqw 8\nqt 96,13,16384\nv_max3_f32 v18, |v104|, |v105|, v18\n"
"qm v,a,a,220,104,184\nqt 98,13,17408\nv_max3_f32 v18, |v106|, |v107|, v18\nqm v,a,a,220,108,188\nqd 249,60,85,1024\nqm v,a,a,188,108,180\n"
"qt 100,13,24576\nv_max3_f32 v18, |v19|, |v20|, v18\nqm v,a,a,188,104,176\nqt 102,13,25600\nv_max3_f32 v18, |v21|, |v22|, v18\nqm v,a,a,156,104,168\n"
"v_lshlrev_b32 v19, 16, v108\nv_lshlrev_b32 v20, 16, v109\nqm v,a,a,156,108,172\nv_lshlrev_b32 v21, 16, v110\nv_lshlrev_b32 v22, 16, v111\nqm v,a,a,124,108,164\n"
"qd 250,60,85,2048\nqm v,a,a,124,104,160\nqw 8\nqt 104,14,16384\nv_max3_f32 v18, |v108|, |v109|, v18\nqm v,a,a,128,112,160\n"
"qt 106,14,17408\nv_max3_f32 v18, |v110|, |v111|, v18\nqm v,a,a,128,116,164\nqt 108,14,24576\nv_max3_f32 v18, |v19|, |v20|, v18\nqm v,a,a,160,116,172\n"
"qd 251,60,85,3072\nqm v,a,a,160,112,168\nqt 110,14,25600\nv_max3_f32 v18, |v21|, |v22|, v18\nqm v,a,a,192,112,176\nv_lshlrev_b32 v19, 16, v112\n"
"v_lshlrev_b32 v20, 16, v113\nqm v,a,a,192,116,180\nv_lshlrev_b32 v21, 16, v114\nv_lshlrev_b32 v22, 16, v115\nqm v,a,a,224,116,188\nv_max3_f32 v18, |v112|, |v113|, v18\n"
"v_max3_f32 v18, |v114|, |v115|, v18\nqm v,a,a,224,112,184\nqw 8\nqt 112,15,16384\nv_max3_f32 v18, |v19|, |v20|, v18\nqm v,a,a,228,120,184\n"
"qt 114,15,17408\nv_max3_f32 v18, |v21|, |v22|, v18\nqm v,a,a,228,124,188\nqt 116,15,24576\nv_lshlrev_b32 v19, 16, v116\nqm v,a,a,196,124,180\n"
"qt 118,15,25600\nv_lshlrev_b32 v20, 16, v117\nqm v,a,a,196,120,176\nv_lshlrev_b32 v21, 16, v118\nv_lshlrev_b32 v22, 16, v119\nqm v,a,a,164,120,168\n"
"v_max3_f32 v18, |v116|, |v117|, v18\nv_max3_f32 v18, |v118|, |v119|, v18\nqm v,a,a,164,124,172\nv_max3_f32 v18, |v19|, |v20|, v18\nv_max3_f32 v18, |v21|, |v22|, v18\nqm v,a,a,132,124,164\n"
"buffer_store_dwordx4 v[72:75], v0, s[56:59], s9 offen offset:0 sc1\ns_add_u32 m0, s87, 0x11000\nqm v,a,a,132,120,160\nqw 8\nqt 120,16,16384\nv_lshlrev_b32 v19, 16, v24\n"
"qm v,a,a,136,96,160\nqd 248,60,86,0\nqm v,a,a,136,100,164\nqt 122,16,17408\nv_lshlrev_b32 v20, 16, v25\nqm v,a,a,168,100,172\n"
"qt 124,16,24576\nv_lshlrev_b32 v21, 16, v26\nqm v,a,a,168,96,168\nqt 126,16,25600\nv_lshlrev_b32 v22, 16, v27\nqm v,a,a,200,96,176\n"
"qd 249,60,86,1024\nqm v,a,a,200,100,180\nv_max3_f32 v18, |v24|, |v25|, v18\nv_max3_f32 v18, |v26|, |v27|, v18\nqm v,a,a,232,100,188\nv_max3_f32 v18, |v19|, |v20|, v18\n"
"v_max3_f32 v18, |v21|, |v22|, v18\nqm v,a,a,232,96,184\nqw 8\nv_lshlrev_b32 v19, 16, v28\nv_lshlrev_b32 v20, 16, v29\nqm v,a,a,236,104,184\n"
"qd 250,60,86,2048\nqm v,a,a,236,108,188\nv_lshlrev_b32 v21, 16, v30\nv_lshlrev_b32 v22, 16, v31\nqm v,a,a,204,108,180\nv_max3_f32 v18, |v28|, |v29|, v18\n"
"v_max3_f32 v18, |v30|, |v31|, v18\nqm v,a,a,204,104,176\nv_max3_f32 v18, |v19|, |v20|, v18\nv_max3_f32 v18, |v21|, |v22|, v18\nqm v,a,a,172,104,168\nqw 0\n"
"qv 36\ns_barrier\nqm v,a,a,172,108,172\nqd 251,60,86,3072\nqm v,a,a,140,108,164\ns_mov_b32 s75, 0x100000\n"
"s_add_u32 s85, s85, s75\ns_add_u32 s86, s86, s75\nqm v,a,a,140,104,160\nqt 96,9,32768\nv_lshlrev_b32 v19, 16, v32\nqm v,a,a,144,112,160\n"
"qt 98,9,33792\nv_lshlrev_b32 v20, 16, v33\nqm v,a,a,144,116,164\nqt 100,9,40960\nv_lshlrev_b32 v21, 16, v34\nqm v,a,a,176,116,172\n"
"ql 128,0,56,81,0\nqm v,a,a,176,112,168\nqt 102,9,41984\nv_lshlrev_b32 v22, 16, v35\nqm v,a,a,208,112,176\nqt 104,10,32768\n"
"v_max3_f32 v18, |v32|, |v33|, v18\nqm v,a,a,208,116,180\nqt 106,10,33792\nv_max3_f32 v18, |v34|, |v35|, v18\nqm v,a,a,240,116,188\nqt 108,10,40960\n"
"v_max3_f32 v18, |v19|, |v20|, v18\nqm v,a,a,240,112,184\nqt 110,10,41984\nv_max3_f32 v18, |v21|, |v22|, v18\nqm v,a,a,244,120,184\nql 132,0,56,81,1024\n"
"qm v,a,a,244,124,188\nqt 112,11,32768\nv_lshlrev_b32 v19, 16, v36\nqm v,a,a,212,124,180\nqt 114,11,33792\nv_lshlrev_b32 v20, 16, v37\n"
"qm v,a,a,212,120,176\nqt 116,11,40960\nv_lshlrev_b32 v21, 16, v38\nqm v,a,a,180,120,168\nqt 118,11,41984\nv_lshlrev_b32 v22, 16, v39\n"
"qm v,a,a,180,124,172\nv_max3_f32 v18, |v36|, |v37|, v18\nv_max3_f32 v18, |v38|, |v39|, v18\nqm v,a,a,148,124,164\nv_max3_f32 v18, |v19|, |v20|, v18\nv_max3_f32 v18, |v21|, |v22|, v18\n"
"qm v,a,a,148,120,160\nqw 8\nqv 27\nqt 120,12,32768\nqm v,a,a,120,96,192\nqt 122,12,33792\n"
"v_lshlrev_b32 v19, 16, v40\nqm v,a,a,120,100,196\nqt 124,12,40960\nv_lshlrev_b32 v20, 16, v41\nqm v,a,a,152,100,204\nqt 126,12,41984\n"
"v_lshlrev_b32 v21, 16, v42\nqm v,a,a,152,96,200\nv_lshlrev_b32 v22, 16, v43\nv_max3_f32 v18, |v40|, |v41|, v18\nqm v,a,a,184,96,208\nbuffer_store_dwordx4 v[76:79], v0, s[56:59], s11 offen offset:0 sc1\n"
"qm v,a,a,184,100,212\nql 136,0,56,82,0\nqm v,a,a,216,100,220\nv_max3_f32 v18, |v42|, |v43|, v18\nv_max3_f32 v18, |v19|, |v20|, v18\nqm v,a,a,216,96,216\n"
"qw 8\nqt 96,13,32768\nv_max3_f32 v18, |v21|, |v22|, v18\nqm v,a,a,220,104,216\nql 140,0,56,82,1024\nqm v,a,a,220,108,220\n"
"qt 98,13,33792\nv_lshlrev_b32 v19, 16, v44\nqm v,a,a,188,108,212\nqt 100,13,40960\nv_lshlrev_b32 v20, 16, v45\nqm v,a,a,188,104,208\n"
"qt 102,13,41984\nv_lshlrev_b32 v21, 16, v46\nqm v,a,a,156,104,200\nql 144,0,56,83,0\nqm v,a,a,156,108,204\nv_lshlrev_b32 v22, 16, v47\n"
"v_max3_f32 v18, |v44|, |v45|, v18\nqm v,a,a,124,108,196\nv_max3_f32 v18, |v46|, |v47|, v18\nv_max3_f32 v18, |v19|, |v20|, v18\nqm v,a,a,124,104,192\nqw 8\n"
"qt 104,14,32768\nv_max3_f32 v18, |v21|, |v22|, v18\nqm v,a,a,128,112,192\nqt 106,14,33792\nv_lshlrev_b32 v19, 16, v48\nqm v,a,a,128,116,196\n"
"ql 148,0,56,83,1024\nqm v,a,a,160,116,204\nqt 108,14,40960\nv_lshlrev_b32 v20, 16, v49\nqm v,a,a,160,112,200\nqt 110,14,41984\n"
"v_lshlrev_b32 v21, 16, v50\nqm v,a,a,192,112,208\nv_lshlrev_b32 v22, 16, v51\nv_max3_f32 v18, |v48|, |v49|, v18\nqm v,a,a,192,116,212\nv_max3_f32 v18, |v50|, |v51|, v18\n"
"v_max3_f32 v18, |v19|, |v20|, v18\nqm v,a,a,224,116,220\nql 152,0,56,84,0\nqm v,a,a,224,112,216\nqw 8\nqt 112,15,32768\n"
"v_max3_f32 v18, |v21|, |v22|, v18\nqm v,a,a,228,120,216\nqt 114,15,33792\nv_lshlrev_b32 v19, 16, v52\nqm v,a,a,228,124,220\nqt 116,15,40960\n"
"v_lshlrev_b32 v20, 16, v53\nqm v,a,a,196,124,212\nqt 118,15,41984\nv_lshlrev_b32 v21, 16, v54\nqm v,a,a,196,120,208\nql 156,0,56,84,1024\n"
"qm v,a,a,164,120,200\nv_lshlrev_b32 v22, 16, v55\nv_max3_f32 v18, |v52|, |v53|, v18\nqm v,a,a,164,124,204\nv_max3_f32 v18, |v54|, |v55|, v18\nv_max3_f32 v18, |v19|, |v20|, v18\n"
"qm v,a,a,132,124,196\nv_max3_f32 v18, |v21|, |v22|, v18\nv_cmp_lt_f32 vcc, 0x58800000, v18\nqm v,a,a,132,120,192\nqw 8\nqt 120,16,32768\n"
"v_mov_b32 v19, 0x7f800000\nqm v,a,a,136,96,192\nql 160,0,56,81,2048\nqm v,a,a,136,100,196\nqt 122,16,33792\nv_mov_b32 v20, 0\n"
"qm v,a,a,168,100,204\nqt 124,16,40960\nv_cndmask_b32 v17, v17, v19, vcc\nqm v,a,a,168,96,200\nqt 126,16,41984\nqm v,a,a,200,96,208\n"
"ql 164,0,56,81,3072\nqm v,a,a,200,100,212\nv_max_f32_dpp v17, v17, v17 row_ror:8 row_mask:0xf bank_mask:0xf\nqm v,a,a,232,100,220\nqm v,a,a,232,96,216\nqw 8\n"
"qt 96,9,49152\nv_max_f32_dpp v17, v17, v17 row_ror:4 row_mask:0xf bank_mask:0xf\nqm v,a,a,236,104,216\nqt 98,9,50176\nqm v,a,a,236,108,220\nql 168,0,56,82,2048\n"
"qm v,a,a,204,108,212\nqt 100,9,57344\nv_max_f32_dpp v17, v17, v17 row_ror:2 row_mask:0xf bank_mask:0xf\nqm v,a,a,204,104,208\nqt 102,9,58368\nqm v,a,a,172,104,200\n"
"v_max_f32_dpp v17, v17, v17 row_ror:1 row_mask:0xf bank_mask:0xf\nqm v,a,a,172,108,204\nqm v,a,a,140,108,196\nql 172,0,56,82,3072\nqm v,a,a,140,104,192\nqw 8\n"
"qt 104,10,49152\nv_mov_b32 v18, v17\nqm v,a,a,144,112,192\nqt 106,10,50176\nqm v,a,a,144,116,196\nqt 108,10,57344\n"
"qm v,a,a,176,116,204\nqt 110,10,58368\nqm v,a,a,176,112,200\nv_permlane16_swap_b32 v18, v17\nqm v,a,a,208,112,208\nqm v,a,a,208,116,212\n"
"v_max_f32 v17, v17, v18\nqm v,a,a,240,116,220\nqm v,a,a,240,112,216\nqw 8\nqt 112,11,49152\nv_mov_b32 v18, v17\n"
"qm v,a,a,244,120,216\nqt 114,11,50176\nqm v,a,a,244,124,220\nqt 116,11,57344\nqm v,a,a,212,124,212\nqt 118,11,58368\n"
"qm v,a,a,212,120,208\nbuffer_store_dwordx4 v[80:83], v0, s[56:59], s9 offen offset:1024 sc1\nqm v,a,a,180,120,200\nql 176,0,56,83,2048\nqm v,a,a,180,124,204\nv_permlane32_swap_b32 v18, v17\n"
"qm v,a,a,148,124,196\nql 180,0,56,83,3072\nqm v,a,a,148,120,192\nqw 8\nqv 29\nqt 120,12,49152\n"
"qm v,a,a,120,96,224\nqt 122,12,50176\nv_max_f32 v17, v17, v18\nqm v,a,a,120,100,228\nqt 124,12,57344\nv_mov_b32 v18, s16\n"
"qm v,a,a,152,100,236\nqt 126,12,58368\nv_mov_b32 v19, s17\nqm v,a,a,152,96,232\nql 184,0,56,84,2048\nqm v,a,a,184,96,240\n"
"s_sub_u32 s34, s13, 0xf000\ns_lshl1_add_u32 s34, s34, 0x3000\nqm v,a,a,184,100,244\nqm v,a,a,216,100,252\nqm v,a,a,216,96,248\nqw 8\n"
"qt 96,13,49152\nqm v,a,a,220,104,248\nql 188,0,56,84,3072\nqm v,a,a,220,108,252\ns_mov_b32 s75, 0x1000\ns_add_u32 s81, s81, s75\n"
"s_add_u32 s82, s82, s75\nqm v,a,a,188,108,244\ns_add_u32 s83, s83, s75\nqt 98,13,50176\nqm v,a,a,188,104,240\nqt 100,13,57344\n"
"qm v,a,a,156,104,232\nqt 102,13,58368\nqm v,a,a,156,108,236\nqm v,a,a,124,108,228\nqm v,a,a,124,104,224\nqw 8\n"
"qt 104,14,49152\nqm v,a,a,128,112,224\nqt 106,14,50176\nqm v,a,a,128,116,228\nqt 108,14,57344\nqm v,a,a,160,116,236\n"
"qt 110,14,58368\nqm v,a,a,160,112,232\nbuffer_store_dwordx4 v[84:87], v0, s[56:59], s11 offen offset:1024 sc1\ns_add_u32 s84, s84, s75\ns_add_u32 m0, s87, 0x0\nqm v,a,a,192,112,240\n"
"qd 248,60,85,0\nqm v,a,a,192,116,244\nqm v,a,a,224,116,252\nqm v,a,a,224,112,248\nqw 8\nqt 112,15,49152\n"
"qm v,a,a,228,120,248\nqd 249,60,85,1024\nqm v,a,a,228,124,252\nqt 114,15,50176\nqm v,a,a,196,124,244\nqt 116,15,57344\n"
"qm v,a,a,196,120,240\nqt 118,15,58368\nqm v,a,a,164,120,232\nqm v,a,a,164,124,236\nqd 250,60,85,2048\nqm v,a,a,132,124,228\n"
"qm v,a,a,132,120,224\nqw 8\nqt 120,16,49152\nqm v,a,a,136,96,224\nqt 122,16,50176\nqm v,a,a,136,100,228\n"
"qt 124,16,57344\nqm v,a,a,168,100,236\nqt 126,16,58368\nqm v,a,a,168,96,232\nqd 251,60,85,3072\nqm v,a,a,200,96,240\n"
"s_add_u32 m0, s87, 0x1000\nqm v,a,a,200,100,244\nqm v,a,a,232,100,252\nqm v,a,a,232,96,248\nqw 8\nqm v,a,a,236,104,248\n"
"qd 248,60,86,0\nqm v,a,a,236,108,252\nqm v,a,a,204,108,244\nqm v,a,a,204,104,240\nqd 249,60,86,1024\nqm v,a,a,172,104,232\n"
"qw 0\nqv 25\ns_barrier\nqm v,a,a,172,108,236\nqt 96,1,0\nqm v,a,a,140,108,228\n"
"qt 98,1,1024\nqm v,a,a,140,104,224\nqt 100,1,8192\nqm v,a,a,144,112,224\nqt 102,1,9216\nqm v,a,a,144,116,228\n"
"qt 104,2,0\nqm v,a,a,176,116,236\nqt 106,2,1024\nqm v,a,a,176,112,232\nqt 108,2,8192\nqm v,a,a,208,112,240\n"
"qt 110,2,9216\nqm v,a,a,208,116,244\nqm v,a,a,240,116,252\nqm v,a,a,240,112,248\nqt 112,3,0\nqm v,a,a,244,120,248\n"
"buffer_store_dwordx4 v[104:107], v0, s[56:59], s9 offen offset:2048 sc1\nqm v,a,a,244,124,252\nqd 250,60,86,2048\nqm v,a,a,212,124,244\nqt 114,3,1024\nqm v,a,a,212,120,240\n"
"qt 116,3,8192\nqm v,a,a,180,120,232\nqd 251,60,86,3072\nqm v,a,a,180,124,236\ns_mov_b32 s75, 0x100000\ns_add_u32 s85, s85, s75\n"
"s_add_u32 s86, s86, s75\nqm v,a,a,148,124,228\nqt 118,3,9216\nqm v,a,a,148,120,224\nqw 8\nqv 19\n"
"qt 120,4,0\nqm v,a,a,120,96,128\nqt 122,4,1024\nqm v,a,a,120,100,132\nql 192,0,56,81,0\nqm v,a,a,152,100,140\n"
"qt 124,4,8192\nqm v,a,a,152,96,136\nqt 126,4,9216\nqm v,a,a,184,96,144\nqm v,a,a,184,100,148\nql 196,0,56,81,1024\n"
"qm v,a,a,216,100,156\nqm v,a,a,216,96,152\nqw 8\nqt 96,5,0\nqm v,a,a,220,104,152\nqt 98,5,1024\n"
"qm v,a,a,220,108,156\nqt 100,5,8192\nqm v,a,a,188,108,148\nql 200,0,56,82,0\nqm v,a,a,188,104,144\nqt 102,5,9216\n"
"qm v,a,a,156,104,136\nqm v,a,a,156,108,140\nqm v,a,a,124,108,132\nqm v,a,a,124,104,128\nqw 8\nqt 104,6,0\n"
"qm v,a,a,128,112,128\nqt 106,6,1024\nqm v,a,a,128,116,132\nqt 108,6,8192\nqm v,a,a,160,116,140\nqt 110,6,9216\n"
"qm v,a,a,160,112,136\nqm v,a,a,192,112,144\nqm v,a,a,192,116,148\nqm v,a,a,224,116,156\nqm v,a,a,224,112,152\nqw 8\n"
"buffer_store_dwordx4 v[108:111], v0, s[56:59], s11 offen offset:2048 sc1\nqm v,a,a,228,120,152\nql 204,0,56,82,1024\nqm v,a,a,228,124,156\nqt 112,7,0\nqm v,a,a,196,124,148\n"
"ql 208,0,56,83,0\nqm v,a,a,196,120,144\nqt 114,7,1024\nqm v,a,a,164,120,136\nqt 116,7,8192\nqm v,a,a,164,124,140\n"
"qt 118,7,9216\nqm v,a,a,132,124,132\nqm v,a,a,132,120,128\nqw 8\nqt 120,8,0\nqm v,a,a,136,96,128\n"
"ql 212,0,56,83,1024\nqm v,a,a,136,100,132\nqt 122,8,1024\nqm v,a,a,168,100,140\nqt 124,8,8192\nqm v,a,a,168,96,136\n"
"qt 126,8,9216\nqm v,a,a,200,96,144\nql 216,0,56,84,0\nqm v,a,a,200,100,148\nqm v,a,a,232,100,156\nqm v,a,a,232,96,152\n"
"qw 8\nqt 96,1,16384\nqm v,a,a,236,104,152\nql 220,0,56,84,1024\nqm v,a,a,236,108,156\nqt 98,1,17408\n"
"qm v,a,a,204,108,148\nqt 100,1,24576\nqm v,a,a,204,104,144\nqt 102,1,25600\nqm v,a,a,172,104,136\nqm v,a,a,172,108,140\n"
"ql 224,0,56,81,2048\nqm v,a,a,140,108,132\nqm v,a,a,140,104,128\nqw 8\nqt 104,2,16384\nqm v,a,a,144,112,128\n"
"qt 106,2,17408\nqm v,a,a,144,116,132\nqt 108,2,24576\nqm v,a,a,176,116,140\nql 228,0,56,81,3072\nqm v,a,a,176,112,136\n"
"qt 110,2,25600\nqm v,a,a,208,112,144\nqm v,a,a,208,116,148\nqm v,a,a,240,116,156\nql 232,0,56,82,2048\nqm v,a,a,240,112,152\n"
"qw 8\nqt 112,3,16384\nqm v,a,a,244,120,152\nqt 114,3,17408\nqm v,a,a,244,124,156\nqt 116,3,24576\n"
"qm v,a,a,212,124,148\nql 236,0,56,82,3072\nqm v,a,a,212,120,144\nqt 118,3,25600\nqm v,a,a,180,120,136\nqm v,a,a,180,124,140\n"
"qm v,a,a,148,124,132\nqm v,a,a,148,120,128\nqw 8\nqv 23\nqt 120,4,16384\nqm v,a,a,120,96,160\n"
"qt 122,4,17408\nqm v,a,a,120,100,164\nqt 124,4,24576\nqm v,a,a,152,100,172\nqt 126,4,25600\nqm v,a,a,152,96,168\n"
"qm v,a,a,184,96,176\nqm v,a,a,184,100,180\nqm v,a,a,216,100,188\nbuffer_store_dwordx4 v[112:115], v0, s[56:59], s9 offen offset:3072 sc1\nqm v,a,a,216,96,184\nqw 8\n"
"qt 96,5,16384\nqm v,a,a,220,104,184\nql 240,0,56,83,2048\nqm v,a,a,220,108,188\nql 244,0,56,83,3072\nqm v,a,a,188,108,180\n"
"qt 98,5,17408\nqm v,a,a,188,104,176\nqt 100,5,24576\nqm v,a,a,156,104,168\nql 248,0,56,84,2048\nqm v,a,a,156,108,172\n"
"qt 102,5,25600\nqm v,a,a,124,108,164\nqm v,a,a,124,104,160\nqw 8\nqt 104,6,16384\nqm v,a,a,128,112,160\n"
"ql 252,0,56,84,3072\nqm v,a,a,128,116,164\ns_mov_b32 s75, 0x1000\ns_add_u32 s81, s81, s75\ns_add_u32 s82, s82, s75\nqm v,a,a,160,116,172\n"
"s_add_u32 s83, s83, s75\nqt 106,6,17408\nqm v,a,a,160,112,168\nqt 108,6,24576\nqm v,a,a,192,112,176\nqt 110,6,25600\n"
"qm v,a,a,192,116,180\nqm v,a,a,224,116,188\nqm v,a,a,224,112,184\nqw 8\nqt 112,7,16384\nqm v,a,a,228,120,184\n"
"buffer_store_dwordx4 v[116:119], v0, s[56:59], s11 offen offset:3072 sc1\ns_add_u32 s84, s84, s75\ns_add_u32 m0, s87, 0x8000\nqm v,a,a,228,124,188\nqd 248,60,85,0\nqm v,a,a,196,124,180\n"
"qt 114,7,17408\nqm v,a,a,196,120,176\nqt 116,7,24576\nqm v,a,a,164,120,168\nqt 118,7,25600\nqm v,a,a,164,124,172\n"
"qd 249,60,85,1024\nqm v,a,a,132,124,164\nqm v,a,a,132,120,160\nqw 8\nqt 120,8,16384\nqm v,a,a,136,96,160\n"
"qt 122,8,17408\nqm v,a,a,136,100,164\nqt 124,8,24576\nqm v,a,a,168,100,172\nqt 126,8,25600\nqm v,a,a,168,96,168\n"
"qd 250,60,85,2048\nqm v,a,a,200,96,176\nqd 251,60,85,3072\nqm v,a,a,200,100,180\ns_add_u32 m0, s87, 0x9000\nqm v,a,a,232,100,188\n"
"qm v,a,a,232,96,184\nqw 8\nqm v,a,a,236,104,184\nqd 248,60,86,0\nqm v,a,a,236,108,188\nqm v,a,a,204,108,180\n"
"qm v,a,a,204,104,176\nqm v,a,a,172,104,168\nqw 0\nqv 24\ns_barrier\nqm v,a,a,172,108,172\n"
"qd 249,60,86,1024\nqm v,a,a,140,108,164\nqt 96,9,0\nqm v,a,a,140,104,160\nqt 98,9,1024\nqm v,a,a,144,112,160\n"
"qt 100,9,8192\nqm v,a,a,144,116,164\nqt 102,9,9216\nqm v,a,a,176,116,172\nqt 104,10,0\nqm v,a,a,176,112,168\n"
"qt 106,10,1024\nqm v,a,a,208,112,176\nqt 108,10,8192\nqm v,a,a,208,116,180\nqt 110,10,9216\nqm v,a,a,240,116,188\n"
"qm v,a,a,240,112,184\nqt 112,11,0\nqm v,a,a,244,120,184\nbuffer_store_dwordx4 v[24:27], v0, s[56:59], s10 offen offset:0 sc1\nqm v,a,a,244,124,188\nqd 250,60,86,2048\n"
"qm v,a,a,212,124,180\nqd 251,60,86,3072\nqm v,a,a,212,120,176\ns_mov_b32 s75, 0x100000\ns_add_u32 s85, s85, s75\ns_add_u32 s86, s86, s75\n"
"qm v,a,a,180,120,168\nqt 114,11,1024\nqm v,a,a,180,124,172\nql 128,0,56,81,0\nqm v,a,a,148,124,164\nqt 116,11,8192\n"
"qm v,a,a,148,120,160\nqw 7\nqv 20\nqt 118,11,9216\nqm v,a,a,120,96,192\nqt 120,12,0\n"
"qm v,a,a,120,100,196\nql 132,0,56,81,1024\nqm v,a,a,152,100,204\nqt 122,12,1024\nqm v,a,a,152,96,200\nqt 124,12,8192\n"
"qm v,a,a,184,96,208\nqt 126,12,9216\nqm v,a,a,184,100,212\nql 136,0,56,82,0\nqm v,a,a,216,100,220\nqm v,a,a,216,96,216\n"
"qw 8\nqt 96,13,0\nqm v,a,a,220,104,216\nqt 98,13,1024\nqm v,a,a,220,108,220\nqt 100,13,8192\n"
"qm v,a,a,188,108,212\nqt 102,13,9216\nqm v,a,a,188,104,208\nqm v,a,a,156,104,200\nqm v,a,a,156,108,204\nqm v,a,a,124,108,196\n"
"qm v,a,a,124,104,192\nqw 8\nqt 104,14,0\nqm v,a,a,128,112,192\nqt 106,14,1024\nqm v,a,a,128,116,196\n"
"buffer_store_dwordx4 v[28:31], v0, s[56:59], s12 offen offset:0 sc1\nqm v,a,a,160,116,204\nql 140,0,56,82,1024\nqm v,a,a,160,112,200\nql 144,0,56,83,0\nqm v,a,a,192,112,208\n"
"qt 108,14,8192\nqm v,a,a,192,116,212\nqt 110,14,9216\nqm v,a,a,224,116,220\nql 148,0,56,83,1024\nqm v,a,a,224,112,216\n"
"qw 8\nqt 112,15,0\nqm v,a,a,228,120,216\nqt 114,15,1024\nqm v,a,a,228,124,220\nqt 116,15,8192\n"
"qm v,a,a,196,124,212\nql 152,0,56,84,0\nqm v,a,a,196,120,208\nqt 118,15,9216\nqm v,a,a,164,120,200\nqm v,a,a,164,124,204\n"
"qm v,a,a,132,124,196\nql 156,0,56,84,1024\nqm v,a,a,132,120,192\nqw 8\nqt 120,16,0\nqm v,a,a,136,96,192\n"
"qt 122,16,1024\nqm v,a,a,136,100,196\nqt 124,16,8192\nqm v,a,a,168,100,204\nql 160,0,56,81,2048\nqm v,a,a,168,96,200\n"
"qt 126,16,9216\nqm v,a,a,200,96,208\nqm v,a,a,200,100,212\nqm v,a,a,232,100,220\nql 164,0,56,81,3072\nqm v,a,a,232,96,216\n"
"qw 8\nqt 96,9,16384\nqm v,a,a,236,104,216\nqt 98,9,17408\nqm v,a,a,236,108,220\nql 168,0,56,82,2048\n"
"qm v,a,a,204,108,212\nqt 100,9,24576\nqm v,a,a,204,104,208\nqt 102,9,25600\nqm v,a,a,172,104,200\nqm v,a,a,172,108,204\n"
"ql 172,0,56,82,3072\nqm v,a,a,140,108,196\nqm v,a,a,140,104,192\nqw 8\nqt 104,10,16384\nqm v,a,a,144,112,192\n"
"qt 106,10,17408\nqm v,a,a,144,116,196\nql 176,0,56,83,2048\nqm v,a,a,176,116,204\nqt 108,10,24576\nqm v,a,a,176,112,200\n"
"qt 110,10,25600\nqm v,a,a,208,112,208\nqm v,a,a,208,116,212\nqm v,a,a,240,116,220\nqm v,a,a,240,112,216\nqw 8\n"
"qt 112,11,16384\nqm v,a,a,244,120,216\nqt 114,11,17408\nqm v,a,a,244,124,220\nqt 116,11,24576\nqm v,a,a,212,124,212\n"
"qt 118,11,25600\nqm v,a,a,212,120,208\nqm v,a,a,180,120,200\nqm v,a,a,180,124,204\nbuffer_store_dwordx4 v[32:35], v0, s[56:59], s10 offen offset:1024 sc1\nqm v,a,a,148,124,196\n"
"ql 180,0,56,83,3072\nqm v,a,a,148,120,192\nqw 8\nqv 26\nqt 120,12,16384\nqm v,a,a,120,96,224\n"
"ql 184,0,56,84,2048\nqm v,a,a,120,100,228\nqt 122,12,17408\nqm v,a,a,152,100,236\nql 188,0,56,84,3072\nqm v,a,a,152,96,232\n"
"s_mov_b32 s75, 0x1000\ns_add_u32 s81, s81, s75\ns_add_u32 s82, s82, s75\nqm v,a,a,184,96,240\ns_add_u32 s83, s83, s75\ns_add_u32 s84, s84, s75\n"
"s_add_u32 m0, s87, 0x10000\nqm v,a,a,184,100,244\nqt 124,12,24576\nqm v,a,a,216,100,252\nqd 248,60,85,0\nqm v,a,a,216,96,248\n"
"qw 7\nqt 126,12,25600\nqm v,a,a,220,104,248\nqt 96,13,16384\nqm v,a,a,220,108,252\nqt 98,13,17408\n"
"qm v,a,a,188,108,244\nqd 249,60,85,1024\nqm v,a,a,188,104,240\nqt 100,13,24576\nqm v,a,a,156,104,232\nqt 102,13,25600\n"
"qm v,a,a,156,108,236\nqm v,a,a,124,108,228\nqd 250,60,85,2048\nqm v,a,a,124,104,224\nqw 8\nqt 104,14,16384\n"
"qm v,a,a,128,112,224\nqt 106,14,17408\nqm v,a,a,128,116,228\nqt 108,14,24576\nqm v,a,a,160,116,236\nqt 110,14,25600\n"
"qm v,a,a,160,112,232\nqm v,a,a,192,112,240\nqm v,a,a,192,116,244\nqm v,a,a,224,116,252\nqm v,a,a,224,112,248\nqw 8\n"
"qt 112,15,16384\nqm v,a,a,228,120,248\nqt 114,15,17408\nqm v,a,a,228,124,252\nbuffer_store_dwordx4 v[36:39], v0, s[56:59], s12 offen offset:1024 sc1\nqm v,a,a,196,124,244\n"
"qd 251,60,85,3072\ns_add_u32 m0, s87, 0x11000\nqm v,a,a,196,120,240\nqd 248,60,86,0\nqm v,a,a,164,120,232\nqt 116,15,24576\n"
"qm v,a,a,164,124,236\nqt 118,15,25600\nqm v,a,a,132,124,228\nqm v,a,a,132,120,224\nqw 8\nqt 120,16,16384\n"
"qm v,a,a,136,96,224\nqd 249,60,86,1024\nqm v,a,a,136,100,228\nqt 122,16,17408\nqm v,a,a,168,100,236\nqt 124,16,24576\n"
"qm v,a,a,168,96,232\nqt 126,16,25600\nqm v,a,a,200,96,240\nqd 250,60,86,2048\nqm v,a,a,200,100,244\nqm v,a,a,232,100,252\n"
"qm v,a,a,232,96,248\nqw 8\nqm v,a,a,236,104,248\nqd 251,60,86,3072\nqm v,a,a,236,108,252\ns_mov_b32 s75, 0x100000\n"
"s_add_u32 s85, s85, s75\ns_add_u32 s86, s86, s75\nqm v,a,a,204,108,244\nql 192,0,56,81,0\nqm v,a,a,204,104,240\nqm v,a,a,172,104,232\n"
"qw 0\nqv 28\ns_barrier\nqm v,a,a,172,108,236\nqt 96,9,32768\nqm v,a,a,140,108,228\n"
"ql 196,0,56,81,1024\nqm v,a,a,140,104,224\nqt 98,9,33792\nqm v,a,a,144,112,224\nqt 100,9,40960\nqm v,a,a,144,116,228\n"
"qt 102,9,41984\nqm v,a,a,176,116,236\nqt 104,10,32768\nqm v,a,a,176,112,232\nqt 106,10,33792\nqm v,a,a,208,112,240\n"
"qt 108,10,40960\nqm v,a,a,208,116,244\nqt 110,10,41984\nqm v,a,a,240,116,252\nqm v,a,a,240,112,248\nqt 112,11,32768\n"
"qm v,a,a,244,120,248\nqt 114,11,33792\nqm v,a,a,244,124,252\nqt 116,11,40960\nqm v,a,a,212,124,244\nbuffer_store_dwordx4 v[40:43], v0, s[56:59], s10 offen offset:2048 sc1\n"
"qm v,a,a,212,120,240\nql 200,0,56,82,0\nqm v,a,a,180,120,232\nql 204,0,56,82,1024\nqm v,a,a,180,124,236\nqt 118,11,41984\n"
"qm v,a,a,148,124,228\nqm v,a,a,148,120,224\nqw 8\nqv 23\nqt 120,12,32768\nqm v,a,a,120,96,128\n"
"ql 208,0,56,83,0\nqm v,a,a,120,100,132\nqt 122,12,33792\nqm v,a,a,152,100,140\nqt 124,12,40960\nqm v,a,a,152,96,136\n"
"ql 212,0,56,83,1024\nqm v,a,a,184,96,144\nqt 126,12,41984\nqm v,a,a,184,100,148\ns_getpc_b64 s[0:1]\nCT3%=:\n"
"s_add_u32 s0, s0, F%=-CT3%=\ns_addc_u32 s1, s1, 0\nqm v,a,a,216,100,156\ns_lshl2_add_u32 s2, s19, s15\ns_mul_i32 s2, s2, 512\nv_lshl_add_u32 v254, v0, 3, s2\n"
"qm v,a,a,216,96,152\nqw 8\ns_mov_b32 s2, FINISH%=-F%=\nqt 96,13,32768\nqm v,a,a,220,104,152\nql 216,0,56,84,0\n"
"qm v,a,a,220,108,156\ns_mov_b32 s3, 0x20000\nqt 98,13,33792\nqm v,a,a,188,108,148\nqt 100,13,40960\nqm v,a,a,188,104,144\n"
"ql 220,0,56,84,1024\nqm v,a,a,156,104,136\nqt 102,13,41984\nqm v,a,a,156,108,140\nqm v,a,a,124,108,132\nqm v,a,a,124,104,128\n"
"qw 8\nqt 104,14,32768\nqm v,a,a,128,112,128\nql 224,0,56,81,2048\nqm v,a,a,128,116,132\nqt 106,14,33792\n"
"qm v,a,a,160,116,140\nqt 108,14,40960\nqm v,a,a,160,112,136\nql 228,0,56,81,3072\nqm v,a,a,192,112,144\nqt 110,14,41984\n"
"qm v,a,a,192,116,148\nqm v,a,a,224,116,156\nql 232,0,56,82,2048\nqm v,a,a,224,112,152\nqw 8\nqt 112,15,32768\n"
"qm v,a,a,228,120,152\nqt 114,15,33792\nqm v,a,a,228,124,156\nqt 116,15,40960\nqm v,a,a,196,124,148\nql 236,0,56,82,3072\n"
"qm v,a,a,196,120,144\nqt 118,15,41984\nqm v,a,a,164,120,136\nqm v,a,a,164,124,140\nqm v,a,a,132,124,132\nql 240,0,56,83,2048\n"
"qm v,a,a,132,120,128\nqw 8\nqt 120,16,32768\nqm v,a,a,136,96,128\nqt 122,16,33792\nqm v,a,a,136,100,132\n"
"qt 124,16,40960\nqm v,a,a,168,100,140\nqt 126,16,41984\nqm v,a,a,168,96,136\nqm v,a,a,200,96,144\nqm v,a,a,200,100,148\n"
"qm v,a,a,232,100,156\nqm v,a,a,232,96,152\nqw 8\nqt 96,9,49152\nqm v,a,a,236,104,152\nqt 98,9,50176\n"
"qm v,a,a,236,108,156\nqt 100,9,57344\nqm v,a,a,204,108,148\nbuffer_store_dwordx4 v[44:47], v0, s[56:59], s12 offen offset:2048 sc1\nqm v,a,a,204,104,144\nql 244,0,56,83,3072\n"
"qm v,a,a,172,104,136\nql 248,0,56,84,2048\nqm v,a,a,172,108,140\nqt 102,9,58368\nqm v,a,a,140,108,132\nqm v,a,a,140,104,128\n"
"qw 8\nqt 104,10,49152\nqm v,a,a,144,112,128\nql 252,0,56,84,3072\nqm v,a,a,144,116,132\ns_mov_b32 s75, 0x1000\n"
"s_add_u32 s81, s81, s75\ns_add_u32 s82, s82, s75\nqm v,a,a,176,116,140\ns_add_u32 s83, s83, s75\ns_add_u32 s84, s84, s75\ns_add_u32 m0, s87, 0x0\n"
"qm v,a,a,176,112,136\nqd 248,60,85,0\nqm v,a,a,208,112,144\nqt 106,10,50176\nqm v,a,a,208,116,148\nqt 108,10,57344\n"
"qm v,a,a,240,116,156\nqt 110,10,58368\nqm v,a,a,240,112,152\nqw 8\nqt 112,11,49152\nqm v,a,a,244,120,152\n"
"qd 249,60,85,1024\nqm v,a,a,244,124,156\nqt 114,11,50176\nqm v,a,a,212,124,148\nqt 116,11,57344\nqm v,a,a,212,120,144\n"
"qd 250,60,85,2048\nqm v,a,a,180,120,136\nqt 118,11,58368\nqm v,a,a,180,124,140\nqm v,a,a,148,124,132\nqm v,a,a,148,120,128\n"
"qw 8\nqv 30\nqt 120,12,49152\nqm v,a,a,120,96,160\nqt 122,12,50176\nqm v,a,a,120,100,164\n"
"qt 124,12,57344\nqm v,a,a,152,100,172\nqt 126,12,58368\nqm v,a,a,152,96,168\nqm v,a,a,184,96,176\nqm v,a,a,184,100,180\n"
"qm v,a,a,216,100,188\nbuffer_store_dwordx4 v[48:51], v0, s[56:59], s10 offen offset:3072 sc1\nqm v,a,a,216,96,184\nqd 251,60,85,3072\ns_add_u32 m0, s87, 0x1000\nqw 8\n"
"qm v,a,a,220,104,184\nqd 248,60,86,0\nqm v,a,a,220,108,188\nqt 96,13,49152\nqm v,a,a,188,108,180\nqt 98,13,50176\n"
"qm v,a,a,188,104,176\nqt 100,13,57344\nqm v,a,a,156,104,168\nqd 249,60,86,1024\nqm v,a,a,156,108,172\nqt 102,13,58368\n"
"qm v,a,a,124,108,164\nqm v,a,a,124,104,160\nqw 8\nqt 104,14,49152\nqm v,a,a,128,112,160\nqd 250,60,86,2048\n"
"qm v,a,a,128,116,164\nqt 106,14,50176\nqm v,a,a,160,116,172\nqt 108,14,57344\nqm v,a,a,160,112,168\nqt 110,14,58368\n"
"qm v,a,a,192,112,176\nqd 251,60,86,3072\nqm v,a,a,192,116,180\ns_mov_b32 s75, 0x100000\ns_add_u32 s85, s85, s75\ns_add_u32 s86, s86, s75\n"
"qm v,a,a,224,116,188\nqm v,a,a,224,112,184\nqw 8\nqt 112,15,49152\nqm v,a,a,228,120,184\nql 128,0,56,81,0\n"
"qm v,a,a,228,124,188\nqt 114,15,50176\nqm v,a,a,196,124,180\nqt 116,15,57344\nqm v,a,a,196,120,176\nql 132,0,56,81,1024\n"
"qm v,a,a,164,120,168\nqt 118,15,58368\nqm v,a,a,164,124,172\nqm v,a,a,132,124,164\nqm v,a,a,132,120,160\nqw 8\n"
"qt 120,16,49152\nqm v,a,a,136,96,160\nqt 122,16,50176\nqm v,a,a,136,100,164\nqt 124,16,57344\nqm v,a,a,168,100,172\n"
"qt 126,16,58368\nqm v,a,a,168,96,168\nqm v,a,a,200,96,176\nqm v,a,a,200,100,180\nqm v,a,a,232,100,188\nqm v,a,a,232,96,184\n"
"qw 8\nbuffer_store_dwordx4 v[52:55], v0, s[56:59], s12 offen offset:3072 sc1\nqm v,a,a,236,104,184\nql 136,0,56,82,0\nqm v,a,a,236,108,188\nql 140,0,56,82,1024\n"
"qm v,a,a,204,108,180\nqm v,a,a,204,104,176\nqm v,a,a,172,104,168\nqw 0\nqv 32\ns_barrier\n"
"qm v,a,a,172,108,172\nql 144,0,56,83,0\nqm v,a,a,140,108,164\nqt 96,1,0\nqm v,a,a,140,104,160\nqt 98,1,1024\n"
"qm v,a,a,144,112,160\nql 148,0,56,83,1024\nqm v,a,a,144,116,164\nqt 100,1,8192\nqm v,a,a,176,116,172\nqt 102,1,9216\n"
"qm v,a,a,176,112,168\nqt 104,2,0\nqm v,a,a,208,112,176\nql 152,0,56,84,0\nqm v,a,a,208,116,180\nqt 106,2,1024\n"
"qm v,a,a,240,116,188\nqt 108,2,8192\nqm v,a,a,240,112,184\nqt 110,2,9216\nqm v,a,a,244,120,184\nql 156,0,56,84,1024\n"
"qm v,a,a,244,124,188\nqt 112,3,0\nqm v,a,a,212,124,180\nqt 114,3,1024\nqm v,a,a,212,120,176\nqt 116,3,8192\n"
"qm v,a,a,180,120,168\nqt 118,3,9216\nqm v,a,a,180,124,172\nqm v,a,a,148,124,164\nqm v,a,a,148,120,160\nqw 8\n"
"qv 27\nqt 120,4,0\nqm v,a,a,120,96,192\nqt 122,4,1024\nqm v,a,a,120,100,196\nqt 124,4,8192\n"
"qm v,a,a,152,100,204\ns_mov_b64 exec, 1\nbuffer_store_dword v17, off, s[56:59], s13 sc1\ns_mov_b64 exec, -1\nqm v,a,a,152,96,200\nqt 126,4,9216\n"
"qm v,a,a,184,96,208\nqm v,a,a,184,100,212\nqm v,a,a,216,100,220\ns_bfm_b64 exec, 4, 0\nbuffer_load_dword a95, v254, s[0:3], 0 offen\ns_mov_b64 exec, -1\n"
"qm v,a,a,216,96,216\nqw 8\nqt 96,5,0\nqm v,a,a,220,104,216\nql 160,0,56,81,2048\nqm v,a,a,220,108,220\n"
"ql 164,0,56,81,3072\nqm v,a,a,188,108,212\nqt 98,5,1024\nqm v,a,a,188,104,208\nqt 100,5,8192\nqm v,a,a,156,104,200\n"
"qt 102,5,9216\nqm v,a,a,156,108,204\nqm v,a,a,124,108,196\nql 168,0,56,82,2048\nqm v,a,a,124,104,192\nqw 8\n"
"qt 104,6,0\nqm v,a,a,128,112,192\nqt 106,6,1024\nqm v,a,a,128,116,196\nqt 108,6,8192\nqm v,a,a,160,116,204\n"
"ql 172,0,56,82,3072\nqm v,a,a,160,112,200\nqt 110,6,9216\nqm v,a,a,192,112,208\nqm v,a,a,192,116,212\nqm v,a,a,224,116,220\n"
"qm v,a,a,224,112,216\nqw 8\nqt 112,7,0\nqm v,a,a,228,120,216\nql 176,0,56,83,2048\nqm v,a,a,228,124,220\n"
"qt 114,7,1024\nqm v,a,a,196,124,212\nqt 116,7,8192\nqm v,a,a,196,120,208\nqt 118,7,9216\nqm v,a,a,164,120,200\n"
"ql 180,0,56,83,3072\nqm v,a,a,164,124,204\nqm v,a,a,132,124,196\nqm v,a,a,132,120,192\nqw 8\nqt 120,8,0\n"
"qm v,a,a,136,96,192\nqt 122,8,1024\nqm v,a,a,136,100,196\nql 184,0,56,84,2048\nqm v,a,a,168,100,204\nqt 124,8,8192\n"
"qm v,a,a,168,96,200\nqt 126,8,9216\nqm v,a,a,200,96,208\nqm v,a,a,200,100,212\nql 188,0,56,84,3072\nqm v,a,a,232,100,220\n"
"s_mov_b32 s75, 0x1000\ns_add_u32 s81, s81, s75\ns_add_u32 s82, s82, s75\nqm v,a,a,232,96,216\nqw 8\ns_add_u32 s83, s83, s75\n"
"s_add_u32 s84, s84, s75\nqm v,a,a,236,104,216\ns_add_u32 m0, s87, 0x8000\nqt 96,1,16384\nqm v,a,a,236,108,220\nqt 98,1,17408\n"
"qm v,a,a,204,108,212\nqd 248,60,85,0\nqm v,a,a,204,104,208\nqt 100,1,24576\nqm v,a,a,172,104,200\nqt 102,1,25600\n"
"qm v,a,a,172,108,204\nqm v,a,a,140,108,196\nqm v,a,a,140,104,192\nqw 8\nqt 104,2,16384\nqm v,a,a,144,112,192\n"
"qd 249,60,85,1024\nqm v,a,a,144,116,196\nqt 106,2,17408\nqm v,a,a,176,116,204\nqt 108,2,24576\nqm v,a,a,176,112,200\n"
"qt 110,2,25600\nqm v,a,a,208,112,208\nqd 250,60,85,2048\nqm v,a,a,208,116,212\nqm v,a,a,240,116,220\nqm v,a,a,240,112,216\n"
"qw 8\nqt 112,3,16384\nqm v,a,a,244,120,216\nqd 251,60,85,3072\nqm v,a,a,244,124,220\ns_add_u32 m0, s87, 0x9000\n"
"qt 114,3,17408\nqm v,a,a,212,124,212\nqt 116,3,24576\nqm v,a,a,212,120,208\nqt 118,3,25600\nqm v,a,a,180,120,200\n"
"qm v,a,a,180,124,204\nqd 248,60,86,0\nqm v,a,a,148,124,196\nqm v,a,a,148,120,192\nqw 8\nqv 33\n"
"qt 120,4,16384\nqm v,a,a,120,96,224\nqt 122,4,17408\nv_lshrrev_b32 v253, 8, v0\nqm v,a,a,120,100,228\nqt 124,4,24576\n"
"v_lshlrev_b32 v253, 2, v253\nqm v,a,a,152,100,236\nqd 249,60,86,1024\nqm v,a,a,152,96,232\nqt 126,4,25600\nv_bfe_u32 v252, v0, 4, 4\n"
"qm v,a,a,184,96,240\nv_sub_u32 v252, v253, v252\nqm v,a,a,184,100,244\ns_lshl_b32 s72, s15, 4\nv_add_u32 v254, s72, v252\nv_subrev_u32 v252, s72, v252\n"
"qm v,a,a,216,100,252\nqv 15\nv_mov_b32 v253, 0xff800000\nqm v,a,a,216,96,248\nqw 8\ns_mov_b32 s12, 0\n"
"qt 96,5,16384\nqm v,a,a,220,104,248\nqd 250,60,86,2048\nqm v,a,a,220,108,252\nqt 98,5,17408\nv_cmp_lt_i32 s[72:73], s12, v254\n"
"qm v,a,a,188,108,244\ns_mov_b32 s12, -1\nqt 100,5,24576\nv_cmp_lt_i32 s[98:99], s12, v254\nqm v,a,a,188,104,240\nqd 251,60,86,3072\n"
"qm v,a,a,156,104,232\ns_mov_b32 s12, -2\nqt 102,5,25600\nv_cndmask_b32 v48, 0, v253, s[72:73]\nqm v,a,a,156,108,236\nv_cndmask_b32 v49, 0, v253, s[98:99]\n"
"v_cmp_lt_i32 s[72:73], s12, v254\nqm v,a,a,124,108,228\ns_mov_b32 s12, -3\nv_cmp_lt_i32 s[98:99], s12, v254\nqm v,a,a,124,104,224\nqw 8\n"
"qt 104,6,16384\nv_cndmask_b32 v50, 0, v253, s[72:73]\nqm v,a,a,128,112,224\nql 192,0,56,81,0\nqm v,a,a,128,116,228\ns_mov_b32 s12, 16\n"
"qt 106,6,17408\nv_cndmask_b32 v51, 0, v253, s[98:99]\nqm v,a,a,160,116,236\nqt 108,6,24576\nv_cmp_lt_i32 s[72:73], s12, v254\nqm v,a,a,160,112,232\n"
"s_mov_b32 s12, 15\nqt 110,6,25600\nv_cmp_lt_i32 s[98:99], s12, v254\nqm v,a,a,192,112,240\ns_mov_b32 s12, 14\nv_cndmask_b32 v44, 0, v253, s[72:73]\n"
"v_cmp_lt_i32 s[72:73], s12, v254\nqm v,a,a,192,116,244\nql 196,0,56,81,1024\nqm v,a,a,224,116,252\ns_mov_b32 s12, 13\nv_cndmask_b32 v45, 0, v253, s[98:99]\n"
"v_cmp_lt_i32 s[98:99], s12, v254\nqm v,a,a,224,112,248\nqw 8\nqt 112,7,16384\nv_cndmask_b32 v46, 0, v253, s[72:73]\nqm v,a,a,228,120,248\n"
"s_mov_b32 s12, 32\nqt 114,7,17408\nv_cndmask_b32 v47, 0, v253, s[98:99]\nqm v,a,a,228,124,252\nqt 116,7,24576\nv_cmp_lt_i32 s[72:73], s12, v254\n"
"qm v,a,a,196,124,244\ns_mov_b32 s12, 31\nqt 118,7,25600\nv_cmp_lt_i32 s[98:99], s12, v254\nqm v,a,a,196,120,240\ns_mov_b32 s12, 30\n"
"v_cndmask_b32 v40, 0, v253, s[72:73]\nv_cmp_lt_i32 s[72:73], s12, v254\nqm v,a,a,164,120,232\ns_mov_b32 s12, 29\nv_cndmask_b32 v41, 0, v253, s[98:99]\nv_cmp_lt_i32 s[98:99], s12, v254\n"
"qm v,a,a,164,124,236\ns_mov_b32 s12, 48\nv_cndmask_b32 v42, 0, v253, s[72:73]\nv_cmp_lt_i32 s[72:73], s12, v254\nqm v,a,a,132,124,228\ns_mov_b32 s12, 47\n"
"v_cndmask_b32 v43, 0, v253, s[98:99]\nv_cmp_lt_i32 s[98:99], s12, v254\nqm v,a,a,132,120,224\nqw 8\nqt 120,8,16384\nv_cndmask_b32 v36, 0, v253, s[72:73]\n"
"qm v,a,a,136,96,224\ns_mov_b64 exec, 1\nbuffer_store_dwordx2 v[18:19], off, s[56:59], s34 sc1\ns_mov_b64 exec, -1\nqm v,a,a,136,100,228\nql 200,0,56,82,0\n"
"qm v,a,a,168,100,236\ns_mov_b32 s12, 46\nqt 122,8,17408\nv_cndmask_b32 v37, 0, v253, s[98:99]\nqm v,a,a,168,96,232\nqt 124,8,24576\n"
"v_cmp_lt_i32 s[72:73], s12, v254\nqm v,a,a,200,96,240\ns_mov_b32 s12, 45\nqt 126,8,25600\nv_cmp_lt_i32 s[98:99], s12, v254\nqm v,a,a,200,100,244\n"
"ql 204,0,56,82,1024\nqm v,a,a,232,100,252\ns_mov_b32 s12, -48\nv_cndmask_b32 v38, 0, v253, s[72:73]\nv_cndmask_b32 v39, 0, v253, s[98:99]\nqm v,a,a,232,96,248\n"
"qw 8\nv_cmp_lt_i32 s[72:73], s12, v252\nqm v,a,a,236,104,248\ns_mov_b32 s12, -49\nv_cmp_lt_i32 s[98:99], s12, v252\nqm v,a,a,236,108,252\n"
"s_mov_b32 s12, -50\nv_cndmask_b32 v32, 0, v253, s[72:73]\nv_cmp_lt_i32 s[72:73], s12, v252\nqm v,a,a,204,108,244\ns_mov_b32 s12, -51\nv_cndmask_b32 v33, 0, v253, s[98:99]\n"
"v_cmp_lt_i32 s[98:99], s12, v252\nqm v,a,a,204,104,240\nql 208,0,56,83,0\nqm v,a,a,172,104,232\nqw 0\ns_barrier\n"
"qm v,a,a,172,108,236\ns_mov_b32 s12, -32\nqt 96,9,0\nv_cndmask_b32 v34, 0, v253, s[72:73]\nqm v,a,a,140,108,228\nqt 98,9,1024\n"
"v_cndmask_b32 v35, 0, v253, s[98:99]\nqm v,a,a,140,104,224\nqt 100,9,8192\nv_cmp_lt_i32 s[72:73], s12, v252\nqm v,a,a,144,112,224\ns_mov_b32 s12, -33\n"
"qt 102,9,9216\nv_cmp_lt_i32 s[98:99], s12, v252\nqm v,a,a,144,116,228\nql 212,0,56,83,1024\nqm v,a,a,176,116,236\ns_mov_b32 s12, -34\n"
"qt 104,10,0\nv_cndmask_b32 v28, 0, v253, s[72:73]\nqm v,a,a,176,112,232\nqt 106,10,1024\nv_cndmask_b32 v29, 0, v253, s[98:99]\nqm v,a,a,208,112,240\n"
"qt 108,10,8192\nv_cmp_lt_i32 s[72:73], s12, v252\nqm v,a,a,208,116,244\ns_mov_b32 s12, -35\nqt 110,10,9216\nv_cmp_lt_i32 s[98:99], s12, v252\n"
"qm v,a,a,240,116,252\ns_mov_b32 s12, -16\nv_cndmask_b32 v30, 0, v253, s[72:73]\nv_cmp_lt_i32 s[72:73], s12, v252\nqm v,a,a,240,112,248\nqt 112,11,0\n"
"v_cndmask_b32 v31, 0, v253, s[98:99]\nqm v,a,a,244,120,248\nql 216,0,56,84,0\nqm v,a,a,244,124,252\ns_mov_b32 s12, -17\nqt 114,11,1024\n"
"v_cmp_lt_i32 s[98:99], s12, v252\nqm v,a,a,212,124,244\ns_mov_b32 s12, -18\nqt 116,11,8192\nv_cndmask_b32 v24, 0, v253, s[72:73]\nqm v,a,a,212,120,240\n"
"qt 118,11,9216\nv_cndmask_b32 v25, 0, v253, s[98:99]\nqm v,a,a,180,120,232\nv_cmp_lt_i32 s[72:73], s12, v252\nqm v,a,a,180,124,236\nql 220,0,56,84,1024\n"
"qm v,a,a,148,124,228\ns_mov_b32 s12, -19\nv_cmp_lt_i32 s[98:99], s12, v252\nv_cndmask_b32 v26, 0, v253, s[72:73]\nqm v,a,a,148,120,224\nqw 8\n"
"qt 120,12,0\nqm v,a,a,120,96,128\ns_lshr_b32 s5, s88, 3\ns_sub_i32 s4, 13, s5\nqt 122,12,1024\nqm v,a,a,120,100,132\n"
"s_min_i32 s4, s4, s5\ns_max_i32 s4, s4, 0\nqt 124,12,8192\nqm v,a,a,152,100,140\ns_and_b32 s4, s4, 1\ns_lshl_b32 s79, s4, 15\n"
"qt 126,12,9216\nqm v,a,a,152,96,136\nql 224,0,56,81,2048\nqm v,a,a,184,96,144\ns_xor_b32 s78, s79, 0x8000\ns_lshl_b32 s5, s15, 12\n"
"s_add_u32 s34, s5, s79\nqm v,a,a,184,100,148\ns_add_u32 s13, s5, s78\ns_lshl_b32 s6, s20, 8\ns_add_u32 s6, s6, 0x3000\nqm v,a,a,216,100,156\n"
"s_lshl_b32 s7, s20, 7\ns_add_u32 s7, s7, 0xf000\nv_lshrrev_b32 v17, 1, v0\nqm v,a,a,216,96,152\nqw 8\nqt 96,13,0\n"
"v_and_b32 v17, 0xf8, v17\nqm v,a,a,220,104,152\nql 228,0,56,81,3072\nqm v,a,a,220,108,156\ns_mov_b32 s12, 0\nqt 98,13,1024\n"
"v_lshrrev_b32 v248, 1, v17\nqm v,a,a,188,108,148\nqt 100,13,8192\nv_cndmask_b32 v27, 0, v253, s[98:99]\nqm v,a,a,188,104,144\nqt 102,13,9216\n"
"v_cmp_lt_i32 s[72:73], s12, v252\nqm v,a,a,156,104,136\ns_mov_b32 s12, -1\nv_cmp_lt_i32 s[98:99], s12, v252\nqb 0,0\nqm v,a,a,156,108,140\n"
"s_mov_b32 s12, -2\nv_cndmask_b32 v20, 0, v253, s[72:73]\nv_cmp_lt_i32 s[72:73], s12, v252\nqm v,a,a,124,108,132\nql 232,0,56,82,2048\nqm v,a,a,124,104,128\n"
"qw 8\nqt 104,14,0\nv_cndmask_b32 v21, 0, v253, s[98:99]\nqm v,a,a,128,112,128\ns_mov_b32 s12, -3\nqt 106,14,1024\n"
"v_cmp_lt_i32 s[98:99], s12, v252\nqm v,a,a,128,116,132\nqt 108,14,8192\nqb 1,0\nqm v,a,a,160,116,140\nqt 110,14,9216\n"
"qb 2,0\nqm v,a,a,160,112,136\nqb 3,0\nqb 4,0\nqm v,a,a,192,112,144\nql 236,0,56,82,3072\n"
"qm v,a,a,192,116,148\nqb 5,0\nqb 6,0\nqm v,a,a,224,116,156\nqb 7,0\nqb 8,0\n"
"qm v,a,a,224,112,152\nqw 8\nqt 112,15,0\nqb 9,0\nqm v,a,a,228,120,152\nqt 114,15,1024\n"
"qb 10,0\nqm v,a,a,228,124,156\nqt 116,15,8192\nqb 11,0\nqm v,a,a,196,124,148\nql 240,0,56,83,2048\n"
"qm v,a,a,196,120,144\nqt 118,15,9216\nqb 12,0\nqm v,a,a,164,120,136\nqb 13,0\nqb 14,0\n"
"qm v,a,a,164,124,140\nqb 15,0\nqb 16,0\nqm v,a,a,132,124,132\nqb 17,0\nqb 18,0\n"
"qm v,a,a,132,120,128\nqw 8\nqt 120,16,0\nqb 19,0\nqm v,a,a,136,96,128\nql 244,0,56,83,3072\n"
"qm v,a,a,136,100,132\nqt 122,16,1024\nqb 20,0\nqm v,a,a,168,100,140\nqt 124,16,8192\nqb 21,0\n"
"qm v,a,a,168,96,136\nqt 126,16,9216\nqb 22,0\nqm v,a,a,200,96,144\nqb 23,0\nqb 24,0\n"
"qm v,a,a,200,100,148\nqb 25,0\nqb 26,0\nqm v,a,a,232,100,156\nql 248,0,56,84,2048\nqm v,a,a,232,96,152\n"
"qw 8\nqt 96,9,16384\nqb 27,0\nqm v,a,a,236,104,152\nqt 98,9,17408\nqb 28,0\n"
"qm v,a,a,236,108,156\nqt 100,9,24576\nqb 29,0\nqm v,a,a,204,108,148\nqt 102,9,25600\nqb 30,0\n"
"qm v,a,a,204,104,144\nqb 31,0\nqb 32,0\nqm v,a,a,172,104,136\nql 252,0,56,84,3072\nqm v,a,a,172,108,140\n"
"qb 33,0\nqb 34,0\nqm v,a,a,140,108,132\nqb 35,0\nqb 36,0\nqm v,a,a,140,104,128\n"
"qw 8\nqt 104,10,16384\nqb 37,0\nqm v,a,a,144,112,128\nqt 106,10,17408\nqb 38,0\n"
"qm v,a,a,144,116,132\nqt 108,10,24576\nqb 39,0\nqm v,a,a,176,116,140\nbuffer_load_dwordx2 v[250:251], v17, s[56:59], s6 offen sc1\nqm v,a,a,176,112,136\n"
"qt 110,10,25600\nqb 40,0\nqm v,a,a,208,112,144\nqb 41,0\nqb 42,0\nqm v,a,a,208,116,148\n"
"qb 43,0\nqb 44,0\nqm v,a,a,240,116,156\nqb 45,0\nqb 46,0\nqm v,a,a,240,112,152\n"
"qw 8\nqt 112,11,16384\nqb 47,0\nqm v,a,a,244,120,152\nqt 114,11,17408\nqb 48,0\n"
"qm v,a,a,244,124,156\nqt 116,11,24576\nqb 49,0\nqm v,a,a,212,124,148\nqt 118,11,25600\nqb 50,0\n"
"qm v,a,a,212,120,144\nqb 51,0\nqb 52,0\nqm v,a,a,180,120,136\ns_lshl_b32 s86, s20, 19\ns_add_u32 s86, s86, s5\n"
"s_add_u32 s86, s86, 0x800000\nqm v,a,a,180,124,140\ns_lshr_b32 s87, s97, 3\ns_lshl_b32 s87, s87, 15\ns_add_u32 s87, s87, s86\nqm v,a,a,148,124,132\n"
"s_lshr_b32 s85, s88, 3\ns_lshl_b32 s85, s85, 15\ns_add_u32 s85, s85, s86\nqm v,a,a,148,120,128\nqw 8\nqv 26\n"
"qt 120,12,16384\nqm v,a,a,120,96,160\ns_mov_b32 s76, s85\nqt 122,12,17408\nqb 53,0\nqm v,a,a,120,100,164\n"
"qt 124,12,24576\nqb 54,0\nqm v,a,a,152,100,172\nqt 126,12,25600\nqb 55,0\nqm v,a,a,152,96,168\n"
"qb 56,0\nqb 57,0\nqm v,a,a,184,96,176\nqb 58,0\nqb 59,0\nqm v,a,a,184,100,180\n"
"qb 60,0\nqb 61,0\nqm v,a,a,216,100,188\nqb 62,0\nqb 63,0\nqm v,a,a,216,96,184\n"
"qw 8\nqt 96,13,16384\nqb 64,0\nqm v,a,a,220,104,184\nqt 98,13,17408\nqb 65,0\n"
"qm v,a,a,220,108,188\nqt 100,13,24576\nqb 66,0\nqm v,a,a,188,108,180\nqt 102,13,25600\nqb 67,0\n"
"qm v,a,a,188,104,176\nqb 68,0\nqb 69,0\nqm v,a,a,156,104,168\nqb 70,0\nqb 71,0\n"
"qm v,a,a,156,108,172\nv_cndmask_b32 v22, 0, v253, s[72:73]\nv_cndmask_b32 v23, 0, v253, s[98:99]\nqm v,a,a,124,108,164\nqm v,a,a,124,104,160\nqw 8\n"
"qt 104,14,16384\nqm v,a,a,128,112,160\nqt 106,14,17408\nqm v,a,a,128,116,164\nqt 108,14,24576\nqm v,a,a,160,116,172\n"
"qt 110,14,25600\nqm v,a,a,160,112,168\nqm v,a,a,192,112,176\nqm v,a,a,192,116,180\nqm v,a,a,224,116,188\nqm v,a,a,224,112,184\n"
"qw 8\nqt 112,15,16384\nqm v,a,a,228,120,184\nqt 114,15,17408\nqm v,a,a,228,124,188\nqt 116,15,24576\n"
"qm v,a,a,196,124,180\nqt 118,15,25600\nqm v,a,a,196,120,176\nqm v,a,a,164,120,168\nqm v,a,a,164,124,172\nqm v,a,a,132,124,164\n"
"qm v,a,a,132,120,160\nqw 8\nqt 120,16,16384\nqm v,a,a,136,96,160\nqt 122,16,17408\nqm v,a,a,136,100,164\n"
"qt 124,16,24576\nqm v,a,a,168,100,172\nqt 126,16,25600\nqm v,a,a,168,96,168\nqm v,a,a,200,96,176\nqv 0\n"
"v_cmp_eq_u64 vcc, s[16:17], v[250:251]\nqm v,a,a,200,100,180\nqm v,a,a,232,100,188\nqm v,a,a,232,96,184\nqw 8\nqm v,a,a,236,104,184\n"
"s_cmp_eq_u64 vcc, exec\ns_cbranch_scc0 PKV_o%=\nPKV_r%=:\nqm v,a,a,236,108,188\nqm v,a,a,204,108,180\ns_mov_b32 m0, s13\n"
"s_add_u32 s13, s13, 0x4000\nqm v,a,a,204,104,176\nqe 0,56,76,0\nqm v,a,a,172,104,168\nqw 0\ns_barrier\n"
"qm v,a,a,172,108,172\nqt 96,9,32768\nqm v,a,a,140,108,164\nqt 98,9,33792\nqm v,a,a,140,104,160\nqt 100,9,40960\n"
"qm v,a,a,144,112,160\nqt 102,9,41984\nqm v,a,a,144,116,164\nqt 104,10,32768\nqm v,a,a,176,116,172\nqe 0,56,76,1024\n"
"qm v,a,a,176,112,168\nqt 106,10,33792\nqm v,a,a,208,112,176\nqt 108,10,40960\nqm v,a,a,208,116,180\nqt 110,10,41984\n"
"qm v,a,a,240,116,188\nqm v,a,a,240,112,184\nqt 112,11,32768\nqm v,a,a,244,120,184\nqt 114,11,33792\nqm v,a,a,244,124,188\n"
"qt 116,11,40960\nqm v,a,a,212,124,180\nqe 0,56,76,2048\nqm v,a,a,212,120,176\nqt 118,11,41984\nqm v,a,a,180,120,168\n"
"qm v,a,a,180,124,172\nqm v,a,a,148,124,164\nqm v,a,a,148,120,160\nqw 8\nqt 120,12,32768\nqm v,a,a,120,96,192\n"
"s_lshl2_add_u32 s9, s14, s15\ns_mul_i32 s8, s9, 7168\ns_min_u32 s8, s8, 7331840\nqm v,a,a,120,100,196\ns_lshl_b32 s9, s9, 13\ns_add_u32 s10, s46, s9\n"
"s_addc_u32 s11, s47, 0\nqm v,a,a,152,100,204\nqe 0,56,76,3072\nqm v,a,a,152,96,200\ns_and_b32 s13, s13, 0xffff\ns_add_u32 s76, s76, 0x4000\n"
"v_mov_b32 v100, s10\nqm v,a,a,184,96,208\nqt 122,12,33792\nv_mov_b32 v101, s11\nqm v,a,a,184,100,212\ns_add_u32 s10, s48, s8\n"
"s_addc_u32 s11, s49, 0\nqt 124,12,40960\nqm v,a,a,216,100,220\nqt 126,12,41984\nv_add_u32 v1, s78, v0\nqm v,a,a,216,96,216\n"
"qw 8\nqt 96,13,32768\nv_add_u32 v3, 0x10000, v0\nqm v,a,a,220,104,216\nqt 98,13,33792\nv_add_u32 v2, s79, v3\n"
"qm v,a,a,220,108,220\nqt 100,13,40960\nv_mov_b32 v252, 0x3f803f80\nqm v,a,a,188,108,212\nbuffer_load_dword v249, v248, s[56:59], s7 offen sc1\nqm v,a,a,188,104,208\n"
"s_mov_b32 m0, s13\ns_add_u32 s13, s13, 0x4000\nqt 102,13,41984\nqm v,a,a,156,104,200\nv_mov_b32 v253, 0x3f803f80\nv_mov_b32 v254, 0x3f803f80\n"
"qm v,a,a,156,108,204\nv_mov_b32 v255, 0x3f803f80\nqm v,a,a,124,108,196\nqm v,a,a,124,104,192\nqw 8\ns_lshl_b64 exec, -1, 8\n"
"v_mov_b32 v100, s10\nv_mov_b32 v101, s11\ns_mov_b64 exec, -1\nqm v,a,a,128,112,192\ns_add_u32 s10, s50, s8\ns_addc_u32 s11, s51, 0\n"
"qt 104,14,32768\nqm v,a,a,128,116,196\ns_lshl_b64 exec, -1, 16\nv_mov_b32 v100, s10\nv_mov_b32 v101, s11\ns_mov_b64 exec, -1\n"
"qm v,a,a,160,116,204\nv_and_b32 v102, 0x70, v0\nv_lshlrev_b32 v102, 3, v102\nqm v,a,a,160,112,200\nqe 0,56,76,0\nqm v,a,a,192,112,208\n"
"v_add_co_u32 v100, vcc, v100, v102\ns_nop 1\nv_addc_co_u32 v101, vcc, 0, v101, vcc\nqm v,a,a,192,116,212\nv_mbcnt_lo_u32_b32 v102, -1, 0\nv_mbcnt_hi_u32_b32 v102, -1, v102\n"
"qm v,a,a,224,116,220\ns_mov_b32 s60, s46\ns_and_b32 s61, s47, 0xffff\ns_mov_b32 s62, 0x800000\nqm v,a,a,224,112,216\nqw 5\n"
"s_mov_b32 s63, 0x20000\ns_lshl_b32 s73, s15, 1\nqm v,a,a,228,120,216\ns_lshl3_add_u32 s73, s22, s73\ns_add_u32 s72, s73, 0\ns_mul_i32 s72, s72, 131072\n"
"qm v,a,a,228,124,220\ns_add_u32 s72, s72, 0x2800000\ns_add_u32 s64, s56, s72\ns_addc_u32 s65, s57, 0\nqm v,a,a,196,124,212\nqe 0,56,76,1024\n"
"qm v,a,a,196,120,208\ns_mov_b32 s66, 131072\ns_mov_b32 s67, 0x20000\nqt 106,14,33792\nqm v,a,a,164,120,200\ns_add_u32 s72, s73, 1\n"
"s_mul_i32 s72, s72, 131072\nqt 108,14,40960\nqm v,a,a,164,124,204\ns_add_u32 s72, s72, 0x2800000\nqt 110,14,41984\nqm v,a,a,132,124,196\n"
"s_add_u32 s68, s56, s72\ns_addc_u32 s69, s57, 0\ns_mov_b32 s70, 131072\nqm v,a,a,132,120,192\nqw 4\ns_mov_b32 s71, 0x20000\n"
"v_lshrrev_b32 v103, 4, v102\nqm v,a,a,136,96,192\nqt 112,15,32768\nv_and_b32 v104, 15, v102\nqm v,a,a,136,100,196\ns_mov_b32 s72, 0x800\n"
"qt 114,15,33792\nv_or_b32 v99, 0, v103\nqm v,a,a,168,100,204\nqe 0,56,76,2048\nqm v,a,a,168,96,200\nqt 116,15,40960\n"
"v_lshlrev_b32 v99, 1, v99\nqm v,a,a,200,96,208\nqt 118,15,41984\nv_xor_b32 v99, v99, v104\nqm v,a,a,200,100,212\nv_lshlrev_b32 v99, 4, v99\n"
"v_mad_u32_u24 v92, v103, s72, v99\nqm v,a,a,232,100,220\nv_or_b32 v99, 4, v103\nv_lshlrev_b32 v99, 1, v99\nqm v,a,a,232,96,216\nqw 4\n"
"qt 120,16,32768\nv_xor_b32 v99, v99, v104\nqm v,a,a,236,104,216\nqt 122,16,33792\nv_lshlrev_b32 v99, 4, v99\nqm v,a,a,236,108,220\n"
"qt 124,16,40960\nv_mad_u32_u24 v93, v103, s72, v99\nqm v,a,a,204,108,212\nqe 0,56,76,3072\ns_and_b32 s13, s13, 0xffff\ns_add_u32 s76, s76, 0x4000\n"
"s_sub_u32 s76, s76, 0x10000\ns_cmp_lt_u32 s76, s86\ns_cselect_b32 s76, s87, s76\nqm v,a,a,204,104,208\nqt 126,16,41984\nv_add_u32 v93, 0x1c00, v93\n"
"qm v,a,a,172,104,200\nv_or_b32 v99, 0, v103\nv_lshlrev_b32 v99, 1, v99\nqm v,a,a,172,108,204\nv_xor_b32 v99, v99, v104\nv_lshlrev_b32 v99, 4, v99\n"
"qm v,a,a,140,108,196\nqt 96,9,49152\nv_mad_u32_u24 v94, v103, s72, v99\nqm v,a,a,140,104,192\nqw 5\nqt 98,9,50176\n"
"v_add_u32 v94, 0x3800, v94\nqm v,a,a,144,112,192\nqt 100,9,57344\nv_or_b32 v99, 4, v103\nqm v,a,a,144,116,196\nqt 102,9,58368\n"
"v_lshlrev_b32 v99, 1, v99\nqm v,a,a,176,116,204\nv_xor_b32 v99, v99, v104\nv_lshlrev_b32 v99, 4, v99\nqm v,a,a,176,112,200\nv_mad_u32_u24 v95, v103, s72, v99\n"
"v_add_u32 v95, 0x5400, v95\nqm v,a,a,208,112,208\ns_lshl_b32 s72, s23, 7\ns_and_b32 s74, s72, 128\nqt 104,10,49152\nqm v,a,a,208,116,212\n"
"qt 106,10,50176\nv_xor_b32 v92, s74, v92\nqm v,a,a,240,116,220\nqt 108,10,57344\nv_xor_b32 v93, s74, v93\nqm v,a,a,240,112,216\n"
"qw 7\nqt 110,10,58368\nv_xor_b32 v94, s74, v94\nqm v,a,a,244,120,216\nv_xor_b32 v95, s74, v95\nv_and_b32 v103, 15, v102\n"
"qm v,a,a,244,124,220\nv_bfe_u32 v104, v102, 4, 1\nv_lshlrev_b32 v103, 11, v103\nqm v,a,a,212,124,212\nqt 112,11,49152\nv_lshl_add_u32 v103, v104, 5, v103\n"
"qm v,a,a,212,120,208\nqt 114,11,50176\nv_lshrrev_b32 v104, 5, v102\nqm v,a,a,180,120,200\nqt 116,11,57344\nv_lshl_add_u32 v96, v104, 4, v103\n"
"qm v,a,a,180,124,204\ns_lshl_b32 s3, s73, 15\ns_add_u32 s3, s3, s72\nqt 118,11,58368\nqm v,a,a,148,124,196\ns_add_u32 s4, s3, 0x8000\n"
"s_lshl_b32 s0, s15, 12\ns_add_u32 s72, s15, 1\nqm v,a,a,148,120,192\nqw 8\ns_and_b32 s72, s72, 2\nqm v,a,a,120,96,224\n"
"s_lshl2_add_u32 s5, s22, s72\ns_lshl_b32 s5, s5, 8\nqt 120,12,49152\nqm v,a,a,120,100,228\ns_add_u32 s5, s5, 0x5000\ns_lshr_b32 s72, s23, 1\n"
"qt 122,12,50176\nqm v,a,a,152,100,236\ns_add_u32 s72, s72, s22\ns_and_b32 s72, s72, 3\nqt 124,12,57344\nqm v,a,a,152,96,232\n"
"s_and_b32 s74, s23, 1\ns_lshl_b32 s75, s74, 3\nqt 126,12,58368\nqm v,a,a,184,96,240\nv_and_b32 v98, 15, v102\nv_subrev_u32 v103, s72, v98\n"
"qm v,a,a,184,100,244\nv_add_u32 v103, 5, v103\nv_and_b32 v103, 3, v103\nqm v,a,a,216,100,252\nv_sub_u32 v104, v98, v103\nv_add_u32 v97, -1, v98\n"
"qm v,a,a,216,96,248\nqw 8\nqt 96,13,49152\nqm v,a,a,220,104,248\nqt 98,13,50176\nqm v,a,a,220,108,252\n"
"qt 100,13,57344\nqm v,a,a,188,108,244\nqt 102,13,58368\nqm v,a,a,188,104,240\nqm v,a,a,156,104,232\nqm v,a,a,156,108,236\n"
"qm v,a,a,124,108,228\nqm v,a,a,124,104,224\nqw 8\nqt 104,14,49152\nqm v,a,a,128,112,224\nqt 106,14,50176\n"
"qm v,a,a,128,116,228\nqt 108,14,57344\nqm v,a,a,160,116,236\nqt 110,14,58368\nqm v,a,a,160,112,232\nqm v,a,a,192,112,240\n"
"qc 120,120\nqc 121,122\nqm v,a,a,192,116,244\nqc 122,124\nqc 123,126\nqm v,a,a,224,116,252\n"
"qc 152,152\nqc 153,154\nqm v,a,a,224,112,248\nqw 8\nqt 112,15,49152\nqc 154,156\n"
"qm v,a,a,228,120,248\nqt 114,15,50176\nqc 155,158\nqm v,a,a,228,124,252\nqt 116,15,57344\nqc 184,184\n"
"qm v,a,a,196,124,244\nqt 118,15,58368\nqc 185,186\nqm v,a,a,196,120,240\nqc 186,188\nqc 187,190\n"
"qm v,a,a,164,120,232\nqc 216,216\nqc 217,218\nqm v,a,a,164,124,236\nqc 218,220\nqc 219,222\n"
"qm v,a,a,132,124,228\nqm v,a,a,132,120,224\nqw 8\nqt 120,16,49152\nqm v,a,a,136,96,224\nqt 122,16,50176\n"
"qm v,a,a,136,100,228\nv_cmp_lt_u32 vcc, 1, v103\ns_nop 1\nv_cndmask_b32 v97, v97, v98, vcc\nqm v,a,a,168,100,236\nv_cmp_eq_u32 vcc, 0, v103\n"
"v_add_u32 v103, 1, v98\ns_nop 0\nv_cndmask_b32 v97, v97, v103, vcc\nqm v,a,a,168,96,232\nv_cmp_le_u32 vcc, 15, v104\ns_nop 1\n"
"v_cndmask_b32 v97, v97, v98, vcc\nqm v,a,a,200,96,240\ns_lshr_b32 s73, s22, 3\nv_bfe_u32 v103, v102, 4, 1\nv_xor_b32 v103, s73, v103\nqm v,a,a,200,100,244\n"
"v_lshrrev_b32 v104, 1, v97\nv_add_u32 v104, s75, v104\nqm v,a,a,232,100,252\nv_lshl_add_u32 v103, v104, 1, v103\nv_and_b32 v97, 1, v97\nqm v,a,a,232,96,248\n"
"qw 6\nv_lshl_add_u32 v97, v103, 1, v97\nv_and_b32 v103, 7, v102\nqm v,a,a,236,104,248\nqt 124,16,57344\nv_add_u32 v103, s75, v103\n"
"qm v,a,a,236,108,252\ns_lshl_b32 s73, s73, 3\nv_lshl_add_u32 v103, v103, 4, s73\nv_bfe_u32 v104, v102, 3, 1\nqm v,a,a,204,108,244\nqt 126,16,58368\n"
"v_lshl_add_u32 v99, v104, 8, v103\nqm v,a,a,204,104,240\ns_lshl_b32 s72, s15, 15\ns_lshr_b32 s73, s23, 1\ns_lshl_b32 s73, s73, 8\nqm v,a,a,172,104,232\n"
"s_add_u32 s72, s72, s73\nv_lshl_add_u32 v98, v97, 17, s72\nv_lshlrev_b32 v97, 11, v97\nqm v,a,a,172,108,236\nqc 128,128\nqc 129,130\n"
"qm v,a,a,140,108,228\nqc 130,132\nqc 131,134\nqm v,a,a,140,104,224\nqw 4\nqc 160,160\n"
"qc 161,162\nqm v,a,a,144,112,224\nqc 162,164\nqc 163,166\nqm v,a,a,144,116,228\nqc 192,192\n"
"qc 193,194\nqm v,a,a,176,116,236\nqc 194,196\nqc 195,198\nqm v,a,a,176,112,232\nqc 224,224\n"
"qc 225,226\nqm v,a,a,208,112,240\nqc 226,228\nqc 227,230\nqm v,a,a,208,116,244\nqc 136,136\n"
"qc 137,138\nqm v,a,a,240,116,252\nqc 138,140\nqc 139,142\nqm v,a,a,240,112,248\nqw 0\n"
"qc 168,168\nqc 169,170\nqm v,a,a,244,120,248\nqc 170,172\nqc 171,174\nqm v,a,a,244,124,252\n"
"qc 200,200\nqc 201,202\nqm v,a,a,212,124,244\nqc 202,204\nqc 203,206\nqm v,a,a,212,120,240\n"
"qc 232,232\nqc 233,234\nqm v,a,a,180,120,232\nqc 234,236\nqc 235,238\nqm v,a,a,180,124,236\n"
"qm v,a,a,148,124,228\nqm v,a,a,148,120,224\nqv 0\ns_barrier\nqr 144,1,0\nqr 148,1,1024\n"
"qr 152,1,2048\nqr 156,1,3072\nqr 160,1,4096\nqr 164,1,5120\nqc 144,144\nqc 145,146\n"
"qc 146,148\nqc 147,150\nqc 176,176\nqc 177,178\nqc 178,180\nqc 179,182\n"
"qc 208,208\nqc 209,210\nqc 210,212\nqc 211,214\nqc 240,240\nqc 241,242\n"
"qc 242,244\nqc 243,246\nqw 4\nF%=:\nv_mfma_f32_16x16x32_bf16 v[124:127], a[144:147], v[120:123], v[20:23]\ns_add_u32 s77, s85, 0x1000000\n"
"s_mov_b32 s85, s76\nqr 168,1,6144\nqm v,a,v,124,148,128\nqw 3\ns_mov_b32 m0, s13\nqr 172,1,7168\n"
"qm v,a,v,124,152,136\ns_add_u32 s13, s13, 0x4000\nqr 176,1,8192\nv_mov_b32 v4, v100\nqm v,a,v,124,156,144\nqe 0,56,76,0\n"
"qz v,a,v,132,156,176\ns_xor_b32 s78, s78, 0x8000\nqr 180,1,9216\nv_mov_b32 v5, v101\nqm v,a,v,132,152,168\nqe 0,56,76,1024\n"
"qm v,a,v,132,148,160\ns_mov_b32 s80, 1024\ns_mov_b32 s81, 0\nqr 184,1,10240\nqm v,a,v,132,144,152\nqw 5\n"
"s_mov_b32 s82, 0x15554\nqr 188,1,11264\nqz v,a,v,148,160,152\nqe 0,56,76,2048\nqm v,a,v,148,164,160\nqw 4\n"
"qr 192,1,12288\nv_mov_b32 v12, v92\nqm v,a,v,148,168,168\nqe 0,56,76,3072\nqm v,a,v,148,172,176\ns_and_b32 s13, s13, 0xffff\n"
"s_add_u32 s76, s76, 0x4000\nqr 196,1,13312\nv_mfma_f32_16x16x32_bf16 v[140:143], a[172:175], v[144:147], v[24:27]\ns_mov_b32 m0, s13\ns_add_u32 s13, s13, 0x4000\nqr 200,1,14336\n"
"qm v,a,v,140,168,136\nqr 204,1,15360\nv_mov_b32 v13, v93\nqm v,a,v,140,164,128\nqe 0,56,76,0\nqm v,a,v,140,160,120\n"
"qw 6\nqr 144,1,16384\nv_mov_b32 v14, v94\nv_mfma_f32_16x16x32_bf16 v[156:159], a[176:179], v[120:123], v[28:31]\nqe 0,56,76,1024\nqm v,a,v,156,180,128\n"
"qw 5\nqr 148,1,17408\nv_mov_b32 v15, v95\nqm v,a,v,156,184,136\nqe 0,56,76,2048\nqm v,a,v,156,188,144\n"
"qr 152,1,18432\nv_mov_b32 v16, v96\nqz v,a,v,164,188,176\nqr 156,1,19456\nv_mov_b32 v17, v97\nqm v,a,v,164,184,168\n"
"qr 160,1,20480\nv_mov_b32 v18, v98\nqm v,a,v,164,180,160\nqr 164,1,21504\nv_max_f32_dpp v249, v249, v249 row_ror:8 row_mask:0xf bank_mask:0xf\nqm v,a,v,164,176,152\n"
"qw 8\nqr 168,1,22528\nv_mov_b32 v19, v99\nqz v,a,v,180,192,152\nqe 0,56,76,3072\nqm v,a,v,180,196,160\n"
"qw 7\ns_and_b32 s13, s13, 0xffff\nqr 172,1,23552\nqm v,a,v,180,200,168\ns_add_u32 s76, s76, 0x4000\ns_sub_u32 s76, s76, 0x10000\n"
"qr 176,1,24576\nqm v,a,v,180,204,176\ns_cmp_lt_u32 s76, s86\ns_cselect_b32 s76, s87, s76\ns_or_b32 m0, s34, 0x10000\nv_mfma_f32_16x16x32_bf16 v[172:175], a[204:207], v[144:147], v[32:35]\n"
"s_add_u32 s34, s34, 0x4000\nqr 180,1,25600\nv_max_f32_dpp v249, v249, v249 row_ror:4 row_mask:0xf bank_mask:0xf\nqm v,a,v,172,200,136\nqe 0,56,77,0\nqm v,a,v,172,196,128\n"
"qr 184,1,26624\nv_max_f32_dpp v249, v249, v249 row_ror:2 row_mask:0xf bank_mask:0xf\nqm v,a,v,172,192,120\nqw 9\nqr 188,1,27648\nv_mov_b32 v250, s16\n"
"v_mfma_f32_16x16x32_bf16 v[196:199], a[144:147], v[152:155], v[36:39]\nqe 0,56,77,1024\nqm v,a,v,196,148,160\nqw 8\nqr 192,1,28672\nv_max_f32_dpp v249, v249, v249 row_ror:1 row_mask:0xf bank_mask:0xf\n"
"qm v,a,v,196,152,168\nqr 196,1,29696\nv_mov_b32 v251, s17\nqm v,a,v,196,156,176\nqw 8\nqr 200,1,30720\n"
"v_mov_b32 v8, v249\nv_mfma_f32_16x16x32_bf16 v[212:215], a[160:163], v[152:155], v[40:43]\nqr 204,1,31744\nv_mov_b32 v64, 0\nqm v,a,v,212,164,160\nqw 8\n"
"v_add_u32 v1, s78, v0\nv_mov_b32 v65, 0\nqm v,a,v,212,168,168\nqe 0,56,77,2048\nqm v,a,v,212,172,176\nqw 6\n"
"v_permlane16_swap_b32 v8, v249\nv_mfma_f32_16x16x32_bf16 v[228:231], a[176:179], v[152:155], v[44:47]\nqe 0,56,77,3072\nqm v,a,v,228,180,160\nqw 4\ns_and_b32 s34, s34, 0xffff\n"
"s_add_u32 s77, s77, 0x4000\nqm v,a,v,228,184,168\ns_or_b32 m0, s34, 0x10000\ns_add_u32 s34, s34, 0x4000\nv_max_f32 v249, v249, v8\nqm v,a,v,228,188,176\n"
"qw 2\nv_mov_b32 v66, 0\nv_mov_b32 v67, 0\nv_mfma_f32_16x16x32_bf16 v[244:247], a[192:195], v[152:155], v[48:51]\nqe 0,56,77,0\nqm v,a,v,244,196,160\n"
"qw 0\nv_mov_b32 v8, v249\nv_mov_b32 v68, 0\nqm v,a,v,244,200,168\nqe 0,56,77,1024\nqm v,a,v,244,204,176\n"
"qe 0,56,77,2048\nqz v,v,v,104,120,120\nqe 0,56,77,3072\nqz v,v,v,108,152,152\ns_and_b32 s34, s34, 0xffff\ns_add_u32 s77, s77, 0x4000\n"
"s_lshr_b32 s82, s82, 1\nqz v,v,v,112,184,184\nv_permlane32_swap_b32 v8, v249\nqz v,v,v,116,216,216\nv_mov_b32 v69, 0\nv_mov_b32 v70, 0\n"
"qm v,v,v,104,128,128\nv_max_f32 v249, v249, v8\nv_mov_b32 v71, 0\nqm v,v,v,108,160,160\nqx 124\nqm v,v,v,112,192,192\n"
"qx 125\nqm v,v,v,116,224,224\nqx 126\nqm v,v,v,104,136,136\nqx 127\nqm v,v,v,108,168,168\n"
"qx 132\nqm v,v,v,112,200,200\nqx 133\nqm v,v,v,116,232,232\nqx 134\nqm v,v,v,104,144,144\n"
"qx 135\nqm v,v,v,108,176,176\nqx 148\nqm v,v,v,112,208,208\nqx 149\nqx 150\n"
"qx 151\nqx 140\nqx 141\nqx 142\nqx 143\nqx 156\n"
"qx 157\nqx 158\nqx 159\nqx 164\nqx 165\nqx 166\n"
"qx 167\nqx 180\nqx 181\nqx 182\nqx 183\nqx 172\n"
"qx 173\nqx 174\nqx 175\nqx 196\nqx 197\nqx 198\n"
"qx 199\nqx 212\nqx 213\nqx 214\nqx 215\nqx 228\n"
"qx 229\nqx 230\nqx 231\nqm v,v,v,116,240,240\ns_bitcmp1_b32 s82, 0\ns_cselect_b64 exec, -1, 0\n"
"global_load_dword a255, v[4:5], off\nv_lshl_add_u64 v[4:5], v[4:5], 0, s[80:81]\ns_mov_b64 exec, -1\nqc 56,124\nqc 57,126\nqc 72,132\n"
"qc 73,134\nqc 74,148\nqc 75,150\nqc 58,140\nqc 59,142\nqc 60,156\n"
"qc 61,158\nqc 76,164\nqc 77,166\nqc 78,180\nqc 79,182\nqc 62,172\n"
"qc 63,174\nqc 80,196\nqc 81,198\nqc 82,212\nqc 83,214\nqc 84,228\n"
"qc 85,230\nqx 244\nqx 245\nqx 246\nv_max3_f32 v9, v104, v105, v106\nv_max3_f32 v9, v9, v107, v108\n"
"v_max3_f32 v9, v9, v109, v110\nv_max3_f32 v9, v9, v111, v112\nv_max3_f32 v9, v9, v113, v114\nv_max3_f32 v9, v9, v115, v116\nv_max3_f32 v9, v9, v117, v118\nv_max_f32 v9, v9, v119\n"
"v_mul_f32 v9, v9, v249\ns_lshl_b32 s72, s15, 2\ns_add_u32 s72, s72, 0x20000\nv_mov_b32 v10, s72\nv_mov_b32 v11, 0x20000\nv_cmp_nge_f32 vcc, 0x45440000, v9\n"
"s_nop 3\ns_cmp_lg_u64 vcc, 0\ns_cselect_b32 s72, 1, 0\nv_mov_b32 v9, s72\nds_write_b32 v10, v9\nqw 0\n"
"qv 1\ns_barrier\nds_read_b128 v[104:107], v11\nqr 144,1,0\nqr 148,1,1024\nqr 152,1,2048\n"
"qr 156,1,3072\nqr 160,1,4096\nqr 164,1,5120\nqx 247\nqc 86,244\nqc 87,246\n"
"qw 6\nv_or3_b32 v9, v104, v105, v106\nv_or_b32 v9, v9, v107\ns_nop 0\nv_readfirstlane_b32 s72, v9\nqw 4\n"
"s_nop 2\ns_cmp_lg_u32 s72, 0\ns_cbranch_scc1 SLOWJ%=\ns_lshr_b32 s84, s88, 3\ns_sub_i32 s10, 13, s84\ns_min_i32 s10, s10, s84\n"
"s_max_i32 s10, s10, 0\ns_add_u32 s11, s84, s10\ns_sub_u32 s11, 15, s11\ns_sub_u32 s84, s84, s10\ns_cmp_eq_u32 s84, 0\ns_cbranch_scc1 XBQ%=\n"
".p2align 3\nL1B%=:\nqz v,a,v,124,144,120\ns_add_u32 s77, s85, 0x1000000\ns_mov_b32 s85, s76\nqr 168,1,6144\n"
"qm v,a,v,124,148,128\nqw 3\ns_mov_b32 m0, s13\nqr 172,1,7168\nqm v,a,v,124,152,136\ns_add_u32 s13, s13, 0x4000\n"
"s_xor_b32 s78, s78, 0x8000\nqr 176,1,8192\nqm v,a,v,124,156,144\nqe 0,56,76,0\nqz v,a,v,132,156,176\ns_xor_b32 s79, s79, 0x8000\n"
"qr 180,1,9216\nqm v,a,v,132,152,168\nqr 184,1,10240\nqm v,a,v,132,148,160\nqr 188,1,11264\nqm v,a,v,132,144,152\n"
"qw 6\nqr 192,1,12288\nqz v,a,v,148,160,152\nqx 124\nqm v,a,v,148,164,160\nqw 5\n"
"qx 125\nqm v,a,v,148,168,168\nqx 126\nqm v,a,v,148,172,176\nqx 127\nqz v,a,v,140,172,144\n"
"qe 0,56,76,1024\nqm v,a,v,140,168,136\nqr 196,1,13312\nqc 88,124\nqm v,a,v,140,164,128\nqx 132\n"
"qm v,a,v,140,160,120\nqw 4\nqx 133\nqz v,a,v,156,176,120\nqr 200,1,14336\nqc 89,126\n"
"qm v,a,v,156,180,128\nqw 3\nqx 134\nqm v,a,v,156,184,136\nqe 0,56,76,2048\nqm v,a,v,156,188,144\n"
"qr 204,1,15360\nqc 104,132\nqz v,a,v,164,188,176\nqx 135\nqm v,a,v,164,184,168\nqx 148\n"
"qm v,a,v,164,180,160\nqe 0,56,76,3072\nqm v,a,v,164,176,152\nqw 2\nqr 144,1,16384\nqc 105,134\n"
"qz v,a,v,180,192,152\ns_and_b32 s13, s13, 0xffff\nqx 149\nqm v,a,v,180,196,160\nqw 1\ns_add_u32 s76, s76, 0x4000\n"
"qr 148,1,17408\nqm v,a,v,180,200,168\ns_mov_b32 m0, s13\ns_add_u32 s13, s13, 0x4000\nqr 152,1,18432\nqm v,a,v,180,204,176\n"
"qx 150\nqz v,a,v,172,204,144\nqr 156,1,19456\nqc 106,148\nqm v,a,v,172,200,136\nqe 0,56,76,0\n"
"qx 151\nqm v,a,v,172,196,128\nqx 140\nqm v,a,v,172,192,120\nqw 2\nqr 160,1,20480\n"
"qc 107,150\nqz v,a,v,188,144,120\nqx 141\nqm v,a,v,188,148,128\nqw 1\nqr 164,1,21504\n"
"qc 90,140\nqm v,a,v,188,152,136\nqe 0,56,76,1024\nqm v,a,v,188,156,144\nqr 168,1,22528\nqx 142\n"
"qz v,a,v,196,156,176\nqr 172,1,23552\nqm v,a,v,196,152,168\nqx 143\nqm v,a,v,196,148,160\nqe 0,56,76,2048\n"
"qm v,a,v,196,144,152\nqw 2\nqx 156\nqz v,a,v,212,160,152\nqr 176,1,24576\nqc 91,142\n"
"qm v,a,v,212,164,160\nqw 1\nqx 157\nqm v,a,v,212,168,168\nqr 180,1,25600\nqc 92,156\n"
"qm v,a,v,212,172,176\nqr 184,1,26624\nqx 158\nqz v,a,v,204,172,144\nqe 0,56,76,3072\nqr 188,1,27648\n"
"qm v,a,v,204,168,136\ns_and_b32 s13, s13, 0xffff\nqx 159\nqm v,a,v,204,164,128\ns_add_u32 s76, s76, 0x4000\nqx 164\n"
"qm v,a,v,204,160,120\nqw 2\ns_sub_u32 s76, s76, 0x10000\nqr 192,1,28672\nqz v,a,v,220,176,120\ns_cmp_lt_u32 s76, s86\n"
"s_cselect_b32 s76, s87, s76\ns_or_b32 m0, s34, 0x10000\ns_add_u32 s34, s34, 0x4000\nqm v,a,v,220,180,128\nqw 1\nqe 0,56,77,0\n"
"qx 165\nqm v,a,v,220,184,136\nqr 196,1,29696\nqc 93,158\nqm v,a,v,220,188,144\nqr 200,1,30720\n"
"qx 166\nqz v,a,v,228,188,176\nqr 204,1,31744\nqc 108,164\nqm v,a,v,228,184,168\nqx 167\n"
"qm v,a,v,228,180,160\nqe 0,56,77,1024\nqm v,a,v,228,176,152\nqw 2\nqx 180\nqz v,a,v,244,192,152\n"
"qx 181\nqm v,a,v,244,196,160\nqw 0\nqr 144,2,0\nqc 109,166\nqm v,a,v,244,200,168\n"
"qr 148,2,8192\nqc 110,180\nqm v,a,v,244,204,176\nqe 0,56,77,2048\nqr 152,2,16384\nqx 182\n"
"qz v,a,v,236,204,144\nqr 156,2,24576\nv_add_u32 v1, s78, v0\nqm v,a,v,236,200,136\nqx 183\nqm v,a,v,236,196,128\n"
"qx 172\nqm v,a,v,236,192,120\nqw 2\nqx 173\nqm a,a,v,0,144,56\nqe 0,56,77,3072\n"
"qm a,a,v,0,148,60\nqw 0\nqr 160,2,1024\nqc 111,182\nqm a,a,v,0,152,64\ns_and_b32 s34, s34, 0xffff\n"
"qx 174\nqm a,a,v,0,156,68\ns_add_u32 s77, s77, 0x4000\ns_or_b32 m0, s34, 0x10000\nqr 164,2,9216\nqm a,a,v,4,156,84\n"
"s_add_u32 s34, s34, 0x4000\nqr 168,2,17408\nqc 94,172\nqm a,a,v,4,152,80\nqe 0,56,77,0\nqr 172,2,25600\n"
"qx 175\nqm a,a,v,4,148,76\nqx 188\nqm a,a,v,4,144,72\nqx 189\nqm a,v,v,64,252,56\n"
"qw 2\nqr 176,2,2048\nqc 95,174\nqm a,a,v,12,160,72\nqe 0,56,77,1024\nqm a,a,v,12,164,76\n"
"qw 1\nqx 190\nqm a,a,v,12,168,80\nqr 180,2,10240\nqc 96,188\nqm a,a,v,12,172,84\n"
"qx 191\nqm a,a,v,8,172,68\nqr 184,2,18432\nqc 97,190\nqm a,a,v,8,168,64\nqr 188,2,26624\n"
"qm a,a,v,8,164,60\nqe 0,56,77,2048\nqx 196\nqm a,a,v,8,160,56\nqx 197\nqm a,v,v,64,252,60\n"
"qw 2\nqx 198\nqm a,a,v,16,176,56\nqe 0,56,77,3072\nqm a,a,v,16,180,60\ns_and_b32 s34, s34, 0xffff\n"
"s_add_u32 s77, s77, 0x4000\ns_lshr_b32 s82, s82, 1\nqw 0\nqm a,a,v,16,184,64\nqr 192,2,3072\nqc 112,196\n"
"qm a,a,v,16,188,68\nqr 196,2,11264\nqx 199\nqm a,a,v,20,188,84\nqr 200,2,19456\nqc 113,198\n"
"qm a,a,v,20,184,80\nqr 204,2,27648\nqx 212\nqm a,a,v,20,180,76\nqx 213\nqm a,a,v,20,176,72\n"
"qx 214\nqm a,v,v,64,252,64\nqw 2\ns_bitcmp1_b32 s82, 0\ns_cselect_b64 exec, -1, 0\nglobal_load_dword a255, v[4:5], off\n"
"v_lshl_add_u64 v[4:5], v[4:5], 0, s[80:81]\ns_mov_b64 exec, -1\nqm a,a,v,28,192,72\nqx 215\nqm a,a,v,28,196,76\nqw 0\n"
"qr 144,2,4096\nqc 114,212\nqm a,a,v,28,200,80\nqr 148,2,12288\nqc 115,214\nqm a,a,v,28,204,84\n"
"qx 204\nqm a,a,v,24,204,68\nqr 152,2,20480\nqm a,a,v,24,200,64\nqr 156,2,28672\nqm a,a,v,24,196,60\n"
"qx 205\nqm a,a,v,24,192,56\nqx 206\nqm a,v,v,64,252,68\nqw 2\nqx 207\n"
"qm a,a,v,32,144,56\nqr 160,2,5120\nqc 98,204\nqm a,a,v,32,148,60\nqw 1\nqx 220\n"
"qm a,a,v,32,152,64\nqr 164,2,13312\nqc 99,206\nqm a,a,v,32,156,68\nqr 168,2,21504\nqm a,a,v,36,156,84\n"
"qx 221\nqm a,a,v,36,152,80\nqr 172,2,29696\nqc 100,220\nqm a,a,v,36,148,76\nqx 222\n"
"qm a,a,v,36,144,72\nqx 223\nqm a,v,v,68,252,72\nqw 2\nqx 228\nqm a,a,v,44,160,72\n"
"qr 176,2,6144\nqc 101,222\nqm a,a,v,44,164,76\nqw 1\nqr 180,2,14336\nqm a,a,v,44,168,80\n"
"qx 229\nqm a,a,v,44,172,84\nqr 184,2,22528\nqc 116,228\nqm a,a,v,40,172,68\nqr 188,2,30720\n"
"qm a,a,v,40,168,64\nqx 230\nqm a,a,v,40,164,60\nqx 231\nqm a,a,v,40,160,56\nqr 192,2,7168\n"
"qc 117,230\nqm a,v,v,68,252,76\nqw 3\nqr 196,2,15360\nqr 200,2,23552\nqr 204,2,31744\n"
"qm a,a,v,48,176,56\nqx 244\nqm a,a,v,48,180,60\nqw 4\nqx 245\nqm a,a,v,48,184,64\n"
"v_add_u32 v2, s79, v3\nqc 118,244\nqm a,a,v,48,188,68\nqx 246\nqm a,a,v,52,188,84\nqx 247\n"
"qm a,a,v,52,184,80\nqw 0\nqv 1\ns_barrier\nqm a,a,v,52,180,76\nqx 236\n"
"qm a,a,v,52,176,72\nqx 237\nqm a,v,v,68,252,80\nqr 144,1,0\nqc 119,246\nqm a,a,v,60,192,72\n"
"qr 148,1,1024\nqc 102,236\nqm a,a,v,60,196,76\nqx 238\nqm a,a,v,60,200,80\nqr 152,1,2048\n"
"qm a,a,v,60,204,84\nqr 156,1,3072\nqm a,a,v,56,204,68\nqx 239\nqm a,a,v,56,200,64\nqr 160,1,4096\n"
"qc 103,238\nqm a,a,v,56,196,60\nqr 164,1,5120\nqm a,a,v,56,192,56\nqm a,v,v,68,252,84\nqw 1\n"
"s_sub_u32 s84, s84, 1\ns_cmp_eq_u32 s84, 0\ns_cbranch_scc1 XAQ%=\nL1A%=:\nqz v,a,v,124,144,120\ns_add_u32 s77, s85, 0x1000000\n"
"s_mov_b32 s85, s76\nqr 168,1,6144\nqm v,a,v,124,148,128\nqw 3\ns_mov_b32 m0, s13\nqr 172,1,7168\n"
"qm v,a,v,124,152,136\ns_add_u32 s13, s13, 0x4000\ns_xor_b32 s78, s78, 0x8000\nqr 176,1,8192\nqm v,a,v,124,156,144\nqe 0,56,76,0\n"
"qz v,a,v,132,156,176\ns_xor_b32 s79, s79, 0x8000\nqr 180,1,9216\nqm v,a,v,132,152,168\nqr 184,1,10240\nqm v,a,v,132,148,160\n"
"qr 188,1,11264\nqm v,a,v,132,144,152\nqw 6\nqr 192,1,12288\nqz v,a,v,148,160,152\nqx 124\n"
"qm v,a,v,148,164,160\nqw 5\nqx 125\nqm v,a,v,148,168,168\nqx 126\nqm v,a,v,148,172,176\n"
"qx 127\nqz v,a,v,140,172,144\nqe 0,56,76,1024\nqm v,a,v,140,168,136\nqr 196,1,13312\nqc 56,124\n"
"qm v,a,v,140,164,128\nqx 132\nqm v,a,v,140,160,120\nqw 4\nqx 133\nqz v,a,v,156,176,120\n"
"qr 200,1,14336\nqc 57,126\nqm v,a,v,156,180,128\nqw 3\nqx 134\nqm v,a,v,156,184,136\n"
"qe 0,56,76,2048\nqm v,a,v,156,188,144\nqr 204,1,15360\nqc 72,132\nqz v,a,v,164,188,176\nqx 135\n"
"qm v,a,v,164,184,168\nqx 148\nqm v,a,v,164,180,160\nqe 0,56,76,3072\nqm v,a,v,164,176,152\nqw 2\n"
"qr 144,1,16384\nqc 73,134\nqz v,a,v,180,192,152\ns_and_b32 s13, s13, 0xffff\nqx 149\nqm v,a,v,180,196,160\n"
"qw 1\ns_add_u32 s76, s76, 0x4000\nqr 148,1,17408\nqm v,a,v,180,200,168\ns_mov_b32 m0, s13\ns_add_u32 s13, s13, 0x4000\n"
"qr 152,1,18432\nqm v,a,v,180,204,176\nqx 150\nqz v,a,v,172,204,144\nqr 156,1,19456\nqc 74,148\n"
"qm v,a,v,172,200,136\nqe 0,56,76,0\nqx 151\nqm v,a,v,172,196,128\nqx 140\nqm v,a,v,172,192,120\n"
"qw 2\nqr 160,1,20480\nqc 75,150\nqz v,a,v,188,144,120\nqx 141\nqm v,a,v,188,148,128\n"
"qw 1\nqr 164,1,21504\nqc 58,140\nqm v,a,v,188,152,136\nqe 0,56,76,1024\nqm v,a,v,188,156,144\n"
"qr 168,1,22528\nqx 142\nqz v,a,v,196,156,176\nqr 172,1,23552\nqm v,a,v,196,152,168\nqx 143\n"
"qm v,a,v,196,148,160\nqe 0,56,76,2048\nqm v,a,v,196,144,152\nqw 2\nqx 156\nqz v,a,v,212,160,152\n"
"qr 176,1,24576\nqc 59,142\nqm v,a,v,212,164,160\nqw 1\nqx 157\nqm v,a,v,212,168,168\n"
"qr 180,1,25600\nqc 60,156\nqm v,a,v,212,172,176\nqr 184,1,26624\nqx 158\nqz v,a,v,204,172,144\n"
"qe 0,56,76,3072\nqr 188,1,27648\nqm v,a,v,204,168,136\ns_and_b32 s13, s13, 0xffff\nqx 159\nqm v,a,v,204,164,128\n"
"s_add_u32 s76, s76, 0x4000\nqx 164\nqm v,a,v,204,160,120\nqw 2\ns_sub_u32 s76, s76, 0x10000\nqr 192,1,28672\n"
"qz v,a,v,220,176,120\ns_cmp_lt_u32 s76, s86\ns_cselect_b32 s76, s87, s76\ns_or_b32 m0, s34, 0x10000\ns_add_u32 s34, s34, 0x4000\nqm v,a,v,220,180,128\n"
"qw 1\nqe 0,56,77,0\nqx 165\nqm v,a,v,220,184,136\nqr 196,1,29696\nqc 61,158\n"
"qm v,a,v,220,188,144\nqr 200,1,30720\nqx 166\nqz v,a,v,228,188,176\nqr 204,1,31744\nqc 76,164\n"
"qm v,a,v,228,184,168\nqx 167\nqm v,a,v,228,180,160\nqe 0,56,77,1024\nqm v,a,v,228,176,152\nqw 2\n"
"qx 180\nqz v,a,v,244,192,152\nqx 181\nqm v,a,v,244,196,160\nqw 0\nqr 144,2,0\n"
"qc 77,166\nqm v,a,v,244,200,168\nqr 148,2,8192\nqc 78,180\nqm v,a,v,244,204,176\nqe 0,56,77,2048\n"
"qr 152,2,16384\nqx 182\nqz v,a,v,236,204,144\nqr 156,2,24576\nv_add_u32 v1, s78, v0\nqm v,a,v,236,200,136\n"
"qx 183\nqm v,a,v,236,196,128\nqx 172\nqm v,a,v,236,192,120\nqw 2\nqx 173\n"
"qm a,a,v,0,144,88\nqe 0,56,77,3072\nqm a,a,v,0,148,92\nqw 0\nqr 160,2,1024\nqc 79,182\n"
"qm a,a,v,0,152,96\ns_and_b32 s34, s34, 0xffff\nqx 174\nqm a,a,v,0,156,100\ns_add_u32 s77, s77, 0x4000\ns_or_b32 m0, s34, 0x10000\n"
"qr 164,2,9216\nqm a,a,v,4,156,116\ns_add_u32 s34, s34, 0x4000\nqr 168,2,17408\nqc 62,172\nqm a,a,v,4,152,112\n"
"qe 0,56,77,0\nqr 172,2,25600\nqx 175\nqm a,a,v,4,148,108\nqx 188\nqm a,a,v,4,144,104\n"
"qx 189\nqm a,v,v,64,252,88\nqw 2\nqr 176,2,2048\nqc 63,174\nqm a,a,v,12,160,104\n"
"qe 0,56,77,1024\nqm a,a,v,12,164,108\nqw 1\nqx 190\nqm a,a,v,12,168,112\nqr 180,2,10240\n"
"qc 64,188\nqm a,a,v,12,172,116\nqx 191\nqm a,a,v,8,172,100\nqr 184,2,18432\nqc 65,190\n"
"qm a,a,v,8,168,96\nqr 188,2,26624\nqm a,a,v,8,164,92\nqe 0,56,77,2048\nqx 196\nqm a,a,v,8,160,88\n"
"qx 197\nqm a,v,v,64,252,92\nqw 2\nqx 198\nqm a,a,v,16,176,88\nqe 0,56,77,3072\n"
"qm a,a,v,16,180,92\ns_and_b32 s34, s34, 0xffff\ns_add_u32 s77, s77, 0x4000\ns_lshr_b32 s82, s82, 1\nqw 0\nqm a,a,v,16,184,96\n"
"qr 192,2,3072\nqc 80,196\nqm a,a,v,16,188,100\nqr 196,2,11264\nqx 199\nqm a,a,v,20,188,116\n"
"qr 200,2,19456\nqc 81,198\nqm a,a,v,20,184,112\nqr 204,2,27648\nqx 212\nqm a,a,v,20,180,108\n"
"qx 213\nqm a,a,v,20,176,104\nqx 214\nqm a,v,v,64,252,96\nqw 2\ns_bitcmp1_b32 s82, 0\n"
"s_cselect_b64 exec, -1, 0\nglobal_load_dword a255, v[4:5], off\nv_lshl_add_u64 v[4:5], v[4:5], 0, s[80:81]\ns_mov_b64 exec, -1\nqm a,a,v,28,192,104\nqx 215\n"
"qm a,a,v,28,196,108\nqw 0\nqr 144,2,4096\nqc 82,212\nqm a,a,v,28,200,112\nqr 148,2,12288\n"
"qc 83,214\nqm a,a,v,28,204,116\nqx 204\nqm a,a,v,24,204,100\nqr 152,2,20480\nqm a,a,v,24,200,96\n"
"qr 156,2,28672\nqm a,a,v,24,196,92\nqx 205\nqm a,a,v,24,192,88\nqx 206\nqm a,v,v,64,252,100\n"
"qw 2\nqx 207\nqm a,a,v,32,144,88\nqr 160,2,5120\nqc 66,204\nqm a,a,v,32,148,92\n"
"qw 1\nqx 220\nqm a,a,v,32,152,96\nqr 164,2,13312\nqc 67,206\nqm a,a,v,32,156,100\n"
"qr 168,2,21504\nqm a,a,v,36,156,116\nqx 221\nqm a,a,v,36,152,112\nqr 172,2,29696\nqc 68,220\n"
"qm a,a,v,36,148,108\nqx 222\nqm a,a,v,36,144,104\nqx 223\nqm a,v,v,68,252,104\nqw 2\n"
"qx 228\nqm a,a,v,44,160,104\nqr 176,2,6144\nqc 69,222\nqm a,a,v,44,164,108\nqw 1\n"
"qr 180,2,14336\nqm a,a,v,44,168,112\nqx 229\nqm a,a,v,44,172,116\nqr 184,2,22528\nqc 84,228\n"
"qm a,a,v,40,172,100\nqr 188,2,30720\nqm a,a,v,40,168,96\nqx 230\nqm a,a,v,40,164,92\nqx 231\n"
"qm a,a,v,40,160,88\nqr 192,2,7168\nqc 85,230\nqm a,v,v,68,252,108\nqw 3\nqr 196,2,15360\n"
"qr 200,2,23552\nqr 204,2,31744\nqm a,a,v,48,176,88\nqx 244\nqm a,a,v,48,180,92\nqw 4\n"
"qx 245\nqm a,a,v,48,184,96\nv_add_u32 v2, s79, v3\nqc 86,244\nqm a,a,v,48,188,100\nqx 246\n"
"qm a,a,v,52,188,116\nqx 247\nqm a,a,v,52,184,112\nqw 0\nqv 1\ns_barrier\n"
"qm a,a,v,52,180,108\nqx 236\nqm a,a,v,52,176,104\nqx 237\nqm a,v,v,68,252,112\nqr 144,1,0\n"
"qc 87,246\nqm a,a,v,60,192,104\nqr 148,1,1024\nqc 70,236\nqm a,a,v,60,196,108\nqx 238\n"
"qm a,a,v,60,200,112\nqr 152,1,2048\nqm a,a,v,60,204,116\nqr 156,1,3072\nqm a,a,v,56,204,100\nqx 239\n"
"qm a,a,v,56,200,96\nqr 160,1,4096\nqc 71,238\nqm a,a,v,56,196,92\nqr 164,1,5120\nqm a,a,v,56,192,88\n"
"qm a,v,v,68,252,116\nqw 4\ns_sub_u32 s84, s84, 1\ns_cmp_lg_u32 s84, 0\ns_cbranch_scc1 L1B%=\nXBQ%=:\n"
"s_mov_b32 s84, s11\ns_cmp_lg_u32 s10, 0\ns_cbranch_scc1 TD1%=\nXB%=:\nv_mfma_f32_16x16x32_bf16 v[124:127], a[144:147], v[184:187], v[20:23]\ns_add_u32 s77, s85, 0x1000000\n"
"s_mov_b32 s85, s76\nqr 168,1,6144\nqm v,a,v,124,148,192\nqw 3\ns_mov_b32 m0, s13\nqr 172,1,7168\n"
"qm v,a,v,124,152,200\ns_add_u32 s13, s13, 0x4000\ns_xor_b32 s78, s78, 0x8000\nqr 176,1,8192\nqm v,a,v,124,156,208\nqe 0,56,76,0\n"
"qz v,a,v,132,156,240\ns_xor_b32 s79, s79, 0x8000\nqr 180,1,9216\nqm v,a,v,132,152,232\nqr 184,1,10240\nqm v,a,v,132,148,224\n"
"qr 188,1,11264\nqm v,a,v,132,144,216\nqw 6\nqr 192,1,12288\nqz v,a,v,148,160,216\nqe 0,56,76,1024\n"
"qm v,a,v,148,164,224\nqw 5\nqr 196,1,13312\nqm v,a,v,148,168,232\nqr 200,1,14336\nqm v,a,v,148,172,240\n"
"qe 0,56,76,2048\nv_mfma_f32_16x16x32_bf16 v[140:143], a[172:175], v[208:211], v[24:27]\nqr 204,1,15360\nqm v,a,v,140,168,200\nqx 124\nqm v,a,v,140,164,192\n"
"qe 0,56,76,3072\nqm v,a,v,140,160,184\nqw 6\ns_and_b32 s13, s13, 0xffff\nqr 144,1,16384\nv_mfma_f32_16x16x32_bf16 v[156:159], a[176:179], v[184:187], v[28:31]\n"
"s_add_u32 s76, s76, 0x4000\nqx 125\nqm v,a,v,156,180,192\nqw 5\ns_mov_b32 m0, s13\nqr 148,1,17408\n"
"qm v,a,v,156,184,200\ns_add_u32 s13, s13, 0x4000\nqr 152,1,18432\nqc 88,124\nqm v,a,v,156,188,208\nqx 126\n"
"qz v,a,v,164,188,240\nqr 156,1,19456\nqm v,a,v,164,184,232\nqe 0,56,76,0\nqm v,a,v,164,180,224\nqx 127\n"
"qm v,a,v,164,176,216\nqw 6\nqr 160,1,20480\nqc 89,126\nqz v,a,v,180,192,216\nqr 164,1,21504\n"
"qm v,a,v,180,196,224\nqw 6\nqx 132\nqm v,a,v,180,200,232\nqr 168,1,22528\nqm v,a,v,180,204,240\n"
"qr 172,1,23552\nv_mfma_f32_16x16x32_bf16 v[172:175], a[204:207], v[208:211], v[32:35]\nqx 133\nqm v,a,v,172,200,200\nqr 176,1,24576\nqc 104,132\n"
"qm v,a,v,172,196,192\nqe 0,56,76,1024\nqm v,a,v,172,192,184\nqw 7\nqr 180,1,25600\nv_mfma_f32_16x16x32_bf16 v[196:199], a[144:147], v[216:219], v[36:39]\n"
"qx 134\nqm v,a,v,196,148,224\nqw 6\nqr 184,1,26624\nqm v,a,v,196,152,232\nqr 188,1,27648\n"
"qm v,a,v,196,156,240\nqw 6\nqx 135\nv_mfma_f32_16x16x32_bf16 v[212:215], a[160:163], v[216:219], v[40:43]\nqr 192,1,28672\nqc 105,134\n"
"qm v,a,v,212,164,224\nqw 5\nqr 196,1,29696\nqm v,a,v,212,168,232\nqe 0,56,76,2048\nqm v,a,v,212,172,240\n"
"qw 4\nqr 200,1,30720\nqx 148\nv_mfma_f32_16x16x32_bf16 v[228:231], a[176:179], v[216:219], v[44:47]\nqr 204,1,31744\nqm v,a,v,228,180,224\n"
"qw 4\nqr 144,2,0\nqx 149\nqm v,a,v,228,184,232\nqr 148,2,8192\nqc 106,148\n"
"qm v,a,v,228,188,240\nqw 4\nqr 152,2,16384\nqx 150\nv_mfma_f32_16x16x32_bf16 v[244:247], a[192:195], v[216:219], v[48:51]\nqe 0,56,76,3072\n"
"qr 156,2,24576\nqm v,a,v,244,196,224\nqw 4\nqx 151\nqm v,a,v,244,200,232\ns_and_b32 s13, s13, 0xffff\n"
"qx 140\nqm v,a,v,244,204,240\nqw 2\ns_add_u32 s76, s76, 0x4000\ns_sub_u32 s76, s76, 0x10000\nqm a,a,v,0,144,56\n"
"s_cmp_lt_u32 s76, s86\ns_cselect_b32 s76, s87, s76\ns_or_b32 m0, s34, 0x10000\ns_add_u32 s34, s34, 0x4000\nqm a,a,v,0,148,60\nqw 0\n"
"qe 0,56,77,0\nqx 141\nqm a,a,v,0,152,64\nqr 160,2,1024\nqc 107,150\nqm a,a,v,0,156,68\n"
"qr 164,2,9216\nqx 142\nqm a,a,v,4,156,84\nqr 168,2,17408\nqc 90,140\nqm a,a,v,4,152,80\n"
"qr 172,2,25600\nqx 143\nqm a,a,v,4,148,76\nqe 0,56,77,1024\nqm a,a,v,4,144,72\nqx 156\n"
"qm a,v,v,64,252,56\nqw 2\nqx 157\nqm a,a,v,12,160,72\nqe 0,56,77,2048\nqm a,a,v,12,164,76\n"
"qw 0\nqr 176,2,2048\nqc 91,142\nqm a,a,v,12,168,80\nqx 158\nqm a,a,v,12,172,84\n"
"qr 180,2,10240\nqc 92,156\nqm a,a,v,8,172,68\nqr 184,2,18432\nqx 159\nqm a,a,v,8,168,64\n"
"qr 188,2,26624\nqc 93,158\nqm a,a,v,8,164,60\nqx 164\nqm a,a,v,8,160,56\nqe 0,56,77,3072\n"
"qm a,v,v,64,252,60\nqw 2\nqx 165\nqm a,a,v,16,176,56\ns_and_b32 s34, s34, 0xffff\nqx 166\n"
"qm a,a,v,16,180,60\nqw 0\ns_add_u32 s77, s77, 0x4000\ns_or_b32 m0, s34, 0x10000\nqm a,a,v,16,184,64\ns_add_u32 s34, s34, 0x4000\n"
"qr 192,2,3072\nqc 108,164\nqm a,a,v,16,188,68\nqe 0,56,77,0\nqr 196,2,11264\nqx 167\n"
"qm a,a,v,20,188,84\nqr 200,2,19456\nqc 109,166\nqm a,a,v,20,184,80\nqr 204,2,27648\nqx 180\n"
"qm a,a,v,20,180,76\nqx 181\nqm a,a,v,20,176,72\nqe 0,56,77,1024\nqm a,v,v,64,252,64\nqw 2\n"
"qx 182\nqm a,a,v,28,192,72\nqx 183\nqm a,a,v,28,196,76\nqw 0\nqr 144,2,4096\n"
"qc 110,180\nqm a,a,v,28,200,80\nqr 148,2,12288\nqc 111,182\nqm a,a,v,28,204,84\nqe 0,56,77,2048\n"
"qm a,a,v,24,204,68\nqr 152,2,20480\nqx 172\nqm a,a,v,24,200,64\nqr 156,2,28672\nv_add_u32 v1, s78, v0\n"
"qm a,a,v,24,196,60\nqx 173\nqm a,a,v,24,192,56\nqe 0,56,77,3072\nqm a,v,v,64,252,68\ns_and_b32 s34, s34, 0xffff\n"
"s_add_u32 s77, s77, 0x4000\ns_lshr_b32 s82, s82, 1\nqw 2\nqx 174\nqm a,a,v,32,144,56\nqx 175\n"
"qm a,a,v,32,148,60\nqw 0\nqr 160,2,5120\nqc 94,172\nqm a,a,v,32,152,64\nqx 196\n"
"qm a,a,v,32,156,68\nqr 164,2,13312\nqc 95,174\nqm a,a,v,36,156,84\nqr 168,2,21504\nqm a,a,v,36,152,80\n"
"qr 172,2,29696\nqx 197\nqm a,a,v,36,148,76\ns_bitcmp1_b32 s82, 0\ns_cselect_b64 exec, -1, 0\nglobal_load_dword a255, v[4:5], off\n"
"v_lshl_add_u64 v[4:5], v[4:5], 0, s[80:81]\ns_mov_b64 exec, -1\nqm a,a,v,36,144,72\nqx 198\nqm a,v,v,68,252,72\nqw 2\n"
"qx 199\nqm a,a,v,44,160,72\nqr 176,2,6144\nqc 112,196\nqm a,a,v,44,164,76\nqw 1\n"
"qx 212\nqm a,a,v,44,168,80\nqr 180,2,14336\nqc 113,198\nqm a,a,v,44,172,84\nqx 213\n"
"qm a,a,v,40,172,68\nqr 184,2,22528\nqc 114,212\nqm a,a,v,40,168,64\nqr 188,2,30720\nqm a,a,v,40,164,60\n"
"qx 214\nqm a,a,v,40,160,56\ns_getpc_b64 s[72:73]\nCT4%=:\ns_add_u32 s72, s72, G2GO%=-CT4%=\ns_addc_u32 s73, s73, 0\n"
"s_lshl2_add_u32 s74, s19, s15\ns_mul_i32 s74, s74, 256\nv_lshl_add_u32 v11, v0, 3, s74\ns_mov_b32 s74, FINISH%=-G2GO%=\ns_mov_b32 s75, 0x20000\nqm a,v,v,68,252,76\n"
"qw 2\nqr 192,2,7168\nqr 196,2,15360\nqr 200,2,23552\nqr 204,2,31744\nqx 215\n"
"qm a,a,v,48,176,56\nqx 228\nqm a,a,v,48,180,60\nqw 4\nqx 229\nqm a,a,v,48,184,64\n"
"qc 115,214\nv_add_u32 v2, s79, v3\nqm a,a,v,48,188,68\nqx 230\nqm a,a,v,52,188,84\nqx 231\n"
"qm a,a,v,52,184,80\ns_bfm_b64 exec, 2, 0\nbuffer_load_dword a255, v11, s[72:75], 0 offen\ns_mov_b64 exec, -1\nqm a,a,v,52,180,76\nqw 0\n"
"qv 2\ns_barrier\nqm a,a,v,52,176,72\nqr 144,1,0\nqc 116,228\nqm a,v,v,68,252,80\n"
"qx 244\nqm a,a,v,60,192,72\nqx 245\nqm a,a,v,60,196,76\nqr 148,1,1024\nqc 117,230\n"
"qm a,a,v,60,200,80\nqx 246\nqm a,a,v,60,204,84\nqr 152,1,2048\nqc 118,244\nqm a,a,v,56,204,68\n"
"qr 156,1,3072\nqm a,a,v,56,200,64\nqx 247\nqm a,a,v,56,196,60\nqr 160,1,4096\nqc 119,246\n"
"qm a,a,v,56,192,56\nqr 164,1,5120\nqm a,v,v,68,252,84\nqw 4\ns_cmp_eq_u32 s84, 1\ns_cbranch_scc1 EL%=\n"
"EA%=:\ns_lshl_b32 s6, s88, 17\ns_lshl_b32 s7, s20, 12\ns_add_u32 s6, s6, s7\ns_add_u32 s6, s6, 0x2800000\ns_xor_b32 s7, s6, 0xe0000\n"
"qa 6,64\nqa 7,68\nqp 6\nqp 7\ns_lshr_b32 s8, s88, 3\ns_lshl2_add_u32 s8, s8, s15\n"
"s_lshl_b32 s8, s8, 8\ns_lshl3_add_u32 s8, s20, s8\ns_add_u32 s8, s8, 0x5000\nqa 120,0\nqa 121,1\nqa 122,2\n"
"qa 123,3\nqa 128,8\nqa 129,9\nqa 130,10\nqa 131,11\nv_pk_mul_f32 v[120:121], v[120:121], v[6:7] op_sel:[0,0] op_sel_hi:[1,0]\n"
"v_pk_mul_f32 v[122:123], v[122:123], v[6:7] op_sel:[0,0] op_sel_hi:[1,0]\nv_pk_mul_f32 v[128:129], v[128:129], v[6:7] op_sel:[0,0] op_sel_hi:[1,0]\nv_pk_mul_f32 v[130:131], v[130:131], v[6:7] op_sel:[0,0] op_sel_hi:[1,0]\nqc 120,120\nqc 121,122\nqc 122,128\n"
"qc 123,130\nbuffer_store_dwordx4 v[120:123], v0, s[56:59], s6 offen offset:0 sc1\nqa 136,4\nqa 137,5\nqa 138,6\nqa 139,7\n"
"qa 144,12\nqa 145,13\nqa 146,14\nqa 147,15\nv_pk_mul_f32 v[136:137], v[136:137], v[6:7] op_sel:[0,1] op_sel_hi:[1,1]\nv_pk_mul_f32 v[138:139], v[138:139], v[6:7] op_sel:[0,1] op_sel_hi:[1,1]\n"
"v_pk_mul_f32 v[144:145], v[144:145], v[6:7] op_sel:[0,1] op_sel_hi:[1,1]\nv_pk_mul_f32 v[146:147], v[146:147], v[6:7] op_sel:[0,1] op_sel_hi:[1,1]\nqc 136,136\nqc 137,138\nqc 138,144\nqc 139,146\n"
"buffer_store_dwordx4 v[136:139], v0, s[56:59], s7 offen offset:0 sc1\nqa 152,16\nqa 153,17\nqa 154,18\nqa 155,19\nqa 160,24\n"
"qa 161,25\nqa 162,26\nqa 163,27\nv_pk_mul_f32 v[152:153], v[152:153], v[6:7] op_sel:[0,0] op_sel_hi:[1,0]\nv_pk_mul_f32 v[154:155], v[154:155], v[6:7] op_sel:[0,0] op_sel_hi:[1,0]\nv_pk_mul_f32 v[160:161], v[160:161], v[6:7] op_sel:[0,0] op_sel_hi:[1,0]\n"
"v_pk_mul_f32 v[162:163], v[162:163], v[6:7] op_sel:[0,0] op_sel_hi:[1,0]\nqc 152,152\nqc 153,154\nqc 154,160\nqc 155,162\nbuffer_store_dwordx4 v[152:155], v0, s[56:59], s6 offen offset:1024 sc1\n"
"qa 168,20\nqa 169,21\nqa 170,22\nqa 171,23\nqa 176,28\nqa 177,29\n"
"qa 178,30\nqa 179,31\nv_pk_mul_f32 v[168:169], v[168:169], v[6:7] op_sel:[0,1] op_sel_hi:[1,1]\nv_pk_mul_f32 v[170:171], v[170:171], v[6:7] op_sel:[0,1] op_sel_hi:[1,1]\nv_pk_mul_f32 v[176:177], v[176:177], v[6:7] op_sel:[0,1] op_sel_hi:[1,1]\nv_pk_mul_f32 v[178:179], v[178:179], v[6:7] op_sel:[0,1] op_sel_hi:[1,1]\n"
"qc 168,168\nqc 169,170\nqc 170,176\nqc 171,178\nbuffer_store_dwordx4 v[168:171], v0, s[56:59], s7 offen offset:1024 sc1\nqa 120,32\n"
"qa 121,33\nqa 122,34\nqa 123,35\nqa 128,40\nqa 129,41\nqa 130,42\n"
"qa 131,43\nv_pk_mul_f32 v[120:121], v[120:121], v[6:7] op_sel:[0,0] op_sel_hi:[1,0]\nv_pk_mul_f32 v[122:123], v[122:123], v[6:7] op_sel:[0,0] op_sel_hi:[1,0]\nv_pk_mul_f32 v[128:129], v[128:129], v[6:7] op_sel:[0,0] op_sel_hi:[1,0]\nv_pk_mul_f32 v[130:131], v[130:131], v[6:7] op_sel:[0,0] op_sel_hi:[1,0]\nqc 120,120\n"
"qc 121,122\nqc 122,128\nqc 123,130\nbuffer_store_dwordx4 v[120:123], v0, s[56:59], s6 offen offset:2048 sc1\nqa 136,36\nqa 137,37\n"
"qa 138,38\nqa 139,39\nqa 144,44\nqa 145,45\nqa 146,46\nqa 147,47\n"
"v_pk_mul_f32 v[136:137], v[136:137], v[6:7] op_sel:[0,1] op_sel_hi:[1,1]\nv_pk_mul_f32 v[138:139], v[138:139], v[6:7] op_sel:[0,1] op_sel_hi:[1,1]\nv_pk_mul_f32 v[144:145], v[144:145], v[6:7] op_sel:[0,1] op_sel_hi:[1,1]\nv_pk_mul_f32 v[146:147], v[146:147], v[6:7] op_sel:[0,1] op_sel_hi:[1,1]\nqc 136,136\nqc 137,138\n"
"qc 138,144\nqc 139,146\nbuffer_store_dwordx4 v[136:139], v0, s[56:59], s7 offen offset:2048 sc1\nqa 152,48\nqa 153,49\nqa 154,50\n"
"qa 155,51\nqa 160,56\nqa 161,57\nqa 162,58\nqa 163,59\nv_pk_mul_f32 v[152:153], v[152:153], v[6:7] op_sel:[0,0] op_sel_hi:[1,0]\n"
"v_pk_mul_f32 v[154:155], v[154:155], v[6:7] op_sel:[0,0] op_sel_hi:[1,0]\nv_pk_mul_f32 v[160:161], v[160:161], v[6:7] op_sel:[0,0] op_sel_hi:[1,0]\nv_pk_mul_f32 v[162:163], v[162:163], v[6:7] op_sel:[0,0] op_sel_hi:[1,0]\nqc 152,152\nqc 153,154\nqc 154,160\n"
"qc 155,162\nbuffer_store_dwordx4 v[152:155], v0, s[56:59], s6 offen offset:3072 sc1\nqa 168,52\nqa 169,53\nqa 170,54\nqa 171,55\n"
"qa 176,60\nqa 177,61\nqa 178,62\nqa 179,63\nv_pk_mul_f32 v[168:169], v[168:169], v[6:7] op_sel:[0,1] op_sel_hi:[1,1]\nv_pk_mul_f32 v[170:171], v[170:171], v[6:7] op_sel:[0,1] op_sel_hi:[1,1]\n"
"v_pk_mul_f32 v[176:177], v[176:177], v[6:7] op_sel:[0,1] op_sel_hi:[1,1]\nv_pk_mul_f32 v[178:179], v[178:179], v[6:7] op_sel:[0,1] op_sel_hi:[1,1]\nqc 168,168\nqc 169,170\nqc 170,176\nqc 171,178\n"
"buffer_store_dwordx4 v[168:171], v0, s[56:59], s7 offen offset:3072 sc1\nqz v,a,v,124,144,184\ns_add_u32 s77, s85, 0x1000000\ns_mov_b32 s85, s76\ns_mov_b32 m0, s13\ns_add_u32 s13, s13, 0x4000\n"
"qm v,a,v,124,148,192\nqw 2\nqr 168,1,6144\nqm v,a,v,124,152,200\ns_xor_b32 s78, s78, 0x8000\ns_xor_b32 s79, s79, 0x8000\n"
"qr 172,1,7168\nqm v,a,v,124,156,208\nqe 0,56,76,0\nqz v,a,v,132,156,240\nqr 176,1,8192\nqm v,a,v,132,152,232\n"
"qr 180,1,9216\nqm v,a,v,132,148,224\nqe 0,56,76,1024\nqm v,a,v,132,144,216\nqw 4\nqr 184,1,10240\n"
"qz v,a,v,148,160,216\nqx 124\nqx 125\nqx 126\nqx 127\nqm v,a,v,148,164,224\n"
"qw 3\nqr 188,1,11264\nqc 56,124\nqm v,a,v,148,168,232\nqr 192,1,12288\nqc 57,126\n"
"qm v,a,v,148,172,240\nqe 0,56,76,2048\nqz v,a,v,140,172,208\nqx 132\nqx 133\nqx 134\n"
"qx 135\nqm v,a,v,140,168,200\nqr 196,1,13312\nqc 72,132\nqm v,a,v,140,164,192\nqe 0,56,76,3072\n"
"qm v,a,v,140,160,184\nqw 4\ns_and_b32 s13, s13, 0xffff\ns_add_u32 s76, s76, 0x4000\nqz v,a,v,156,176,184\nqx 148\n"
"qx 149\nqx 150\nqx 151\nqm v,a,v,156,180,192\nqw 2\ns_mov_b32 m0, s13\n"
"s_add_u32 s13, s13, 0x4000\nqm v,a,v,156,184,200\nqr 200,1,14336\nqc 73,134\nqm v,a,v,156,188,208\nqr 204,1,15360\n"
"qc 74,148\nqz v,a,v,164,188,240\nqx 140\nqx 141\nqx 142\nqx 143\n"
"qm v,a,v,164,184,232\nqr 144,1,16384\nqc 75,150\nqm v,a,v,164,180,224\nqe 0,56,76,0\nqm v,a,v,164,176,216\n"
"qw 3\nqc 58,140\nqc 59,142\nqz v,a,v,180,192,216\nqv 5\nqx 156\n"
"qx 157\nqm v,a,v,180,196,224\nqw 1\nqx 158\nqm v,a,v,180,200,232\nqr 148,1,17408\n"
"qc 60,156\nqm v,a,v,180,204,240\nqr 152,1,18432\nqx 159\nqz v,a,v,172,204,208\nqr 156,1,19456\n"
"qc 61,158\nqm v,a,v,172,200,200\nqx 164\nqm v,a,v,172,196,192\ns_mov_b64 exec, 1\nbuffer_store_dwordx2 v[250:251], off, s[56:59], s8 sc1\n"
"s_mov_b64 exec, -1\nqm v,a,v,172,192,184\nqw 2\nqx 165\nqz v,a,v,188,144,184\nqr 160,1,20480\n"
"qc 76,164\nqm v,a,v,188,148,192\nqw 1\nqx 166\nqm v,a,v,188,152,200\nqr 164,1,21504\n"
"qm v,a,v,188,156,208\nqe 0,56,76,1024\nqr 168,1,22528\nqx 167\nqz v,a,v,196,156,240\nqr 172,1,23552\n"
"qc 77,166\nqm v,a,v,196,152,232\nqx 180\nqm v,a,v,196,148,224\nqx 181\nqm v,a,v,196,144,216\n"
"qw 2\nqr 176,1,24576\nqc 78,180\nqz v,a,v,212,160,216\nqe 0,56,76,2048\nqm v,a,v,212,164,224\n"
"qw 1\nqx 182\nqm v,a,v,212,168,232\nqr 180,1,25600\nqm v,a,v,212,172,240\nqe 0,56,76,3072\n"
"s_and_b32 s13, s13, 0xffff\ns_add_u32 s76, s76, 0x4000\ns_sub_u32 s76, s76, 0x10000\ns_cmp_lt_u32 s76, s86\ns_cselect_b32 s76, s87, s76\ns_or_b32 m0, s34, 0x10000\n"
"s_add_u32 s34, s34, 0x4000\nqr 184,1,26624\nqx 183\nqz v,a,v,204,172,208\nqr 188,1,27648\nqc 79,182\n"
"qm v,a,v,204,168,200\nqx 172\nqm v,a,v,204,164,192\nqx 173\nqm v,a,v,204,160,184\nqw 2\n"
"qr 192,1,28672\nqc 62,172\nqz v,a,v,220,176,184\nqx 174\nqm v,a,v,220,180,192\nqw 1\n"
"qr 196,1,29696\nqm v,a,v,220,184,200\nqe 0,56,77,0\nqm v,a,v,220,188,208\nqr 200,1,30720\nqx 175\n"
"qz v,a,v,228,188,240\nqr 204,1,31744\nqc 63,174\nqm v,a,v,228,184,232\nqx 188\nqm v,a,v,228,180,224\n"
"qe 0,56,77,1024\nqm v,a,v,228,176,216\nqw 2\nqx 189\nqz v,a,v,244,192,216\nqr 144,2,0\n"
"qc 64,188\nqm v,a,v,244,196,224\nqw 1\nqx 190\nqm v,a,v,244,200,232\nqr 148,2,8192\n"
"v_add_u32 v1, s78, v0\nqm v,a,v,244,204,240\nqx 191\nqz v,a,v,236,204,208\nqe 0,56,77,2048\nqm v,a,v,236,200,200\n"
"qr 152,2,16384\nqx 196\nqm v,a,v,236,196,192\nqr 156,2,24576\nqc 65,190\nqm v,a,v,236,192,184\n"
"qe 0,56,77,3072\ns_and_b32 s34, s34, 0xffff\ns_add_u32 s77, s77, 0x4000\ns_or_b32 m0, s34, 0x10000\ns_add_u32 s34, s34, 0x4000\nqx 197\n"
"qw 2\nqz a,a,v,72,144,88\nqr 160,2,1024\nqc 80,196\nqm a,a,v,72,148,92\nqr 164,2,9216\n"
"qx 198\nqz a,a,v,76,148,108\nqx 199\nqm a,a,v,76,144,104\nqw 2\nqr 168,2,17408\n"
"qc 81,198\nqm a,a,v,76,152,112\nqr 172,2,25600\nqm a,a,v,76,156,116\nqx 212\nqz a,v,v,136,252,88\n"
"qw 2\nqe 0,56,77,0\nqz a,a,v,80,160,88\nqr 176,2,2048\nqx 213\nqm a,a,v,80,164,92\n"
"qr 180,2,10240\nqc 82,212\nqz a,a,v,84,164,108\nqx 214\nqm a,a,v,84,160,104\nqw 2\n"
"qx 215\nqm a,a,v,84,168,112\nqe 0,56,77,1024\nqr 184,2,18432\nqm a,a,v,84,172,116\nqr 188,2,26624\n"
"qc 83,214\nqm a,v,v,136,252,92\nqw 2\nqr 192,2,3072\nqx 204\nqz a,a,v,88,176,88\n"
"qr 196,2,11264\nqm a,a,v,88,180,92\nqx 205\nqz a,a,v,92,180,108\nqe 0,56,77,2048\nqm a,a,v,92,176,104\n"
"qw 2\nqr 200,2,19456\nqx 206\nqm a,a,v,92,184,112\nqr 204,2,27648\nqc 66,204\n"
"qm a,a,v,92,188,116\nqe 0,56,77,3072\ns_and_b32 s34, s34, 0xffff\ns_add_u32 s77, s77, 0x4000\ns_lshr_b32 s82, s82, 1\nqx 207\n"
"qr 144,2,4096\nqw 3\nqz a,a,v,96,192,88\nqr 148,2,12288\nqc 67,206\nqm a,a,v,96,196,92\n"
"qx 220\nqz a,a,v,100,196,108\nqx 221\nqm a,a,v,100,192,104\nqw 2\nqr 152,2,20480\n"
"qc 68,220\nqm a,a,v,100,200,112\nqr 156,2,28672\nqm a,a,v,100,204,116\nqw 2\nqx 222\n"
"qz a,a,v,104,144,88\ns_bitcmp1_b32 s82, 0\ns_cselect_b64 exec, -1, 0\nglobal_load_dword a255, v[4:5], off\nv_lshl_add_u64 v[4:5], v[4:5], 0, s[80:81]\ns_mov_b64 exec, -1\n"
"qr 160,2,5120\nqm a,a,v,104,148,92\nqr 164,2,13312\nqx 223\nqz a,a,v,108,148,108\nqx 228\n"
"qm a,a,v,108,144,104\nqw 2\nqr 168,2,21504\nqc 69,222\nqm a,a,v,108,152,112\nqx 229\n"
"qm a,a,v,108,156,116\nqr 172,2,29696\nqc 84,228\nqz a,v,v,140,252,104\nqw 2\nqx 230\n"
"qz a,a,v,112,160,88\nqr 176,2,6144\nqm a,a,v,112,164,92\nqr 180,2,14336\nqz a,a,v,116,164,108\nqr 184,2,22528\n"
"qr 188,2,30720\nqr 192,2,7168\nqr 196,2,15360\nqr 200,2,23552\nqr 204,2,31744\nqx 231\n"
"qm a,a,v,116,160,104\nqw 8\nqx 244\nqm a,a,v,116,168,112\nqx 245\nqm a,a,v,116,172,116\n"
"qc 85,230\nv_add_u32 v2, s79, v3\nqm a,v,v,140,252,108\nqw 6\nqx 246\nqz a,a,v,120,176,88\n"
"qx 247\nqm a,a,v,120,180,92\nqw 0\nqv 1\ns_barrier\nqz a,a,v,124,180,108\n"
"qr 144,1,0\nqc 86,244\nqm a,a,v,124,176,104\nqx 236\nqm a,a,v,124,184,112\nqx 237\n"
"qm a,a,v,124,188,116\nqr 148,1,1024\nqc 87,246\nqm a,v,v,140,252,112\nqx 238\nqz a,a,v,128,192,88\n"
"qr 152,1,2048\nqc 70,236\nqm a,a,v,128,196,92\nqr 156,1,3072\nqz a,a,v,132,196,108\nqx 239\n"
"qm a,a,v,132,192,104\nqr 160,1,4096\nqc 71,238\nqm a,a,v,132,200,112\nqr 164,1,5120\nqm a,a,v,132,204,116\n"
"qm a,v,v,140,252,116\nqw 4\ns_sub_u32 s84, s84, 2\n.p2align 3\nL2B%=:\nqz v,a,v,124,144,184\n"
"s_add_u32 s77, s85, 0x1000000\ns_mov_b32 s85, s76\nqr 168,1,6144\nqm v,a,v,124,148,192\nqw 3\ns_mov_b32 m0, s13\n"
"qr 172,1,7168\nqm v,a,v,124,152,200\ns_add_u32 s13, s13, 0x4000\ns_xor_b32 s78, s78, 0x8000\nqr 176,1,8192\nqm v,a,v,124,156,208\n"
"qe 0,56,76,0\nqz v,a,v,132,156,240\ns_xor_b32 s79, s79, 0x8000\nqr 180,1,9216\nqm v,a,v,132,152,232\nqr 184,1,10240\n"
"qm v,a,v,132,148,224\nqr 188,1,11264\nqm v,a,v,132,144,216\nqw 6\nqr 192,1,12288\nqz v,a,v,148,160,216\n"
"qx 124\nqm v,a,v,148,164,224\nqw 5\nqx 125\nqm v,a,v,148,168,232\nqx 126\n"
"qm v,a,v,148,172,240\nqx 127\nqz v,a,v,140,172,208\nqe 0,56,76,1024\nqm v,a,v,140,168,200\nqr 196,1,13312\n"
"qc 88,124\nqm v,a,v,140,164,192\nqx 132\nqm v,a,v,140,160,184\nqw 4\nqx 133\n"
"qz v,a,v,156,176,184\nqr 200,1,14336\nqc 89,126\nqm v,a,v,156,180,192\nqw 3\nqx 134\n"
"qm v,a,v,156,184,200\nqe 0,56,76,2048\nqm v,a,v,156,188,208\nqr 204,1,15360\nqc 104,132\nqz v,a,v,164,188,240\n"
"qx 135\nqm v,a,v,164,184,232\nqx 148\nqm v,a,v,164,180,224\nqe 0,56,76,3072\nqm v,a,v,164,176,216\n"
"qw 2\nqr 144,1,16384\nqc 105,134\nqz v,a,v,180,192,216\ns_and_b32 s13, s13, 0xffff\nqx 149\n"
"qm v,a,v,180,196,224\nqw 1\ns_add_u32 s76, s76, 0x4000\nqr 148,1,17408\nqm v,a,v,180,200,232\ns_mov_b32 m0, s13\n"
"s_add_u32 s13, s13, 0x4000\nqr 152,1,18432\nqm v,a,v,180,204,240\nqx 150\nqz v,a,v,172,204,208\nqr 156,1,19456\n"
"qc 106,148\nqm v,a,v,172,200,200\nqe 0,56,76,0\nqx 151\nqm v,a,v,172,196,192\nqx 140\n"
"qm v,a,v,172,192,184\nqw 2\nqr 160,1,20480\nqc 107,150\nqz v,a,v,188,144,184\nqx 141\n"
"qm v,a,v,188,148,192\nqw 1\nqr 164,1,21504\nqc 90,140\nqm v,a,v,188,152,200\nqe 0,56,76,1024\n"
"qm v,a,v,188,156,208\nqr 168,1,22528\nqx 142\nqz v,a,v,196,156,240\nqr 172,1,23552\nqm v,a,v,196,152,232\n"
"qx 143\nqm v,a,v,196,148,224\nqe 0,56,76,2048\nqm v,a,v,196,144,216\nqw 2\nqx 156\n"
"qz v,a,v,212,160,216\nqr 176,1,24576\nqc 91,142\nqm v,a,v,212,164,224\nqw 1\nqx 157\n"
"qm v,a,v,212,168,232\nqr 180,1,25600\nqc 92,156\nqm v,a,v,212,172,240\nqr 184,1,26624\nqx 158\n"
"qz v,a,v,204,172,208\nqe 0,56,76,3072\nqr 188,1,27648\nqm v,a,v,204,168,200\ns_and_b32 s13, s13, 0xffff\nqx 159\n"
"qm v,a,v,204,164,192\ns_add_u32 s76, s76, 0x4000\nqx 164\nqm v,a,v,204,160,184\nqw 2\ns_sub_u32 s76, s76, 0x10000\n"
"qr 192,1,28672\nqz v,a,v,220,176,184\ns_cmp_lt_u32 s76, s86\ns_cselect_b32 s76, s87, s76\ns_or_b32 m0, s34, 0x10000\ns_add_u32 s34, s34, 0x4000\n"
"qm v,a,v,220,180,192\nqw 1\nqe 0,56,77,0\nqx 165\nqm v,a,v,220,184,200\nqr 196,1,29696\n"
"qc 93,158\nqm v,a,v,220,188,208\nqr 200,1,30720\nqx 166\nqz v,a,v,228,188,240\nqr 204,1,31744\n"
"qc 108,164\nqm v,a,v,228,184,232\nqx 167\nqm v,a,v,228,180,224\nqe 0,56,77,1024\nqm v,a,v,228,176,216\n"
"qw 2\nqx 180\nqz v,a,v,244,192,216\nqx 181\nqm v,a,v,244,196,224\nqw 0\n"
"qr 144,2,0\nqc 109,166\nqm v,a,v,244,200,232\nqr 148,2,8192\nqc 110,180\nqm v,a,v,244,204,240\n"
"qe 0,56,77,2048\nqr 152,2,16384\nqx 182\nqz v,a,v,236,204,208\nqr 156,2,24576\nv_add_u32 v1, s78, v0\n"
"qm v,a,v,236,200,200\nqx 183\nqm v,a,v,236,196,192\nqx 172\nqm v,a,v,236,192,184\nqw 2\n"
"qx 173\nqm a,a,v,72,144,56\nqe 0,56,77,3072\nqm a,a,v,72,148,60\nqw 0\nqr 160,2,1024\n"
"qc 111,182\nqm a,a,v,72,152,64\ns_and_b32 s34, s34, 0xffff\nqx 174\nqm a,a,v,72,156,68\ns_add_u32 s77, s77, 0x4000\n"
"s_or_b32 m0, s34, 0x10000\nqr 164,2,9216\nqm a,a,v,76,156,84\ns_add_u32 s34, s34, 0x4000\nqr 168,2,17408\nqc 94,172\n"
"qm a,a,v,76,152,80\nqe 0,56,77,0\nqr 172,2,25600\nqx 175\nqm a,a,v,76,148,76\nqx 188\n"
"qm a,a,v,76,144,72\nqx 189\nqm a,v,v,136,252,56\nqw 2\nqr 176,2,2048\nqc 95,174\n"
"qm a,a,v,84,160,72\nqe 0,56,77,1024\nqm a,a,v,84,164,76\nqw 1\nqx 190\nqm a,a,v,84,168,80\n"
"qr 180,2,10240\nqc 96,188\nqm a,a,v,84,172,84\nqx 191\nqm a,a,v,80,172,68\nqr 184,2,18432\n"
"qc 97,190\nqm a,a,v,80,168,64\nqr 188,2,26624\nqm a,a,v,80,164,60\nqe 0,56,77,2048\nqx 196\n"
"qm a,a,v,80,160,56\nqx 197\nqm a,v,v,136,252,60\nqw 2\nqx 198\nqm a,a,v,88,176,56\n"
"qe 0,56,77,3072\nqm a,a,v,88,180,60\ns_and_b32 s34, s34, 0xffff\ns_add_u32 s77, s77, 0x4000\ns_lshr_b32 s82, s82, 1\nqw 0\n"
"qm a,a,v,88,184,64\nqr 192,2,3072\nqc 112,196\nqm a,a,v,88,188,68\nqr 196,2,11264\nqx 199\n"
"qm a,a,v,92,188,84\nqr 200,2,19456\nqc 113,198\nqm a,a,v,92,184,80\nqr 204,2,27648\nqx 212\n"
"qm a,a,v,92,180,76\nqx 213\nqm a,a,v,92,176,72\nqx 214\nqm a,v,v,136,252,64\nqw 2\n"
"s_bitcmp1_b32 s82, 0\ns_cselect_b64 exec, -1, 0\nglobal_load_dword a255, v[4:5], off\nv_lshl_add_u64 v[4:5], v[4:5], 0, s[80:81]\ns_mov_b64 exec, -1\nqm a,a,v,100,192,72\n"
"qx 215\nqm a,a,v,100,196,76\nqw 0\nqr 144,2,4096\nqc 114,212\nqm a,a,v,100,200,80\n"
"qr 148,2,12288\nqc 115,214\nqm a,a,v,100,204,84\nqx 204\nqm a,a,v,96,204,68\nqr 152,2,20480\n"
"qm a,a,v,96,200,64\nqr 156,2,28672\nqm a,a,v,96,196,60\nqx 205\nqm a,a,v,96,192,56\nqx 206\n"
"qm a,v,v,136,252,68\nqw 2\nqx 207\nqm a,a,v,104,144,56\nqr 160,2,5120\nqc 98,204\n"
"qm a,a,v,104,148,60\nqw 1\nqx 220\nqm a,a,v,104,152,64\nqr 164,2,13312\nqc 99,206\n"
"qm a,a,v,104,156,68\nqr 168,2,21504\nqm a,a,v,108,156,84\nqx 221\nqm a,a,v,108,152,80\nqr 172,2,29696\n"
"qc 100,220\nqm a,a,v,108,148,76\nqx 222\nqm a,a,v,108,144,72\nqx 223\nqm a,v,v,140,252,72\n"
"qw 2\nqx 228\nqm a,a,v,116,160,72\nqr 176,2,6144\nqc 101,222\nqm a,a,v,116,164,76\n"
"qw 1\nqr 180,2,14336\nqm a,a,v,116,168,80\nqx 229\nqm a,a,v,116,172,84\nqr 184,2,22528\n"
"qc 116,228\nqm a,a,v,112,172,68\nqr 188,2,30720\nqm a,a,v,112,168,64\nqx 230\nqm a,a,v,112,164,60\n"
"qx 231\nqm a,a,v,112,160,56\nqr 192,2,7168\nqc 117,230\nqm a,v,v,140,252,76\nqw 3\n"
"qr 196,2,15360\nqr 200,2,23552\nqr 204,2,31744\nqm a,a,v,120,176,56\nqx 244\nqm a,a,v,120,180,60\n"
"qw 4\nqx 245\nqm a,a,v,120,184,64\nv_add_u32 v2, s79, v3\nqc 118,244\nqm a,a,v,120,188,68\n"
"qx 246\nqm a,a,v,124,188,84\nqx 247\nqm a,a,v,124,184,80\nqw 0\nqv 1\n"
"s_barrier\nqm a,a,v,124,180,76\nqx 236\nqm a,a,v,124,176,72\nqx 237\nqm a,v,v,140,252,80\n"
"qr 144,1,0\nqc 119,246\nqm a,a,v,132,192,72\nqr 148,1,1024\nqc 102,236\nqm a,a,v,132,196,76\n"
"qx 238\nqm a,a,v,132,200,80\nqr 152,1,2048\nqm a,a,v,132,204,84\nqr 156,1,3072\nqm a,a,v,128,204,68\n"
"qx 239\nqm a,a,v,128,200,64\nqr 160,1,4096\nqc 103,238\nqm a,a,v,128,196,60\nqr 164,1,5120\n"
"qm a,a,v,128,192,56\nqm a,v,v,140,252,84\nqw 4\nCHK%=:\ns_cmp_eq_u32 s84, 1\ns_cbranch_scc1 L2L%=\n"
"L2A%=:\nqz v,a,v,124,144,184\ns_add_u32 s77, s85, 0x1000000\ns_mov_b32 s85, s76\nqr 168,1,6144\nqm v,a,v,124,148,192\n"
"qw 3\ns_mov_b32 m0, s13\nqr 172,1,7168\nqm v,a,v,124,152,200\ns_add_u32 s13, s13, 0x4000\ns_xor_b32 s78, s78, 0x8000\n"
"qr 176,1,8192\nqm v,a,v,124,156,208\nqe 0,56,76,0\nqz v,a,v,132,156,240\ns_xor_b32 s79, s79, 0x8000\nqr 180,1,9216\n"
"qm v,a,v,132,152,232\nqr 184,1,10240\nqm v,a,v,132,148,224\nqr 188,1,11264\nqm v,a,v,132,144,216\nqw 6\n"
"qr 192,1,12288\nqz v,a,v,148,160,216\nqx 124\nqm v,a,v,148,164,224\nqw 5\nqx 125\n"
"qm v,a,v,148,168,232\nqx 126\nqm v,a,v,148,172,240\nqx 127\nqz v,a,v,140,172,208\nqe 0,56,76,1024\n"
"qm v,a,v,140,168,200\nqr 196,1,13312\nqc 56,124\nqm v,a,v,140,164,192\nqx 132\nqm v,a,v,140,160,184\n"
"qw 4\nqx 133\nqz v,a,v,156,176,184\nqr 200,1,14336\nqc 57,126\nqm v,a,v,156,180,192\n"
"qw 3\nqx 134\nqm v,a,v,156,184,200\nqe 0,56,76,2048\nqm v,a,v,156,188,208\nqr 204,1,15360\n"
"qc 72,132\nqz v,a,v,164,188,240\nqx 135\nqm v,a,v,164,184,232\nqx 148\nqm v,a,v,164,180,224\n"
"qe 0,56,76,3072\nqm v,a,v,164,176,216\nqw 2\nqr 144,1,16384\nqc 73,134\nqz v,a,v,180,192,216\n"
"s_and_b32 s13, s13, 0xffff\nqx 149\nqm v,a,v,180,196,224\nqw 1\ns_add_u32 s76, s76, 0x4000\nqr 148,1,17408\n"
"qm v,a,v,180,200,232\ns_mov_b32 m0, s13\ns_add_u32 s13, s13, 0x4000\nqr 152,1,18432\nqm v,a,v,180,204,240\nqx 150\n"
"qz v,a,v,172,204,208\nqr 156,1,19456\nqc 74,148\nqm v,a,v,172,200,200\nqe 0,56,76,0\nqx 151\n"
"qm v,a,v,172,196,192\nqx 140\nqm v,a,v,172,192,184\nqw 2\nqr 160,1,20480\nqc 75,150\n"
"qz v,a,v,188,144,184\nqx 141\nqm v,a,v,188,148,192\nqw 1\nqr 164,1,21504\nqc 58,140\n"
"qm v,a,v,188,152,200\nqe 0,56,76,1024\nqm v,a,v,188,156,208\nqr 168,1,22528\nqx 142\nqz v,a,v,196,156,240\n"
"qr 172,1,23552\nqm v,a,v,196,152,232\nqx 143\nqm v,a,v,196,148,224\nqe 0,56,76,2048\nqm v,a,v,196,144,216\n"
"qw 2\nqx 156\nqz v,a,v,212,160,216\nqr 176,1,24576\nqc 59,142\nqm v,a,v,212,164,224\n"
"qw 1\nqx 157\nqm v,a,v,212,168,232\nqr 180,1,25600\nqc 60,156\nqm v,a,v,212,172,240\n"
"qr 184,1,26624\nqx 158\nqz v,a,v,204,172,208\nqe 0,56,76,3072\nqr 188,1,27648\nqm v,a,v,204,168,200\n"
"s_and_b32 s13, s13, 0xffff\nqx 159\nqm v,a,v,204,164,192\ns_add_u32 s76, s76, 0x4000\nqx 164\nqm v,a,v,204,160,184\n"
"qw 2\ns_sub_u32 s76, s76, 0x10000\nqr 192,1,28672\nqz v,a,v,220,176,184\ns_cmp_lt_u32 s76, s86\ns_cselect_b32 s76, s87, s76\n"
"s_or_b32 m0, s34, 0x10000\ns_add_u32 s34, s34, 0x4000\nqm v,a,v,220,180,192\nqw 1\nqe 0,56,77,0\nqx 165\n"
"qm v,a,v,220,184,200\nqr 196,1,29696\nqc 61,158\nqm v,a,v,220,188,208\nqr 200,1,30720\nqx 166\n"
"qz v,a,v,228,188,240\nqr 204,1,31744\nqc 76,164\nqm v,a,v,228,184,232\nqx 167\nqm v,a,v,228,180,224\n"
"qe 0,56,77,1024\nqm v,a,v,228,176,216\nqw 2\nqx 180\nqz v,a,v,244,192,216\nqx 181\n"
"qm v,a,v,244,196,224\nqw 0\nqr 144,2,0\nqc 77,166\nqm v,a,v,244,200,232\nqr 148,2,8192\n"
"qc 78,180\nqm v,a,v,244,204,240\nqe 0,56,77,2048\nqr 152,2,16384\nqx 182\nqz v,a,v,236,204,208\n"
"qr 156,2,24576\nv_add_u32 v1, s78, v0\nqm v,a,v,236,200,200\nqx 183\nqm v,a,v,236,196,192\nqx 172\n"
"qm v,a,v,236,192,184\nqw 2\nqx 173\nqm a,a,v,72,144,88\nqe 0,56,77,3072\nqm a,a,v,72,148,92\n"
"qw 0\nqr 160,2,1024\nqc 79,182\nqm a,a,v,72,152,96\ns_and_b32 s34, s34, 0xffff\nqx 174\n"
"qm a,a,v,72,156,100\ns_add_u32 s77, s77, 0x4000\ns_or_b32 m0, s34, 0x10000\nqr 164,2,9216\nqm a,a,v,76,156,116\ns_add_u32 s34, s34, 0x4000\n"
"qr 168,2,17408\nqc 62,172\nqm a,a,v,76,152,112\nqe 0,56,77,0\nqr 172,2,25600\nqx 175\n"
"qm a,a,v,76,148,108\nqx 188\nqm a,a,v,76,144,104\nqx 189\nqm a,v,v,136,252,88\nqw 2\n"
"qr 176,2,2048\nqc 63,174\nqm a,a,v,84,160,104\nqe 0,56,77,1024\nqm a,a,v,84,164,108\nqw 1\n"
"qx 190\nqm a,a,v,84,168,112\nqr 180,2,10240\nqc 64,188\nqm a,a,v,84,172,116\nqx 191\n"
"qm a,a,v,80,172,100\nqr 184,2,18432\nqc 65,190\nqm a,a,v,80,168,96\nqr 188,2,26624\nqm a,a,v,80,164,92\n"
"qe 0,56,77,2048\nqx 196\nqm a,a,v,80,160,88\nqx 197\nqm a,v,v,136,252,92\nqw 2\n"
"qx 198\nqm a,a,v,88,176,88\nqe 0,56,77,3072\nqm a,a,v,88,180,92\ns_and_b32 s34, s34, 0xffff\ns_add_u32 s77, s77, 0x4000\n"
"s_lshr_b32 s82, s82, 1\nqw 0\nqm a,a,v,88,184,96\nqr 192,2,3072\nqc 80,196\nqm a,a,v,88,188,100\n"
"qr 196,2,11264\nqx 199\nqm a,a,v,92,188,116\nqr 200,2,19456\nqc 81,198\nqm a,a,v,92,184,112\n"
"qr 204,2,27648\nqx 212\nqm a,a,v,92,180,108\nqx 213\nqm a,a,v,92,176,104\nqx 214\n"
"qm a,v,v,136,252,96\nqw 2\ns_bitcmp1_b32 s82, 0\ns_cselect_b64 exec, -1, 0\nglobal_load_dword a255, v[4:5], off\nv_lshl_add_u64 v[4:5], v[4:5], 0, s[80:81]\n"
"s_mov_b64 exec, -1\nqm a,a,v,100,192,104\nqx 215\nqm a,a,v,100,196,108\nqw 0\nqr 144,2,4096\n"
"qc 82,212\nqm a,a,v,100,200,112\nqr 148,2,12288\nqc 83,214\nqm a,a,v,100,204,116\nqx 204\n"
"qm a,a,v,96,204,100\nqr 152,2,20480\nqm a,a,v,96,200,96\nqr 156,2,28672\nqm a,a,v,96,196,92\nqx 205\n"
"qm a,a,v,96,192,88\nqx 206\nqm a,v,v,136,252,100\nqw 2\nqx 207\nqm a,a,v,104,144,88\n"
"qr 160,2,5120\nqc 66,204\nqm a,a,v,104,148,92\nqw 1\nqx 220\nqm a,a,v,104,152,96\n"
"qr 164,2,13312\nqc 67,206\nqm a,a,v,104,156,100\nqr 168,2,21504\nqm a,a,v,108,156,116\nqx 221\n"
"qm a,a,v,108,152,112\nqr 172,2,29696\nqc 68,220\nqm a,a,v,108,148,108\nqx 222\nqm a,a,v,108,144,104\n"
"qx 223\nqm a,v,v,140,252,104\nqw 2\nqx 228\nqm a,a,v,116,160,104\nqr 176,2,6144\n"
"qc 69,222\nqm a,a,v,116,164,108\nqw 1\nqr 180,2,14336\nqm a,a,v,116,168,112\nqx 229\n"
"qm a,a,v,116,172,116\nqr 184,2,22528\nqc 84,228\nqm a,a,v,112,172,100\nqr 188,2,30720\nqm a,a,v,112,168,96\n"
"qx 230\nqm a,a,v,112,164,92\nqx 231\nqm a,a,v,112,160,88\nqr 192,2,7168\nqc 85,230\n"
"qm a,v,v,140,252,108\nqw 3\nqr 196,2,15360\nqr 200,2,23552\nqr 204,2,31744\nqm a,a,v,120,176,88\n"
"qx 244\nqm a,a,v,120,180,92\nqw 4\nqx 245\nqm a,a,v,120,184,96\nv_add_u32 v2, s79, v3\n"
"qc 86,244\nqm a,a,v,120,188,100\nqx 246\nqm a,a,v,124,188,116\nqx 247\nqm a,a,v,124,184,112\n"
"qw 0\nqv 1\ns_barrier\nqm a,a,v,124,180,108\nqx 236\nqm a,a,v,124,176,104\n"
"qx 237\nqm a,v,v,140,252,112\nqr 144,1,0\nqc 87,246\nqm a,a,v,132,192,104\nqr 148,1,1024\n"
"qc 70,236\nqm a,a,v,132,196,108\nqx 238\nqm a,a,v,132,200,112\nqr 152,1,2048\nqm a,a,v,132,204,116\n"
"qr 156,1,3072\nqm a,a,v,128,204,100\nqx 239\nqm a,a,v,128,200,96\nqr 160,1,4096\nqc 71,238\n"
"qm a,a,v,128,196,92\nqr 164,1,5120\nqm a,a,v,128,192,88\nqm a,v,v,140,252,116\nqw 4\ns_sub_u32 s84, s84, 2\n"
"s_branch L2B%=\nXAQ%=:\ns_mov_b32 s84, s11\ns_cmp_lg_u32 s10, 0\ns_cbranch_scc1 TD0%=\ns_cmp_eq_u32 s84, 0\n"
"s_cbranch_scc1 XP%=\nXA%=:\nv_mfma_f32_16x16x32_bf16 v[124:127], a[144:147], v[184:187], v[20:23]\ns_add_u32 s77, s85, 0x1000000\ns_mov_b32 s85, s76\nqr 168,1,6144\n"
"qm v,a,v,124,148,192\nqw 3\ns_mov_b32 m0, s13\nqr 172,1,7168\nqm v,a,v,124,152,200\ns_add_u32 s13, s13, 0x4000\n"
"s_xor_b32 s78, s78, 0x8000\nqr 176,1,8192\nqm v,a,v,124,156,208\nqe 0,56,76,0\nqz v,a,v,132,156,240\ns_xor_b32 s79, s79, 0x8000\n"
"qr 180,1,9216\nqm v,a,v,132,152,232\nqr 184,1,10240\nqm v,a,v,132,148,224\nqr 188,1,11264\nqm v,a,v,132,144,216\n"
"qw 6\nqr 192,1,12288\nqz v,a,v,148,160,216\nqe 0,56,76,1024\nqm v,a,v,148,164,224\nqw 5\n"
"qr 196,1,13312\nqm v,a,v,148,168,232\nqr 200,1,14336\nqm v,a,v,148,172,240\nqe 0,56,76,2048\nv_mfma_f32_16x16x32_bf16 v[140:143], a[172:175], v[208:211], v[24:27]\n"
"qr 204,1,15360\nqm v,a,v,140,168,200\nqx 124\nqm v,a,v,140,164,192\nqe 0,56,76,3072\nqm v,a,v,140,160,184\n"
"qw 6\ns_and_b32 s13, s13, 0xffff\nqr 144,1,16384\nv_mfma_f32_16x16x32_bf16 v[156:159], a[176:179], v[184:187], v[28:31]\ns_add_u32 s76, s76, 0x4000\nqx 125\n"
"qm v,a,v,156,180,192\nqw 5\ns_mov_b32 m0, s13\nqr 148,1,17408\nqm v,a,v,156,184,200\ns_add_u32 s13, s13, 0x4000\n"
"qr 152,1,18432\nqc 56,124\nqm v,a,v,156,188,208\nqx 126\nqz v,a,v,164,188,240\nqr 156,1,19456\n"
"qm v,a,v,164,184,232\nqe 0,56,76,0\nqm v,a,v,164,180,224\nqx 127\nqm v,a,v,164,176,216\nqw 6\n"
"qr 160,1,20480\nqc 57,126\nqz v,a,v,180,192,216\nqr 164,1,21504\nqm v,a,v,180,196,224\nqw 6\n"
"qx 132\nqm v,a,v,180,200,232\nqr 168,1,22528\nqm v,a,v,180,204,240\nqr 172,1,23552\nv_mfma_f32_16x16x32_bf16 v[172:175], a[204:207], v[208:211], v[32:35]\n"
"qx 133\nqm v,a,v,172,200,200\nqr 176,1,24576\nqc 72,132\nqm v,a,v,172,196,192\nqe 0,56,76,1024\n"
"qm v,a,v,172,192,184\nqw 7\nqr 180,1,25600\nv_mfma_f32_16x16x32_bf16 v[196:199], a[144:147], v[216:219], v[36:39]\nqx 134\nqm v,a,v,196,148,224\n"
"qw 6\nqr 184,1,26624\nqm v,a,v,196,152,232\nqr 188,1,27648\nqm v,a,v,196,156,240\nqw 6\n"
"qx 135\nv_mfma_f32_16x16x32_bf16 v[212:215], a[160:163], v[216:219], v[40:43]\nqr 192,1,28672\nqc 73,134\nqm v,a,v,212,164,224\nqw 5\n"
"qr 196,1,29696\nqm v,a,v,212,168,232\nqe 0,56,76,2048\nqm v,a,v,212,172,240\nqw 4\nqr 200,1,30720\n"
"qx 148\nv_mfma_f32_16x16x32_bf16 v[228:231], a[176:179], v[216:219], v[44:47]\nqr 204,1,31744\nqm v,a,v,228,180,224\nqw 4\nqr 144,2,0\n"
"qx 149\nqm v,a,v,228,184,232\nqr 148,2,8192\nqc 74,148\nqm v,a,v,228,188,240\nqw 4\n"
"qr 152,2,16384\nqx 150\nv_mfma_f32_16x16x32_bf16 v[244:247], a[192:195], v[216:219], v[48:51]\nqe 0,56,76,3072\nqr 156,2,24576\nqm v,a,v,244,196,224\n"
"qw 4\nqx 151\nqm v,a,v,244,200,232\ns_and_b32 s13, s13, 0xffff\nqx 140\nqm v,a,v,244,204,240\n"
"qw 2\ns_add_u32 s76, s76, 0x4000\ns_sub_u32 s76, s76, 0x10000\nqm a,a,v,0,144,88\ns_cmp_lt_u32 s76, s86\ns_cselect_b32 s76, s87, s76\n"
"s_or_b32 m0, s34, 0x10000\ns_add_u32 s34, s34, 0x4000\nqm a,a,v,0,148,92\nqw 0\nqe 0,56,77,0\nqx 141\n"
"qm a,a,v,0,152,96\nqr 160,2,1024\nqc 75,150\nqm a,a,v,0,156,100\nqr 164,2,9216\nqx 142\n"
"qm a,a,v,4,156,116\nqr 168,2,17408\nqc 58,140\nqm a,a,v,4,152,112\nqr 172,2,25600\nqx 143\n"
"qm a,a,v,4,148,108\nqe 0,56,77,1024\nqm a,a,v,4,144,104\nqx 156\nqm a,v,v,64,252,88\nqw 2\n"
"qx 157\nqm a,a,v,12,160,104\nqe 0,56,77,2048\nqm a,a,v,12,164,108\nqw 0\nqr 176,2,2048\n"
"qc 59,142\nqm a,a,v,12,168,112\nqx 158\nqm a,a,v,12,172,116\nqr 180,2,10240\nqc 60,156\n"
"qm a,a,v,8,172,100\nqr 184,2,18432\nqx 159\nqm a,a,v,8,168,96\nqr 188,2,26624\nqc 61,158\n"
"qm a,a,v,8,164,92\nqx 164\nqm a,a,v,8,160,88\nqe 0,56,77,3072\nqm a,v,v,64,252,92\nqw 2\n"
"qx 165\nqm a,a,v,16,176,88\ns_and_b32 s34, s34, 0xffff\nqx 166\nqm a,a,v,16,180,92\nqw 0\n"
"s_add_u32 s77, s77, 0x4000\ns_or_b32 m0, s34, 0x10000\nqm a,a,v,16,184,96\ns_add_u32 s34, s34, 0x4000\nqr 192,2,3072\nqc 76,164\n"
"qm a,a,v,16,188,100\nqe 0,56,77,0\nqr 196,2,11264\nqx 167\nqm a,a,v,20,188,116\nqr 200,2,19456\n"
"qc 77,166\nqm a,a,v,20,184,112\nqr 204,2,27648\nqx 180\nqm a,a,v,20,180,108\nqx 181\n"
"qm a,a,v,20,176,104\nqe 0,56,77,1024\nqm a,v,v,64,252,96\nqw 2\nqx 182\nqm a,a,v,28,192,104\n"
"qx 183\nqm a,a,v,28,196,108\nqw 0\nqr 144,2,4096\nqc 78,180\nqm a,a,v,28,200,112\n"
"qr 148,2,12288\nqc 79,182\nqm a,a,v,28,204,116\nqe 0,56,77,2048\nqm a,a,v,24,204,100\nqr 152,2,20480\n"
"qx 172\nqm a,a,v,24,200,96\nqr 156,2,28672\nv_add_u32 v1, s78, v0\nqm a,a,v,24,196,92\nqx 173\n"
"qm a,a,v,24,192,88\nqe 0,56,77,3072\nqm a,v,v,64,252,100\ns_and_b32 s34, s34, 0xffff\ns_add_u32 s77, s77, 0x4000\ns_lshr_b32 s82, s82, 1\n"
"qw 2\nqx 174\nqm a,a,v,32,144,88\nqx 175\nqm a,a,v,32,148,92\nqw 0\n"
"qr 160,2,5120\nqc 62,172\nqm a,a,v,32,152,96\nqx 196\nqm a,a,v,32,156,100\nqr 164,2,13312\n"
"qc 63,174\nqm a,a,v,36,156,116\nqr 168,2,21504\nqm a,a,v,36,152,112\nqr 172,2,29696\nqx 197\n"
"qm a,a,v,36,148,108\ns_bitcmp1_b32 s82, 0\ns_cselect_b64 exec, -1, 0\nglobal_load_dword a255, v[4:5], off\nv_lshl_add_u64 v[4:5], v[4:5], 0, s[80:81]\ns_mov_b64 exec, -1\n"
"qm a,a,v,36,144,104\nqx 198\nqm a,v,v,68,252,104\nqw 2\nqx 199\nqm a,a,v,44,160,104\n"
"qr 176,2,6144\nqc 80,196\nqm a,a,v,44,164,108\nqw 1\nqx 212\nqm a,a,v,44,168,112\n"
"qr 180,2,14336\nqc 81,198\nqm a,a,v,44,172,116\nqx 213\nqm a,a,v,40,172,100\nqr 184,2,22528\n"
"qc 82,212\nqm a,a,v,40,168,96\nqr 188,2,30720\nqm a,a,v,40,164,92\nqx 214\nqm a,a,v,40,160,88\n"
"s_getpc_b64 s[72:73]\nCT5%=:\ns_add_u32 s72, s72, G2GO%=-CT5%=\ns_addc_u32 s73, s73, 0\ns_lshl2_add_u32 s74, s19, s15\ns_mul_i32 s74, s74, 256\n"
"v_lshl_add_u32 v11, v0, 3, s74\ns_mov_b32 s74, FINISH%=-G2GO%=\ns_mov_b32 s75, 0x20000\nqm a,v,v,68,252,108\nqw 2\nqr 192,2,7168\n"
"qr 196,2,15360\nqr 200,2,23552\nqr 204,2,31744\nqx 215\nqm a,a,v,48,176,88\nqx 228\n"
"qm a,a,v,48,180,92\nqw 4\nqx 229\nqm a,a,v,48,184,96\nqc 83,214\nv_add_u32 v2, s79, v3\n"
"qm a,a,v,48,188,100\nqx 230\nqm a,a,v,52,188,116\nqx 231\nqm a,a,v,52,184,112\ns_bfm_b64 exec, 2, 0\n"
"buffer_load_dword a255, v11, s[72:75], 0 offen\ns_mov_b64 exec, -1\nqm a,a,v,52,180,108\nqw 0\nqv 2\ns_barrier\n"
"qm a,a,v,52,176,104\nqr 144,1,0\nqc 84,228\nqm a,v,v,68,252,112\nqx 244\nqm a,a,v,60,192,104\n"
"qx 245\nqm a,a,v,60,196,108\nqr 148,1,1024\nqc 85,230\nqm a,a,v,60,200,112\nqx 246\n"
"qm a,a,v,60,204,116\nqr 152,1,2048\nqc 86,244\nqm a,a,v,56,204,100\nqr 156,1,3072\nqm a,a,v,56,200,96\n"
"qx 247\nqm a,a,v,56,196,92\nqr 160,1,4096\nqc 87,246\nqm a,a,v,56,192,88\nqr 164,1,5120\n"
"qm a,v,v,68,252,116\nqw 4\nEB%=:\ns_lshl_b32 s6, s88, 17\ns_lshl_b32 s7, s20, 12\ns_add_u32 s6, s6, s7\n"
"s_add_u32 s6, s6, 0x2800000\ns_xor_b32 s7, s6, 0xe0000\nqa 6,64\ns_nop 0\nqa 7,68\nqp 6\n"
"qp 7\ns_lshr_b32 s8, s88, 3\ns_lshl2_add_u32 s8, s8, s15\ns_lshl_b32 s8, s8, 8\ns_lshl3_add_u32 s8, s20, s8\ns_add_u32 s8, s8, 0x5000\n"
"qa 120,0\nqa 121,1\nqa 122,2\nqa 123,3\nqa 128,8\nqa 129,9\n"
"qa 130,10\nqa 131,11\nv_pk_mul_f32 v[120:121], v[120:121], v[6:7] op_sel:[0,0] op_sel_hi:[1,0]\nv_pk_mul_f32 v[122:123], v[122:123], v[6:7] op_sel:[0,0] op_sel_hi:[1,0]\nv_pk_mul_f32 v[128:129], v[128:129], v[6:7] op_sel:[0,0] op_sel_hi:[1,0]\nv_pk_mul_f32 v[130:131], v[130:131], v[6:7] op_sel:[0,0] op_sel_hi:[1,0]\n"
"qc 120,120\nqc 121,122\nqc 122,128\nqc 123,130\nbuffer_store_dwordx4 v[120:123], v0, s[56:59], s6 offen offset:0 sc1\nqa 136,4\n"
"qa 137,5\nqa 138,6\nqa 139,7\nqa 144,12\nqa 145,13\nqa 146,14\n"
"qa 147,15\nv_pk_mul_f32 v[136:137], v[136:137], v[6:7] op_sel:[0,1] op_sel_hi:[1,1]\nv_pk_mul_f32 v[138:139], v[138:139], v[6:7] op_sel:[0,1] op_sel_hi:[1,1]\nv_pk_mul_f32 v[144:145], v[144:145], v[6:7] op_sel:[0,1] op_sel_hi:[1,1]\nv_pk_mul_f32 v[146:147], v[146:147], v[6:7] op_sel:[0,1] op_sel_hi:[1,1]\nqc 136,136\n"
"qc 137,138\nqc 138,144\nqc 139,146\nbuffer_store_dwordx4 v[136:139], v0, s[56:59], s7 offen offset:0 sc1\nqa 152,16\nqa 153,17\n"
"qa 154,18\nqa 155,19\nqa 160,24\nqa 161,25\nqa 162,26\nqa 163,27\n"
"v_pk_mul_f32 v[152:153], v[152:153], v[6:7] op_sel:[0,0] op_sel_hi:[1,0]\nv_pk_mul_f32 v[154:155], v[154:155], v[6:7] op_sel:[0,0] op_sel_hi:[1,0]\nv_pk_mul_f32 v[160:161], v[160:161], v[6:7] op_sel:[0,0] op_sel_hi:[1,0]\nv_pk_mul_f32 v[162:163], v[162:163], v[6:7] op_sel:[0,0] op_sel_hi:[1,0]\nqc 152,152\nqc 153,154\n"
"qc 154,160\nqc 155,162\nbuffer_store_dwordx4 v[152:155], v0, s[56:59], s6 offen offset:1024 sc1\nqa 168,20\nqa 169,21\nqa 170,22\n"
"qa 171,23\nqa 176,28\nqa 177,29\nqa 178,30\nqa 179,31\nv_pk_mul_f32 v[168:169], v[168:169], v[6:7] op_sel:[0,1] op_sel_hi:[1,1]\n"
"v_pk_mul_f32 v[170:171], v[170:171], v[6:7] op_sel:[0,1] op_sel_hi:[1,1]\nv_pk_mul_f32 v[176:177], v[176:177], v[6:7] op_sel:[0,1] op_sel_hi:[1,1]\nv_pk_mul_f32 v[178:179], v[178:179], v[6:7] op_sel:[0,1] op_sel_hi:[1,1]\nqc 168,168\nqc 169,170\nqc 170,176\n"
"qc 171,178\nbuffer_store_dwordx4 v[168:171], v0, s[56:59], s7 offen offset:1024 sc1\nqa 120,32\nqa 121,33\nqa 122,34\nqa 123,35\n"
"qa 128,40\nqa 129,41\nqa 130,42\nqa 131,43\nv_pk_mul_f32 v[120:121], v[120:121], v[6:7] op_sel:[0,0] op_sel_hi:[1,0]\nv_pk_mul_f32 v[122:123], v[122:123], v[6:7] op_sel:[0,0] op_sel_hi:[1,0]\n"
"v_pk_mul_f32 v[128:129], v[128:129], v[6:7] op_sel:[0,0] op_sel_hi:[1,0]\nv_pk_mul_f32 v[130:131], v[130:131], v[6:7] op_sel:[0,0] op_sel_hi:[1,0]\nqc 120,120\nqc 121,122\nqc 122,128\nqc 123,130\n"
"buffer_store_dwordx4 v[120:123], v0, s[56:59], s6 offen offset:2048 sc1\nqa 136,36\nqa 137,37\nqa 138,38\nqa 139,39\nqa 144,44\n"
"qa 145,45\nqa 146,46\nqa 147,47\nv_pk_mul_f32 v[136:137], v[136:137], v[6:7] op_sel:[0,1] op_sel_hi:[1,1]\nv_pk_mul_f32 v[138:139], v[138:139], v[6:7] op_sel:[0,1] op_sel_hi:[1,1]\nv_pk_mul_f32 v[144:145], v[144:145], v[6:7] op_sel:[0,1] op_sel_hi:[1,1]\n"
"v_pk_mul_f32 v[146:147], v[146:147], v[6:7] op_sel:[0,1] op_sel_hi:[1,1]\nqc 136,136\nqc 137,138\nqc 138,144\nqc 139,146\nbuffer_store_dwordx4 v[136:139], v0, s[56:59], s7 offen offset:2048 sc1\n"
"qa 152,48\nqa 153,49\nqa 154,50\nqa 155,51\nqa 160,56\nqa 161,57\n"
"qa 162,58\nqa 163,59\nv_pk_mul_f32 v[152:153], v[152:153], v[6:7] op_sel:[0,0] op_sel_hi:[1,0]\nv_pk_mul_f32 v[154:155], v[154:155], v[6:7] op_sel:[0,0] op_sel_hi:[1,0]\nv_pk_mul_f32 v[160:161], v[160:161], v[6:7] op_sel:[0,0] op_sel_hi:[1,0]\nv_pk_mul_f32 v[162:163], v[162:163], v[6:7] op_sel:[0,0] op_sel_hi:[1,0]\n"
"qc 152,152\nqc 153,154\nqc 154,160\nqc 155,162\nbuffer_store_dwordx4 v[152:155], v0, s[56:59], s6 offen offset:3072 sc1\nqa 168,52\n"
"qa 169,53\nqa 170,54\nqa 171,55\nqa 176,60\nqa 177,61\nqa 178,62\n"
"qa 179,63\nv_pk_mul_f32 v[168:169], v[168:169], v[6:7] op_sel:[0,1] op_sel_hi:[1,1]\nv_pk_mul_f32 v[170:171], v[170:171], v[6:7] op_sel:[0,1] op_sel_hi:[1,1]\nv_pk_mul_f32 v[176:177], v[176:177], v[6:7] op_sel:[0,1] op_sel_hi:[1,1]\nv_pk_mul_f32 v[178:179], v[178:179], v[6:7] op_sel:[0,1] op_sel_hi:[1,1]\nqc 168,168\n"
"qc 169,170\nqc 170,176\nqc 171,178\nbuffer_store_dwordx4 v[168:171], v0, s[56:59], s7 offen offset:3072 sc1\nqz v,a,v,124,144,184\ns_add_u32 s77, s85, 0x1000000\n"
"s_mov_b32 s85, s76\ns_mov_b32 m0, s13\ns_add_u32 s13, s13, 0x4000\nqm v,a,v,124,148,192\nqw 2\nqr 168,1,6144\n"
"qm v,a,v,124,152,200\ns_xor_b32 s78, s78, 0x8000\ns_xor_b32 s79, s79, 0x8000\nqr 172,1,7168\nqm v,a,v,124,156,208\nqe 0,56,76,0\n"
"qz v,a,v,132,156,240\nqr 176,1,8192\nqm v,a,v,132,152,232\nqr 180,1,9216\nqm v,a,v,132,148,224\nqe 0,56,76,1024\n"
"qm v,a,v,132,144,216\nqw 4\nqr 184,1,10240\nqz v,a,v,148,160,216\nqx 124\nqx 125\n"
"qx 126\nqx 127\nqm v,a,v,148,164,224\nqw 3\nqr 188,1,11264\nqc 88,124\n"
"qm v,a,v,148,168,232\nqr 192,1,12288\nqc 89,126\nqm v,a,v,148,172,240\nqe 0,56,76,2048\nqz v,a,v,140,172,208\n"
"qx 132\nqx 133\nqx 134\nqx 135\nqm v,a,v,140,168,200\nqr 196,1,13312\n"
"qc 104,132\nqm v,a,v,140,164,192\nqe 0,56,76,3072\nqm v,a,v,140,160,184\nqw 4\ns_and_b32 s13, s13, 0xffff\n"
"s_add_u32 s76, s76, 0x4000\nqz v,a,v,156,176,184\nqx 148\nqx 149\nqx 150\nqx 151\n"
"qm v,a,v,156,180,192\nqw 2\ns_mov_b32 m0, s13\ns_add_u32 s13, s13, 0x4000\nqm v,a,v,156,184,200\nqr 200,1,14336\n"
"qc 105,134\nqm v,a,v,156,188,208\nqr 204,1,15360\nqc 106,148\nqz v,a,v,164,188,240\nqx 140\n"
"qx 141\nqx 142\nqx 143\nqm v,a,v,164,184,232\nqr 144,1,16384\nqc 107,150\n"
"qm v,a,v,164,180,224\nqe 0,56,76,0\nqm v,a,v,164,176,216\nqw 3\nqc 90,140\nqc 91,142\n"
"qz v,a,v,180,192,216\nqv 5\nqx 156\nqx 157\nqm v,a,v,180,196,224\nqw 1\n"
"qx 158\nqm v,a,v,180,200,232\nqr 148,1,17408\nqc 92,156\nqm v,a,v,180,204,240\nqr 152,1,18432\n"
"qx 159\nqz v,a,v,172,204,208\nqr 156,1,19456\nqc 93,158\nqm v,a,v,172,200,200\nqx 164\n"
"qm v,a,v,172,196,192\ns_mov_b64 exec, 1\nbuffer_store_dwordx2 v[250:251], off, s[56:59], s8 sc1\ns_mov_b64 exec, -1\nqm v,a,v,172,192,184\nqw 2\n"
"qx 165\nqz v,a,v,188,144,184\nqr 160,1,20480\nqc 108,164\nqm v,a,v,188,148,192\nqw 1\n"
"qx 166\nqm v,a,v,188,152,200\nqr 164,1,21504\nqm v,a,v,188,156,208\nqe 0,56,76,1024\nqr 168,1,22528\n"
"qx 167\nqz v,a,v,196,156,240\nqr 172,1,23552\nqc 109,166\nqm v,a,v,196,152,232\nqx 180\n"
"qm v,a,v,196,148,224\nqx 181\nqm v,a,v,196,144,216\nqw 2\nqr 176,1,24576\nqc 110,180\n"
"qz v,a,v,212,160,216\nqe 0,56,76,2048\nqm v,a,v,212,164,224\nqw 1\nqx 182\nqm v,a,v,212,168,232\n"
"qr 180,1,25600\nqm v,a,v,212,172,240\nqe 0,56,76,3072\ns_and_b32 s13, s13, 0xffff\ns_add_u32 s76, s76, 0x4000\ns_sub_u32 s76, s76, 0x10000\n"
"s_cmp_lt_u32 s76, s86\ns_cselect_b32 s76, s87, s76\ns_or_b32 m0, s34, 0x10000\ns_add_u32 s34, s34, 0x4000\nqr 184,1,26624\nqx 183\n"
"qz v,a,v,204,172,208\nqr 188,1,27648\nqc 111,182\nqm v,a,v,204,168,200\nqx 172\nqm v,a,v,204,164,192\n"
"qx 173\nqm v,a,v,204,160,184\nqw 2\nqr 192,1,28672\nqc 94,172\nqz v,a,v,220,176,184\n"
"qx 174\nqm v,a,v,220,180,192\nqw 1\nqr 196,1,29696\nqm v,a,v,220,184,200\nqe 0,56,77,0\n"
"qm v,a,v,220,188,208\nqr 200,1,30720\nqx 175\nqz v,a,v,228,188,240\nqr 204,1,31744\nqc 95,174\n"
"qm v,a,v,228,184,232\nqx 188\nqm v,a,v,228,180,224\nqe 0,56,77,1024\nqm v,a,v,228,176,216\nqw 2\n"
"qx 189\nqz v,a,v,244,192,216\nqr 144,2,0\nqc 96,188\nqm v,a,v,244,196,224\nqw 1\n"
"qx 190\nqm v,a,v,244,200,232\nqr 148,2,8192\nv_add_u32 v1, s78, v0\nqm v,a,v,244,204,240\nqx 191\n"
"qz v,a,v,236,204,208\nqe 0,56,77,2048\nqm v,a,v,236,200,200\nqr 152,2,16384\nqx 196\nqm v,a,v,236,196,192\n"
"qr 156,2,24576\nqc 97,190\nqm v,a,v,236,192,184\nqe 0,56,77,3072\ns_and_b32 s34, s34, 0xffff\ns_add_u32 s77, s77, 0x4000\n"
"s_or_b32 m0, s34, 0x10000\ns_add_u32 s34, s34, 0x4000\nqx 197\nqw 2\nqz a,a,v,72,144,56\nqr 160,2,1024\n"
"qc 112,196\nqm a,a,v,72,148,60\nqr 164,2,9216\nqx 198\nqz a,a,v,76,148,76\nqx 199\n"
"qm a,a,v,76,144,72\nqw 2\nqr 168,2,17408\nqc 113,198\nqm a,a,v,76,152,80\nqr 172,2,25600\n"
"qm a,a,v,76,156,84\nqx 212\nqz a,v,v,136,252,56\nqw 2\nqe 0,56,77,0\nqz a,a,v,80,160,56\n"
"qr 176,2,2048\nqx 213\nqm a,a,v,80,164,60\nqr 180,2,10240\nqc 114,212\nqz a,a,v,84,164,76\n"
"qx 214\nqm a,a,v,84,160,72\nqw 2\nqx 215\nqm a,a,v,84,168,80\nqe 0,56,77,1024\n"
"qr 184,2,18432\nqm a,a,v,84,172,84\nqr 188,2,26624\nqc 115,214\nqm a,v,v,136,252,60\nqw 2\n"
"qr 192,2,3072\nqx 204\nqz a,a,v,88,176,56\nqr 196,2,11264\nqm a,a,v,88,180,60\nqx 205\n"
"qz a,a,v,92,180,76\nqe 0,56,77,2048\nqm a,a,v,92,176,72\nqw 2\nqr 200,2,19456\nqx 206\n"
"qm a,a,v,92,184,80\nqr 204,2,27648\nqc 98,204\nqm a,a,v,92,188,84\nqe 0,56,77,3072\ns_and_b32 s34, s34, 0xffff\n"
"s_add_u32 s77, s77, 0x4000\ns_lshr_b32 s82, s82, 1\nqx 207\nqr 144,2,4096\nqw 3\nqz a,a,v,96,192,56\n"
"qr 148,2,12288\nqc 99,206\nqm a,a,v,96,196,60\nqx 220\nqz a,a,v,100,196,76\nqx 221\n"
"qm a,a,v,100,192,72\nqw 2\nqr 152,2,20480\nqc 100,220\nqm a,a,v,100,200,80\nqr 156,2,28672\n"
"qm a,a,v,100,204,84\nqw 2\nqx 222\nqz a,a,v,104,144,56\ns_bitcmp1_b32 s82, 0\ns_cselect_b64 exec, -1, 0\n"
"global_load_dword a255, v[4:5], off\nv_lshl_add_u64 v[4:5], v[4:5], 0, s[80:81]\ns_mov_b64 exec, -1\nqr 160,2,5120\nqm a,a,v,104,148,60\nqr 164,2,13312\n"
"qx 223\nqz a,a,v,108,148,76\nqx 228\nqm a,a,v,108,144,72\nqw 2\nqr 168,2,21504\n"
"qc 101,222\nqm a,a,v,108,152,80\nqx 229\nqm a,a,v,108,156,84\nqr 172,2,29696\nqc 116,228\n"
"qz a,v,v,140,252,72\nqw 2\nqx 230\nqz a,a,v,112,160,56\nqr 176,2,6144\nqm a,a,v,112,164,60\n"
"qr 180,2,14336\nqz a,a,v,116,164,76\nqr 184,2,22528\nqr 188,2,30720\nqr 192,2,7168\nqr 196,2,15360\n"
"qr 200,2,23552\nqr 204,2,31744\nqx 231\nqm a,a,v,116,160,72\nqw 8\nqx 244\n"
"qm a,a,v,116,168,80\nqx 245\nqm a,a,v,116,172,84\nqc 117,230\nv_add_u32 v2, s79, v3\nqm a,v,v,140,252,76\n"
"qw 6\nqx 246\nqz a,a,v,120,176,56\nqx 247\nqm a,a,v,120,180,60\nqw 0\n"
"qv 1\ns_barrier\nqz a,a,v,124,180,76\nqr 144,1,0\nqc 118,244\nqm a,a,v,124,176,72\n"
"qx 236\nqm a,a,v,124,184,80\nqx 237\nqm a,a,v,124,188,84\nqr 148,1,1024\nqc 119,246\n"
"qm a,v,v,140,252,80\nqx 238\nqz a,a,v,128,192,56\nqr 152,1,2048\nqc 102,236\nqm a,a,v,128,196,60\n"
"qr 156,1,3072\nqz a,a,v,132,196,76\nqx 239\nqm a,a,v,132,192,72\nqr 160,1,4096\nqc 103,238\n"
"qm a,a,v,132,200,80\nqr 164,1,5120\nqm a,a,v,132,204,84\nqm a,v,v,140,252,84\nqw 4\ns_sub_u32 s84, s84, 1\n"
"s_branch CHK%=\nPXN_o%=:\ns_mov_b32 s35, 200000\nPXN_s%=:\nbuffer_load_dwordx2 v[254:255], v252, s[56:59], s34 offen offset:0 sc1\nqv 0\n"
"v_cmp_eq_u64 vcc, s[16:17], v[254:255]\ns_nop 3\ns_cmp_eq_u64 vcc, exec\ns_cbranch_scc1 PXN_sx%=\ns_sleep 3\ns_sub_u32 s35, s35, 1\n"
"s_cmp_lg_u32 s35, 0\ns_cbranch_scc1 PXN_s%=\nPXN_sx%=:\ns_branch PXN_r%=\nPKV_o%=:\ns_mov_b32 s35, 200000\n"
"PKV_s%=:\nbuffer_load_dwordx2 v[250:251], v17, s[56:59], s6 offen offset:0 sc1\nqv 0\nv_cmp_eq_u64 vcc, s[16:17], v[250:251]\ns_nop 3\ns_cmp_eq_u64 vcc, exec\n"
"s_cbranch_scc1 PKV_sx%=\ns_sleep 3\ns_sub_u32 s35, s35, 1\ns_cmp_lg_u32 s35, 0\ns_cbranch_scc1 PKV_s%=\nPKV_sx%=:\n"
"s_branch PKV_r%=\nSLOWJ%=:\ns_branch SLOW%=\nTD0%=:\nqz v,a,v,124,144,120\ns_add_u32 s77, s85, 0x1000000\n"
"qb 72,0\nqb 73,0\nqm v,a,v,124,148,128\nqw 2\nqr 168,1,6144\nqb 74,0\n"
"qm v,a,v,124,152,136\ns_mov_b32 s85, s76\nqr 172,1,7168\nqb 75,0\nqm v,a,v,124,156,144\ns_mov_b32 m0, s13\n"
"s_add_u32 s13, s13, 0x4000\nqb 76,0\nqz v,a,v,132,156,176\nqe 0,56,76,0\nqm v,a,v,132,152,168\ns_xor_b32 s79, s79, 0x8000\n"
"qb 77,0\nqb 78,0\nqm v,a,v,132,148,160\nqb 79,0\nqb 80,0\nqm v,a,v,132,144,152\n"
"qb 81,0\nqb 82,0\nqz v,a,v,140,144,184\nqx 124\nqm v,a,v,140,148,192\nqx 125\n"
"qm v,a,v,140,152,200\nqx 126\nqm v,a,v,140,156,208\nqx 127\nqz v,a,v,148,156,240\nqc 56,124\n"
"qc 57,126\nqm v,a,v,148,152,232\nqx 132\nqm v,a,v,148,148,224\nqx 133\nqm v,a,v,148,144,216\n"
"qw 2\nqx 134\nqz v,a,v,180,160,216\nqx 135\nqm v,a,v,180,164,224\nqw 0\n"
"qr 176,1,8192\nqc 64,132\nqm v,a,v,180,168,232\nqx 140\nqm v,a,v,180,172,240\nqe 0,56,76,1024\n"
"qz v,a,v,172,172,208\nqx 141\nqm v,a,v,172,168,200\nqr 180,1,9216\nqb 83,0\nqm v,a,v,172,164,192\n"
"qr 184,1,10240\nqc 65,134\nqm v,a,v,172,160,184\nqx 142\nqz v,a,v,164,160,152\nqr 188,1,11264\n"
"qb 84,0\nqm v,a,v,164,164,160\nqx 143\nqm v,a,v,164,168,168\nqb 85,0\nqb 86,0\n"
"qm v,a,v,164,172,176\nqc 72,140\nqb 87,0\nqz v,a,v,156,172,144\nqx 148\nqm v,a,v,156,168,136\n"
"qx 149\nqm v,a,v,156,164,128\nqe 0,56,76,2048\nqm v,a,v,156,160,120\nqw 2\nqb 88,0\n"
"qc 73,142\nqz v,a,v,188,176,120\nqx 150\nqm v,a,v,188,180,128\nqw 0\nqb 89,0\n"
"qb 90,0\nqm v,a,v,188,184,136\nqx 151\nqm v,a,v,188,188,144\nqc 80,148\nqb 91,0\n"
"qz v,a,v,196,188,176\nqx 180\nqm v,a,v,196,184,168\nqr 192,1,12288\nqb 92,0\nqm v,a,v,196,180,160\n"
"qx 181\nqm v,a,v,196,176,152\nqr 196,1,13312\nqb 93,0\nqz v,a,v,204,176,184\nqr 200,1,14336\n"
"qc 81,150\nqm v,a,v,204,180,192\nqx 182\nqm v,a,v,204,184,200\nqr 204,1,15360\nqb 94,0\n"
"qm v,a,v,204,188,208\nqe 0,56,76,3072\nqz v,a,v,212,188,240\ns_and_b32 s13, s13, 0xffff\nqx 183\nqm v,a,v,212,184,232\n"
"s_add_u32 s76, s76, 0x4000\nqb 95,0\nqc 82,180\nqm v,a,v,212,180,224\ns_or_b32 m0, s34, 0x10000\nqx 172\n"
"qm v,a,v,212,176,216\nqw 2\nqb 96,0\nqb 97,0\nqz v,a,v,244,192,216\ns_add_u32 s34, s34, 0x4000\n"
"qx 173\nqm v,a,v,244,196,224\nqw 0\nqc 83,182\nqb 98,0\nqm v,a,v,244,200,232\n"
"qx 174\nqm v,a,v,244,204,240\nqb 99,0\nqb 100,0\nqz v,a,v,236,204,208\nqx 175\n"
"qm v,a,v,236,200,200\nqe 0,56,77,0\nqm v,a,v,236,196,192\nqc 74,172\nqb 101,0\nqm v,a,v,236,192,184\n"
"qx 164\nqz v,a,v,228,192,152\nqr 144,2,0\nqb 102,0\nqm v,a,v,228,196,160\nqx 165\n"
"qm v,a,v,228,200,168\nqr 148,2,8192\nqc 75,174\nqm v,a,v,228,204,176\nqr 152,2,16384\nqx 166\n"
"qz v,a,v,220,204,144\nqr 156,2,24576\nqb 103,0\nqm v,a,v,220,200,136\nqx 167\nqm v,a,v,220,196,128\n"
"qb 104,0\nqb 105,0\nqm v,a,v,220,192,120\nqw 2\nqc 66,164\nqx 156\n"
"qm a,a,v,0,144,88\nqe 0,56,77,1024\nqm a,a,v,0,148,92\nqw 0\nqb 106,0\nqx 157\n"
"qm a,a,v,0,152,96\nqr 160,2,1024\nqb 107,0\nqm a,a,v,0,156,100\nqr 164,2,9216\nqc 67,166\n"
"qx 158\nqm a,a,v,4,156,116\nqr 168,2,17408\nqb 108,0\nqm a,a,v,4,152,112\nqr 172,2,25600\n"
"qx 159\nqm a,a,v,4,148,108\nqb 109,0\nqc 58,156\nqm a,a,v,4,144,104\nqx 188\n"
"qm a,v,v,64,252,88\nqw 2\nqb 110,0\nqb 111,0\nqm a,a,v,12,160,104\nqx 189\n"
"qm a,a,v,12,164,108\nqw 0\nqe 0,56,77,2048\nqm a,a,v,12,168,112\nqr 176,2,2048\nqb 112,0\n"
"qc 59,158\nqx 190\nqm a,a,v,12,172,116\nqr 180,2,10240\nqb 113,0\nqm a,a,v,8,172,100\n"
"qr 184,2,18432\nqx 191\nqm a,a,v,8,168,96\nqr 188,2,26624\nqb 114,0\nqm a,a,v,8,164,92\n"
"qc 60,188\nqx 196\nqm a,a,v,8,160,88\nqb 115,0\nqb 116,0\nqm a,v,v,64,252,92\n"
"qw 2\nqx 197\nqm a,a,v,16,176,88\nqe 0,56,77,3072\nqm a,a,v,16,180,92\ns_and_b32 s34, s34, 0xffff\n"
"s_add_u32 s77, s77, 0x4000\ns_lshr_b32 s82, s82, 1\nqw 0\nqc 61,190\nqx 198\nqm a,a,v,16,184,96\n"
"qr 192,2,3072\nqb 117,0\nqm a,a,v,16,188,100\nqr 196,2,11264\nqb 118,0\nqx 199\n"
"qm a,a,v,20,188,116\nqr 200,2,19456\nqb 119,0\nqm a,a,v,20,184,112\nqr 204,2,27648\nqc 68,196\n"
"qx 204\nqm a,a,v,20,180,108\nqb 120,0\nqb 121,0\nqm a,a,v,20,176,104\nqx 205\n"
"qm a,v,v,64,252,96\nqw 2\ns_bitcmp1_b32 s82, 0\ns_cselect_b64 exec, -1, 0\nglobal_load_dword a255, v[4:5], off\nv_lshl_add_u64 v[4:5], v[4:5], 0, s[80:81]\n"
"s_mov_b64 exec, -1\nqm a,a,v,28,192,104\nqc 69,198\nqx 206\nqm a,a,v,28,196,108\nqw 0\n"
"qr 144,2,4096\nqb 122,0\nqm a,a,v,28,200,112\nqx 207\nqm a,a,v,28,204,116\nqr 148,2,12288\n"
"qb 123,0\nqm a,a,v,24,204,100\nqr 152,2,20480\nqc 76,204\nqx 212\nqm a,a,v,24,200,96\n"
"qr 156,2,28672\nqb 124,0\nqm a,a,v,24,196,92\nqb 125,0\nqx 213\nqm a,a,v,24,192,88\n"
"qb 126,0\nqc 77,206\nqm a,v,v,64,252,100\nqw 2\nqx 214\nqm a,a,v,32,144,88\n"
"qb 127,0\nqb 128,0\nqm a,a,v,32,148,92\nqw 0\nqx 215\nqm a,a,v,32,152,96\n"
"qr 160,2,5120\nqc 84,212\nqm a,a,v,32,156,100\nqr 164,2,13312\nqx 244\nqm a,a,v,36,156,116\n"
"qr 168,2,21504\nqb 129,0\nqm a,a,v,36,152,112\nqr 172,2,29696\nqx 245\nqm a,a,v,36,148,108\n"
"qb 130,0\nqb 131,0\nqm a,a,v,36,144,104\nqc 85,214\nqx 246\nqm a,v,v,68,252,104\n"
"qw 2\nqb 132,0\nqb 133,0\nqm a,a,v,44,160,104\nqx 247\nqm a,a,v,44,164,108\n"
"qw 0\nqr 176,2,6144\nqc 86,244\nqm a,a,v,44,168,112\nqx 236\nqm a,a,v,44,172,116\n"
"qr 180,2,14336\nqb 134,0\nqm a,a,v,40,172,100\nqr 184,2,22528\nqx 237\nqm a,a,v,40,168,96\n"
"qr 188,2,30720\nqb 135,0\nqm a,a,v,40,164,92\nqc 87,246\nqx 238\nqm a,a,v,40,160,88\n"
"qr 192,2,7168\nqb 136,0\nqm a,v,v,68,252,108\nqw 3\nqr 196,2,15360\nqr 200,2,23552\n"
"qr 204,2,31744\nqx 239\nqm a,a,v,48,176,88\nqb 137,0\nqb 138,0\nqm a,a,v,48,180,92\n"
"qw 4\nqc 78,236\nqx 228\nqm a,a,v,48,184,96\nqb 139,0\nqb 140,0\n"
"qm a,a,v,48,188,100\nqx 229\nqm a,a,v,52,188,116\nqc 79,238\nv_add_u32 v2, s79, v3\nqm a,a,v,52,184,112\n"
"qx 230\nqm a,a,v,52,180,108\nqw 0\nqv 1\ns_barrier\nqb 141,0\n"
"qm a,a,v,52,176,104\nqx 231\nqm a,v,v,68,252,112\nqb 142,0\nqc 70,228\nqm a,a,v,60,192,104\n"
"qx 220\nqm a,a,v,60,196,108\nqr 144,1,16384\nqb 143,0\nqm a,a,v,60,200,112\nqx 221\n"
"qm a,a,v,60,204,116\nqr 148,1,17408\nqc 71,230\nqm a,a,v,56,204,100\nqr 152,1,18432\nqx 222\n"
"qm a,a,v,56,200,96\nqr 156,1,19456\nqc 62,220\nqm a,a,v,56,196,92\nqr 160,1,20480\nqx 223\n"
"qm a,a,v,56,192,88\nqr 164,1,21504\nqc 63,222\nqm a,v,v,68,252,116\nqw 4\n.p2align 3\n"
"DB0%=:\nqz v,a,v,124,144,120\ns_mov_b32 m0, s13\ns_add_u32 s13, s13, 0x4000\nqr 168,1,22528\nqm v,a,v,124,148,128\n"
"qw 3\nqr 172,1,23552\nqm v,a,v,124,152,136\nqe 0,56,76,0\nqm v,a,v,124,156,144\nqe 0,56,76,1024\n"
"qz v,a,v,132,156,176\ns_xor_b32 s78, s78, 0x8000\nqr 176,1,24576\nqm v,a,v,132,152,168\nqr 180,1,25600\nqm v,a,v,132,148,160\n"
"qr 184,1,26624\nqm v,a,v,132,144,152\nqr 188,1,27648\nqz v,a,v,140,144,184\nqx 124\nqm v,a,v,140,148,192\n"
"qx 125\nqm v,a,v,140,152,200\nqx 126\nqm v,a,v,140,156,208\nqx 127\nqz v,a,v,148,156,240\n"
"qc 88,124\nqc 89,126\nqm v,a,v,148,152,232\nqx 132\nqm v,a,v,148,148,224\nqx 133\n"
"qm v,a,v,148,144,216\nqw 6\nqx 134\nqz v,a,v,180,160,216\nqx 135\nqm v,a,v,180,164,224\n"
"qw 4\nqc 96,132\nqc 97,134\nqm v,a,v,180,168,232\nqx 140\nqm v,a,v,180,172,240\n"
"qx 141\nqz v,a,v,172,172,208\nqx 142\nqm v,a,v,172,168,200\nqx 143\nqm v,a,v,172,164,192\n"
"qc 104,140\nqc 105,142\nqm v,a,v,172,160,184\nqx 148\nqz v,a,v,164,160,152\nqx 149\n"
"qm v,a,v,164,164,160\nqe 0,56,76,2048\nqm v,a,v,164,168,168\nqx 150\nqm v,a,v,164,172,176\nqx 151\n"
"qz v,a,v,156,172,144\nqr 192,1,28672\nqc 112,148\nqm v,a,v,156,168,136\nqx 180\nqm v,a,v,156,164,128\n"
"qx 181\nqm v,a,v,156,160,120\nqw 3\nqr 196,1,29696\nqc 113,150\nqz v,a,v,188,176,120\n"
"qr 200,1,30720\nqc 114,180\nqm v,a,v,188,180,128\nqw 3\nqx 182\nqm v,a,v,188,184,136\n"
"qr 204,1,31744\nqm v,a,v,188,188,144\nqe 0,56,76,3072\nqz v,a,v,196,188,176\ns_and_b32 s13, s13, 0xffff\nqx 183\n"
"qm v,a,v,196,184,168\ns_add_u32 s76, s76, 0x4000\nqx 172\nqm v,a,v,196,180,160\ns_sub_u32 s76, s76, 0x10000\nqx 173\n"
"qm v,a,v,196,176,152\ns_cmp_lt_u32 s76, s86\ns_cselect_b32 s76, s87, s76\nqc 115,182\nqz v,a,v,204,176,184\ns_or_b32 m0, s34, 0x10000\n"
"qx 174\nqm v,a,v,204,180,192\ns_add_u32 s34, s34, 0x4000\nqx 175\nqm v,a,v,204,184,200\nqe 0,56,77,0\n"
"qm v,a,v,204,188,208\nqr 144,2,0\nqc 106,172\nqz v,a,v,212,188,240\nqx 164\nqm v,a,v,212,184,232\n"
"qx 165\nqm v,a,v,212,180,224\nqr 148,2,8192\nqc 107,174\nqm v,a,v,212,176,216\nqw 4\n"
"qx 166\nqz v,a,v,244,192,216\nqx 167\nqm v,a,v,244,196,224\nqw 2\nqr 152,2,1024\n"
"qc 98,164\nqm v,a,v,244,200,232\nqx 156\nqm v,a,v,244,204,240\nqe 0,56,77,1024\nqz v,a,v,236,204,208\n"
"qx 157\nqm v,a,v,236,200,200\nqr 156,2,9216\nqc 99,166\nqm v,a,v,236,196,192\nqx 158\n"
"qm v,a,v,236,192,184\nqx 159\nqz v,a,v,228,192,152\nqc 90,156\nqc 91,158\nqm v,a,v,228,196,160\n"
"qx 188\nqm v,a,v,228,200,168\nqx 189\nqm v,a,v,228,204,176\nqe 0,56,77,2048\nqz v,a,v,220,204,144\n"
"qr 160,2,2048\nqc 92,188\nqm v,a,v,220,200,136\nqx 190\nqm v,a,v,220,196,128\nqr 164,2,10240\n"
"v_add_u32 v1, s78, v0\nqm v,a,v,220,192,120\nqw 4\nqx 191\nqm a,a,v,0,144,56\nqx 196\n"
"qm a,a,v,0,148,60\nqx 197\nqm a,a,v,4,148,68\nqe 0,56,77,3072\nqm a,a,v,4,144,64\ns_and_b32 s34, s34, 0xffff\n"
"s_add_u32 s77, s77, 0x4000\ns_lshr_b32 s82, s82, 1\nqm a,a,v,72,144,72\nqr 168,2,3072\nqc 93,190\nqm a,a,v,72,148,76\n"
"qx 198\nqm a,a,v,76,148,84\nqr 172,2,11264\nqc 100,196\nqm a,a,v,76,144,80\nqx 199\n"
"qm a,v,v,64,252,56\nqw 4\nqx 204\nqm a,a,v,84,152,80\nqx 205\nqm a,a,v,84,156,84\n"
"qr 176,2,4096\nqc 101,198\nqm a,a,v,80,156,76\nqx 206\nqm a,a,v,80,152,72\nqx 207\n"
"qm a,a,v,12,152,64\nqr 180,2,12288\nqc 108,204\nqm a,a,v,12,156,68\nqx 212\nqm a,a,v,8,156,60\n"
"qx 213\nqm a,a,v,8,152,56\nqr 184,2,5120\nqc 109,206\nqm a,v,v,64,252,60\nqw 5\n"
"qx 214\nqm a,a,v,16,160,56\nqx 215\nqm a,a,v,16,164,60\nqr 188,2,13312\nqc 116,212\n"
"qm a,a,v,20,164,68\nqx 244\nqm a,a,v,20,160,64\nqx 245\nqm a,a,v,88,160,72\nqc 117,214\n"
"qc 118,244\nqm a,a,v,88,164,76\nqx 246\nqm a,a,v,92,164,84\nqx 247\nqm a,a,v,92,160,80\n"
"qx 236\nqm a,v,v,68,252,64\nqw 4\ns_bitcmp1_b32 s82, 0\ns_cselect_b64 exec, -1, 0\nglobal_load_dword a255, v[4:5], off\n"
"v_lshl_add_u64 v[4:5], v[4:5], 0, s[80:81]\ns_mov_b64 exec, -1\nqm a,a,v,100,168,80\nqr 192,2,6144\nqc 119,246\nqm a,a,v,100,172,84\n"
"qr 196,2,14336\nqm a,a,v,96,172,76\nqx 237\nqm a,a,v,96,168,72\nqx 238\nqm a,a,v,28,168,64\n"
"qx 239\nqm a,a,v,28,172,68\nqr 200,2,7168\nqc 110,236\nqm a,a,v,24,172,60\nqr 204,2,15360\n"
"qc 111,238\nqm a,a,v,24,168,56\nqx 228\nqm a,v,v,68,252,68\nqw 6\nqx 229\n"
"qm a,a,v,32,176,56\nqx 230\nqm a,a,v,32,180,60\nqx 231\nqm a,a,v,36,180,68\nqc 102,228\n"
"qc 103,230\nqm a,a,v,36,176,64\nqx 220\nqm a,a,v,104,176,72\nqx 221\nqm a,a,v,104,180,76\n"
"qx 222\nqm a,a,v,108,180,84\nqx 223\nqm a,a,v,108,176,80\nqc 94,220\nqc 95,222\n"
"qm a,v,v,136,252,72\nqw 4\nqm a,a,v,116,184,80\nqm a,a,v,116,188,84\nqm a,a,v,112,188,76\nqm a,a,v,112,184,72\n"
"qm a,a,v,44,184,64\nqm a,a,v,44,188,68\nqm a,a,v,40,188,60\nqm a,a,v,40,184,56\nqm a,v,v,136,252,76\nqw 2\n"
"qm a,a,v,48,192,56\nqm a,a,v,48,196,60\nqm a,a,v,52,196,68\nqm a,a,v,52,192,64\nqm a,a,v,120,192,72\nqm a,a,v,120,196,76\n"
"qw 0\nqv 1\ns_barrier\nqm a,a,v,124,196,84\nqr 144,1,0\nqm a,a,v,124,192,80\n"
"qr 148,1,1024\nqm a,v,v,140,252,80\nqr 152,1,2048\nqm a,a,v,132,200,80\nqr 156,1,3072\nqm a,a,v,132,204,84\n"
"qr 160,1,4096\nqm a,a,v,128,204,76\nqr 164,1,5120\nqm a,a,v,128,200,72\nqm a,a,v,60,200,64\nqm a,a,v,60,204,68\n"
"qm a,a,v,56,204,60\nqm a,a,v,56,200,56\nqm a,v,v,140,252,84\nqw 4\ns_sub_u32 s10, s10, 1\ns_cmp_eq_u32 s10, 0\n"
"s_cbranch_scc1 XD0%=\nDA0%=:\nqz v,a,v,124,144,120\ns_add_u32 s77, s85, 0x1000000\ns_mov_b32 s85, s76\nqr 168,1,6144\n"
"qm v,a,v,124,148,128\nqw 3\ns_mov_b32 m0, s13\nqr 172,1,7168\nqm v,a,v,124,152,136\ns_add_u32 s13, s13, 0x4000\n"
"s_xor_b32 s79, s79, 0x8000\nqr 176,1,8192\nqm v,a,v,124,156,144\nqe 0,56,76,0\nqz v,a,v,132,156,176\nqe 0,56,76,1024\n"
"qm v,a,v,132,152,168\nqr 180,1,9216\nqm v,a,v,132,148,160\nqr 184,1,10240\nqm v,a,v,132,144,152\nqr 188,1,11264\n"
"qz v,a,v,140,144,184\nqx 124\nqm v,a,v,140,148,192\nqx 125\nqm v,a,v,140,152,200\nqx 126\n"
"qm v,a,v,140,156,208\nqx 127\nqz v,a,v,148,156,240\nqc 56,124\nqc 57,126\nqm v,a,v,148,152,232\n"
"qx 132\nqm v,a,v,148,148,224\nqx 133\nqm v,a,v,148,144,216\nqw 6\nqx 134\n"
"qz v,a,v,180,160,216\nqx 135\nqm v,a,v,180,164,224\nqw 4\nqc 64,132\nqc 65,134\n"
"qm v,a,v,180,168,232\nqx 140\nqm v,a,v,180,172,240\nqx 141\nqz v,a,v,172,172,208\nqx 142\n"
"qm v,a,v,172,168,200\nqx 143\nqm v,a,v,172,164,192\nqc 72,140\nqc 73,142\nqm v,a,v,172,160,184\n"
"qx 148\nqz v,a,v,164,160,152\nqx 149\nqm v,a,v,164,164,160\nqe 0,56,76,2048\nqm v,a,v,164,168,168\n"
"qx 150\nqm v,a,v,164,172,176\nqx 151\nqz v,a,v,156,172,144\nqr 192,1,12288\nqc 80,148\n"
"qm v,a,v,156,168,136\nqx 180\nqm v,a,v,156,164,128\nqx 181\nqm v,a,v,156,160,120\nqw 3\n"
"qr 196,1,13312\nqc 81,150\nqz v,a,v,188,176,120\nqr 200,1,14336\nqc 82,180\nqm v,a,v,188,180,128\n"
"qw 3\nqx 182\nqm v,a,v,188,184,136\nqr 204,1,15360\nqm v,a,v,188,188,144\nqe 0,56,76,3072\n"
"qz v,a,v,196,188,176\ns_and_b32 s13, s13, 0xffff\nqx 183\nqm v,a,v,196,184,168\ns_add_u32 s76, s76, 0x4000\nqx 172\n"
"qm v,a,v,196,180,160\ns_or_b32 m0, s34, 0x10000\nqx 173\nqm v,a,v,196,176,152\ns_add_u32 s34, s34, 0x4000\nqc 83,182\n"
"qc 74,172\nqz v,a,v,204,176,184\nqx 174\nqm v,a,v,204,180,192\nqx 175\nqm v,a,v,204,184,200\n"
"qe 0,56,77,0\nqm v,a,v,204,188,208\nqx 164\nqz v,a,v,212,188,240\nqr 144,2,16384\nqc 75,174\n"
"qm v,a,v,212,184,232\nqx 165\nqm v,a,v,212,180,224\nqr 148,2,24576\nqc 66,164\nqm v,a,v,212,176,216\n"
"qw 4\nqx 166\nqz v,a,v,244,192,216\nqx 167\nqm v,a,v,244,196,224\nqw 2\n"
"qx 156\nqm v,a,v,244,200,232\nqe 0,56,77,1024\nqm v,a,v,244,204,240\nqx 157\nqz v,a,v,236,204,208\n"
"qr 152,2,17408\nqc 67,166\nqm v,a,v,236,200,200\nqr 156,2,25600\nqc 58,156\nqm v,a,v,236,196,192\n"
"qx 158\nqm v,a,v,236,192,184\nqx 159\nqz v,a,v,228,192,152\nqx 188\nqm v,a,v,228,196,160\n"
"qx 189\nqm v,a,v,228,200,168\nqe 0,56,77,2048\nqm v,a,v,228,204,176\nqr 160,2,18432\nqc 59,158\n"
"qz v,a,v,220,204,144\nqx 190\nqm v,a,v,220,200,136\nqr 164,2,26624\nqc 60,188\nqm v,a,v,220,196,128\n"
"qx 191\nqm v,a,v,220,192,120\nqw 4\nqx 196\nqm a,a,v,0,144,88\nqx 197\n"
"qm a,a,v,0,148,92\nqe 0,56,77,3072\nqm a,a,v,4,148,100\ns_and_b32 s34, s34, 0xffff\ns_add_u32 s77, s77, 0x4000\ns_lshr_b32 s82, s82, 1\n"
"qm a,a,v,4,144,96\nqr 168,2,19456\nqc 61,190\nqm a,a,v,72,144,104\nqx 198\nqm a,a,v,72,148,108\n"
"qr 172,2,27648\nqc 68,196\nqm a,a,v,76,148,116\nqx 199\nqm a,a,v,76,144,112\nqx 204\n"
"qm a,v,v,64,252,88\nqw 4\nqx 205\nqm a,a,v,84,152,112\nqr 176,2,20480\nqc 69,198\n"
"qm a,a,v,84,156,116\nqx 206\nqm a,a,v,80,156,108\nqx 207\nqm a,a,v,80,152,104\nqr 180,2,28672\n"
"qc 76,204\nqm a,a,v,12,152,96\nqx 212\nqm a,a,v,12,156,100\nqx 213\nqm a,a,v,8,156,92\n"
"qr 184,2,21504\nqc 77,206\nqm a,a,v,8,152,88\nqx 214\nqm a,v,v,64,252,92\nqw 5\n"
"qx 215\nqm a,a,v,16,160,88\nqr 188,2,29696\nqc 84,212\nqm a,a,v,16,164,92\nqx 244\n"
"qm a,a,v,20,164,100\nqx 245\nqm a,a,v,20,160,96\nqc 85,214\nqc 86,244\nqm a,a,v,88,160,104\n"
"qx 246\nqm a,a,v,88,164,108\nqx 247\nqm a,a,v,92,164,116\nqx 236\nqm a,a,v,92,160,112\n"
"qr 192,2,22528\nqc 87,246\nqm a,v,v,68,252,96\nqw 5\ns_bitcmp1_b32 s82, 0\ns_cselect_b64 exec, -1, 0\n"
"global_load_dword a255, v[4:5], off\nv_lshl_add_u64 v[4:5], v[4:5], 0, s[80:81]\ns_mov_b64 exec, -1\nqm a,a,v,100,168,112\nqr 196,2,30720\nqm a,a,v,100,172,116\n"
"qx 237\nqm a,a,v,96,172,108\nqx 238\nqm a,a,v,96,168,104\nqx 239\nqm a,a,v,28,168,96\n"
"qr 200,2,23552\nqc 78,236\nqm a,a,v,28,172,100\nqr 204,2,31744\nqc 79,238\nqm a,a,v,24,172,92\n"
"qx 228\nqm a,a,v,24,168,88\nqx 229\nqm a,v,v,68,252,100\nqw 6\nv_add_u32 v2, s79, v3\n"
"qc 70,228\nqm a,a,v,32,176,88\nqx 230\nqm a,a,v,32,180,92\nqx 231\nqm a,a,v,36,180,100\n"
"qx 220\nqm a,a,v,36,176,96\nqx 221\nqm a,a,v,104,176,104\nqc 71,230\nqc 62,220\n"
"qm a,a,v,104,180,108\nqx 222\nqm a,a,v,108,180,116\nqx 223\nqm a,a,v,108,176,112\nqc 63,222\n"
"qm a,v,v,136,252,104\nqw 4\nqm a,a,v,116,184,112\nqm a,a,v,116,188,116\nqm a,a,v,112,188,108\nqm a,a,v,112,184,104\n"
"qm a,a,v,44,184,96\nqm a,a,v,44,188,100\nqm a,a,v,40,188,92\nqm a,a,v,40,184,88\nqm a,v,v,136,252,108\nqw 2\n"
"qm a,a,v,48,192,88\nqm a,a,v,48,196,92\nqm a,a,v,52,196,100\nqm a,a,v,52,192,96\nqm a,a,v,120,192,104\nqm a,a,v,120,196,108\n"
"qw 0\nqv 1\ns_barrier\nqm a,a,v,124,196,116\nqr 144,1,16384\nqm a,a,v,124,192,112\n"
"qr 148,1,17408\nqm a,v,v,140,252,112\nqr 152,1,18432\nqm a,a,v,132,200,112\nqr 156,1,19456\nqm a,a,v,132,204,116\n"
"qr 160,1,20480\nqm a,a,v,128,204,108\nqr 164,1,21504\nqm a,a,v,128,200,104\nqm a,a,v,60,200,96\nqm a,a,v,60,204,100\n"
"qm a,a,v,56,204,92\nqm a,a,v,56,200,88\nqm a,v,v,140,252,116\nqw 4\ns_branch DB0%=\nXD0%=:\n"
"v_mfma_f32_16x16x32_bf16 v[124:127], a[144:147], v[184:187], v[20:23]\ns_add_u32 s77, s85, 0x1000000\ns_mov_b32 s85, s76\nqr 168,1,6144\nqm v,a,v,124,148,192\nqw 3\n"
"s_mov_b32 m0, s13\nqr 172,1,7168\nqm v,a,v,124,152,200\ns_add_u32 s13, s13, 0x4000\ns_xor_b32 s78, s78, 0x8000\nqr 176,1,8192\n"
"qm v,a,v,124,156,208\nqe 0,56,76,0\nqz v,a,v,132,156,240\ns_xor_b32 s79, s79, 0x8000\nqr 180,1,9216\nqm v,a,v,132,152,232\n"
"qr 184,1,10240\nqm v,a,v,132,148,224\nqr 188,1,11264\nqm v,a,v,132,144,216\nqw 6\nqr 192,1,12288\n"
"qz v,a,v,148,160,216\nqe 0,56,76,1024\nqm v,a,v,148,164,224\nqw 5\nqr 196,1,13312\nqm v,a,v,148,168,232\n"
"qr 200,1,14336\nqm v,a,v,148,172,240\nqe 0,56,76,2048\nv_mfma_f32_16x16x32_bf16 v[140:143], a[172:175], v[208:211], v[24:27]\nqr 204,1,15360\nqm v,a,v,140,168,200\n"
"qx 124\nqm v,a,v,140,164,192\nqe 0,56,76,3072\nqm v,a,v,140,160,184\nqw 6\ns_and_b32 s13, s13, 0xffff\n"
"qr 144,1,16384\nv_mfma_f32_16x16x32_bf16 v[156:159], a[176:179], v[184:187], v[28:31]\ns_add_u32 s76, s76, 0x4000\nqx 125\nqm v,a,v,156,180,192\nqw 5\n"
"s_mov_b32 m0, s13\nqr 148,1,17408\nqm v,a,v,156,184,200\ns_add_u32 s13, s13, 0x4000\nqr 152,1,18432\nqc 56,124\n"
"qm v,a,v,156,188,208\nqx 126\nqz v,a,v,164,188,240\nqr 156,1,19456\nqm v,a,v,164,184,232\nqe 0,56,76,0\n"
"qm v,a,v,164,180,224\nqx 127\nqm v,a,v,164,176,216\nqw 6\nqr 160,1,20480\nqc 57,126\n"
"qz v,a,v,180,192,216\nqr 164,1,21504\nqm v,a,v,180,196,224\nqw 6\nqx 132\nqm v,a,v,180,200,232\n"
"qr 168,1,22528\nqm v,a,v,180,204,240\nqr 172,1,23552\nv_mfma_f32_16x16x32_bf16 v[172:175], a[204:207], v[208:211], v[32:35]\nqx 133\nqm v,a,v,172,200,200\n"
"qr 176,1,24576\nqc 72,132\nqm v,a,v,172,196,192\nqe 0,56,76,1024\nqm v,a,v,172,192,184\nqw 7\n"
"qr 180,1,25600\nv_mfma_f32_16x16x32_bf16 v[196:199], a[144:147], v[216:219], v[36:39]\nqx 134\nqm v,a,v,196,148,224\nqw 6\nqr 184,1,26624\n"
"qm v,a,v,196,152,232\nqr 188,1,27648\nqm v,a,v,196,156,240\nqw 6\nqx 135\nv_mfma_f32_16x16x32_bf16 v[212:215], a[160:163], v[216:219], v[40:43]\n"
"qr 192,1,28672\nqc 73,134\nqm v,a,v,212,164,224\nqw 5\nqr 196,1,29696\nqm v,a,v,212,168,232\n"
"qe 0,56,76,2048\nqm v,a,v,212,172,240\nqw 4\nqr 200,1,30720\nqx 148\nv_mfma_f32_16x16x32_bf16 v[228:231], a[176:179], v[216:219], v[44:47]\n"
"qr 204,1,31744\nqm v,a,v,228,180,224\nqw 4\nqr 144,2,16384\nqx 149\nqm v,a,v,228,184,232\n"
"qr 148,2,24576\nqc 74,148\nqm v,a,v,228,188,240\nqw 4\nqx 150\nv_mfma_f32_16x16x32_bf16 v[244:247], a[192:195], v[216:219], v[48:51]\n"
"qe 0,56,76,3072\nqm v,a,v,244,196,224\nqw 2\nqx 151\nqm v,a,v,244,200,232\ns_and_b32 s13, s13, 0xffff\n"
"qx 140\nqm v,a,v,244,204,240\nqw 0\ns_add_u32 s76, s76, 0x4000\ns_sub_u32 s76, s76, 0x10000\nqm a,a,v,0,144,88\n"
"s_cmp_lt_u32 s76, s86\ns_cselect_b32 s76, s87, s76\ns_or_b32 m0, s34, 0x10000\ns_add_u32 s34, s34, 0x4000\nqm a,a,v,0,148,92\nqe 0,56,77,0\n"
"qx 141\nqm a,a,v,4,148,100\nqr 152,2,17408\nqc 75,150\nqm a,a,v,4,144,96\nqr 156,2,25600\n"
"qx 142\nqm a,a,v,72,144,104\nqx 143\nqm a,a,v,72,148,108\nqe 0,56,77,1024\nqm a,a,v,76,148,116\n"
"qc 58,140\nqc 59,142\nqm a,a,v,76,144,112\nqx 156\nqm a,v,v,64,252,88\nqw 0\n"
"qx 157\nqm a,a,v,84,152,112\nqe 0,56,77,2048\nqm a,a,v,84,156,116\nqx 158\nqm a,a,v,80,156,108\n"
"qr 160,2,18432\nqc 60,156\nqm a,a,v,80,152,104\nqr 164,2,26624\nv_add_u32 v1, s78, v0\nqm a,a,v,12,152,96\n"
"qx 159\nqm a,a,v,12,156,100\nqx 164\nqm a,a,v,8,156,92\nqe 0,56,77,3072\nqm a,a,v,8,152,88\n"
"s_and_b32 s34, s34, 0xffff\nqx 165\nqm a,v,v,64,252,92\nqw 0\ns_add_u32 s77, s77, 0x4000\nqc 61,158\n"
"qm a,a,v,16,160,88\ns_or_b32 m0, s34, 0x10000\nqx 166\nqm a,a,v,16,164,92\ns_add_u32 s34, s34, 0x4000\nqr 168,2,19456\n"
"qc 76,164\nqm a,a,v,20,164,100\nqe 0,56,77,0\nqm a,a,v,20,160,96\nqr 172,2,27648\nqx 167\n"
"qm a,a,v,88,160,104\nqx 180\nqm a,a,v,88,164,108\nqx 181\nqm a,a,v,92,164,116\nqe 0,56,77,1024\n"
"qm a,a,v,92,160,112\nqr 176,2,20480\nqc 77,166\nqm a,v,v,68,252,96\nqw 1\nqx 182\n"
"qm a,a,v,100,168,112\nqx 183\nqm a,a,v,100,172,116\nqr 180,2,28672\nqc 78,180\nqm a,a,v,96,172,108\n"
"qe 0,56,77,2048\nqm a,a,v,96,168,104\nqx 172\nqm a,a,v,28,168,96\nqx 173\nqm a,a,v,28,172,100\n"
"qe 0,56,77,3072\nqm a,a,v,24,172,92\ns_and_b32 s34, s34, 0xffff\ns_add_u32 s77, s77, 0x4000\ns_lshr_b32 s82, s82, 1\nqm a,a,v,24,168,88\n"
"qr 184,2,21504\nqc 79,182\nqm a,v,v,68,252,100\nqw 1\nqx 174\nqm a,a,v,32,176,88\n"
"qx 175\nqm a,a,v,32,180,92\nqr 188,2,29696\nqc 62,172\nqm a,a,v,36,180,100\nqx 196\n"
"qm a,a,v,36,176,96\nqx 197\nqm a,a,v,104,176,104\nqc 63,174\nqc 80,196\nqm a,a,v,104,180,108\n"
"qx 198\nqm a,a,v,108,180,116\ns_bitcmp1_b32 s82, 0\ns_cselect_b64 exec, -1, 0\nglobal_load_dword a255, v[4:5], off\nv_lshl_add_u64 v[4:5], v[4:5], 0, s[80:81]\n"
"s_mov_b64 exec, -1\nqm a,a,v,108,176,112\nqx 199\nqm a,v,v,136,252,104\nqw 0\nqx 212\n"
"qm a,a,v,116,184,112\nqr 192,2,22528\nqc 81,198\nqm a,a,v,116,188,116\nqr 196,2,30720\nqm a,a,v,112,188,108\n"
"qx 213\nqm a,a,v,112,184,104\nqx 214\nqm a,a,v,44,184,96\ns_getpc_b64 s[72:73]\nCT6%=:\n"
"s_add_u32 s72, s72, G2GO%=-CT6%=\ns_addc_u32 s73, s73, 0\nqm a,a,v,44,188,100\ns_lshl2_add_u32 s74, s19, s15\ns_mul_i32 s74, s74, 256\nv_lshl_add_u32 v11, v0, 3, s74\n"
"qm a,a,v,40,188,92\ns_mov_b32 s74, FINISH%=-G2GO%=\ns_mov_b32 s75, 0x20000\nqr 200,2,23552\nqm a,a,v,40,184,88\nqx 215\n"
"qm a,v,v,136,252,108\nqw 1\nqr 204,2,31744\nqc 82,212\nqm a,a,v,48,192,88\nqx 228\n"
"qm a,a,v,48,196,92\nqx 229\nqm a,a,v,52,196,100\nqc 83,214\nv_add_u32 v2, s79, v3\nqm a,a,v,52,192,96\n"
"qx 230\nqm a,a,v,120,192,104\nqx 231\nqm a,a,v,120,196,108\ns_bfm_b64 exec, 2, 0\nbuffer_load_dword a255, v11, s[72:75], 0 offen\n"
"s_mov_b64 exec, -1\nqm a,a,v,124,196,116\nqw 0\nqv 2\ns_barrier\nqm a,a,v,124,192,112\n"
"qr 144,1,0\nqc 84,228\nqm a,v,v,140,252,112\nqx 244\nqm a,a,v,132,200,112\nqx 245\n"
"qm a,a,v,132,204,116\nqr 148,1,1024\nqc 85,230\nqm a,a,v,128,204,108\nqx 246\nqm a,a,v,128,200,104\n"
"qr 152,1,2048\nqc 86,244\nqm a,a,v,60,200,96\nqr 156,1,3072\nqm a,a,v,60,204,100\nqx 247\n"
"qm a,a,v,56,204,92\nqr 160,1,4096\nqc 87,246\nqm a,a,v,56,200,88\nqr 164,1,5120\nqm a,v,v,140,252,116\n"
"qw 4\nED0%=:\ns_lshl_b32 s6, s88, 17\ns_lshl_b32 s7, s20, 12\ns_add_u32 s6, s6, s7\ns_add_u32 s6, s6, 0x2800000\n"
"s_xor_b32 s7, s6, 0xe0000\nqa 6,64\nqa 7,68\nqp 6\nqp 7\ns_lshr_b32 s8, s88, 3\n"
"s_lshl2_add_u32 s8, s8, s15\ns_lshl_b32 s8, s8, 8\ns_lshl3_add_u32 s8, s20, s8\ns_add_u32 s8, s8, 0x5000\nqa 120,0\nqa 121,1\n"
"qa 122,2\nqa 123,3\nqa 128,8\nqa 129,9\nqa 130,10\nqa 131,11\n"
"v_pk_mul_f32 v[120:121], v[120:121], v[6:7] op_sel:[0,0] op_sel_hi:[1,0]\nv_pk_mul_f32 v[122:123], v[122:123], v[6:7] op_sel:[0,0] op_sel_hi:[1,0]\nv_pk_mul_f32 v[128:129], v[128:129], v[6:7] op_sel:[0,0] op_sel_hi:[1,0]\nv_pk_mul_f32 v[130:131], v[130:131], v[6:7] op_sel:[0,0] op_sel_hi:[1,0]\nqc 120,120\nqc 121,122\n"
"qc 122,128\nqc 123,130\nbuffer_store_dwordx4 v[120:123], v0, s[56:59], s6 offen offset:0 sc1\nqa 136,4\nqa 137,5\nqa 138,6\n"
"qa 139,7\nqa 144,12\nqa 145,13\nqa 146,14\nqa 147,15\nv_pk_mul_f32 v[136:137], v[136:137], v[6:7] op_sel:[0,1] op_sel_hi:[1,1]\n"
"v_pk_mul_f32 v[138:139], v[138:139], v[6:7] op_sel:[0,1] op_sel_hi:[1,1]\nv_pk_mul_f32 v[144:145], v[144:145], v[6:7] op_sel:[0,1] op_sel_hi:[1,1]\nv_pk_mul_f32 v[146:147], v[146:147], v[6:7] op_sel:[0,1] op_sel_hi:[1,1]\nqc 136,136\nqc 137,138\nqc 138,144\n"
"qc 139,146\nbuffer_store_dwordx4 v[136:139], v0, s[56:59], s7 offen offset:0 sc1\nqa 152,16\nqa 153,17\nqa 154,18\nqa 155,19\n"
"qa 160,24\nqa 161,25\nqa 162,26\nqa 163,27\nv_pk_mul_f32 v[152:153], v[152:153], v[6:7] op_sel:[0,0] op_sel_hi:[1,0]\nv_pk_mul_f32 v[154:155], v[154:155], v[6:7] op_sel:[0,0] op_sel_hi:[1,0]\n"
"v_pk_mul_f32 v[160:161], v[160:161], v[6:7] op_sel:[0,0] op_sel_hi:[1,0]\nv_pk_mul_f32 v[162:163], v[162:163], v[6:7] op_sel:[0,0] op_sel_hi:[1,0]\nqc 152,152\nqc 153,154\nqc 154,160\nqc 155,162\n"
"buffer_store_dwordx4 v[152:155], v0, s[56:59], s6 offen offset:1024 sc1\nqa 168,20\nqa 169,21\nqa 170,22\nqa 171,23\nqa 176,28\n"
"qa 177,29\nqa 178,30\nqa 179,31\nv_pk_mul_f32 v[168:169], v[168:169], v[6:7] op_sel:[0,1] op_sel_hi:[1,1]\nv_pk_mul_f32 v[170:171], v[170:171], v[6:7] op_sel:[0,1] op_sel_hi:[1,1]\nv_pk_mul_f32 v[176:177], v[176:177], v[6:7] op_sel:[0,1] op_sel_hi:[1,1]\n"
"v_pk_mul_f32 v[178:179], v[178:179], v[6:7] op_sel:[0,1] op_sel_hi:[1,1]\nqc 168,168\nqc 169,170\nqc 170,176\nqc 171,178\nbuffer_store_dwordx4 v[168:171], v0, s[56:59], s7 offen offset:1024 sc1\n"
"qa 120,32\nqa 121,33\nqa 122,34\nqa 123,35\nqa 128,40\nqa 129,41\n"
"qa 130,42\nqa 131,43\nv_pk_mul_f32 v[120:121], v[120:121], v[6:7] op_sel:[0,0] op_sel_hi:[1,0]\nv_pk_mul_f32 v[122:123], v[122:123], v[6:7] op_sel:[0,0] op_sel_hi:[1,0]\nv_pk_mul_f32 v[128:129], v[128:129], v[6:7] op_sel:[0,0] op_sel_hi:[1,0]\nv_pk_mul_f32 v[130:131], v[130:131], v[6:7] op_sel:[0,0] op_sel_hi:[1,0]\n"
"qc 120,120\nqc 121,122\nqc 122,128\nqc 123,130\nbuffer_store_dwordx4 v[120:123], v0, s[56:59], s6 offen offset:2048 sc1\nqa 136,36\n"
"qa 137,37\nqa 138,38\nqa 139,39\nqa 144,44\nqa 145,45\nqa 146,46\n"
"qa 147,47\nv_pk_mul_f32 v[136:137], v[136:137], v[6:7] op_sel:[0,1] op_sel_hi:[1,1]\nv_pk_mul_f32 v[138:139], v[138:139], v[6:7] op_sel:[0,1] op_sel_hi:[1,1]\nv_pk_mul_f32 v[144:145], v[144:145], v[6:7] op_sel:[0,1] op_sel_hi:[1,1]\nv_pk_mul_f32 v[146:147], v[146:147], v[6:7] op_sel:[0,1] op_sel_hi:[1,1]\nqc 136,136\n"
"qc 137,138\nqc 138,144\nqc 139,146\nbuffer_store_dwordx4 v[136:139], v0, s[56:59], s7 offen offset:2048 sc1\nqa 152,48\nqa 153,49\n"
"qa 154,50\nqa 155,51\nqa 160,56\nqa 161,57\nqa 162,58\nqa 163,59\n"
"v_pk_mul_f32 v[152:153], v[152:153], v[6:7] op_sel:[0,0] op_sel_hi:[1,0]\nv_pk_mul_f32 v[154:155], v[154:155], v[6:7] op_sel:[0,0] op_sel_hi:[1,0]\nv_pk_mul_f32 v[160:161], v[160:161], v[6:7] op_sel:[0,0] op_sel_hi:[1,0]\nv_pk_mul_f32 v[162:163], v[162:163], v[6:7] op_sel:[0,0] op_sel_hi:[1,0]\nqc 152,152\nqc 153,154\n"
"qc 154,160\nqc 155,162\nbuffer_store_dwordx4 v[152:155], v0, s[56:59], s6 offen offset:3072 sc1\nqa 168,52\nqa 169,53\nqa 170,54\n"
"qa 171,55\nqa 176,60\nqa 177,61\nqa 178,62\nqa 179,63\nv_pk_mul_f32 v[168:169], v[168:169], v[6:7] op_sel:[0,1] op_sel_hi:[1,1]\n"
"v_pk_mul_f32 v[170:171], v[170:171], v[6:7] op_sel:[0,1] op_sel_hi:[1,1]\nv_pk_mul_f32 v[176:177], v[176:177], v[6:7] op_sel:[0,1] op_sel_hi:[1,1]\nv_pk_mul_f32 v[178:179], v[178:179], v[6:7] op_sel:[0,1] op_sel_hi:[1,1]\nqc 168,168\nqc 169,170\nqc 170,176\n"
"qc 171,178\nbuffer_store_dwordx4 v[168:171], v0, s[56:59], s7 offen offset:3072 sc1\nqz v,a,v,124,144,184\ns_add_u32 s77, s85, 0x1000000\ns_mov_b32 s85, s76\ns_mov_b32 m0, s13\n"
"s_add_u32 s13, s13, 0x4000\nqm v,a,v,124,148,192\nqw 2\nqr 168,1,6144\nqm v,a,v,124,152,200\ns_xor_b32 s78, s78, 0x8000\n"
"s_xor_b32 s79, s79, 0x8000\nqr 172,1,7168\nqm v,a,v,124,156,208\nqe 0,56,76,0\nqz v,a,v,132,156,240\nqr 176,1,8192\n"
"qm v,a,v,132,152,232\nqr 180,1,9216\nqm v,a,v,132,148,224\nqe 0,56,76,1024\nqm v,a,v,132,144,216\nqw 4\n"
"qr 184,1,10240\nqz v,a,v,148,160,216\nqx 124\nqx 125\nqx 126\nqx 127\n"
"qm v,a,v,148,164,224\nqw 3\nqr 188,1,11264\nqc 88,124\nqm v,a,v,148,168,232\nqr 192,1,12288\n"
"qc 89,126\nqm v,a,v,148,172,240\nqe 0,56,76,2048\nqz v,a,v,140,172,208\nqx 132\nqx 133\n"
"qx 134\nqx 135\nqm v,a,v,140,168,200\nqr 196,1,13312\nqc 104,132\nqm v,a,v,140,164,192\n"
"qe 0,56,76,3072\nqm v,a,v,140,160,184\nqw 4\ns_and_b32 s13, s13, 0xffff\ns_add_u32 s76, s76, 0x4000\nqz v,a,v,156,176,184\n"
"qx 148\nqx 149\nqx 150\nqx 151\nqm v,a,v,156,180,192\nqw 2\n"
"s_mov_b32 m0, s13\ns_add_u32 s13, s13, 0x4000\nqm v,a,v,156,184,200\nqr 200,1,14336\nqc 105,134\nqm v,a,v,156,188,208\n"
"qr 204,1,15360\nqc 106,148\nqz v,a,v,164,188,240\nqx 140\nqx 141\nqx 142\n"
"qx 143\nqm v,a,v,164,184,232\nqr 144,1,16384\nqc 107,150\nqm v,a,v,164,180,224\nqe 0,56,76,0\n"
"qm v,a,v,164,176,216\nqw 3\nqc 90,140\nqc 91,142\nqz v,a,v,180,192,216\nqv 5\n"
"qx 156\nqx 157\nqm v,a,v,180,196,224\nqw 1\nqx 158\nqm v,a,v,180,200,232\n"
"qr 148,1,17408\nqc 92,156\nqm v,a,v,180,204,240\nqr 152,1,18432\nqx 159\nqz v,a,v,172,204,208\n"
"qr 156,1,19456\nqc 93,158\nqm v,a,v,172,200,200\nqx 164\nqm v,a,v,172,196,192\ns_mov_b64 exec, 1\n"
"buffer_store_dwordx2 v[250:251], off, s[56:59], s8 sc1\ns_mov_b64 exec, -1\nqm v,a,v,172,192,184\nqw 2\nqx 165\nqz v,a,v,188,144,184\n"
"qr 160,1,20480\nqc 108,164\nqm v,a,v,188,148,192\nqw 1\nqx 166\nqm v,a,v,188,152,200\n"
"qr 164,1,21504\nqm v,a,v,188,156,208\nqe 0,56,76,1024\nqr 168,1,22528\nqx 167\nqz v,a,v,196,156,240\n"
"qr 172,1,23552\nqc 109,166\nqm v,a,v,196,152,232\nqx 180\nqm v,a,v,196,148,224\nqx 181\n"
"qm v,a,v,196,144,216\nqw 2\nqr 176,1,24576\nqc 110,180\nqz v,a,v,212,160,216\nqe 0,56,76,2048\n"
"qm v,a,v,212,164,224\nqw 1\nqx 182\nqm v,a,v,212,168,232\nqr 180,1,25600\nqm v,a,v,212,172,240\n"
"qe 0,56,76,3072\ns_and_b32 s13, s13, 0xffff\ns_add_u32 s76, s76, 0x4000\ns_sub_u32 s76, s76, 0x10000\ns_cmp_lt_u32 s76, s86\ns_cselect_b32 s76, s87, s76\n"
"s_or_b32 m0, s34, 0x10000\ns_add_u32 s34, s34, 0x4000\nqr 184,1,26624\nqx 183\nqz v,a,v,204,172,208\nqr 188,1,27648\n"
"qc 111,182\nqm v,a,v,204,168,200\nqx 172\nqm v,a,v,204,164,192\nqx 173\nqm v,a,v,204,160,184\n"
"qw 2\nqr 192,1,28672\nqc 94,172\nqz v,a,v,220,176,184\nqx 174\nqm v,a,v,220,180,192\n"
"qw 1\nqr 196,1,29696\nqm v,a,v,220,184,200\nqe 0,56,77,0\nqm v,a,v,220,188,208\nqr 200,1,30720\n"
"qx 175\nqz v,a,v,228,188,240\nqr 204,1,31744\nqc 95,174\nqm v,a,v,228,184,232\nqx 188\n"
"qm v,a,v,228,180,224\nqe 0,56,77,1024\nqm v,a,v,228,176,216\nqw 2\nqx 189\nqz v,a,v,244,192,216\n"
"qr 144,2,0\nqc 96,188\nqm v,a,v,244,196,224\nqw 1\nqx 190\nqm v,a,v,244,200,232\n"
"qr 148,2,8192\nv_add_u32 v1, s78, v0\nqm v,a,v,244,204,240\nqx 191\nqz v,a,v,236,204,208\nqe 0,56,77,2048\n"
"qm v,a,v,236,200,200\nqr 152,2,16384\nqx 196\nqm v,a,v,236,196,192\nqr 156,2,24576\nqc 97,190\n"
"qm v,a,v,236,192,184\nqe 0,56,77,3072\ns_and_b32 s34, s34, 0xffff\ns_add_u32 s77, s77, 0x4000\ns_or_b32 m0, s34, 0x10000\ns_add_u32 s34, s34, 0x4000\n"
"qx 197\nqw 2\nqm a,a,v,72,144,56\nqr 160,2,1024\nqc 112,196\nqm a,a,v,72,148,60\n"
"qr 164,2,9216\nqx 198\nqm a,a,v,76,148,76\nqx 199\nqm a,a,v,76,144,72\nqw 2\n"
"qr 168,2,17408\nqc 113,198\nqm a,a,v,76,152,80\nqr 172,2,25600\nqm a,a,v,76,156,84\nqx 212\n"
"qm a,v,v,136,252,56\nqw 2\nqe 0,56,77,0\nqm a,a,v,80,160,56\nqr 176,2,2048\nqx 213\n"
"qm a,a,v,80,164,60\nqr 180,2,10240\nqc 114,212\nqm a,a,v,84,164,76\nqx 214\nqm a,a,v,84,160,72\n"
"qw 2\nqx 215\nqm a,a,v,84,168,80\nqe 0,56,77,1024\nqr 184,2,18432\nqm a,a,v,84,172,84\n"
"qr 188,2,26624\nqc 115,214\nqm a,v,v,136,252,60\nqw 2\nqr 192,2,3072\nqx 204\n"
"qm a,a,v,88,176,56\nqr 196,2,11264\nqm a,a,v,88,180,60\nqx 205\nqm a,a,v,92,180,76\nqe 0,56,77,2048\n"
"qm a,a,v,92,176,72\nqw 2\nqr 200,2,19456\nqx 206\nqm a,a,v,92,184,80\nqr 204,2,27648\n"
"qc 98,204\nqm a,a,v,92,188,84\nqe 0,56,77,3072\ns_and_b32 s34, s34, 0xffff\ns_add_u32 s77, s77, 0x4000\ns_lshr_b32 s82, s82, 1\n"
"qx 207\nqr 144,2,4096\nqw 3\nqm a,a,v,96,192,56\nqr 148,2,12288\nqc 99,206\n"
"qm a,a,v,96,196,60\nqx 220\nqm a,a,v,100,196,76\nqx 221\nqm a,a,v,100,192,72\nqw 2\n"
"qr 152,2,20480\nqc 100,220\nqm a,a,v,100,200,80\nqr 156,2,28672\nqm a,a,v,100,204,84\nqw 2\n"
"qx 222\nqm a,a,v,104,144,56\ns_bitcmp1_b32 s82, 0\ns_cselect_b64 exec, -1, 0\nglobal_load_dword a255, v[4:5], off\nv_lshl_add_u64 v[4:5], v[4:5], 0, s[80:81]\n"
"s_mov_b64 exec, -1\nqr 160,2,5120\nqm a,a,v,104,148,60\nqr 164,2,13312\nqx 223\nqm a,a,v,108,148,76\n"
"qx 228\nqm a,a,v,108,144,72\nqw 2\nqr 168,2,21504\nqc 101,222\nqm a,a,v,108,152,80\n"
"qx 229\nqm a,a,v,108,156,84\nqr 172,2,29696\nqc 116,228\nqm a,v,v,140,252,72\nqw 2\n"
"qx 230\nqm a,a,v,112,160,56\nqr 176,2,6144\nqm a,a,v,112,164,60\nqr 180,2,14336\nqm a,a,v,116,164,76\n"
"qr 184,2,22528\nqr 188,2,30720\nqr 192,2,7168\nqr 196,2,15360\nqr 200,2,23552\nqr 204,2,31744\n"
"qx 231\nqm a,a,v,116,160,72\nqw 8\nqx 244\nqm a,a,v,116,168,80\nqx 245\n"
"qm a,a,v,116,172,84\nqc 117,230\nv_add_u32 v2, s79, v3\nqm a,v,v,140,252,76\nqw 6\nqx 246\n"
"qm a,a,v,120,176,56\nqx 247\nqm a,a,v,120,180,60\nqw 0\nqv 1\ns_barrier\n"
"qm a,a,v,124,180,76\nqr 144,1,0\nqc 118,244\nqm a,a,v,124,176,72\nqx 236\nqm a,a,v,124,184,80\n"
"qx 237\nqm a,a,v,124,188,84\nqr 148,1,1024\nqc 119,246\nqm a,v,v,140,252,80\nqx 238\n"
"qm a,a,v,128,192,56\nqr 152,1,2048\nqc 102,236\nqm a,a,v,128,196,60\nqr 156,1,3072\nqm a,a,v,132,196,76\n"
"qx 239\nqm a,a,v,132,192,72\nqr 160,1,4096\nqc 103,238\nqm a,a,v,132,200,80\nqr 164,1,5120\n"
"qm a,a,v,132,204,84\nqm a,v,v,140,252,84\nqw 4\ns_sub_u32 s84, s84, 1\ns_branch CHK%=\nTD1%=:\n"
"qz v,a,v,124,144,120\ns_add_u32 s77, s85, 0x1000000\nqb 72,0\nqb 73,0\nqm v,a,v,124,148,128\nqw 2\n"
"qr 168,1,6144\nqb 74,0\nqm v,a,v,124,152,136\ns_mov_b32 s85, s76\nqr 172,1,7168\nqb 75,0\n"
"qm v,a,v,124,156,144\ns_mov_b32 m0, s13\ns_add_u32 s13, s13, 0x4000\nqb 76,0\nqz v,a,v,132,156,176\nqe 0,56,76,0\n"
"qm v,a,v,132,152,168\ns_xor_b32 s79, s79, 0x8000\nqb 77,0\nqb 78,0\nqm v,a,v,132,148,160\nqb 79,0\n"
"qb 80,0\nqm v,a,v,132,144,152\nqb 81,0\nqb 82,0\nqz v,a,v,140,144,184\nqx 124\n"
"qm v,a,v,140,148,192\nqx 125\nqm v,a,v,140,152,200\nqx 126\nqm v,a,v,140,156,208\nqx 127\n"
"qz v,a,v,148,156,240\nqc 88,124\nqc 89,126\nqm v,a,v,148,152,232\nqx 132\nqm v,a,v,148,148,224\n"
"qx 133\nqm v,a,v,148,144,216\nqw 2\nqx 134\nqz v,a,v,180,160,216\nqx 135\n"
"qm v,a,v,180,164,224\nqw 0\nqr 176,1,8192\nqc 96,132\nqm v,a,v,180,168,232\nqx 140\n"
"qm v,a,v,180,172,240\nqe 0,56,76,1024\nqz v,a,v,172,172,208\nqx 141\nqm v,a,v,172,168,200\nqr 180,1,9216\n"
"qb 83,0\nqm v,a,v,172,164,192\nqr 184,1,10240\nqc 97,134\nqm v,a,v,172,160,184\nqx 142\n"
"qz v,a,v,164,160,152\nqr 188,1,11264\nqb 84,0\nqm v,a,v,164,164,160\nqx 143\nqm v,a,v,164,168,168\n"
"qb 85,0\nqb 86,0\nqm v,a,v,164,172,176\nqc 104,140\nqb 87,0\nqz v,a,v,156,172,144\n"
"qx 148\nqm v,a,v,156,168,136\nqx 149\nqm v,a,v,156,164,128\nqe 0,56,76,2048\nqm v,a,v,156,160,120\n"
"qw 2\nqb 88,0\nqc 105,142\nqz v,a,v,188,176,120\nqx 150\nqm v,a,v,188,180,128\n"
"qw 0\nqb 89,0\nqb 90,0\nqm v,a,v,188,184,136\nqx 151\nqm v,a,v,188,188,144\n"
"qc 112,148\nqb 91,0\nqz v,a,v,196,188,176\nqx 180\nqm v,a,v,196,184,168\nqr 192,1,12288\n"
"qb 92,0\nqm v,a,v,196,180,160\nqx 181\nqm v,a,v,196,176,152\nqr 196,1,13312\nqb 93,0\n"
"qz v,a,v,204,176,184\nqr 200,1,14336\nqc 113,150\nqm v,a,v,204,180,192\nqx 182\nqm v,a,v,204,184,200\n"
"qr 204,1,15360\nqb 94,0\nqm v,a,v,204,188,208\nqe 0,56,76,3072\nqz v,a,v,212,188,240\ns_and_b32 s13, s13, 0xffff\n"
"qx 183\nqm v,a,v,212,184,232\ns_add_u32 s76, s76, 0x4000\nqb 95,0\nqc 114,180\nqm v,a,v,212,180,224\n"
"s_or_b32 m0, s34, 0x10000\nqx 172\nqm v,a,v,212,176,216\nqw 2\nqb 96,0\nqb 97,0\n"
"qz v,a,v,244,192,216\ns_add_u32 s34, s34, 0x4000\nqx 173\nqm v,a,v,244,196,224\nqw 0\nqc 115,182\n"
"qb 98,0\nqm v,a,v,244,200,232\nqx 174\nqm v,a,v,244,204,240\nqb 99,0\nqb 100,0\n"
"qz v,a,v,236,204,208\nqx 175\nqm v,a,v,236,200,200\nqe 0,56,77,0\nqm v,a,v,236,196,192\nqc 106,172\n"
"qb 101,0\nqm v,a,v,236,192,184\nqx 164\nqz v,a,v,228,192,152\nqr 144,2,0\nqb 102,0\n"
"qm v,a,v,228,196,160\nqx 165\nqm v,a,v,228,200,168\nqr 148,2,8192\nqc 107,174\nqm v,a,v,228,204,176\n"
"qr 152,2,16384\nqx 166\nqz v,a,v,220,204,144\nqr 156,2,24576\nqb 103,0\nqm v,a,v,220,200,136\n"
"qx 167\nqm v,a,v,220,196,128\nqb 104,0\nqb 105,0\nqm v,a,v,220,192,120\nqw 2\n"
"qc 98,164\nqx 156\nqm a,a,v,0,144,56\nqe 0,56,77,1024\nqm a,a,v,0,148,60\nqw 0\n"
"qb 106,0\nqx 157\nqm a,a,v,0,152,64\nqr 160,2,1024\nqb 107,0\nqm a,a,v,0,156,68\n"
"qr 164,2,9216\nqc 99,166\nqx 158\nqm a,a,v,4,156,84\nqr 168,2,17408\nqb 108,0\n"
"qm a,a,v,4,152,80\nqr 172,2,25600\nqx 159\nqm a,a,v,4,148,76\nqb 109,0\nqc 90,156\n"
"qm a,a,v,4,144,72\nqx 188\nqm a,v,v,64,252,56\nqw 2\nqb 110,0\nqb 111,0\n"
"qm a,a,v,12,160,72\nqx 189\nqm a,a,v,12,164,76\nqw 0\nqe 0,56,77,2048\nqm a,a,v,12,168,80\n"
"qr 176,2,2048\nqb 112,0\nqc 91,158\nqx 190\nqm a,a,v,12,172,84\nqr 180,2,10240\n"
"qb 113,0\nqm a,a,v,8,172,68\nqr 184,2,18432\nqx 191\nqm a,a,v,8,168,64\nqr 188,2,26624\n"
"qb 114,0\nqm a,a,v,8,164,60\nqc 92,188\nqx 196\nqm a,a,v,8,160,56\nqb 115,0\n"
"qb 116,0\nqm a,v,v,64,252,60\nqw 2\nqx 197\nqm a,a,v,16,176,56\nqe 0,56,77,3072\n"
"qm a,a,v,16,180,60\ns_and_b32 s34, s34, 0xffff\ns_add_u32 s77, s77, 0x4000\ns_lshr_b32 s82, s82, 1\nqw 0\nqc 93,190\n"
"qx 198\nqm a,a,v,16,184,64\nqr 192,2,3072\nqb 117,0\nqm a,a,v,16,188,68\nqr 196,2,11264\n"
"qb 118,0\nqx 199\nqm a,a,v,20,188,84\nqr 200,2,19456\nqb 119,0\nqm a,a,v,20,184,80\n"
"qr 204,2,27648\nqc 100,196\nqx 204\nqm a,a,v,20,180,76\nqb 120,0\nqb 121,0\n"
"qm a,a,v,20,176,72\nqx 205\nqm a,v,v,64,252,64\nqw 2\ns_bitcmp1_b32 s82, 0\ns_cselect_b64 exec, -1, 0\n"
"global_load_dword a255, v[4:5], off\nv_lshl_add_u64 v[4:5], v[4:5], 0, s[80:81]\ns_mov_b64 exec, -1\nqm a,a,v,28,192,72\nqc 101,198\nqx 206\n"
"qm a,a,v,28,196,76\nqw 0\nqr 144,2,4096\nqb 122,0\nqm a,a,v,28,200,80\nqx 207\n"
"qm a,a,v,28,204,84\nqr 148,2,12288\nqb 123,0\nqm a,a,v,24,204,68\nqr 152,2,20480\nqc 108,204\n"
"qx 212\nqm a,a,v,24,200,64\nqr 156,2,28672\nqb 124,0\nqm a,a,v,24,196,60\nqb 125,0\n"
"qx 213\nqm a,a,v,24,192,56\nqb 126,0\nqc 109,206\nqm a,v,v,64,252,68\nqw 2\n"
"qx 214\nqm a,a,v,32,144,56\nqb 127,0\nqb 128,0\nqm a,a,v,32,148,60\nqw 0\n"
"qx 215\nqm a,a,v,32,152,64\nqr 160,2,5120\nqc 116,212\nqm a,a,v,32,156,68\nqr 164,2,13312\n"
"qx 244\nqm a,a,v,36,156,84\nqr 168,2,21504\nqb 129,0\nqm a,a,v,36,152,80\nqr 172,2,29696\n"
"qx 245\nqm a,a,v,36,148,76\nqb 130,0\nqb 131,0\nqm a,a,v,36,144,72\nqc 117,214\n"
"qx 246\nqm a,v,v,68,252,72\nqw 2\nqb 132,0\nqb 133,0\nqm a,a,v,44,160,72\n"
"qx 247\nqm a,a,v,44,164,76\nqw 0\nqr 176,2,6144\nqc 118,244\nqm a,a,v,44,168,80\n"
"qx 236\nqm a,a,v,44,172,84\nqr 180,2,14336\nqb 134,0\nqm a,a,v,40,172,68\nqr 184,2,22528\n"
"qx 237\nqm a,a,v,40,168,64\nqr 188,2,30720\nqb 135,0\nqm a,a,v,40,164,60\nqc 119,246\n"
"qx 238\nqm a,a,v,40,160,56\nqr 192,2,7168\nqb 136,0\nqm a,v,v,68,252,76\nqw 3\n"
"qr 196,2,15360\nqr 200,2,23552\nqr 204,2,31744\nqx 239\nqm a,a,v,48,176,56\nqb 137,0\n"
"qb 138,0\nqm a,a,v,48,180,60\nqw 4\nqc 110,236\nqx 228\nqm a,a,v,48,184,64\n"
"qb 139,0\nqb 140,0\nqm a,a,v,48,188,68\nqx 229\nqm a,a,v,52,188,84\nqc 111,238\n"
"v_add_u32 v2, s79, v3\nqm a,a,v,52,184,80\nqx 230\nqm a,a,v,52,180,76\nqw 0\nqv 1\n"
"s_barrier\nqb 141,0\nqm a,a,v,52,176,72\nqx 231\nqm a,v,v,68,252,80\nqb 142,0\n"
"qc 102,228\nqm a,a,v,60,192,72\nqx 220\nqm a,a,v,60,196,76\nqr 144,1,16384\nqb 143,0\n"
"qm a,a,v,60,200,80\nqx 221\nqm a,a,v,60,204,84\nqr 148,1,17408\nqc 103,230\nqm a,a,v,56,204,68\n"
"qr 152,1,18432\nqx 222\nqm a,a,v,56,200,64\nqr 156,1,19456\nqc 94,220\nqm a,a,v,56,196,60\n"
"qr 160,1,20480\nqx 223\nqm a,a,v,56,192,56\nqr 164,1,21504\nqc 95,222\nqm a,v,v,68,252,84\n"
"qw 4\n.p2align 3\nDB1%=:\nqz v,a,v,124,144,120\ns_mov_b32 m0, s13\ns_add_u32 s13, s13, 0x4000\n"
"qr 168,1,22528\nqm v,a,v,124,148,128\nqw 3\nqr 172,1,23552\nqm v,a,v,124,152,136\nqe 0,56,76,0\n"
"qm v,a,v,124,156,144\nqe 0,56,76,1024\nqz v,a,v,132,156,176\ns_xor_b32 s78, s78, 0x8000\nqr 176,1,24576\nqm v,a,v,132,152,168\n"
"qr 180,1,25600\nqm v,a,v,132,148,160\nqr 184,1,26624\nqm v,a,v,132,144,152\nqr 188,1,27648\nqz v,a,v,140,144,184\n"
"qx 124\nqm v,a,v,140,148,192\nqx 125\nqm v,a,v,140,152,200\nqx 126\nqm v,a,v,140,156,208\n"
"qx 127\nqz v,a,v,148,156,240\nqc 56,124\nqc 57,126\nqm v,a,v,148,152,232\nqx 132\n"
"qm v,a,v,148,148,224\nqx 133\nqm v,a,v,148,144,216\nqw 6\nqx 134\nqz v,a,v,180,160,216\n"
"qx 135\nqm v,a,v,180,164,224\nqw 4\nqc 64,132\nqc 65,134\nqm v,a,v,180,168,232\n"
"qx 140\nqm v,a,v,180,172,240\nqx 141\nqz v,a,v,172,172,208\nqx 142\nqm v,a,v,172,168,200\n"
"qx 143\nqm v,a,v,172,164,192\nqc 72,140\nqc 73,142\nqm v,a,v,172,160,184\nqx 148\n"
"qz v,a,v,164,160,152\nqx 149\nqm v,a,v,164,164,160\nqe 0,56,76,2048\nqm v,a,v,164,168,168\nqx 150\n"
"qm v,a,v,164,172,176\nqx 151\nqz v,a,v,156,172,144\nqr 192,1,28672\nqc 80,148\nqm v,a,v,156,168,136\n"
"qx 180\nqm v,a,v,156,164,128\nqx 181\nqm v,a,v,156,160,120\nqw 3\nqr 196,1,29696\n"
"qc 81,150\nqz v,a,v,188,176,120\nqr 200,1,30720\nqc 82,180\nqm v,a,v,188,180,128\nqw 3\n"
"qx 182\nqm v,a,v,188,184,136\nqr 204,1,31744\nqm v,a,v,188,188,144\nqe 0,56,76,3072\nqz v,a,v,196,188,176\n"
"s_and_b32 s13, s13, 0xffff\nqx 183\nqm v,a,v,196,184,168\ns_add_u32 s76, s76, 0x4000\nqx 172\nqm v,a,v,196,180,160\n"
"s_sub_u32 s76, s76, 0x10000\nqx 173\nqm v,a,v,196,176,152\ns_cmp_lt_u32 s76, s86\ns_cselect_b32 s76, s87, s76\nqc 83,182\n"
"qz v,a,v,204,176,184\ns_or_b32 m0, s34, 0x10000\nqx 174\nqm v,a,v,204,180,192\ns_add_u32 s34, s34, 0x4000\nqx 175\n"
"qm v,a,v,204,184,200\nqe 0,56,77,0\nqm v,a,v,204,188,208\nqr 144,2,0\nqc 74,172\nqz v,a,v,212,188,240\n"
"qx 164\nqm v,a,v,212,184,232\nqx 165\nqm v,a,v,212,180,224\nqr 148,2,8192\nqc 75,174\n"
"qm v,a,v,212,176,216\nqw 4\nqx 166\nqz v,a,v,244,192,216\nqx 167\nqm v,a,v,244,196,224\n"
"qw 2\nqr 152,2,1024\nqc 66,164\nqm v,a,v,244,200,232\nqx 156\nqm v,a,v,244,204,240\n"
"qe 0,56,77,1024\nqz v,a,v,236,204,208\nqx 157\nqm v,a,v,236,200,200\nqr 156,2,9216\nqc 67,166\n"
"qm v,a,v,236,196,192\nqx 158\nqm v,a,v,236,192,184\nqx 159\nqz v,a,v,228,192,152\nqc 58,156\n"
"qc 59,158\nqm v,a,v,228,196,160\nqx 188\nqm v,a,v,228,200,168\nqx 189\nqm v,a,v,228,204,176\n"
"qe 0,56,77,2048\nqz v,a,v,220,204,144\nqr 160,2,2048\nqc 60,188\nqm v,a,v,220,200,136\nqx 190\n"
"qm v,a,v,220,196,128\nqr 164,2,10240\nv_add_u32 v1, s78, v0\nqm v,a,v,220,192,120\nqw 4\nqx 191\n"
"qm a,a,v,0,144,88\nqx 196\nqm a,a,v,0,148,92\nqx 197\nqm a,a,v,4,148,100\nqe 0,56,77,3072\n"
"qm a,a,v,4,144,96\ns_and_b32 s34, s34, 0xffff\ns_add_u32 s77, s77, 0x4000\ns_lshr_b32 s82, s82, 1\nqm a,a,v,72,144,104\nqr 168,2,3072\n"
"qc 61,190\nqm a,a,v,72,148,108\nqx 198\nqm a,a,v,76,148,116\nqr 172,2,11264\nqc 68,196\n"
"qm a,a,v,76,144,112\nqx 199\nqm a,v,v,64,252,88\nqw 4\nqx 204\nqm a,a,v,84,152,112\n"
"qx 205\nqm a,a,v,84,156,116\nqr 176,2,4096\nqc 69,198\nqm a,a,v,80,156,108\nqx 206\n"
"qm a,a,v,80,152,104\nqx 207\nqm a,a,v,12,152,96\nqr 180,2,12288\nqc 76,204\nqm a,a,v,12,156,100\n"
"qx 212\nqm a,a,v,8,156,92\nqx 213\nqm a,a,v,8,152,88\nqr 184,2,5120\nqc 77,206\n"
"qm a,v,v,64,252,92\nqw 5\nqx 214\nqm a,a,v,16,160,88\nqx 215\nqm a,a,v,16,164,92\n"
"qr 188,2,13312\nqc 84,212\nqm a,a,v,20,164,100\nqx 244\nqm a,a,v,20,160,96\nqx 245\n"
"qm a,a,v,88,160,104\nqc 85,214\nqc 86,244\nqm a,a,v,88,164,108\nqx 246\nqm a,a,v,92,164,116\n"
"qx 247\nqm a,a,v,92,160,112\nqx 236\nqm a,v,v,68,252,96\nqw 4\ns_bitcmp1_b32 s82, 0\n"
"s_cselect_b64 exec, -1, 0\nglobal_load_dword a255, v[4:5], off\nv_lshl_add_u64 v[4:5], v[4:5], 0, s[80:81]\ns_mov_b64 exec, -1\nqm a,a,v,100,168,112\nqr 192,2,6144\n"
"qc 87,246\nqm a,a,v,100,172,116\nqr 196,2,14336\nqm a,a,v,96,172,108\nqx 237\nqm a,a,v,96,168,104\n"
"qx 238\nqm a,a,v,28,168,96\nqx 239\nqm a,a,v,28,172,100\nqr 200,2,7168\nqc 78,236\n"
"qm a,a,v,24,172,92\nqr 204,2,15360\nqc 79,238\nqm a,a,v,24,168,88\nqx 228\nqm a,v,v,68,252,100\n"
"qw 6\nqx 229\nqm a,a,v,32,176,88\nqx 230\nqm a,a,v,32,180,92\nqx 231\n"
"qm a,a,v,36,180,100\nqc 70,228\nqc 71,230\nqm a,a,v,36,176,96\nqx 220\nqm a,a,v,104,176,104\n"
"qx 221\nqm a,a,v,104,180,108\nqx 222\nqm a,a,v,108,180,116\nqx 223\nqm a,a,v,108,176,112\n"
"qc 62,220\nqc 63,222\nqm a,v,v,136,252,104\nqw 4\nqm a,a,v,116,184,112\nqm a,a,v,116,188,116\n"
"qm a,a,v,112,188,108\nqm a,a,v,112,184,104\nqm a,a,v,44,184,96\nqm a,a,v,44,188,100\nqm a,a,v,40,188,92\nqm a,a,v,40,184,88\n"
"qm a,v,v,136,252,108\nqw 2\nqm a,a,v,48,192,88\nqm a,a,v,48,196,92\nqm a,a,v,52,196,100\nqm a,a,v,52,192,96\n"
"qm a,a,v,120,192,104\nqm a,a,v,120,196,108\nqw 0\nqv 1\ns_barrier\nqm a,a,v,124,196,116\n"
"qr 144,1,0\nqm a,a,v,124,192,112\nqr 148,1,1024\nqm a,v,v,140,252,112\nqr 152,1,2048\nqm a,a,v,132,200,112\n"
"qr 156,1,3072\nqm a,a,v,132,204,116\nqr 160,1,4096\nqm a,a,v,128,204,108\nqr 164,1,5120\nqm a,a,v,128,200,104\n"
"qm a,a,v,60,200,96\nqm a,a,v,60,204,100\nqm a,a,v,56,204,92\nqm a,a,v,56,200,88\nqm a,v,v,140,252,116\nqw 4\n"
"s_sub_u32 s10, s10, 1\ns_cmp_eq_u32 s10, 0\ns_cbranch_scc1 XD1%=\nDA1%=:\nqz v,a,v,124,144,120\ns_add_u32 s77, s85, 0x1000000\n"
"s_mov_b32 s85, s76\nqr 168,1,6144\nqm v,a,v,124,148,128\nqw 3\ns_mov_b32 m0, s13\nqr 172,1,7168\n"
"qm v,a,v,124,152,136\ns_add_u32 s13, s13, 0x4000\ns_xor_b32 s79, s79, 0x8000\nqr 176,1,8192\nqm v,a,v,124,156,144\nqe 0,56,76,0\n"
"qz v,a,v,132,156,176\nqe 0,56,76,1024\nqm v,a,v,132,152,168\nqr 180,1,9216\nqm v,a,v,132,148,160\nqr 184,1,10240\n"
"qm v,a,v,132,144,152\nqr 188,1,11264\nqz v,a,v,140,144,184\nqx 124\nqm v,a,v,140,148,192\nqx 125\n"
"qm v,a,v,140,152,200\nqx 126\nqm v,a,v,140,156,208\nqx 127\nqz v,a,v,148,156,240\nqc 88,124\n"
"qc 89,126\nqm v,a,v,148,152,232\nqx 132\nqm v,a,v,148,148,224\nqx 133\nqm v,a,v,148,144,216\n"
"qw 6\nqx 134\nqz v,a,v,180,160,216\nqx 135\nqm v,a,v,180,164,224\nqw 4\n"
"qc 96,132\nqc 97,134\nqm v,a,v,180,168,232\nqx 140\nqm v,a,v,180,172,240\nqx 141\n"
"qz v,a,v,172,172,208\nqx 142\nqm v,a,v,172,168,200\nqx 143\nqm v,a,v,172,164,192\nqc 104,140\n"
"qc 105,142\nqm v,a,v,172,160,184\nqx 148\nqz v,a,v,164,160,152\nqx 149\nqm v,a,v,164,164,160\n"
"qe 0,56,76,2048\nqm v,a,v,164,168,168\nqx 150\nqm v,a,v,164,172,176\nqx 151\nqz v,a,v,156,172,144\n"
"qr 192,1,12288\nqc 112,148\nqm v,a,v,156,168,136\nqx 180\nqm v,a,v,156,164,128\nqx 181\n"
"qm v,a,v,156,160,120\nqw 3\nqr 196,1,13312\nqc 113,150\nqz v,a,v,188,176,120\nqr 200,1,14336\n"
"qc 114,180\nqm v,a,v,188,180,128\nqw 3\nqx 182\nqm v,a,v,188,184,136\nqr 204,1,15360\n"
"qm v,a,v,188,188,144\nqe 0,56,76,3072\nqz v,a,v,196,188,176\ns_and_b32 s13, s13, 0xffff\nqx 183\nqm v,a,v,196,184,168\n"
"s_add_u32 s76, s76, 0x4000\nqx 172\nqm v,a,v,196,180,160\ns_or_b32 m0, s34, 0x10000\nqx 173\nqm v,a,v,196,176,152\n"
"s_add_u32 s34, s34, 0x4000\nqc 115,182\nqc 106,172\nqz v,a,v,204,176,184\nqx 174\nqm v,a,v,204,180,192\n"
"qx 175\nqm v,a,v,204,184,200\nqe 0,56,77,0\nqm v,a,v,204,188,208\nqx 164\nqz v,a,v,212,188,240\n"
"qr 144,2,16384\nqc 107,174\nqm v,a,v,212,184,232\nqx 165\nqm v,a,v,212,180,224\nqr 148,2,24576\n"
"qc 98,164\nqm v,a,v,212,176,216\nqw 4\nqx 166\nqz v,a,v,244,192,216\nqx 167\n"
"qm v,a,v,244,196,224\nqw 2\nqx 156\nqm v,a,v,244,200,232\nqe 0,56,77,1024\nqm v,a,v,244,204,240\n"
"qx 157\nqz v,a,v,236,204,208\nqr 152,2,17408\nqc 99,166\nqm v,a,v,236,200,200\nqr 156,2,25600\n"
"qc 90,156\nqm v,a,v,236,196,192\nqx 158\nqm v,a,v,236,192,184\nqx 159\nqz v,a,v,228,192,152\n"
"qx 188\nqm v,a,v,228,196,160\nqx 189\nqm v,a,v,228,200,168\nqe 0,56,77,2048\nqm v,a,v,228,204,176\n"
"qr 160,2,18432\nqc 91,158\nqz v,a,v,220,204,144\nqx 190\nqm v,a,v,220,200,136\nqr 164,2,26624\n"
"qc 92,188\nqm v,a,v,220,196,128\nqx 191\nqm v,a,v,220,192,120\nqw 4\nqx 196\n"
"qm a,a,v,0,144,56\nqx 197\nqm a,a,v,0,148,60\nqe 0,56,77,3072\nqm a,a,v,4,148,68\ns_and_b32 s34, s34, 0xffff\n"
"s_add_u32 s77, s77, 0x4000\ns_lshr_b32 s82, s82, 1\nqm a,a,v,4,144,64\nqr 168,2,19456\nqc 93,190\nqm a,a,v,72,144,72\n"
"qx 198\nqm a,a,v,72,148,76\nqr 172,2,27648\nqc 100,196\nqm a,a,v,76,148,84\nqx 199\n"
"qm a,a,v,76,144,80\nqx 204\nqm a,v,v,64,252,56\nqw 4\nqx 205\nqm a,a,v,84,152,80\n"
"qr 176,2,20480\nqc 101,198\nqm a,a,v,84,156,84\nqx 206\nqm a,a,v,80,156,76\nqx 207\n"
"qm a,a,v,80,152,72\nqr 180,2,28672\nqc 108,204\nqm a,a,v,12,152,64\nqx 212\nqm a,a,v,12,156,68\n"
"qx 213\nqm a,a,v,8,156,60\nqr 184,2,21504\nqc 109,206\nqm a,a,v,8,152,56\nqx 214\n"
"qm a,v,v,64,252,60\nqw 5\nqx 215\nqm a,a,v,16,160,56\nqr 188,2,29696\nqc 116,212\n"
"qm a,a,v,16,164,60\nqx 244\nqm a,a,v,20,164,68\nqx 245\nqm a,a,v,20,160,64\nqc 117,214\n"
"qc 118,244\nqm a,a,v,88,160,72\nqx 246\nqm a,a,v,88,164,76\nqx 247\nqm a,a,v,92,164,84\n"
"qx 236\nqm a,a,v,92,160,80\nqr 192,2,22528\nqc 119,246\nqm a,v,v,68,252,64\nqw 5\n"
"s_bitcmp1_b32 s82, 0\ns_cselect_b64 exec, -1, 0\nglobal_load_dword a255, v[4:5], off\nv_lshl_add_u64 v[4:5], v[4:5], 0, s[80:81]\ns_mov_b64 exec, -1\nqm a,a,v,100,168,80\n"
"qr 196,2,30720\nqm a,a,v,100,172,84\nqx 237\nqm a,a,v,96,172,76\nqx 238\nqm a,a,v,96,168,72\n"
"qx 239\nqm a,a,v,28,168,64\nqr 200,2,23552\nqc 110,236\nqm a,a,v,28,172,68\nqr 204,2,31744\n"
"qc 111,238\nqm a,a,v,24,172,60\nqx 228\nqm a,a,v,24,168,56\nqx 229\nqm a,v,v,68,252,68\n"
"qw 6\nv_add_u32 v2, s79, v3\nqc 102,228\nqm a,a,v,32,176,56\nqx 230\nqm a,a,v,32,180,60\n"
"qx 231\nqm a,a,v,36,180,68\nqx 220\nqm a,a,v,36,176,64\nqx 221\nqm a,a,v,104,176,72\n"
"qc 103,230\nqc 94,220\nqm a,a,v,104,180,76\nqx 222\nqm a,a,v,108,180,84\nqx 223\n"
"qm a,a,v,108,176,80\nqc 95,222\nqm a,v,v,136,252,72\nqw 4\nqm a,a,v,116,184,80\nqm a,a,v,116,188,84\n"
"qm a,a,v,112,188,76\nqm a,a,v,112,184,72\nqm a,a,v,44,184,64\nqm a,a,v,44,188,68\nqm a,a,v,40,188,60\nqm a,a,v,40,184,56\n"
"qm a,v,v,136,252,76\nqw 2\nqm a,a,v,48,192,56\nqm a,a,v,48,196,60\nqm a,a,v,52,196,68\nqm a,a,v,52,192,64\n"
"qm a,a,v,120,192,72\nqm a,a,v,120,196,76\nqw 0\nqv 1\ns_barrier\nqm a,a,v,124,196,84\n"
"qr 144,1,16384\nqm a,a,v,124,192,80\nqr 148,1,17408\nqm a,v,v,140,252,80\nqr 152,1,18432\nqm a,a,v,132,200,80\n"
"qr 156,1,19456\nqm a,a,v,132,204,84\nqr 160,1,20480\nqm a,a,v,128,204,76\nqr 164,1,21504\nqm a,a,v,128,200,72\n"
"qm a,a,v,60,200,64\nqm a,a,v,60,204,68\nqm a,a,v,56,204,60\nqm a,a,v,56,200,56\nqm a,v,v,140,252,84\nqw 4\n"
"s_branch DB1%=\nXD1%=:\nv_mfma_f32_16x16x32_bf16 v[124:127], a[144:147], v[184:187], v[20:23]\ns_add_u32 s77, s85, 0x1000000\ns_mov_b32 s85, s76\nqr 168,1,6144\n"
"qm v,a,v,124,148,192\nqw 3\ns_mov_b32 m0, s13\nqr 172,1,7168\nqm v,a,v,124,152,200\ns_add_u32 s13, s13, 0x4000\n"
"s_xor_b32 s78, s78, 0x8000\nqr 176,1,8192\nqm v,a,v,124,156,208\nqe 0,56,76,0\nqz v,a,v,132,156,240\ns_xor_b32 s79, s79, 0x8000\n"
"qr 180,1,9216\nqm v,a,v,132,152,232\nqr 184,1,10240\nqm v,a,v,132,148,224\nqr 188,1,11264\nqm v,a,v,132,144,216\n"
"qw 6\nqr 192,1,12288\nqz v,a,v,148,160,216\nqe 0,56,76,1024\nqm v,a,v,148,164,224\nqw 5\n"
"qr 196,1,13312\nqm v,a,v,148,168,232\nqr 200,1,14336\nqm v,a,v,148,172,240\nqe 0,56,76,2048\nv_mfma_f32_16x16x32_bf16 v[140:143], a[172:175], v[208:211], v[24:27]\n"
"qr 204,1,15360\nqm v,a,v,140,168,200\nqx 124\nqm v,a,v,140,164,192\nqe 0,56,76,3072\nqm v,a,v,140,160,184\n"
"qw 6\ns_and_b32 s13, s13, 0xffff\nqr 144,1,16384\nv_mfma_f32_16x16x32_bf16 v[156:159], a[176:179], v[184:187], v[28:31]\ns_add_u32 s76, s76, 0x4000\nqx 125\n"
"qm v,a,v,156,180,192\nqw 5\ns_mov_b32 m0, s13\nqr 148,1,17408\nqm v,a,v,156,184,200\ns_add_u32 s13, s13, 0x4000\n"
"qr 152,1,18432\nqc 88,124\nqm v,a,v,156,188,208\nqx 126\nqz v,a,v,164,188,240\nqr 156,1,19456\n"
"qm v,a,v,164,184,232\nqe 0,56,76,0\nqm v,a,v,164,180,224\nqx 127\nqm v,a,v,164,176,216\nqw 6\n"
"qr 160,1,20480\nqc 89,126\nqz v,a,v,180,192,216\nqr 164,1,21504\nqm v,a,v,180,196,224\nqw 6\n"
"qx 132\nqm v,a,v,180,200,232\nqr 168,1,22528\nqm v,a,v,180,204,240\nqr 172,1,23552\nv_mfma_f32_16x16x32_bf16 v[172:175], a[204:207], v[208:211], v[32:35]\n"
"qx 133\nqm v,a,v,172,200,200\nqr 176,1,24576\nqc 104,132\nqm v,a,v,172,196,192\nqe 0,56,76,1024\n"
"qm v,a,v,172,192,184\nqw 7\nqr 180,1,25600\nv_mfma_f32_16x16x32_bf16 v[196:199], a[144:147], v[216:219], v[36:39]\nqx 134\nqm v,a,v,196,148,224\n"
"qw 6\nqr 184,1,26624\nqm v,a,v,196,152,232\nqr 188,1,27648\nqm v,a,v,196,156,240\nqw 6\n"
"qx 135\nv_mfma_f32_16x16x32_bf16 v[212:215], a[160:163], v[216:219], v[40:43]\nqr 192,1,28672\nqc 105,134\nqm v,a,v,212,164,224\nqw 5\n"
"qr 196,1,29696\nqm v,a,v,212,168,232\nqe 0,56,76,2048\nqm v,a,v,212,172,240\nqw 4\nqr 200,1,30720\n"
"qx 148\nv_mfma_f32_16x16x32_bf16 v[228:231], a[176:179], v[216:219], v[44:47]\nqr 204,1,31744\nqm v,a,v,228,180,224\nqw 4\nqr 144,2,16384\n"
"qx 149\nqm v,a,v,228,184,232\nqr 148,2,24576\nqc 106,148\nqm v,a,v,228,188,240\nqw 4\n"
"qx 150\nv_mfma_f32_16x16x32_bf16 v[244:247], a[192:195], v[216:219], v[48:51]\nqe 0,56,76,3072\nqm v,a,v,244,196,224\nqw 2\nqx 151\n"
"qm v,a,v,244,200,232\ns_and_b32 s13, s13, 0xffff\nqx 140\nqm v,a,v,244,204,240\nqw 0\ns_add_u32 s76, s76, 0x4000\n"
"s_sub_u32 s76, s76, 0x10000\nqm a,a,v,0,144,56\ns_cmp_lt_u32 s76, s86\ns_cselect_b32 s76, s87, s76\ns_or_b32 m0, s34, 0x10000\ns_add_u32 s34, s34, 0x4000\n"
"qm a,a,v,0,148,60\nqe 0,56,77,0\nqx 141\nqm a,a,v,4,148,68\nqr 152,2,17408\nqc 107,150\n"
"qm a,a,v,4,144,64\nqr 156,2,25600\nqx 142\nqm a,a,v,72,144,72\nqx 143\nqm a,a,v,72,148,76\n"
"qe 0,56,77,1024\nqm a,a,v,76,148,84\nqc 90,140\nqc 91,142\nqm a,a,v,76,144,80\nqx 156\n"
"qm a,v,v,64,252,56\nqw 0\nqx 157\nqm a,a,v,84,152,80\nqe 0,56,77,2048\nqm a,a,v,84,156,84\n"
"qx 158\nqm a,a,v,80,156,76\nqr 160,2,18432\nqc 92,156\nqm a,a,v,80,152,72\nqr 164,2,26624\n"
"v_add_u32 v1, s78, v0\nqm a,a,v,12,152,64\nqx 159\nqm a,a,v,12,156,68\nqx 164\nqm a,a,v,8,156,60\n"
"qe 0,56,77,3072\nqm a,a,v,8,152,56\ns_and_b32 s34, s34, 0xffff\nqx 165\nqm a,v,v,64,252,60\nqw 0\n"
"s_add_u32 s77, s77, 0x4000\nqc 93,158\nqm a,a,v,16,160,56\ns_or_b32 m0, s34, 0x10000\nqx 166\nqm a,a,v,16,164,60\n"
"s_add_u32 s34, s34, 0x4000\nqr 168,2,19456\nqc 108,164\nqm a,a,v,20,164,68\nqe 0,56,77,0\nqm a,a,v,20,160,64\n"
"qr 172,2,27648\nqx 167\nqm a,a,v,88,160,72\nqx 180\nqm a,a,v,88,164,76\nqx 181\n"
"qm a,a,v,92,164,84\nqe 0,56,77,1024\nqm a,a,v,92,160,80\nqr 176,2,20480\nqc 109,166\nqm a,v,v,68,252,64\n"
"qw 1\nqx 182\nqm a,a,v,100,168,80\nqx 183\nqm a,a,v,100,172,84\nqr 180,2,28672\n"
"qc 110,180\nqm a,a,v,96,172,76\nqe 0,56,77,2048\nqm a,a,v,96,168,72\nqx 172\nqm a,a,v,28,168,64\n"
"qx 173\nqm a,a,v,28,172,68\nqe 0,56,77,3072\nqm a,a,v,24,172,60\ns_and_b32 s34, s34, 0xffff\ns_add_u32 s77, s77, 0x4000\n"
"s_lshr_b32 s82, s82, 1\nqm a,a,v,24,168,56\nqr 184,2,21504\nqc 111,182\nqm a,v,v,68,252,68\nqw 1\n"
"qx 174\nqm a,a,v,32,176,56\nqx 175\nqm a,a,v,32,180,60\nqr 188,2,29696\nqc 94,172\n"
"qm a,a,v,36,180,68\nqx 196\nqm a,a,v,36,176,64\nqx 197\nqm a,a,v,104,176,72\nqc 95,174\n"
"qc 112,196\nqm a,a,v,104,180,76\nqx 198\nqm a,a,v,108,180,84\ns_bitcmp1_b32 s82, 0\ns_cselect_b64 exec, -1, 0\n"
"global_load_dword a255, v[4:5], off\nv_lshl_add_u64 v[4:5], v[4:5], 0, s[80:81]\ns_mov_b64 exec, -1\nqm a,a,v,108,176,80\nqx 199\nqm a,v,v,136,252,72\n"
"qw 0\nqx 212\nqm a,a,v,116,184,80\nqr 192,2,22528\nqc 113,198\nqm a,a,v,116,188,84\n"
"qr 196,2,30720\nqm a,a,v,112,188,76\nqx 213\nqm a,a,v,112,184,72\nqx 214\nqm a,a,v,44,184,64\n"
"s_getpc_b64 s[72:73]\nCT7%=:\ns_add_u32 s72, s72, G2GO%=-CT7%=\ns_addc_u32 s73, s73, 0\nqm a,a,v,44,188,68\ns_lshl2_add_u32 s74, s19, s15\n"
"s_mul_i32 s74, s74, 256\nv_lshl_add_u32 v11, v0, 3, s74\nqm a,a,v,40,188,60\ns_mov_b32 s74, FINISH%=-G2GO%=\ns_mov_b32 s75, 0x20000\nqr 200,2,23552\n"
"qm a,a,v,40,184,56\nqx 215\nqm a,v,v,136,252,76\nqw 1\nqr 204,2,31744\nqc 114,212\n"
"qm a,a,v,48,192,56\nqx 228\nqm a,a,v,48,196,60\nqx 229\nqm a,a,v,52,196,68\nqc 115,214\n"
"v_add_u32 v2, s79, v3\nqm a,a,v,52,192,64\nqx 230\nqm a,a,v,120,192,72\nqx 231\nqm a,a,v,120,196,76\n"
"s_bfm_b64 exec, 2, 0\nbuffer_load_dword a255, v11, s[72:75], 0 offen\ns_mov_b64 exec, -1\nqm a,a,v,124,196,84\nqw 0\nqv 2\n"
"s_barrier\nqm a,a,v,124,192,80\nqr 144,1,0\nqc 116,228\nqm a,v,v,140,252,80\nqx 244\n"
"qm a,a,v,132,200,80\nqx 245\nqm a,a,v,132,204,84\nqr 148,1,1024\nqc 117,230\nqm a,a,v,128,204,76\n"
"qx 246\nqm a,a,v,128,200,72\nqr 152,1,2048\nqc 118,244\nqm a,a,v,60,200,64\nqr 156,1,3072\n"
"qm a,a,v,60,204,68\nqx 247\nqm a,a,v,56,204,60\nqr 160,1,4096\nqc 119,246\nqm a,a,v,56,200,56\n"
"qr 164,1,5120\nqm a,v,v,140,252,84\nqw 4\nED1%=:\ns_lshl_b32 s6, s88, 17\ns_lshl_b32 s7, s20, 12\n"
"s_add_u32 s6, s6, s7\ns_add_u32 s6, s6, 0x2800000\ns_xor_b32 s7, s6, 0xe0000\nqa 6,64\nqa 7,68\nqp 6\n"
"qp 7\ns_lshr_b32 s8, s88, 3\ns_lshl2_add_u32 s8, s8, s15\ns_lshl_b32 s8, s8, 8\ns_lshl3_add_u32 s8, s20, s8\ns_add_u32 s8, s8, 0x5000\n"
"qa 120,0\nqa 121,1\nqa 122,2\nqa 123,3\nqa 128,8\nqa 129,9\n"
"qa 130,10\nqa 131,11\nv_pk_mul_f32 v[120:121], v[120:121], v[6:7] op_sel:[0,0] op_sel_hi:[1,0]\nv_pk_mul_f32 v[122:123], v[122:123], v[6:7] op_sel:[0,0] op_sel_hi:[1,0]\nv_pk_mul_f32 v[128:129], v[128:129], v[6:7] op_sel:[0,0] op_sel_hi:[1,0]\nv_pk_mul_f32 v[130:131], v[130:131], v[6:7] op_sel:[0,0] op_sel_hi:[1,0]\n"
"qc 120,120\nqc 121,122\nqc 122,128\nqc 123,130\nbuffer_store_dwordx4 v[120:123], v0, s[56:59], s6 offen offset:0 sc1\nqa 136,4\n"
"qa 137,5\nqa 138,6\nqa 139,7\nqa 144,12\nqa 145,13\nqa 146,14\n"
"qa 147,15\nv_pk_mul_f32 v[136:137], v[136:137], v[6:7] op_sel:[0,1] op_sel_hi:[1,1]\nv_pk_mul_f32 v[138:139], v[138:139], v[6:7] op_sel:[0,1] op_sel_hi:[1,1]\nv_pk_mul_f32 v[144:145], v[144:145], v[6:7] op_sel:[0,1] op_sel_hi:[1,1]\nv_pk_mul_f32 v[146:147], v[146:147], v[6:7] op_sel:[0,1] op_sel_hi:[1,1]\nqc 136,136\n"
"qc 137,138\nqc 138,144\nqc 139,146\nbuffer_store_dwordx4 v[136:139], v0, s[56:59], s7 offen offset:0 sc1\nqa 152,16\nqa 153,17\n"
"qa 154,18\nqa 155,19\nqa 160,24\nqa 161,25\nqa 162,26\nqa 163,27\n"
"v_pk_mul_f32 v[152:153], v[152:153], v[6:7] op_sel:[0,0] op_sel_hi:[1,0]\nv_pk_mul_f32 v[154:155], v[154:155], v[6:7] op_sel:[0,0] op_sel_hi:[1,0]\nv_pk_mul_f32 v[160:161], v[160:161], v[6:7] op_sel:[0,0] op_sel_hi:[1,0]\nv_pk_mul_f32 v[162:163], v[162:163], v[6:7] op_sel:[0,0] op_sel_hi:[1,0]\nqc 152,152\nqc 153,154\n"
"qc 154,160\nqc 155,162\nbuffer_store_dwordx4 v[152:155], v0, s[56:59], s6 offen offset:1024 sc1\nqa 168,20\nqa 169,21\nqa 170,22\n"
"qa 171,23\nqa 176,28\nqa 177,29\nqa 178,30\nqa 179,31\nv_pk_mul_f32 v[168:169], v[168:169], v[6:7] op_sel:[0,1] op_sel_hi:[1,1]\n"
"v_pk_mul_f32 v[170:171], v[170:171], v[6:7] op_sel:[0,1] op_sel_hi:[1,1]\nv_pk_mul_f32 v[176:177], v[176:177], v[6:7] op_sel:[0,1] op_sel_hi:[1,1]\nv_pk_mul_f32 v[178:179], v[178:179], v[6:7] op_sel:[0,1] op_sel_hi:[1,1]\nqc 168,168\nqc 169,170\nqc 170,176\n"
"qc 171,178\nbuffer_store_dwordx4 v[168:171], v0, s[56:59], s7 offen offset:1024 sc1\nqa 120,32\nqa 121,33\nqa 122,34\nqa 123,35\n"
"qa 128,40\nqa 129,41\nqa 130,42\nqa 131,43\nv_pk_mul_f32 v[120:121], v[120:121], v[6:7] op_sel:[0,0] op_sel_hi:[1,0]\nv_pk_mul_f32 v[122:123], v[122:123], v[6:7] op_sel:[0,0] op_sel_hi:[1,0]\n"
"v_pk_mul_f32 v[128:129], v[128:129], v[6:7] op_sel:[0,0] op_sel_hi:[1,0]\nv_pk_mul_f32 v[130:131], v[130:131], v[6:7] op_sel:[0,0] op_sel_hi:[1,0]\nqc 120,120\nqc 121,122\nqc 122,128\nqc 123,130\n"
"buffer_store_dwordx4 v[120:123], v0, s[56:59], s6 offen offset:2048 sc1\nqa 136,36\nqa 137,37\nqa 138,38\nqa 139,39\nqa 144,44\n"
"qa 145,45\nqa 146,46\nqa 147,47\nv_pk_mul_f32 v[136:137], v[136:137], v[6:7] op_sel:[0,1] op_sel_hi:[1,1]\nv_pk_mul_f32 v[138:139], v[138:139], v[6:7] op_sel:[0,1] op_sel_hi:[1,1]\nv_pk_mul_f32 v[144:145], v[144:145], v[6:7] op_sel:[0,1] op_sel_hi:[1,1]\n"
"v_pk_mul_f32 v[146:147], v[146:147], v[6:7] op_sel:[0,1] op_sel_hi:[1,1]\nqc 136,136\nqc 137,138\nqc 138,144\nqc 139,146\nbuffer_store_dwordx4 v[136:139], v0, s[56:59], s7 offen offset:2048 sc1\n"
"qa 152,48\nqa 153,49\nqa 154,50\nqa 155,51\nqa 160,56\nqa 161,57\n"
"qa 162,58\nqa 163,59\nv_pk_mul_f32 v[152:153], v[152:153], v[6:7] op_sel:[0,0] op_sel_hi:[1,0]\nv_pk_mul_f32 v[154:155], v[154:155], v[6:7] op_sel:[0,0] op_sel_hi:[1,0]\nv_pk_mul_f32 v[160:161], v[160:161], v[6:7] op_sel:[0,0] op_sel_hi:[1,0]\nv_pk_mul_f32 v[162:163], v[162:163], v[6:7] op_sel:[0,0] op_sel_hi:[1,0]\n"
"qc 152,152\nqc 153,154\nqc 154,160\nqc 155,162\nbuffer_store_dwordx4 v[152:155], v0, s[56:59], s6 offen offset:3072 sc1\nqa 168,52\n"
"qa 169,53\nqa 170,54\nqa 171,55\nqa 176,60\nqa 177,61\nqa 178,62\n"
"qa 179,63\nv_pk_mul_f32 v[168:169], v[168:169], v[6:7] op_sel:[0,1] op_sel_hi:[1,1]\nv_pk_mul_f32 v[170:171], v[170:171], v[6:7] op_sel:[0,1] op_sel_hi:[1,1]\nv_pk_mul_f32 v[176:177], v[176:177], v[6:7] op_sel:[0,1] op_sel_hi:[1,1]\nv_pk_mul_f32 v[178:179], v[178:179], v[6:7] op_sel:[0,1] op_sel_hi:[1,1]\nqc 168,168\n"
"qc 169,170\nqc 170,176\nqc 171,178\nbuffer_store_dwordx4 v[168:171], v0, s[56:59], s7 offen offset:3072 sc1\nqz v,a,v,124,144,184\ns_add_u32 s77, s85, 0x1000000\n"
"s_mov_b32 s85, s76\ns_mov_b32 m0, s13\ns_add_u32 s13, s13, 0x4000\nqm v,a,v,124,148,192\nqw 2\nqr 168,1,6144\n"
"qm v,a,v,124,152,200\ns_xor_b32 s78, s78, 0x8000\ns_xor_b32 s79, s79, 0x8000\nqr 172,1,7168\nqm v,a,v,124,156,208\nqe 0,56,76,0\n"
"qz v,a,v,132,156,240\nqr 176,1,8192\nqm v,a,v,132,152,232\nqr 180,1,9216\nqm v,a,v,132,148,224\nqe 0,56,76,1024\n"
"qm v,a,v,132,144,216\nqw 4\nqr 184,1,10240\nqz v,a,v,148,160,216\nqx 124\nqx 125\n"
"qx 126\nqx 127\nqm v,a,v,148,164,224\nqw 3\nqr 188,1,11264\nqc 56,124\n"
"qm v,a,v,148,168,232\nqr 192,1,12288\nqc 57,126\nqm v,a,v,148,172,240\nqe 0,56,76,2048\nqz v,a,v,140,172,208\n"
"qx 132\nqx 133\nqx 134\nqx 135\nqm v,a,v,140,168,200\nqr 196,1,13312\n"
"qc 72,132\nqm v,a,v,140,164,192\nqe 0,56,76,3072\nqm v,a,v,140,160,184\nqw 4\ns_and_b32 s13, s13, 0xffff\n"
"s_add_u32 s76, s76, 0x4000\nqz v,a,v,156,176,184\nqx 148\nqx 149\nqx 150\nqx 151\n"
"qm v,a,v,156,180,192\nqw 2\ns_mov_b32 m0, s13\ns_add_u32 s13, s13, 0x4000\nqm v,a,v,156,184,200\nqr 200,1,14336\n"
"qc 73,134\nqm v,a,v,156,188,208\nqr 204,1,15360\nqc 74,148\nqz v,a,v,164,188,240\nqx 140\n"
"qx 141\nqx 142\nqx 143\nqm v,a,v,164,184,232\nqr 144,1,16384\nqc 75,150\n"
"qm v,a,v,164,180,224\nqe 0,56,76,0\nqm v,a,v,164,176,216\nqw 3\nqc 58,140\nqc 59,142\n"
"qz v,a,v,180,192,216\nqv 5\nqx 156\nqx 157\nqm v,a,v,180,196,224\nqw 1\n"
"qx 158\nqm v,a,v,180,200,232\nqr 148,1,17408\nqc 60,156\nqm v,a,v,180,204,240\nqr 152,1,18432\n"
"qx 159\nqz v,a,v,172,204,208\nqr 156,1,19456\nqc 61,158\nqm v,a,v,172,200,200\nqx 164\n"
"qm v,a,v,172,196,192\ns_mov_b64 exec, 1\nbuffer_store_dwordx2 v[250:251], off, s[56:59], s8 sc1\ns_mov_b64 exec, -1\nqm v,a,v,172,192,184\nqw 2\n"
"qx 165\nqz v,a,v,188,144,184\nqr 160,1,20480\nqc 76,164\nqm v,a,v,188,148,192\nqw 1\n"
"qx 166\nqm v,a,v,188,152,200\nqr 164,1,21504\nqm v,a,v,188,156,208\nqe 0,56,76,1024\nqr 168,1,22528\n"
"qx 167\nqz v,a,v,196,156,240\nqr 172,1,23552\nqc 77,166\nqm v,a,v,196,152,232\nqx 180\n"
"qm v,a,v,196,148,224\nqx 181\nqm v,a,v,196,144,216\nqw 2\nqr 176,1,24576\nqc 78,180\n"
"qz v,a,v,212,160,216\nqe 0,56,76,2048\nqm v,a,v,212,164,224\nqw 1\nqx 182\nqm v,a,v,212,168,232\n"
"qr 180,1,25600\nqm v,a,v,212,172,240\nqe 0,56,76,3072\ns_and_b32 s13, s13, 0xffff\ns_add_u32 s76, s76, 0x4000\ns_sub_u32 s76, s76, 0x10000\n"
"s_cmp_lt_u32 s76, s86\ns_cselect_b32 s76, s87, s76\ns_or_b32 m0, s34, 0x10000\ns_add_u32 s34, s34, 0x4000\nqr 184,1,26624\nqx 183\n"
"qz v,a,v,204,172,208\nqr 188,1,27648\nqc 79,182\nqm v,a,v,204,168,200\nqx 172\nqm v,a,v,204,164,192\n"
"qx 173\nqm v,a,v,204,160,184\nqw 2\nqr 192,1,28672\nqc 62,172\nqz v,a,v,220,176,184\n"
"qx 174\nqm v,a,v,220,180,192\nqw 1\nqr 196,1,29696\nqm v,a,v,220,184,200\nqe 0,56,77,0\n"
"qm v,a,v,220,188,208\nqr 200,1,30720\nqx 175\nqz v,a,v,228,188,240\nqr 204,1,31744\nqc 63,174\n"
"qm v,a,v,228,184,232\nqx 188\nqm v,a,v,228,180,224\nqe 0,56,77,1024\nqm v,a,v,228,176,216\nqw 2\n"
"qx 189\nqz v,a,v,244,192,216\nqr 144,2,0\nqc 64,188\nqm v,a,v,244,196,224\nqw 1\n"
"qx 190\nqm v,a,v,244,200,232\nqr 148,2,8192\nv_add_u32 v1, s78, v0\nqm v,a,v,244,204,240\nqx 191\n"
"qz v,a,v,236,204,208\nqe 0,56,77,2048\nqm v,a,v,236,200,200\nqr 152,2,16384\nqx 196\nqm v,a,v,236,196,192\n"
"qr 156,2,24576\nqc 65,190\nqm v,a,v,236,192,184\nqe 0,56,77,3072\ns_and_b32 s34, s34, 0xffff\ns_add_u32 s77, s77, 0x4000\n"
"s_or_b32 m0, s34, 0x10000\ns_add_u32 s34, s34, 0x4000\nqx 197\nqw 2\nqm a,a,v,72,144,88\nqr 160,2,1024\n"
"qc 80,196\nqm a,a,v,72,148,92\nqr 164,2,9216\nqx 198\nqm a,a,v,76,148,108\nqx 199\n"
"qm a,a,v,76,144,104\nqw 2\nqr 168,2,17408\nqc 81,198\nqm a,a,v,76,152,112\nqr 172,2,25600\n"
"qm a,a,v,76,156,116\nqx 212\nqm a,v,v,136,252,88\nqw 2\nqe 0,56,77,0\nqm a,a,v,80,160,88\n"
"qr 176,2,2048\nqx 213\nqm a,a,v,80,164,92\nqr 180,2,10240\nqc 82,212\nqm a,a,v,84,164,108\n"
"qx 214\nqm a,a,v,84,160,104\nqw 2\nqx 215\nqm a,a,v,84,168,112\nqe 0,56,77,1024\n"
"qr 184,2,18432\nqm a,a,v,84,172,116\nqr 188,2,26624\nqc 83,214\nqm a,v,v,136,252,92\nqw 2\n"
"qr 192,2,3072\nqx 204\nqm a,a,v,88,176,88\nqr 196,2,11264\nqm a,a,v,88,180,92\nqx 205\n"
"qm a,a,v,92,180,108\nqe 0,56,77,2048\nqm a,a,v,92,176,104\nqw 2\nqr 200,2,19456\nqx 206\n"
"qm a,a,v,92,184,112\nqr 204,2,27648\nqc 66,204\nqm a,a,v,92,188,116\nqe 0,56,77,3072\ns_and_b32 s34, s34, 0xffff\n"
"s_add_u32 s77, s77, 0x4000\ns_lshr_b32 s82, s82, 1\nqx 207\nqr 144,2,4096\nqw 3\nqm a,a,v,96,192,88\n"
"qr 148,2,12288\nqc 67,206\nqm a,a,v,96,196,92\nqx 220\nqm a,a,v,100,196,108\nqx 221\n"
"qm a,a,v,100,192,104\nqw 2\nqr 152,2,20480\nqc 68,220\nqm a,a,v,100,200,112\nqr 156,2,28672\n"
"qm a,a,v,100,204,116\nqw 2\nqx 222\nqm a,a,v,104,144,88\ns_bitcmp1_b32 s82, 0\ns_cselect_b64 exec, -1, 0\n"
"global_load_dword a255, v[4:5], off\nv_lshl_add_u64 v[4:5], v[4:5], 0, s[80:81]\ns_mov_b64 exec, -1\nqr 160,2,5120\nqm a,a,v,104,148,92\nqr 164,2,13312\n"
"qx 223\nqm a,a,v,108,148,108\nqx 228\nqm a,a,v,108,144,104\nqw 2\nqr 168,2,21504\n"
"qc 69,222\nqm a,a,v,108,152,112\nqx 229\nqm a,a,v,108,156,116\nqr 172,2,29696\nqc 84,228\n"
"qm a,v,v,140,252,104\nqw 2\nqx 230\nqm a,a,v,112,160,88\nqr 176,2,6144\nqm a,a,v,112,164,92\n"
"qr 180,2,14336\nqm a,a,v,116,164,108\nqr 184,2,22528\nqr 188,2,30720\nqr 192,2,7168\nqr 196,2,15360\n"
"qr 200,2,23552\nqr 204,2,31744\nqx 231\nqm a,a,v,116,160,104\nqw 8\nqx 244\n"
"qm a,a,v,116,168,112\nqx 245\nqm a,a,v,116,172,116\nqc 85,230\nv_add_u32 v2, s79, v3\nqm a,v,v,140,252,108\n"
"qw 6\nqx 246\nqm a,a,v,120,176,88\nqx 247\nqm a,a,v,120,180,92\nqw 0\n"
"qv 1\ns_barrier\nqm a,a,v,124,180,108\nqr 144,1,0\nqc 86,244\nqm a,a,v,124,176,104\n"
"qx 236\nqm a,a,v,124,184,112\nqx 237\nqm a,a,v,124,188,116\nqr 148,1,1024\nqc 87,246\n"
"qm a,v,v,140,252,112\nqx 238\nqm a,a,v,128,192,88\nqr 152,1,2048\nqc 70,236\nqm a,a,v,128,196,92\n"
"qr 156,1,3072\nqm a,a,v,132,196,108\nqx 239\nqm a,a,v,132,192,104\nqr 160,1,4096\nqc 71,238\n"
"qm a,a,v,132,200,112\nqr 164,1,5120\nqm a,a,v,132,204,116\nqm a,v,v,140,252,116\nqw 4\ns_sub_u32 s84, s84, 2\n"
"s_branch L2B%=\nXP%=:\nqm a,v,v,64,252,88\ns_add_u32 s77, s85, 0x1000000\ns_or_b32 m0, s34, 0x10000\ns_add_u32 s34, s34, 0x4000\n"
"qm a,v,v,64,252,92\nqr 144,2,0\nqm a,v,v,64,252,96\ns_xor_b32 s79, s79, 0x8000\nqr 148,2,8192\nqm a,v,v,64,252,100\n"
"qr 152,2,16384\nqm a,v,v,68,252,104\nqr 156,2,24576\nqm a,v,v,68,252,108\nqe 0,56,77,0\nqm a,v,v,68,252,112\n"
"qr 160,2,1024\nqm a,v,v,68,252,116\nqw 3\nqr 164,2,9216\nqm a,a,v,0,144,88\nqe 0,56,77,1024\n"
"qm a,a,v,0,148,92\nqw 2\nqr 168,2,17408\nqm a,a,v,0,152,96\nqr 172,2,25600\nqm a,a,v,0,156,100\n"
"qe 0,56,77,2048\nqm a,a,v,4,156,116\nqr 176,2,2048\nqm a,a,v,4,152,112\nqr 180,2,10240\nqm a,a,v,4,148,108\n"
"qe 0,56,77,3072\nqm a,a,v,4,144,104\nqw 4\ns_and_b32 s34, s34, 0xffff\ns_add_u32 s77, s77, 0x4000\nqm a,a,v,12,160,104\n"
"s_or_b32 m0, s34, 0x10000\ns_add_u32 s34, s34, 0x4000\nqr 184,2,18432\nqm a,a,v,12,164,108\nqw 3\nqr 188,2,26624\n"
"qm a,a,v,12,168,112\nqe 0,56,77,0\nqm a,a,v,12,172,116\nqr 192,2,3072\nqm a,a,v,8,172,100\nqr 196,2,11264\n"
"qm a,a,v,8,168,96\nqr 200,2,19456\nqm a,a,v,8,164,92\nqr 204,2,27648\nqm a,a,v,8,160,88\nqw 6\n"
"qr 144,2,4096\nqm a,a,v,16,176,88\nqe 0,56,77,1024\nqm a,a,v,16,180,92\nqw 5\nqr 148,2,12288\n"
"qm a,a,v,16,184,96\nqr 152,2,20480\nqm a,a,v,16,188,100\nqr 156,2,28672\nqm a,a,v,20,188,116\nqe 0,56,77,2048\n"
"qm a,a,v,20,184,112\nqr 160,2,5120\nqm a,a,v,20,180,108\nqr 164,2,13312\nqm a,a,v,20,176,104\nqw 8\n"
"qr 168,2,21504\nqm a,a,v,28,192,104\nqe 0,56,77,3072\nqm a,a,v,28,196,108\nqw 7\ns_and_b32 s34, s34, 0xffff\n"
"s_add_u32 s77, s77, 0x4000\nqm a,a,v,28,200,112\nqr 172,2,29696\nv_readlane_b32 s2, v18, 0\nqm a,a,v,28,204,116\nqr 176,2,6144\n"
"qm a,a,v,24,204,100\ns_add_u32 m0, s0, 0x0\nqr 180,2,14336\nqm a,a,v,24,200,96\nqd 12,60,2,0\nqm a,a,v,24,196,92\n"
"qr 184,2,22528\nqm a,a,v,24,192,88\nqw 9\nqr 188,2,30720\nqm a,a,v,32,144,88\nqr 192,2,7168\n"
"qm a,a,v,32,148,92\nqw 9\nqr 196,2,15360\nqm a,a,v,32,152,96\nqd 13,60,2,1024\nqm a,a,v,32,156,100\n"
"qr 200,2,23552\nqm a,a,v,36,156,116\nqr 204,2,31744\nqm a,a,v,36,152,112\nv_add_u32 v2, s79, v3\nqm a,a,v,36,148,108\n"
"qd 14,60,2,2048\nqm a,a,v,36,144,104\nqw 10\nqm a,a,v,44,160,104\nqr 144,1,0\nqm a,a,v,44,164,108\n"
"qw 9\nqr 148,1,1024\nqm a,a,v,44,168,112\nqr 152,1,2048\nqm a,a,v,44,172,116\nqd 15,60,2,3072\n"
"qm a,a,v,40,172,100\nqr 156,1,3072\nv_readlane_b32 s2, v18, 1\nqm a,a,v,40,168,96\nqm a,a,v,40,164,92\ns_add_u32 m0, s0, 0x4000\n"
"qm a,a,v,40,160,88\nqw 10\nqm a,a,v,48,176,88\nqd 12,60,2,0\nqm a,a,v,48,180,92\nqw 8\n"
"qr 160,1,4096\nqm a,a,v,48,184,96\nqr 164,1,5120\nqm a,a,v,48,188,100\nqm a,a,v,52,188,116\nqd 13,60,2,1024\n"
"qm a,a,v,52,184,112\nqm a,a,v,52,180,108\nqm a,a,v,52,176,104\nqw 8\nqm a,a,v,60,192,104\nqm a,a,v,60,196,108\n"
"qw 6\nqm a,a,v,60,200,112\nqd 14,60,2,2048\nqm a,a,v,60,204,116\nqm a,a,v,56,204,100\nqm a,a,v,56,200,96\n"
"qm a,a,v,56,196,92\nqd 15,60,2,3072\nqm a,a,v,56,192,88\nXQ%=:\ns_lshl_b32 s6, s88, 17\ns_lshl_b32 s7, s20, 12\n"
"s_add_u32 s6, s6, s7\ns_add_u32 s6, s6, 0x2800000\ns_xor_b32 s7, s6, 0xe0000\nqa 6,64\nqa 7,68\nqp 6\n"
"qp 7\ns_lshr_b32 s8, s88, 3\ns_lshl2_add_u32 s8, s8, s15\ns_lshl_b32 s8, s8, 8\ns_lshl3_add_u32 s8, s20, s8\ns_add_u32 s8, s8, 0x5000\n"
"qa 120,0\nqa 121,1\nqa 122,2\nqa 123,3\nqa 128,8\nqa 129,9\n"
"qa 130,10\nqa 131,11\nv_pk_mul_f32 v[120:121], v[120:121], v[6:7] op_sel:[0,0] op_sel_hi:[1,0]\nv_pk_mul_f32 v[122:123], v[122:123], v[6:7] op_sel:[0,0] op_sel_hi:[1,0]\nv_pk_mul_f32 v[128:129], v[128:129], v[6:7] op_sel:[0,0] op_sel_hi:[1,0]\nv_pk_mul_f32 v[130:131], v[130:131], v[6:7] op_sel:[0,0] op_sel_hi:[1,0]\n"
"qc 120,120\nqc 121,122\nqc 122,128\nqc 123,130\nbuffer_store_dwordx4 v[120:123], v0, s[56:59], s6 offen offset:0 sc1\nqa 136,4\n"
"qa 137,5\nqa 138,6\nqa 139,7\nqa 144,12\nqa 145,13\nqa 146,14\n"
"qa 147,15\nv_pk_mul_f32 v[136:137], v[136:137], v[6:7] op_sel:[0,1] op_sel_hi:[1,1]\nv_pk_mul_f32 v[138:139], v[138:139], v[6:7] op_sel:[0,1] op_sel_hi:[1,1]\nv_pk_mul_f32 v[144:145], v[144:145], v[6:7] op_sel:[0,1] op_sel_hi:[1,1]\nv_pk_mul_f32 v[146:147], v[146:147], v[6:7] op_sel:[0,1] op_sel_hi:[1,1]\nqc 136,136\n"
"qc 137,138\nqc 138,144\nqc 139,146\nbuffer_store_dwordx4 v[136:139], v0, s[56:59], s7 offen offset:0 sc1\nqa 152,16\nqa 153,17\n"
"qa 154,18\nqa 155,19\nqa 160,24\nqa 161,25\nqa 162,26\nqa 163,27\n"
"v_pk_mul_f32 v[152:153], v[152:153], v[6:7] op_sel:[0,0] op_sel_hi:[1,0]\nv_pk_mul_f32 v[154:155], v[154:155], v[6:7] op_sel:[0,0] op_sel_hi:[1,0]\nv_pk_mul_f32 v[160:161], v[160:161], v[6:7] op_sel:[0,0] op_sel_hi:[1,0]\nv_pk_mul_f32 v[162:163], v[162:163], v[6:7] op_sel:[0,0] op_sel_hi:[1,0]\nqc 152,152\nqc 153,154\n"
"qc 154,160\nqc 155,162\nbuffer_store_dwordx4 v[152:155], v0, s[56:59], s6 offen offset:1024 sc1\nqa 168,20\nqa 169,21\nqa 170,22\n"
"qa 171,23\nqa 176,28\nqa 177,29\nqa 178,30\nqa 179,31\nv_pk_mul_f32 v[168:169], v[168:169], v[6:7] op_sel:[0,1] op_sel_hi:[1,1]\n"
"v_pk_mul_f32 v[170:171], v[170:171], v[6:7] op_sel:[0,1] op_sel_hi:[1,1]\nv_pk_mul_f32 v[176:177], v[176:177], v[6:7] op_sel:[0,1] op_sel_hi:[1,1]\nv_pk_mul_f32 v[178:179], v[178:179], v[6:7] op_sel:[0,1] op_sel_hi:[1,1]\nqc 168,168\nqc 169,170\nqc 170,176\n"
"qc 171,178\nbuffer_store_dwordx4 v[168:171], v0, s[56:59], s7 offen offset:1024 sc1\nqa 120,32\nqa 121,33\nqa 122,34\nqa 123,35\n"
"qa 128,40\nqa 129,41\nqa 130,42\nqa 131,43\nv_pk_mul_f32 v[120:121], v[120:121], v[6:7] op_sel:[0,0] op_sel_hi:[1,0]\nv_pk_mul_f32 v[122:123], v[122:123], v[6:7] op_sel:[0,0] op_sel_hi:[1,0]\n"
"v_pk_mul_f32 v[128:129], v[128:129], v[6:7] op_sel:[0,0] op_sel_hi:[1,0]\nv_pk_mul_f32 v[130:131], v[130:131], v[6:7] op_sel:[0,0] op_sel_hi:[1,0]\nqc 120,120\nqc 121,122\nqc 122,128\nqc 123,130\n"
"buffer_store_dwordx4 v[120:123], v0, s[56:59], s6 offen offset:2048 sc1\nqa 136,36\nqa 137,37\nqa 138,38\nqa 139,39\nqa 144,44\n"
"qa 145,45\nqa 146,46\nqa 147,47\nv_pk_mul_f32 v[136:137], v[136:137], v[6:7] op_sel:[0,1] op_sel_hi:[1,1]\nv_pk_mul_f32 v[138:139], v[138:139], v[6:7] op_sel:[0,1] op_sel_hi:[1,1]\nv_pk_mul_f32 v[144:145], v[144:145], v[6:7] op_sel:[0,1] op_sel_hi:[1,1]\n"
"v_pk_mul_f32 v[146:147], v[146:147], v[6:7] op_sel:[0,1] op_sel_hi:[1,1]\nqc 136,136\nqc 137,138\nqc 138,144\nqc 139,146\nbuffer_store_dwordx4 v[136:139], v0, s[56:59], s7 offen offset:2048 sc1\n"
"qa 152,48\nqa 153,49\nqa 154,50\nqa 155,51\nqa 160,56\nqa 161,57\n"
"qa 162,58\nqa 163,59\nv_pk_mul_f32 v[152:153], v[152:153], v[6:7] op_sel:[0,0] op_sel_hi:[1,0]\nv_pk_mul_f32 v[154:155], v[154:155], v[6:7] op_sel:[0,0] op_sel_hi:[1,0]\nv_pk_mul_f32 v[160:161], v[160:161], v[6:7] op_sel:[0,0] op_sel_hi:[1,0]\nv_pk_mul_f32 v[162:163], v[162:163], v[6:7] op_sel:[0,0] op_sel_hi:[1,0]\n"
"qc 152,152\nqc 153,154\nqc 154,160\nqc 155,162\nbuffer_store_dwordx4 v[152:155], v0, s[56:59], s6 offen offset:3072 sc1\nqa 168,52\n"
"qa 169,53\nqa 170,54\nqa 171,55\nqa 176,60\nqa 177,61\nqa 178,62\n"
"qa 179,63\nv_pk_mul_f32 v[168:169], v[168:169], v[6:7] op_sel:[0,1] op_sel_hi:[1,1]\nv_pk_mul_f32 v[170:171], v[170:171], v[6:7] op_sel:[0,1] op_sel_hi:[1,1]\nv_pk_mul_f32 v[176:177], v[176:177], v[6:7] op_sel:[0,1] op_sel_hi:[1,1]\nv_pk_mul_f32 v[178:179], v[178:179], v[6:7] op_sel:[0,1] op_sel_hi:[1,1]\nqc 168,168\n"
"qc 169,170\nqc 170,176\nqc 171,178\nbuffer_store_dwordx4 v[168:171], v0, s[56:59], s7 offen offset:3072 sc1\nqw 4\nv_mfma_f32_16x16x32_bf16 v[124:127], a[144:147], v[184:187], v[20:23]\n"
"qr 168,1,6144\nqm v,a,v,124,148,192\nqw 3\nqr 172,1,7168\nqm v,a,v,124,152,200\nqr 176,1,8192\n"
"qm v,a,v,124,156,208\nqr 180,1,9216\nqz v,a,v,132,156,240\nqr 184,1,10240\nqm v,a,v,132,152,232\nqr 188,1,11264\n"
"qm v,a,v,132,148,224\nqr 192,1,12288\nqm v,a,v,132,144,216\nqw 7\nqr 196,1,13312\nqz v,a,v,148,160,216\n"
"qr 200,1,14336\nqm v,a,v,148,164,224\nqw 7\nqr 204,1,15360\nqm v,a,v,148,168,232\nqr 144,1,16384\n"
"qm v,a,v,148,172,240\nqr 148,1,17408\nv_mfma_f32_16x16x32_bf16 v[140:143], a[172:175], v[208:211], v[24:27]\nqr 152,1,18432\nqm v,a,v,140,168,200\nqr 156,1,19456\n"
"qm v,a,v,140,164,192\nqx 124\nqm v,a,v,140,160,184\nqw 10\nqx 125\nv_mfma_f32_16x16x32_bf16 v[156:159], a[176:179], v[184:187], v[28:31]\n"
"qr 160,1,20480\nqc 56,124\nqm v,a,v,156,180,192\nqw 9\nqr 164,1,21504\nqm v,a,v,156,184,200\n"
"qr 168,1,22528\nqm v,a,v,156,188,208\nqr 172,1,23552\nqz v,a,v,164,188,240\nqx 126\nqm v,a,v,164,184,232\n"
"qx 127\nqm v,a,v,164,180,224\nqx 132\nqm v,a,v,164,176,216\nqw 10\nqx 133\n"
"qz v,a,v,180,192,216\nqv 0\nqr 176,1,24576\nqc 57,126\nqm v,a,v,180,196,224\nqw 9\n"
"qr 180,1,25600\nqc 72,132\nqm v,a,v,180,200,232\nqr 184,1,26624\nqm v,a,v,180,204,240\nqr 188,1,27648\n"
"v_mfma_f32_16x16x32_bf16 v[172:175], a[204:207], v[208:211], v[32:35]\nqx 134\nqm v,a,v,172,200,200\nqx 135\nqm v,a,v,172,196,192\ns_mov_b64 exec, 1\n"
"buffer_store_dwordx2 v[250:251], off, s[56:59], s8 sc1\ns_mov_b64 exec, -1\nqm v,a,v,172,192,184\nqw 10\nqx 148\nv_mfma_f32_16x16x32_bf16 v[196:199], a[144:147], v[216:219], v[36:39]\n"
"qr 192,1,28672\nqc 73,134\nqm v,a,v,196,148,224\nqw 9\nqr 196,1,29696\nqm v,a,v,196,152,232\n"
"qr 200,1,30720\nqm v,a,v,196,156,240\nqw 9\nqr 204,1,31744\nv_mfma_f32_16x16x32_bf16 v[212:215], a[160:163], v[216:219], v[40:43]\ns_getpc_b64 s[72:73]\n"
"CT8%=:\ns_add_u32 s72, s72, G2GO%=-CT8%=\ns_addc_u32 s73, s73, 0\nqm v,a,v,212,164,224\nqw 8\ns_lshl2_add_u32 s74, s19, s15\n"
"s_mul_i32 s74, s74, 256\nqm v,a,v,212,168,232\nv_lshl_add_u32 v11, v0, 3, s74\nqm v,a,v,212,172,240\nqw 6\ns_mov_b32 s74, FINISH%=-G2GO%=\n"
"s_mov_b32 s75, 0x20000\nv_mfma_f32_16x16x32_bf16 v[228:231], a[176:179], v[216:219], v[44:47]\nqx 149\nqm v,a,v,228,180,224\nqw 4\nqx 150\n"
"qm v,a,v,228,184,232\nqx 151\nqm v,a,v,228,188,240\nqw 2\nqx 140\nv_mfma_f32_16x16x32_bf16 v[244:247], a[192:195], v[216:219], v[48:51]\n"
"qx 141\nqm v,a,v,244,196,224\nqw 0\nqx 142\nqm v,a,v,244,200,232\nqx 143\n"
"qx 156\nqx 157\nqx 158\nqx 159\nqx 164\nqx 165\n"
"qx 166\nqx 167\nqx 180\nqx 181\nqx 182\nqx 183\n"
"qm v,a,v,244,204,240\ns_bfm_b64 exec, 2, 0\nbuffer_load_dword a255, v11, s[72:75], 0 offen\ns_mov_b64 exec, -1\nqc 74,148\nqc 75,150\n"
"qc 58,140\nqc 59,142\nqc 60,156\nqc 61,158\nqc 76,164\nqc 77,166\n"
"qc 78,180\nqc 79,182\ns_mov_b32 s72, s36\ns_and_b32 s73, s37, 0xffff\ns_mov_b32 s74, 0x400000\ns_mov_b32 s75, 0x20000\n"
"buffer_load_dwordx4 v[20:23], v16, s[72:75], s3 offen offset:0\nbuffer_load_dwordx4 v[24:27], v16, s[72:75], s3 offen offset:64\nbuffer_load_dwordx4 v[28:31], v16, s[72:75], s4 offen offset:0\nbuffer_load_dwordx4 v[32:35], v16, s[72:75], s4 offen offset:64\nqx 172\nqx 173\n"
"qx 174\nqx 175\nqc 62,172\nqc 63,174\nqx 196\nqx 197\n"
"qx 198\nqx 199\nqc 80,196\nqc 81,198\nqx 212\nqx 213\n"
"qx 214\nqw 0\ns_barrier\nqr 144,2,0\nqr 148,2,8192\nqr 152,2,16384\n"
"qr 156,2,24576\nqr 160,2,1024\nqr 164,2,9216\nqx 215\nqc 82,212\nqc 83,214\n"
"qx 228\nqx 229\nqx 230\nqx 231\nqc 84,228\nqc 85,230\n"
"qx 244\nqx 245\nqx 246\nqx 247\nqc 86,244\nqc 87,246\n"
"FINE%=:\nqz a,v,v,136,252,56\nqr 168,2,17408\nv_mbcnt_lo_u32_b32 v8, -1, 0\nqm a,v,v,136,252,60\nqr 172,2,25600\n"
"v_mbcnt_hi_u32_b32 v8, -1, v8\nqz a,v,v,140,252,72\nbuffer_load_dwordx2 v[248:249], v19, s[56:59], s5 offen sc1\nqm a,v,v,140,252,76\nqr 176,2,2048\nv_lshrrev_b32 v9, 4, v8\n"
"qm a,v,v,140,252,80\nqr 180,2,10240\nv_bfe_u32 v10, v8, 2, 2\nqm a,v,v,140,252,84\nqw 8\nqr 184,2,18432\n"
"v_lshl_add_u32 v11, v9, 2, v10\nqz a,a,v,72,144,56\nqr 188,2,26624\nv_readlane_b32 s2, v18, 2\nqm a,a,v,72,148,60\nqr 192,2,3072\n"
"v_and_b32 v9, 7, v11\nqz a,a,v,76,148,76\ns_add_u32 m0, s0, 0x8000\nqr 196,2,11264\nv_and_b32 v10, 3, v8\nqm a,a,v,76,144,72\n"
"qw 10\nqr 200,2,19456\nv_lshlrev_b32 v10, 3, v10\nqm a,a,v,76,152,80\nqd 12,60,2,0\nqm a,a,v,76,156,84\n"
"qw 9\nqr 204,2,27648\nv_lshl_add_u32 v10, v11, 8, v10\nqz a,a,v,80,160,56\nqd 13,60,2,1024\nqm a,a,v,80,164,60\n"
"qr 144,2,4096\nv_lshl_add_u32 v10, v9, 5, v10\nqz a,a,v,84,164,76\nqd 14,60,2,2048\nqm a,a,v,84,160,72\nqw 9\n"
"qr 148,2,12288\nv_mov_b32 v36, v10\nqm a,a,v,84,168,80\nqr 152,2,20480\nv_xor_b32 v37, 32, v10\nqm a,a,v,84,172,84\n"
"qw 9\nqr 156,2,28672\nv_xor_b32 v38, 64, v10\nqz a,a,v,88,176,56\nqd 15,60,2,3072\nqm a,a,v,88,180,60\n"
"qr 160,2,5120\nv_xor_b32 v39, 96, v10\nqz a,a,v,92,180,76\nqr 164,2,13312\nv_xor_b32 v40, 128, v10\nqm a,a,v,92,176,72\n"
"qw 10\nqr 168,2,21504\nv_xor_b32 v41, 160, v10\nqm a,a,v,92,184,80\nqr 172,2,29696\nv_xor_b32 v42, 192, v10\n"
"qm a,a,v,92,188,84\nqw 10\nqr 176,2,6144\nv_xor_b32 v43, 224, v10\nqz a,a,v,96,192,56\nqv 7\n"
"qr 180,2,14336\nqm a,a,v,96,196,60\nqr 184,2,22528\nqz a,a,v,100,196,76\nqr 188,2,30720\nqm a,a,v,100,192,72\n"
"qw 12\nv_permlane16_swap_b32 v20, v22\nqm a,a,v,100,200,80\nqr 192,2,7168\nqm a,a,v,100,204,84\nqw 11\n"
"qr 196,2,15360\nqz a,a,v,104,144,56\nqr 200,2,23552\nqm a,a,v,104,148,60\nqr 204,2,31744\nqz a,a,v,108,148,76\n"
"v_permlane16_swap_b32 v21, v23\nqm a,a,v,108,144,72\nqw 12\nv_permlane16_swap_b32 v24, v26\nqm a,a,v,108,152,80\nqv 4\n"
"v_cmp_eq_u64 vcc, s[16:17], v[248:249]\nqm a,a,v,108,156,84\nqw 10\nv_permlane16_swap_b32 v25, v27\nqz a,a,v,112,160,56\nv_permlane16_swap_b32 v28, v30\n"
"qm a,a,v,112,164,60\nv_permlane16_swap_b32 v29, v31\nqz a,a,v,116,164,76\ns_cmp_eq_u64 vcc, exec\ns_cbranch_scc0 PEFINE_o%=\nPEFINE_r%=:\n"
"qm a,a,v,116,160,72\nqw 8\nv_permlane16_swap_b32 v32, v34\nqm a,a,v,116,168,80\nv_readlane_b32 s1, v17, 0\nqm a,a,v,116,172,84\n"
"qw 7\nv_permlane16_swap_b32 v33, v35\nqz a,a,v,120,176,56\nqw 0\ns_barrier\nqt 144,36,0\n"
"qm a,a,v,120,180,60\nqt 146,36,4096\nv_lshlrev_b32 v88, 16, v20\nqz a,a,v,124,180,76\nqt 148,36,8192\nv_and_b32 v89, 0xffff0000, v20\n"
"qm a,a,v,124,176,72\nqt 150,36,12288\nbuffer_load_dwordx4 v[152:155], v0, s[64:67], s1 offen offset:0 sc1\nbuffer_load_dwordx4 v[156:159], v0, s[64:67], s1 offen offset:1024 sc1\nbuffer_load_dwordx4 v[160:163], v0, s[68:71], s1 offen offset:0 sc1\nbuffer_load_dwordx4 v[164:167], v0, s[68:71], s1 offen offset:1024 sc1\n"
"v_readlane_b32 s1, v17, 1\nqm a,a,v,124,184,80\nqt 152,37,0\nv_lshlrev_b32 v90, 16, v21\nqm a,a,v,124,188,84\nqt 154,37,4096\n"
"v_and_b32 v91, 0xffff0000, v21\nqz a,a,v,128,192,56\nqt 156,37,8192\nv_lshlrev_b32 v92, 16, v22\nqm a,a,v,128,196,60\nqt 158,37,12288\n"
"v_and_b32 v93, 0xffff0000, v22\nqz a,a,v,132,196,76\nqt 160,38,0\nv_lshlrev_b32 v94, 16, v23\nqm a,a,v,132,192,72\nqt 162,38,4096\n"
"v_and_b32 v95, 0xffff0000, v23\nqm a,a,v,132,200,80\nqt 164,38,8192\nbuffer_load_dwordx4 v[168:171], v0, s[64:67], s1 offen offset:0 sc1\nv_lshlrev_b32 v96, 16, v24\nv_and_b32 v97, 0xffff0000, v24\n"
"v_lshlrev_b32 v98, 16, v25\nv_and_b32 v99, 0xffff0000, v25\nv_lshlrev_b32 v100, 16, v26\nv_and_b32 v101, 0xffff0000, v26\nv_lshlrev_b32 v102, 16, v27\nv_and_b32 v103, 0xffff0000, v27\n"
"v_lshlrev_b32 v104, 16, v28\nv_and_b32 v105, 0xffff0000, v28\nv_lshlrev_b32 v106, 16, v29\nv_and_b32 v107, 0xffff0000, v29\nv_lshlrev_b32 v108, 16, v30\nv_and_b32 v109, 0xffff0000, v30\n"
"v_lshlrev_b32 v110, 16, v31\nv_and_b32 v111, 0xffff0000, v31\nv_lshlrev_b32 v112, 16, v32\nv_and_b32 v113, 0xffff0000, v32\nv_lshlrev_b32 v114, 16, v33\nv_and_b32 v115, 0xffff0000, v33\n"
"v_lshlrev_b32 v116, 16, v34\nv_and_b32 v117, 0xffff0000, v34\nv_lshlrev_b32 v118, 16, v35\nv_and_b32 v119, 0xffff0000, v35\nqm a,a,v,132,204,84\nqt 166,38,12288\n"
"buffer_load_dwordx4 v[172:175], v0, s[64:67], s1 offen offset:1024 sc1\nbuffer_load_dwordx4 v[176:179], v0, s[68:71], s1 offen offset:0 sc1\nbuffer_load_dwordx4 v[180:183], v0, s[68:71], s1 offen offset:1024 sc1\ns_branch G2GO%=\nEL%=:\ns_lshl_b32 s6, s88, 17\n"
"s_lshl_b32 s7, s20, 12\ns_add_u32 s6, s6, s7\ns_add_u32 s6, s6, 0x2800000\ns_xor_b32 s7, s6, 0xe0000\nqa 6,64\nqa 7,68\n"
"qp 6\nqp 7\ns_lshr_b32 s8, s88, 3\ns_lshl2_add_u32 s8, s8, s15\ns_lshl_b32 s8, s8, 8\ns_lshl3_add_u32 s8, s20, s8\n"
"s_add_u32 s8, s8, 0x5000\nqa 120,0\nqa 121,1\nqa 122,2\nqa 123,3\nqa 128,8\n"
"qa 129,9\nqa 130,10\nqa 131,11\nv_pk_mul_f32 v[120:121], v[120:121], v[6:7] op_sel:[0,0] op_sel_hi:[1,0]\nv_pk_mul_f32 v[122:123], v[122:123], v[6:7] op_sel:[0,0] op_sel_hi:[1,0]\nv_pk_mul_f32 v[128:129], v[128:129], v[6:7] op_sel:[0,0] op_sel_hi:[1,0]\n"
"v_pk_mul_f32 v[130:131], v[130:131], v[6:7] op_sel:[0,0] op_sel_hi:[1,0]\nqc 120,120\nqc 121,122\nqc 122,128\nqc 123,130\nbuffer_store_dwordx4 v[120:123], v0, s[56:59], s6 offen offset:0 sc1\n"
"qa 136,4\nqa 137,5\nqa 138,6\nqa 139,7\nqa 144,12\nqa 145,13\n"
"qa 146,14\nqa 147,15\nv_pk_mul_f32 v[136:137], v[136:137], v[6:7] op_sel:[0,1] op_sel_hi:[1,1]\nv_pk_mul_f32 v[138:139], v[138:139], v[6:7] op_sel:[0,1] op_sel_hi:[1,1]\nv_pk_mul_f32 v[144:145], v[144:145], v[6:7] op_sel:[0,1] op_sel_hi:[1,1]\nv_pk_mul_f32 v[146:147], v[146:147], v[6:7] op_sel:[0,1] op_sel_hi:[1,1]\n"
"qc 136,136\nqc 137,138\nqc 138,144\nqc 139,146\nbuffer_store_dwordx4 v[136:139], v0, s[56:59], s7 offen offset:0 sc1\nqa 152,16\n"
"qa 153,17\nqa 154,18\nqa 155,19\nqa 160,24\nqa 161,25\nqa 162,26\n"
"qa 163,27\nv_pk_mul_f32 v[152:153], v[152:153], v[6:7] op_sel:[0,0] op_sel_hi:[1,0]\nv_pk_mul_f32 v[154:155], v[154:155], v[6:7] op_sel:[0,0] op_sel_hi:[1,0]\nv_pk_mul_f32 v[160:161], v[160:161], v[6:7] op_sel:[0,0] op_sel_hi:[1,0]\nv_pk_mul_f32 v[162:163], v[162:163], v[6:7] op_sel:[0,0] op_sel_hi:[1,0]\nqc 152,152\n"
"qc 153,154\nqc 154,160\nqc 155,162\nbuffer_store_dwordx4 v[152:155], v0, s[56:59], s6 offen offset:1024 sc1\nqa 168,20\nqa 169,21\n"
"qa 170,22\nqa 171,23\nqa 176,28\nqa 177,29\nqa 178,30\nqa 179,31\n"
"v_pk_mul_f32 v[168:169], v[168:169], v[6:7] op_sel:[0,1] op_sel_hi:[1,1]\nv_pk_mul_f32 v[170:171], v[170:171], v[6:7] op_sel:[0,1] op_sel_hi:[1,1]\nv_pk_mul_f32 v[176:177], v[176:177], v[6:7] op_sel:[0,1] op_sel_hi:[1,1]\nv_pk_mul_f32 v[178:179], v[178:179], v[6:7] op_sel:[0,1] op_sel_hi:[1,1]\nqc 168,168\nqc 169,170\n"
"qc 170,176\nqc 171,178\nbuffer_store_dwordx4 v[168:171], v0, s[56:59], s7 offen offset:1024 sc1\nqa 120,32\nqa 121,33\nqa 122,34\n"
"qa 123,35\nqa 128,40\nqa 129,41\nqa 130,42\nqa 131,43\nv_pk_mul_f32 v[120:121], v[120:121], v[6:7] op_sel:[0,0] op_sel_hi:[1,0]\n"
"v_pk_mul_f32 v[122:123], v[122:123], v[6:7] op_sel:[0,0] op_sel_hi:[1,0]\nv_pk_mul_f32 v[128:129], v[128:129], v[6:7] op_sel:[0,0] op_sel_hi:[1,0]\nv_pk_mul_f32 v[130:131], v[130:131], v[6:7] op_sel:[0,0] op_sel_hi:[1,0]\nqc 120,120\nqc 121,122\nqc 122,128\n"
"qc 123,130\nbuffer_store_dwordx4 v[120:123], v0, s[56:59], s6 offen offset:2048 sc1\nqa 136,36\nqa 137,37\nqa 138,38\nqa 139,39\n"
"qa 144,44\nqa 145,45\nqa 146,46\nqa 147,47\nv_pk_mul_f32 v[136:137], v[136:137], v[6:7] op_sel:[0,1] op_sel_hi:[1,1]\nv_pk_mul_f32 v[138:139], v[138:139], v[6:7] op_sel:[0,1] op_sel_hi:[1,1]\n"
"v_pk_mul_f32 v[144:145], v[144:145], v[6:7] op_sel:[0,1] op_sel_hi:[1,1]\nv_pk_mul_f32 v[146:147], v[146:147], v[6:7] op_sel:[0,1] op_sel_hi:[1,1]\nqc 136,136\nqc 137,138\nqc 138,144\nqc 139,146\n"
"buffer_store_dwordx4 v[136:139], v0, s[56:59], s7 offen offset:2048 sc1\nqa 152,48\nqa 153,49\nqa 154,50\nqa 155,51\nqa 160,56\n"
"qa 161,57\nqa 162,58\nqa 163,59\nv_pk_mul_f32 v[152:153], v[152:153], v[6:7] op_sel:[0,0] op_sel_hi:[1,0]\nv_pk_mul_f32 v[154:155], v[154:155], v[6:7] op_sel:[0,0] op_sel_hi:[1,0]\nv_pk_mul_f32 v[160:161], v[160:161], v[6:7] op_sel:[0,0] op_sel_hi:[1,0]\n"
"v_pk_mul_f32 v[162:163], v[162:163], v[6:7] op_sel:[0,0] op_sel_hi:[1,0]\nqc 152,152\nqc 153,154\nqc 154,160\nqc 155,162\nbuffer_store_dwordx4 v[152:155], v0, s[56:59], s6 offen offset:3072 sc1\n"
"qa 168,52\nqa 169,53\nqa 170,54\nqa 171,55\nqa 176,60\nqa 177,61\n"
"qa 178,62\nqa 179,63\nv_pk_mul_f32 v[168:169], v[168:169], v[6:7] op_sel:[0,1] op_sel_hi:[1,1]\nv_pk_mul_f32 v[170:171], v[170:171], v[6:7] op_sel:[0,1] op_sel_hi:[1,1]\nv_pk_mul_f32 v[176:177], v[176:177], v[6:7] op_sel:[0,1] op_sel_hi:[1,1]\nv_pk_mul_f32 v[178:179], v[178:179], v[6:7] op_sel:[0,1] op_sel_hi:[1,1]\n"
"qc 168,168\nqc 169,170\nqc 170,176\nqc 171,178\nbuffer_store_dwordx4 v[168:171], v0, s[56:59], s7 offen offset:3072 sc1\nqz v,a,v,124,144,184\n"
"s_add_u32 s77, s85, 0x1000000\ns_or_b32 m0, s34, 0x10000\ns_add_u32 s34, s34, 0x4000\nqm v,a,v,124,148,192\nqw 2\nqr 168,1,6144\n"
"qm v,a,v,124,152,200\ns_xor_b32 s79, s79, 0x8000\nqr 172,1,7168\nqm v,a,v,124,156,208\nqe 0,56,77,0\nqz v,a,v,132,156,240\n"
"qr 176,1,8192\nqm v,a,v,132,152,232\nqr 180,1,9216\nqm v,a,v,132,148,224\nqr 184,1,10240\nqm v,a,v,132,144,216\n"
"qw 5\nqr 188,1,11264\nqz v,a,v,148,160,216\nqx 124\nqx 125\nqx 126\n"
"qx 127\nqm v,a,v,148,164,224\nqw 4\nqc 56,124\nqc 57,126\nqm v,a,v,148,168,232\n"
"qe 0,56,77,1024\nqm v,a,v,148,172,240\nqe 0,56,77,2048\nqz v,a,v,140,172,208\nqx 132\nqx 133\n"
"qx 134\nqx 135\nqm v,a,v,140,168,200\nqr 192,1,12288\nqc 72,132\nqm v,a,v,140,164,192\n"
"qr 196,1,13312\nqc 73,134\nqm v,a,v,140,160,184\nqw 4\nqr 200,1,14336\nqz v,a,v,156,176,184\n"
"qx 148\nqx 149\nqx 150\nqx 151\nqm v,a,v,156,180,192\nqw 3\n"
"qr 204,1,15360\nqc 74,148\nqm v,a,v,156,184,200\nqe 0,56,77,3072\nqm v,a,v,156,188,208\ns_and_b32 s34, s34, 0xffff\n"
"s_add_u32 s77, s77, 0x4000\ns_or_b32 m0, s34, 0x10000\ns_add_u32 s34, s34, 0x4000\nqz v,a,v,164,188,240\nqx 140\nqx 141\n"
"qx 142\nqx 143\nqm v,a,v,164,184,232\nqr 144,1,16384\nqc 75,150\nqm v,a,v,164,180,224\n"
"qe 0,56,77,0\nqm v,a,v,164,176,216\nqw 3\nqc 58,140\nqc 59,142\nqz v,a,v,180,192,216\n"
"qv 5\nqx 156\nqx 157\nqm v,a,v,180,196,224\nqw 1\nqx 158\n"
"qm v,a,v,180,200,232\nqr 148,1,17408\nqc 60,156\nqm v,a,v,180,204,240\nqr 152,1,18432\nqx 159\n"
"qz v,a,v,172,204,208\nqr 156,1,19456\nqc 61,158\nqm v,a,v,172,200,200\nqx 164\nqm v,a,v,172,196,192\n"
"qx 165\nqm v,a,v,172,192,184\nqw 2\ns_mov_b64 exec, 1\nbuffer_store_dwordx2 v[250:251], off, s[56:59], s8 sc1\ns_mov_b64 exec, -1\n"
"qz v,a,v,188,144,184\nqr 160,1,20480\nqc 76,164\nqm v,a,v,188,148,192\nqw 1\nqx 166\n"
"qm v,a,v,188,152,200\nqr 164,1,21504\nqm v,a,v,188,156,208\nqr 168,1,22528\nqx 167\nqz v,a,v,196,156,240\n"
"qe 0,56,77,1024\nqr 172,1,23552\nqm v,a,v,196,152,232\nqx 180\nqm v,a,v,196,148,224\nqx 181\n"
"qm v,a,v,196,144,216\nqw 2\nqr 176,1,24576\nqc 77,166\nqz v,a,v,212,160,216\nqe 0,56,77,2048\n"
"qm v,a,v,212,164,224\nqw 1\nqx 182\nqm v,a,v,212,168,232\nqr 180,1,25600\nqc 78,180\n"
"qm v,a,v,212,172,240\nqe 0,56,77,3072\ns_and_b32 s34, s34, 0xffff\ns_add_u32 s77, s77, 0x4000\nv_readlane_b32 s2, v18, 0\nqr 184,1,26624\n"
"qx 183\nqz v,a,v,204,172,208\nqr 188,1,27648\nqc 79,182\nqm v,a,v,204,168,200\ns_add_u32 m0, s0, 0x0\n"
"qx 172\nqm v,a,v,204,164,192\nqx 173\nqm v,a,v,204,160,184\nqw 2\nqr 192,1,28672\n"
"qc 62,172\nqz v,a,v,220,176,184\nqx 174\nqm v,a,v,220,180,192\nqw 1\nqr 196,1,29696\n"
"qm v,a,v,220,184,200\nqr 200,1,30720\nqm v,a,v,220,188,208\nqx 175\nqz v,a,v,228,188,240\nqd 12,60,2,0\n"
"qr 204,1,31744\nqm v,a,v,228,184,232\nqx 188\nqm v,a,v,228,180,224\nqx 189\nqm v,a,v,228,176,216\n"
"qw 2\nqr 144,2,0\nqc 63,174\nqz v,a,v,244,192,216\nqx 190\nqm v,a,v,244,196,224\n"
"qw 1\nqr 148,2,8192\nqc 64,188\nqm v,a,v,244,200,232\nqd 13,60,2,1024\nqm v,a,v,244,204,240\n"
"qx 191\nqz v,a,v,236,204,208\nqr 152,2,16384\nqc 65,190\nqm v,a,v,236,200,200\nqx 196\n"
"qm v,a,v,236,196,192\nqr 156,2,24576\nqm v,a,v,236,192,184\nqw 2\nqd 14,60,2,2048\nqx 197\n"
"qz a,a,v,72,144,88\nqr 160,2,1024\nqc 80,196\nqm a,a,v,72,148,92\nqd 15,60,2,3072\nqr 164,2,9216\n"
"v_readlane_b32 s2, v18, 1\nqx 198\nqz a,a,v,76,148,108\nqx 199\nqm a,a,v,76,144,104\ns_add_u32 m0, s0, 0x4000\n"
"qw 2\nqr 168,2,17408\nqm a,a,v,76,152,112\nqr 172,2,25600\nqc 81,198\nqm a,a,v,76,156,116\n"
"qx 212\nqz a,v,v,136,252,88\nqw 2\nqx 213\nqz a,a,v,80,160,88\nqr 176,2,2048\n"
"qc 82,212\nqm a,a,v,80,164,92\nqr 180,2,10240\nqz a,a,v,84,164,108\nqx 214\nqm a,a,v,84,160,104\n"
"qw 2\nqd 12,60,2,0\nqm a,a,v,84,168,112\nqr 184,2,18432\nqx 215\nqm a,a,v,84,172,116\n"
"qr 188,2,26624\nqc 83,214\nqm a,v,v,136,252,92\nqw 2\nqr 192,2,3072\nqx 204\n"
"qz a,a,v,88,176,88\nqr 196,2,11264\nqm a,a,v,88,180,92\nqx 205\nqz a,a,v,92,180,108\nqd 13,60,2,1024\n"
"qm a,a,v,92,176,104\nqw 2\nqr 200,2,19456\nqx 206\nqm a,a,v,92,184,112\nqr 204,2,27648\n"
"qc 66,204\nqm a,a,v,92,188,116\nqw 2\nqr 144,2,4096\nqx 207\nqz a,a,v,96,192,88\n"
"qr 148,2,12288\nqc 67,206\nqm a,a,v,96,196,92\nqd 14,60,2,2048\nqx 220\nqz a,a,v,100,196,108\n"
"qx 221\nqm a,a,v,100,192,104\nqw 2\nqr 152,2,20480\nqc 68,220\nqm a,a,v,100,200,112\n"
"qr 156,2,28672\nqm a,a,v,100,204,116\nqw 2\nqx 222\nqz a,a,v,104,144,88\nqr 160,2,5120\n"
"qm a,a,v,104,148,92\nqd 15,60,2,3072\nqx 223\ns_mov_b32 s72, s36\ns_and_b32 s73, s37, 0xffff\ns_mov_b32 s74, 0x400000\n"
"s_mov_b32 s75, 0x20000\nqr 164,2,13312\nqz a,a,v,108,148,108\nqx 228\nqm a,a,v,108,144,104\nqw 2\n"
"qr 168,2,21504\nqc 69,222\nqm a,a,v,108,152,112\nqx 229\nqm a,a,v,108,156,116\nqr 172,2,29696\n"
"qc 84,228\nqz a,v,v,140,252,104\nqw 2\nqx 230\nqz a,a,v,112,160,88\nqr 176,2,6144\n"
"qm a,a,v,112,164,92\nqr 180,2,14336\nbuffer_load_dwordx4 v[20:23], v16, s[72:75], s3 offen offset:0\nqz a,a,v,116,164,108\nqr 184,2,22528\nqr 188,2,30720\n"
"qr 192,2,7168\nqr 196,2,15360\nqr 200,2,23552\nqr 204,2,31744\nqx 231\nqm a,a,v,116,160,104\n"
"qw 8\nqx 244\nqm a,a,v,116,168,112\nqx 245\nqm a,a,v,116,172,116\nqc 85,230\n"
"v_add_u32 v2, s79, v3\nqm a,v,v,140,252,108\nqw 6\nqx 246\nqz a,a,v,120,176,88\nbuffer_load_dwordx4 v[24:27], v16, s[72:75], s3 offen offset:64\n"
"qm a,a,v,120,180,92\nqw 0\nqv 10\ns_barrier\nqz a,a,v,124,180,108\nqx 247\n"
"qm a,a,v,124,176,104\nqr 144,2,0\nqc 86,244\nqm a,a,v,124,184,112\nqx 236\nqm a,a,v,124,188,116\n"
"qx 237\nqm a,v,v,140,252,112\nqr 148,2,8192\nqc 87,246\nqz a,a,v,128,192,88\nbuffer_load_dwordx4 v[28:31], v16, s[72:75], s4 offen offset:0\n"
"qm a,a,v,128,196,92\nqx 238\nqz a,a,v,132,196,108\nqr 152,2,16384\nqc 70,236\nqm a,a,v,132,192,104\n"
"qr 156,2,24576\nqm a,a,v,132,200,112\nqr 160,2,1024\nqx 239\nqm a,a,v,132,204,116\nqr 164,2,9216\n"
"qc 71,238\nqm a,v,v,140,252,116\nbuffer_load_dwordx4 v[32:35], v16, s[72:75], s4 offen offset:64\ns_branch FIN%=\nL2L%=:\nqz v,a,v,124,144,184\n"
"s_add_u32 s77, s85, 0x1000000\ns_or_b32 m0, s34, 0x10000\ns_add_u32 s34, s34, 0x4000\nqm v,a,v,124,148,192\nqw 2\nqr 168,1,6144\n"
"qm v,a,v,124,152,200\ns_xor_b32 s79, s79, 0x8000\nqr 172,1,7168\nqm v,a,v,124,156,208\nqe 0,56,77,0\nqz v,a,v,132,156,240\n"
"qr 176,1,8192\nqm v,a,v,132,152,232\nqr 180,1,9216\nqm v,a,v,132,148,224\nqr 184,1,10240\nqm v,a,v,132,144,216\n"
"qw 5\nqr 188,1,11264\nqz v,a,v,148,160,216\nqx 124\nqm v,a,v,148,164,224\nqw 4\n"
"qx 125\nqm v,a,v,148,168,232\nqx 126\nqm v,a,v,148,172,240\nqe 0,56,77,1024\nqz v,a,v,140,172,208\n"
"qx 127\nqm v,a,v,140,168,200\nqr 192,1,12288\nqc 56,124\nqm v,a,v,140,164,192\nqx 132\n"
"qm v,a,v,140,160,184\nqw 3\nqx 133\nqz v,a,v,156,176,184\nqr 196,1,13312\nqc 57,126\n"
"qm v,a,v,156,180,192\nqw 2\nqr 200,1,14336\nqc 72,132\nqm v,a,v,156,184,200\nqe 0,56,77,2048\n"
"qm v,a,v,156,188,208\nqx 134\nqz v,a,v,164,188,240\nqr 204,1,15360\nqm v,a,v,164,184,232\nqx 135\n"
"qm v,a,v,164,180,224\nqx 148\nqm v,a,v,164,176,216\nqw 2\nqr 144,1,16384\nqc 73,134\n"
"qz v,a,v,180,192,216\nqx 149\nqm v,a,v,180,196,224\nqw 1\nqr 148,1,17408\nqc 74,148\n"
"qm v,a,v,180,200,232\nqe 0,56,77,3072\ns_and_b32 s34, s34, 0xffff\ns_add_u32 s77, s77, 0x4000\ns_or_b32 m0, s34, 0x10000\ns_add_u32 s34, s34, 0x4000\n"
"qm v,a,v,180,204,240\nqr 152,1,18432\nqx 150\nqz v,a,v,172,204,208\nqr 156,1,19456\nqm v,a,v,172,200,200\n"
"qx 151\nqm v,a,v,172,196,192\nqx 140\nqm v,a,v,172,192,184\nqw 2\nqr 160,1,20480\n"
"qc 75,150\nqz v,a,v,188,144,184\nqe 0,56,77,0\nqm v,a,v,188,148,192\nqw 1\nqx 141\n"
"qm v,a,v,188,152,200\nqr 164,1,21504\nqc 58,140\nqm v,a,v,188,156,208\nqr 168,1,22528\nqx 142\n"
"qz v,a,v,196,156,240\nqr 172,1,23552\nqm v,a,v,196,152,232\nqx 143\nqm v,a,v,196,148,224\nqx 156\n"
"qm v,a,v,196,144,216\nqw 2\nqr 176,1,24576\nqc 59,142\nqz v,a,v,212,160,216\nqe 0,56,77,1024\n"
"qm v,a,v,212,164,224\nqw 1\nqx 157\nqm v,a,v,212,168,232\nqr 180,1,25600\nqc 60,156\n"
"qm v,a,v,212,172,240\nqr 184,1,26624\nqx 158\nqz v,a,v,204,172,208\nqr 188,1,27648\nqm v,a,v,204,168,200\n"
"qx 159\nqm v,a,v,204,164,192\nqe 0,56,77,2048\nqm v,a,v,204,160,184\nqw 2\nqx 164\n"
"qz v,a,v,220,176,184\nqx 165\nqm v,a,v,220,180,192\nqw 0\nqr 192,1,28672\nqc 61,158\n"
"qm v,a,v,220,184,200\nqr 196,1,29696\nqc 76,164\nqm v,a,v,220,188,208\nqe 0,56,77,3072\ns_and_b32 s34, s34, 0xffff\n"
"s_add_u32 s77, s77, 0x4000\nv_readlane_b32 s2, v18, 0\nqx 166\nqr 200,1,30720\nqz v,a,v,228,188,240\nqr 204,1,31744\n"
"qm v,a,v,228,184,232\ns_add_u32 m0, s0, 0x0\nqx 167\nqm v,a,v,228,180,224\nqx 180\nqm v,a,v,228,176,216\n"
"qw 2\nqx 181\nqz v,a,v,244,192,216\nqr 144,2,0\nqc 77,166\nqm v,a,v,244,196,224\n"
"qw 1\nqr 148,2,8192\nqc 78,180\nqm v,a,v,244,200,232\nqx 182\nqm v,a,v,244,204,240\n"
"qr 152,2,16384\nqz v,a,v,236,204,208\nqd 12,60,2,0\nqr 156,2,24576\nqm v,a,v,236,200,200\nqx 183\n"
"qm v,a,v,236,196,192\nqx 172\nqm v,a,v,236,192,184\nqw 2\nqx 173\nqm a,a,v,72,144,88\n"
"qr 160,2,1024\nqc 79,182\nqm a,a,v,72,148,92\nqw 1\nqx 174\nqm a,a,v,72,152,96\n"
"qd 13,60,2,1024\nqm a,a,v,72,156,100\nqr 164,2,9216\nqc 62,172\nqm a,a,v,76,156,116\nqr 168,2,17408\n"
"qm a,a,v,76,152,112\nqr 172,2,25600\nqx 175\nqm a,a,v,76,148,108\nqx 188\nqm a,a,v,76,144,104\n"
"qx 189\nqm a,v,v,136,252,88\nqw 2\nqr 176,2,2048\nqc 63,174\nqm a,a,v,84,160,104\n"
"qd 14,60,2,2048\nqm a,a,v,84,164,108\nqw 1\nqx 190\nqm a,a,v,84,168,112\nqr 180,2,10240\n"
"qc 64,188\nqm a,a,v,84,172,116\nqx 191\nqm a,a,v,80,172,100\nqr 184,2,18432\nqc 65,190\n"
"qm a,a,v,80,168,96\nqr 188,2,26624\nqm a,a,v,80,164,92\nqd 15,60,2,3072\nv_readlane_b32 s2, v18, 1\nqx 196\n"
"qm a,a,v,80,160,88\nqx 197\nqm a,v,v,136,252,92\ns_add_u32 m0, s0, 0x4000\nqw 2\nqr 192,2,3072\n"
"qm a,a,v,88,176,88\nqx 198\nqm a,a,v,88,180,92\nqw 1\nqx 199\nqm a,a,v,88,184,96\n"
"qr 196,2,11264\nqc 80,196\nqm a,a,v,88,188,100\nqr 200,2,19456\nqc 81,198\nqm a,a,v,92,188,116\n"
"qx 212\nqm a,a,v,92,184,112\nqr 204,2,27648\nqm a,a,v,92,180,108\nqd 12,60,2,0\nqm a,a,v,92,176,104\n"
"qx 213\nqm a,v,v,136,252,96\nqw 2\nqx 214\nqm a,a,v,100,192,104\nqx 215\n"
"qm a,a,v,100,196,108\nqw 0\nqr 144,2,4096\nqc 82,212\nqm a,a,v,100,200,112\nqr 148,2,12288\n"
"qc 83,214\nqm a,a,v,100,204,116\nqd 13,60,2,1024\nqm a,a,v,96,204,100\nqr 152,2,20480\nqx 204\n"
"qm a,a,v,96,200,96\nqr 156,2,28672\nqm a,a,v,96,196,92\nqx 205\nqm a,a,v,96,192,88\nqx 206\n"
"qm a,v,v,136,252,100\nqw 2\nqx 207\nqm a,a,v,104,144,88\nqd 14,60,2,2048\nqm a,a,v,104,148,92\n"
"qw 0\nqr 160,2,5120\nqc 66,204\nqm a,a,v,104,152,96\nqx 220\nqm a,a,v,104,156,100\n"
"qr 164,2,13312\nqc 67,206\nqm a,a,v,108,156,116\nqr 168,2,21504\nqm a,a,v,108,152,112\nqr 172,2,29696\n"
"qx 221\nqm a,a,v,108,148,108\nqx 222\nqm a,a,v,108,144,104\nqd 15,60,2,3072\nqm a,v,v,140,252,104\n"
"qw 2\nqx 223\nqm a,a,v,116,160,104\ns_mov_b32 s72, s36\ns_and_b32 s73, s37, 0xffff\ns_mov_b32 s74, 0x400000\n"
"s_mov_b32 s75, 0x20000\nqm a,a,v,116,164,108\nqw 0\nqr 176,2,6144\nqc 68,220\nqm a,a,v,116,168,112\n"
"qx 228\nqm a,a,v,116,172,116\nqr 180,2,14336\nqc 69,222\nqm a,a,v,112,172,100\nqr 184,2,22528\n"
"qx 229\nqm a,a,v,112,168,96\nqr 188,2,30720\nqc 84,228\nqm a,a,v,112,164,92\nqx 230\n"
"qm a,a,v,112,160,88\nbuffer_load_dwordx4 v[20:23], v16, s[72:75], s3 offen offset:0\nqm a,v,v,140,252,108\nqw 2\nqr 192,2,7168\nqr 196,2,15360\n"
"qr 200,2,23552\nqr 204,2,31744\nqx 231\nqm a,a,v,120,176,88\nqx 244\nqm a,a,v,120,180,92\n"
"qw 4\nqx 245\nqm a,a,v,120,184,96\nqc 85,230\nv_add_u32 v2, s79, v3\nqm a,a,v,120,188,100\n"
"qx 246\nqm a,a,v,124,188,116\nbuffer_load_dwordx4 v[24:27], v16, s[72:75], s3 offen offset:64\nqm a,a,v,124,184,112\nqw 0\nqv 10\n"
"s_barrier\nqm a,a,v,124,180,108\nqx 247\nqm a,a,v,124,176,104\nqr 144,2,0\nqc 86,244\n"
"qm a,v,v,140,252,112\nqx 236\nqm a,a,v,132,192,104\nqx 237\nqm a,a,v,132,196,108\nqr 148,2,8192\n"
"qc 87,246\nqm a,a,v,132,200,112\nbuffer_load_dwordx4 v[28:31], v16, s[72:75], s4 offen offset:0\nqm a,a,v,132,204,116\nqx 238\nqm a,a,v,128,204,100\n"
"qr 152,2,16384\nqc 70,236\nqm a,a,v,128,200,96\nqr 156,2,24576\nqm a,a,v,128,196,92\nqr 160,2,1024\n"
"qx 239\nqm a,a,v,128,192,88\nqr 164,2,9216\nqc 71,238\nqm a,v,v,140,252,116\nbuffer_load_dwordx4 v[32:35], v16, s[72:75], s4 offen offset:64\n"
"FIN%=:\nqm a,v,v,136,252,56\nqr 168,2,17408\nv_mbcnt_lo_u32_b32 v8, -1, 0\nqm a,v,v,136,252,60\nqr 172,2,25600\n"
"v_mbcnt_hi_u32_b32 v8, -1, v8\nqm a,v,v,136,252,64\nbuffer_load_dwordx2 v[248:249], v19, s[56:59], s5 offen sc1\nqm a,v,v,136,252,68\nqr 176,2,2048\nv_lshrrev_b32 v9, 4, v8\n"
"qm a,v,v,140,252,72\nqr 180,2,10240\nv_bfe_u32 v10, v8, 2, 2\nqm a,v,v,140,252,76\nqr 184,2,18432\nv_lshl_add_u32 v11, v9, 2, v10\n"
"qm a,v,v,140,252,80\nqr 188,2,26624\nv_readlane_b32 s2, v18, 2\nqm a,v,v,140,252,84\nqw 10\nqr 192,2,3072\n"
"v_and_b32 v9, 7, v11\nqm a,a,v,72,144,56\ns_add_u32 m0, s0, 0x8000\nqr 196,2,11264\nv_and_b32 v10, 3, v8\nqm a,a,v,72,148,60\n"
"qw 10\nqr 200,2,19456\nv_lshlrev_b32 v10, 3, v10\nqm a,a,v,72,152,64\nqd 12,60,2,0\nqm a,a,v,72,156,68\n"
"qr 204,2,27648\nv_lshl_add_u32 v10, v11, 8, v10\nqm a,a,v,76,156,84\nv_lshl_add_u32 v10, v9, 5, v10\nv_mov_b32 v36, v10\nqm a,a,v,76,152,80\n"
"qd 13,60,2,1024\nqm a,a,v,76,148,76\nv_xor_b32 v37, 32, v10\nv_xor_b32 v38, 64, v10\nqm a,a,v,76,144,72\nqw 10\n"
"v_xor_b32 v39, 96, v10\nv_xor_b32 v40, 128, v10\nqm a,a,v,84,160,72\nqr 144,2,4096\nv_xor_b32 v41, 160, v10\nqm a,a,v,84,164,76\n"
"qw 9\nqr 148,2,12288\nv_xor_b32 v42, 192, v10\nqm a,a,v,84,168,80\nqr 152,2,20480\nv_xor_b32 v43, 224, v10\n"
"qm a,a,v,84,172,84\nqd 14,60,2,2048\nqm a,a,v,80,172,68\nqr 156,2,28672\nqm a,a,v,80,168,64\nqm a,a,v,80,164,60\n"
"qm a,a,v,80,160,56\nqw 10\nqm a,a,v,88,176,56\nqd 15,60,2,3072\nqm a,a,v,88,180,60\nqw 8\n"
"qr 160,2,5120\nqm a,a,v,88,184,64\nqr 164,2,13312\nqm a,a,v,88,188,68\nqr 168,2,21504\nqm a,a,v,92,188,84\n"
"qr 172,2,29696\nqm a,a,v,92,184,80\nqm a,a,v,92,180,76\nqm a,a,v,92,176,72\nqw 10\nqm a,a,v,100,192,72\n"
"qr 176,2,6144\nqm a,a,v,100,196,76\nqw 9\nqr 180,2,14336\nqm a,a,v,100,200,80\nqv 4\n"
"qr 184,2,22528\nv_cmp_eq_u64 vcc, s[16:17], v[248:249]\nqm a,a,v,100,204,84\nqr 188,2,30720\nqm a,a,v,96,204,68\nv_permlane16_swap_b32 v20, v22\n"
"qm a,a,v,96,200,64\nv_permlane16_swap_b32 v21, v23\nqm a,a,v,96,196,60\ns_cmp_eq_u64 vcc, exec\ns_cbranch_scc0 PEFIN_o%=\nPEFIN_r%=:\n"
"qm a,a,v,96,192,56\nqw 10\nv_permlane16_swap_b32 v24, v26\nqm a,a,v,104,144,56\nqr 192,2,7168\nv_readlane_b32 s1, v17, 0\n"
"qm a,a,v,104,148,60\nqw 9\nqr 196,2,15360\nqm a,a,v,104,152,64\ns_nop 0\nbuffer_load_dwordx4 v[152:155], v0, s[64:67], s1 offen offset:0 sc1\n"
"qm a,a,v,104,156,68\nqr 200,2,23552\nqm a,a,v,108,156,84\nbuffer_load_dwordx4 v[156:159], v0, s[64:67], s1 offen offset:1024 sc1\nqm a,a,v,108,152,80\nqr 204,2,31744\n"
"qm a,a,v,108,148,76\nv_permlane16_swap_b32 v25, v27\nqm a,a,v,108,144,72\nqw 10\nv_permlane16_swap_b32 v28, v30\nqm a,a,v,116,160,72\n"
"buffer_load_dwordx4 v[160:163], v0, s[68:71], s1 offen offset:0 sc1\nqm a,a,v,116,164,76\nqw 8\nv_permlane16_swap_b32 v29, v31\nqm a,a,v,116,168,80\nv_permlane16_swap_b32 v32, v34\n"
"qm a,a,v,116,172,84\nv_permlane16_swap_b32 v33, v35\nqm a,a,v,112,172,68\nbuffer_load_dwordx4 v[164:167], v0, s[68:71], s1 offen offset:1024 sc1\nqm a,a,v,112,168,64\nv_readlane_b32 s1, v17, 1\n"
"v_lshlrev_b32 v88, 16, v20\nqm a,a,v,112,164,60\nv_and_b32 v89, 0xffff0000, v20\nv_lshlrev_b32 v90, 16, v21\nqm a,a,v,112,160,56\nqw 6\n"
"v_and_b32 v91, 0xffff0000, v21\nv_lshlrev_b32 v92, 16, v22\nqm a,a,v,120,176,56\nv_and_b32 v93, 0xffff0000, v22\nv_lshlrev_b32 v94, 16, v23\nqm a,a,v,120,180,60\n"
"qw 4\nv_and_b32 v95, 0xffff0000, v23\nv_lshlrev_b32 v96, 16, v24\nqm a,a,v,120,184,64\nbuffer_load_dwordx4 v[168:171], v0, s[64:67], s1 offen offset:0 sc1\nqm a,a,v,120,188,68\n"
"qw 0\ns_barrier\nqm a,a,v,124,188,84\nqt 144,36,0\nv_and_b32 v97, 0xffff0000, v24\nqm a,a,v,124,184,80\n"
"qt 146,36,4096\nv_lshlrev_b32 v98, 16, v25\nqm a,a,v,124,180,76\nqt 148,36,8192\nv_and_b32 v99, 0xffff0000, v25\nqm a,a,v,124,176,72\n"
"qt 150,36,12288\nv_lshlrev_b32 v100, 16, v26\nqm a,a,v,132,192,72\nqt 152,37,0\nv_and_b32 v101, 0xffff0000, v26\nqm a,a,v,132,196,76\n"
"qt 154,37,4096\nv_lshlrev_b32 v102, 16, v27\nqm a,a,v,132,200,80\nqt 156,37,8192\nv_and_b32 v103, 0xffff0000, v27\nqm a,a,v,132,204,84\n"
"qt 158,37,12288\nbuffer_load_dwordx4 v[172:175], v0, s[64:67], s1 offen offset:1024 sc1\nqm a,a,v,128,204,68\nqt 160,38,0\nv_lshlrev_b32 v104, 16, v28\nqm a,a,v,128,200,64\n"
"qt 162,38,4096\nv_and_b32 v105, 0xffff0000, v28\nqm a,a,v,128,196,60\nqt 164,38,8192\nv_lshlrev_b32 v106, 16, v29\nv_and_b32 v107, 0xffff0000, v29\n"
"v_lshlrev_b32 v108, 16, v30\nv_and_b32 v109, 0xffff0000, v30\nv_lshlrev_b32 v110, 16, v31\nv_and_b32 v111, 0xffff0000, v31\nv_lshlrev_b32 v112, 16, v32\nv_and_b32 v113, 0xffff0000, v32\n"
"v_lshlrev_b32 v114, 16, v33\nv_and_b32 v115, 0xffff0000, v33\nv_lshlrev_b32 v116, 16, v34\nv_and_b32 v117, 0xffff0000, v34\nv_lshlrev_b32 v118, 16, v35\nv_and_b32 v119, 0xffff0000, v35\n"
"qm a,a,v,128,192,56\nqt 166,38,12288\nbuffer_load_dwordx4 v[176:179], v0, s[68:71], s1 offen offset:0 sc1\nbuffer_load_dwordx4 v[180:183], v0, s[68:71], s1 offen offset:1024 sc1\nG2GO%=:\ns_lshl_b32 s6, s97, 17\n"
"s_lshl_b32 s7, s20, 12\ns_add_u32 s6, s6, s7\ns_add_u32 s6, s6, 0x2800000\ns_xor_b32 s7, s6, 0xe0000\nqa 6,136\nqa 7,140\n"
"qp 6\nqp 7\ns_lshr_b32 s9, s97, 3\ns_lshl2_add_u32 s9, s9, s15\ns_lshl_b32 s9, s9, 8\ns_lshl3_add_u32 s9, s20, s9\n"
"s_add_u32 s9, s9, 0x5000\nv_readlane_b32 s1, v17, 2\ns_nop 4\nbuffer_load_dwordx4 v[184:187], v0, s[64:67], s1 offen offset:0 sc1\nqa 56,72\nqa 57,73\n"
"qa 58,74\nqa 59,75\nqa 60,80\nqa 61,81\nqa 62,82\nqa 63,83\n"
"buffer_load_dwordx4 v[188:191], v0, s[64:67], s1 offen offset:1024 sc1\nv_pk_mul_f32 v[56:57], v[56:57], v[6:7] op_sel:[0,0] op_sel_hi:[1,0]\nv_pk_mul_f32 v[58:59], v[58:59], v[6:7] op_sel:[0,0] op_sel_hi:[1,0]\nv_pk_mul_f32 v[60:61], v[60:61], v[6:7] op_sel:[0,0] op_sel_hi:[1,0]\nv_pk_mul_f32 v[62:63], v[62:63], v[6:7] op_sel:[0,0] op_sel_hi:[1,0]\nqc 56,56\n"
"qc 57,58\nqc 58,60\nqc 59,62\nbuffer_store_dwordx4 v[56:59], v0, s[56:59], s6 offen offset:0 sc1\nbuffer_load_dwordx4 v[192:195], v0, s[68:71], s1 offen offset:0 sc1\nqa 64,76\n"
"qa 65,77\nqa 66,78\nqa 67,79\nqa 68,84\nqa 69,85\nqa 70,86\n"
"qa 71,87\nbuffer_load_dwordx4 v[196:199], v0, s[68:71], s1 offen offset:1024 sc1\nv_readlane_b32 s2, v18, 3\ns_add_u32 m0, s0, 0xc000\nv_pk_mul_f32 v[64:65], v[64:65], v[6:7] op_sel:[0,1] op_sel_hi:[1,1]\nv_pk_mul_f32 v[66:67], v[66:67], v[6:7] op_sel:[0,1] op_sel_hi:[1,1]\n"
"v_pk_mul_f32 v[68:69], v[68:69], v[6:7] op_sel:[0,1] op_sel_hi:[1,1]\nv_pk_mul_f32 v[70:71], v[70:71], v[6:7] op_sel:[0,1] op_sel_hi:[1,1]\nqc 64,64\nqc 65,66\nqc 66,68\nqc 67,70\n"
"buffer_store_dwordx4 v[64:67], v0, s[56:59], s7 offen offset:0 sc1\nqd 12,60,2,0\nqa 72,88\nqa 73,89\nqa 74,90\nqa 75,91\n"
"qa 76,96\nqa 77,97\nqa 78,98\nqa 79,99\nqd 13,60,2,1024\nv_pk_mul_f32 v[72:73], v[72:73], v[6:7] op_sel:[0,0] op_sel_hi:[1,0]\n"
"v_pk_mul_f32 v[74:75], v[74:75], v[6:7] op_sel:[0,0] op_sel_hi:[1,0]\nv_pk_mul_f32 v[76:77], v[76:77], v[6:7] op_sel:[0,0] op_sel_hi:[1,0]\nv_pk_mul_f32 v[78:79], v[78:79], v[6:7] op_sel:[0,0] op_sel_hi:[1,0]\nqc 72,72\nqc 73,74\nqc 74,76\n"
"qc 75,78\nbuffer_store_dwordx4 v[72:75], v0, s[56:59], s6 offen offset:1024 sc1\nqd 14,60,2,2048\nqa 80,92\nqa 81,93\nqa 82,94\n"
"qa 83,95\nqa 84,100\nqa 85,101\nqa 86,102\nqa 87,103\nqd 15,60,2,3072\n"
"v_readlane_b32 s1, v17, 3\nv_pk_mul_f32 v[80:81], v[80:81], v[6:7] op_sel:[0,1] op_sel_hi:[1,1]\nv_pk_mul_f32 v[82:83], v[82:83], v[6:7] op_sel:[0,1] op_sel_hi:[1,1]\nv_pk_mul_f32 v[84:85], v[84:85], v[6:7] op_sel:[0,1] op_sel_hi:[1,1]\nv_pk_mul_f32 v[86:87], v[86:87], v[6:7] op_sel:[0,1] op_sel_hi:[1,1]\nqc 80,80\n"
"qc 81,82\nqc 82,84\nqc 83,86\nbuffer_store_dwordx4 v[80:83], v0, s[56:59], s7 offen offset:1024 sc1\nbuffer_load_dwordx4 v[200:203], v0, s[64:67], s1 offen offset:0 sc1\nqa 56,104\n"
"qa 57,105\nqa 58,106\nqa 59,107\nqa 60,112\nqa 61,113\nqa 62,114\n"
"qa 63,115\nbuffer_load_dwordx4 v[204:207], v0, s[64:67], s1 offen offset:1024 sc1\nv_pk_mul_f32 v[56:57], v[56:57], v[6:7] op_sel:[0,0] op_sel_hi:[1,0]\nv_pk_mul_f32 v[58:59], v[58:59], v[6:7] op_sel:[0,0] op_sel_hi:[1,0]\nv_pk_mul_f32 v[60:61], v[60:61], v[6:7] op_sel:[0,0] op_sel_hi:[1,0]\nv_pk_mul_f32 v[62:63], v[62:63], v[6:7] op_sel:[0,0] op_sel_hi:[1,0]\n"
"qc 56,56\nqc 57,58\nqc 58,60\nqc 59,62\nbuffer_store_dwordx4 v[56:59], v0, s[56:59], s6 offen offset:2048 sc1\nbuffer_load_dwordx4 v[208:211], v0, s[68:71], s1 offen offset:0 sc1\n"
"qa 64,108\nqa 65,109\nqa 66,110\nqa 67,111\nqa 68,116\nqa 69,117\n"
"qa 70,118\nqa 71,119\nbuffer_load_dwordx4 v[212:215], v0, s[68:71], s1 offen offset:1024 sc1\nv_readlane_b32 s2, v18, 4\ns_add_u32 m0, s0, 0x10000\nv_pk_mul_f32 v[64:65], v[64:65], v[6:7] op_sel:[0,1] op_sel_hi:[1,1]\n"
"v_pk_mul_f32 v[66:67], v[66:67], v[6:7] op_sel:[0,1] op_sel_hi:[1,1]\nv_pk_mul_f32 v[68:69], v[68:69], v[6:7] op_sel:[0,1] op_sel_hi:[1,1]\nv_pk_mul_f32 v[70:71], v[70:71], v[6:7] op_sel:[0,1] op_sel_hi:[1,1]\nqc 64,64\nqc 65,66\nqc 66,68\n"
"qc 67,70\nbuffer_store_dwordx4 v[64:67], v0, s[56:59], s7 offen offset:2048 sc1\nqd 12,60,2,0\nqa 72,120\nqa 73,121\nqa 74,122\n"
"qa 75,123\nqa 76,128\nqa 77,129\nqa 78,130\nqa 79,131\nqd 13,60,2,1024\n"
"v_pk_mul_f32 v[72:73], v[72:73], v[6:7] op_sel:[0,0] op_sel_hi:[1,0]\nv_pk_mul_f32 v[74:75], v[74:75], v[6:7] op_sel:[0,0] op_sel_hi:[1,0]\nv_pk_mul_f32 v[76:77], v[76:77], v[6:7] op_sel:[0,0] op_sel_hi:[1,0]\nv_pk_mul_f32 v[78:79], v[78:79], v[6:7] op_sel:[0,0] op_sel_hi:[1,0]\nqc 72,72\nqc 73,74\n"
"qc 74,76\nqc 75,78\nbuffer_store_dwordx4 v[72:75], v0, s[56:59], s6 offen offset:3072 sc1\nqd 14,60,2,2048\nqa 80,124\nqa 81,125\n"
"qa 82,126\nqa 83,127\nqa 84,132\nqa 85,133\nqa 86,134\nqa 87,135\n"
"qd 15,60,2,3072\nv_pk_mul_f32 v[80:81], v[80:81], v[6:7] op_sel:[0,1] op_sel_hi:[1,1]\nv_pk_mul_f32 v[82:83], v[82:83], v[6:7] op_sel:[0,1] op_sel_hi:[1,1]\nv_pk_mul_f32 v[84:85], v[84:85], v[6:7] op_sel:[0,1] op_sel_hi:[1,1]\nv_pk_mul_f32 v[86:87], v[86:87], v[6:7] op_sel:[0,1] op_sel_hi:[1,1]\nqc 80,80\n"
"qc 81,82\nqc 82,84\nqc 83,86\nbuffer_store_dwordx4 v[80:83], v0, s[56:59], s7 offen offset:3072 sc1\nqw 8\nqv 28\n"
"qm v,a,v,88,144,152\nqt 168,39,0\nqt 170,39,4096\nqm v,a,v,88,148,156\nqt 172,39,8192\nqt 174,39,12288\n"
"qm v,a,v,104,148,164\nqt 176,40,0\nqt 178,40,4096\nqm v,a,v,104,144,160\nqw 10\nqt 180,40,8192\n"
"qt 182,40,12288\nqm v,a,v,108,152,160\nqt 184,41,0\nqt 186,41,4096\nqm v,a,v,108,156,164\nqt 188,41,8192\n"
"v_readlane_b32 s2, v18, 5\nqm v,a,v,92,156,156\nqt 190,41,12288\nqm v,a,v,92,152,152\nqw 12\ns_add_u32 m0, s0, 0x14000\n"
"qt 192,42,0\nqm v,a,v,96,160,152\nqt 194,42,4096\nqm v,a,v,96,164,156\nqt 196,42,8192\nqm v,a,v,112,164,164\n"
"qt 198,42,12288\nqm v,a,v,112,160,160\nqw 12\nqt 200,43,0\nqm v,a,v,116,168,160\nqt 202,43,4096\n"
"qm v,a,v,116,172,164\nqt 204,43,8192\nqt 206,43,12288\ns_barrier\nqm v,a,v,100,172,156\nqt 144,36,16384\n"
"qm v,a,v,100,168,152\nqw 13\nqt 146,36,20480\nqz v,a,v,120,176,152\nqt 148,36,24576\nqm v,a,v,120,180,156\n"
"qt 150,36,28672\nqz v,a,v,136,180,164\nqt 152,37,16384\nqm v,a,v,136,176,160\nqw 13\nqt 154,37,20480\n"
"qz v,a,v,140,184,160\nqt 156,37,24576\nqm v,a,v,140,188,164\nqt 158,37,28672\nqz v,a,v,124,188,156\nqt 160,38,16384\n"
"qm v,a,v,124,184,152\nqw 13\nqt 162,38,20480\nqz v,a,v,128,192,152\nqt 164,38,24576\nqm v,a,v,128,196,156\n"
"qt 166,38,28672\nqz v,a,v,144,196,164\nqt 168,39,16384\nqm v,a,v,144,192,160\nqw 13\nqd 12,60,2,0\n"
"qz v,a,v,148,200,160\nqt 170,39,20480\nqm v,a,v,148,204,164\nqt 172,39,24576\nqz v,a,v,132,204,156\nqt 174,39,28672\n"
"qm v,a,v,132,200,152\nqw 12\nqv 25\nqt 176,40,16384\nqd 13,60,2,1024\nqm v,a,v,88,144,168\n"
"s_mov_b32 s76, s48\nqt 178,40,20480\nv_readlane_b32 s1, v17, 4\nqm v,a,v,88,148,172\ns_and_b32 s77, s49, 0xffff\ns_mov_b32 s78, 0x700000\n"
"qt 180,40,24576\nqm v,a,v,104,148,180\nqt 182,40,28672\nbuffer_load_dwordx4 v[216:219], v0, s[64:67], s1 offen offset:0 sc1\nqm v,a,v,104,144,176\nqw 12\n"
"s_mov_b32 s79, 0x20000\nqt 184,41,16384\nqm v,a,v,108,152,176\ns_sub_u32 s83, 7, s21\ns_lshl_b32 s83, s83, 5\nqt 186,41,20480\n"
"qm v,a,v,108,156,180\nqt 188,41,24576\nbuffer_load_dwordx4 v[220:223], v0, s[64:67], s1 offen offset:1024 sc1\nqm v,a,v,92,156,172\ns_add_u32 s12, s83, s20\ns_mul_i32 s80, s12, 2341\n"
"qt 190,41,28672\nqm v,a,v,92,152,168\nqw 12\ns_lshr_b32 s80, s80, 16\nqt 192,42,16384\nqm v,a,v,96,160,168\n"
"qt 194,42,20480\nqd 14,60,2,2048\nqm v,a,v,96,164,172\ns_mul_i32 s81, s80, 28\ns_sub_u32 s81, s12, s81\nqt 196,42,24576\n"
"qm v,a,v,112,164,180\ns_lshl2_add_u32 s82, s80, s15\ns_mul_i32 s82, s82, 458752\nqt 198,42,28672\nqm v,a,v,112,160,176\nqw 12\n"
"qt 200,43,16384\nqd 15,60,2,3072\nqm v,a,v,116,168,176\ns_lshl_b32 s83, s81, 8\ns_add_u32 s82, s82, s83\nqt 202,43,20480\n"
"qm v,a,v,116,172,180\nqt 204,43,24576\nqt 206,43,28672\nqv 19\ns_barrier\nqm v,a,v,100,172,172\n"
"buffer_load_dwordx4 v[224:227], v0, s[68:71], s1 offen offset:0 sc1\nqm v,a,v,100,168,168\nqw 12\nqt 144,36,32768\nv_lshrrev_b32 v46, 8, v0\nqm v,a,v,120,176,168\n"
"qt 146,36,36864\nv_mul_u32_u24 v44, 0x1c00, v46\nqm v,a,v,120,180,172\nqt 148,36,40960\nbuffer_load_dwordx4 v[228:231], v0, s[68:71], s1 offen offset:1024 sc1\nqm v,a,v,136,180,180\n"
"qt 150,36,45056\nv_bfe_u32 v45, v0, 4, 4\nqm v,a,v,136,176,176\nqw 12\nqt 152,37,32768\nv_lshl_add_u32 v44, v45, 4, v44\n"
"qm v,a,v,140,184,176\ns_mul_i32 s83, s80, 14336\ns_lshl_b32 s72, s81, 9\nqt 154,37,36864\nqm v,a,v,140,188,180\ns_add_u32 s83, s83, s72\n"
"qt 156,37,40960\nv_readlane_b32 s1, v17, 5\nqm v,a,v,124,188,172\ns_add_u32 s83, s83, 0x50000\ns_mul_i32 s81, s81, 4\nqt 158,37,45056\n"
"qm v,a,v,124,184,168\nqw 12\ns_add_u32 s81, s81, s80\nqt 160,38,32768\nqm v,a,v,128,192,168\ns_lshl3_add_u32 s81, s81, 0x70000\n"
"s_lshl_b32 s72, s15, 8\nqt 162,38,36864\nqm v,a,v,128,196,172\ns_add_u32 s72, s72, s12\nqt 164,38,40960\nqm v,a,v,144,196,180\n"
"qt 166,38,45056\nqm v,a,v,144,192,176\nqw 12\nqt 168,39,32768\nqm v,a,v,148,200,176\nqt 170,39,36864\n"
"qm v,a,v,148,204,180\nqt 172,39,40960\nqm v,a,v,132,204,172\nqt 174,39,45056\nqm v,a,v,132,200,168\nqw 12\n"
"qt 176,40,32768\nqm v,a,v,88,144,184\nqv 8\nqt 178,40,36864\nqm v,a,v,88,148,188\nqt 180,40,40960\n"
"qm v,a,v,104,148,196\nqt 182,40,45056\nqm v,a,v,104,144,192\nqw 12\nqt 184,41,32768\ns_mov_b64 exec, 1\n"
"buffer_store_dwordx2 v[250:251], off, s[56:59], s9 sc1\ns_mov_b64 exec, -1\nbuffer_load_dwordx4 v[232:235], v0, s[64:67], s1 offen offset:0 sc1\nqm v,a,v,108,152,192\nqt 186,41,36864\nqm v,a,v,108,156,196\n"
"qt 188,41,40960\nbuffer_load_dwordx4 v[236:239], v0, s[64:67], s1 offen offset:1024 sc1\nqm v,a,v,92,156,188\nqt 190,41,45056\nv_readlane_b32 s2, v18, 6\nqm v,a,v,92,152,184\n"
"qw 12\nqt 192,42,32768\nqm v,a,v,96,160,184\ns_add_u32 m0, s0, 0x18000\nqt 194,42,36864\nqm v,a,v,96,164,188\n"
"qt 196,42,40960\nqm v,a,v,112,164,196\nqt 198,42,45056\nqm v,a,v,112,160,192\ns_cmp_lt_u32 s72, 112\ns_cselect_b32 s83, s83, 0x6000000\n"
"s_cselect_b32 s81, s81, 0x6000000\nqw 12\nqt 200,43,32768\nqd 12,60,2,0\nqm v,a,v,116,168,192\nqt 202,43,36864\n"
"v_lshlrev_b32 v46, 4, v46\nqm v,a,v,116,172,196\nqt 204,43,40960\nv_lshl_add_u32 v47, v45, 5, v46\nqm v,a,v,100,172,188\nqt 206,43,45056\n"
"v_mov_b32 v8, 0x3f80\nqm v,a,v,100,168,184\nqw 12\nqt 144,36,49152\nqd 13,60,2,1024\nqm v,a,v,120,176,184\n"
"qt 146,36,53248\nv_mov_b32 v9, 0x3f800000\nqm v,a,v,120,180,188\nqt 148,36,57344\nqm v,a,v,136,180,196\nqt 150,36,61440\n"
"buffer_load_dwordx4 v[240:243], v0, s[68:71], s1 offen offset:0 sc1\nqm v,a,v,136,176,192\nv_add_u32 v36, 0x10000, v36\nv_cmp_eq_u32 vcc, 0, v45\nv_cmp_eq_u32 s[98:99], 1, v45\ns_nop 0\n"
"v_cndmask_b32 v48, 0, v8, vcc\nv_cndmask_b32 v48, v48, v9, s[98:99]\nqt 152,37,49152\nqw 13\nqm v,a,v,140,184,192\nqt 154,37,53248\n"
"v_cmp_eq_u32 vcc, 2, v45\nv_cmp_eq_u32 s[98:99], 3, v45\ns_nop 0\nv_cndmask_b32 v49, 0, v8, vcc\nv_cndmask_b32 v49, v49, v9, s[98:99]\nqm v,a,v,140,188,196\n"
"qt 156,37,57344\nbuffer_load_dwordx4 v[244:247], v0, s[68:71], s1 offen offset:1024 sc1\nqm v,a,v,124,188,188\nqt 158,37,61440\nv_cmp_eq_u32 vcc, 4, v45\nv_cmp_eq_u32 s[98:99], 5, v45\n"
"s_nop 0\nv_cndmask_b32 v50, 0, v8, vcc\nv_cndmask_b32 v50, v50, v9, s[98:99]\nqm v,a,v,124,184,184\nv_add_u32 v37, 0x10000, v37\nv_cmp_eq_u32 vcc, 6, v45\n"
"v_cmp_eq_u32 s[98:99], 7, v45\ns_nop 0\nv_cndmask_b32 v51, 0, v8, vcc\nv_cndmask_b32 v51, v51, v9, s[98:99]\nqt 160,38,49152\nqw 13\n"
"qm v,a,v,128,192,184\nqt 162,38,53248\nqd 14,60,2,2048\nqm v,a,v,128,196,188\nqt 164,38,57344\nv_add_u32 v45, 0x20400, v47\n"
"qm v,a,v,144,196,196\ns_lshl_b32 s72, s15, 9\nqt 166,38,61440\nv_add_u32 v38, 0x10000, v38\nqm v,a,v,144,192,192\nqw 12\n"
"qt 168,39,49152\nqd 15,60,2,3072\nqm v,a,v,148,200,192\nqt 170,39,53248\nv_add_u32 v46, s72, v45\nqm v,a,v,148,204,196\n"
"qt 172,39,57344\nv_readlane_b32 s1, v17, 6\nqm v,a,v,132,204,188\nqt 174,39,61440\nv_add_u32 v39, 0x10000, v39\nqm v,a,v,132,200,184\n"
"qw 12\nqt 176,40,49152\nqm v,a,v,88,144,200\nqt 178,40,53248\nqm v,a,v,88,148,204\nqt 180,40,57344\n"
"buffer_load_dwordx4 v[152:155], v0, s[64:67], s1 offen offset:0 sc1\nbuffer_load_dwordx4 a[0:3], v44, s[76:79], s82 offen\ns_add_u32 s82, s82, 28672\nqm v,a,v,104,148,212\nqt 182,40,61440\nv_add_u32 v40, 0x10000, v40\n"
"qm v,a,v,104,144,208\nbuffer_load_dwordx4 a[4:7], v44, s[76:79], s82 offen\ns_add_u32 s82, s82, 28672\nqt 184,41,49152\nqw 13\nqm v,a,v,108,152,208\n"
"qt 186,41,53248\nqm v,a,v,108,156,212\nbuffer_load_dwordx4 v[156:159], v0, s[64:67], s1 offen offset:1024 sc1\nqt 188,41,57344\nv_readlane_b32 s2, v18, 7\nqm v,a,v,92,156,204\n"
"qt 190,41,61440\nv_add_u32 v41, 0x10000, v41\nqm v,a,v,92,152,200\ns_add_u32 m0, s0, 0x1c000\nqw 12\nqt 192,42,49152\n"
"qm v,a,v,96,160,200\nbuffer_load_dwordx4 a[8:11], v44, s[76:79], s82 offen\ns_add_u32 s82, s82, 28672\nqt 194,42,53248\nqm v,a,v,96,164,204\nqt 196,42,57344\n"
"qd 12,60,2,0\nqm v,a,v,112,164,212\nqt 198,42,61440\nv_add_u32 v42, 0x10000, v42\nqm v,a,v,112,160,208\nqt 200,43,49152\n"
"buffer_load_dwordx4 a[12:15], v44, s[76:79], s82 offen\ns_add_u32 s82, s82, 28672\nqw 13\nqm v,a,v,116,168,208\nqt 202,43,53248\nqm v,a,v,116,172,212\n"
"qt 204,43,57344\nqt 206,43,61440\nv_add_u32 v43, 0x10000, v43\nqv 18\ns_barrier\nqd 13,60,2,1024\n"
"qm v,a,v,100,172,204\nbuffer_load_dwordx4 a[16:19], v44, s[76:79], s82 offen\ns_add_u32 s82, s82, 28672\nqm v,a,v,100,168,200\nqw 12\nqt 144,36,0\n"
"qm v,a,v,120,176,200\nqt 146,36,4096\nbuffer_load_dwordx4 v[160:163], v0, s[68:71], s1 offen offset:0 sc1\nqm v,a,v,120,180,204\nqt 148,36,8192\nqm v,a,v,136,180,212\n"
"qt 150,36,12288\nbuffer_load_dwordx4 a[20:23], v44, s[76:79], s82 offen\ns_add_u32 s82, s82, 28672\nqm v,a,v,136,176,208\nqw 12\nqt 152,37,0\n"
"qm v,a,v,140,184,208\nqt 154,37,4096\nbuffer_load_dwordx4 v[164:167], v0, s[68:71], s1 offen offset:1024 sc1\nqm v,a,v,140,188,212\nqt 156,37,8192\nbuffer_load_dwordx4 a[24:27], v44, s[76:79], s82 offen\n"
"s_add_u32 s82, s82, 28672\nqm v,a,v,124,188,204\nqt 158,37,12288\nqm v,a,v,124,184,200\nqw 12\nqt 160,38,0\n"
"qd 14,60,2,2048\nqm v,a,v,128,192,200\nqt 162,38,4096\nqm v,a,v,128,196,204\nqt 164,38,8192\nbuffer_load_dwordx4 a[28:31], v44, s[76:79], s82 offen\n"
"s_add_u32 s82, s82, 28672\nqm v,a,v,144,196,212\nqt 166,38,12288\nqm v,a,v,144,192,208\nqw 12\nqt 168,39,0\n"
"qd 15,60,2,3072\nv_readlane_b32 s1, v17, 7\nqm v,a,v,148,200,208\nqt 170,39,4096\nqm v,a,v,148,204,212\nqt 172,39,8192\n"
"buffer_load_dwordx4 a[32:35], v44, s[76:79], s82 offen\nqm v,a,v,132,204,204\ns_add_u32 s82, s82, 28672\nqt 174,39,12288\nqm v,a,v,132,200,200\nqw 12\n"
"qv 26\nqt 176,40,0\nqm v,a,v,88,144,216\nqt 178,40,4096\nbuffer_load_dwordx4 a[36:39], v44, s[76:79], s82 offen\nqm v,a,v,88,148,220\n"
"s_add_u32 s82, s82, 28672\nqt 180,40,8192\nqm v,a,v,104,148,228\nqt 182,40,12288\nqm v,a,v,104,144,224\nqw 12\n"
"qt 184,41,0\nbuffer_load_dwordx4 a[40:43], v44, s[76:79], s82 offen\nqm v,a,v,108,152,224\ns_add_u32 s82, s82, 28672\nqt 186,41,4096\nqm v,a,v,108,156,228\n"
"qt 188,41,8192\nqm v,a,v,92,156,220\nqt 190,41,12288\nbuffer_load_dwordx4 a[44:47], v44, s[76:79], s82 offen\nqm v,a,v,92,152,216\ns_add_u32 s82, s82, 28672\n"
"qw 12\nqt 192,42,0\nqm v,a,v,96,160,216\nqt 194,42,4096\nqm v,a,v,96,164,220\nqt 196,42,8192\n"
"buffer_load_dwordx4 a[48:51], v44, s[76:79], s82 offen\nqm v,a,v,112,164,228\ns_add_u32 s82, s82, 28672\nqt 198,42,12288\nqm v,a,v,112,160,224\nqw 12\n"
"qt 200,43,0\nqm v,a,v,116,168,224\nqt 202,43,4096\nbuffer_load_dwordx4 a[52:55], v44, s[76:79], s82 offen\nqm v,a,v,116,172,228\ns_add_u32 s82, s82, 28672\n"
"qt 204,43,8192\nqm v,a,v,100,172,220\nqt 206,43,12288\nqm v,a,v,100,168,216\nqw 12\nqt 144,36,16384\n"
"buffer_load_dwordx4 a[56:59], v44, s[76:79], s82 offen\nqm v,a,v,120,176,216\ns_add_u32 s82, s82, 28672\nqt 146,36,20480\nqm v,a,v,120,180,220\nqt 148,36,24576\n"
"qm v,a,v,136,180,228\nqt 150,36,28672\nbuffer_load_dwordx4 a[60:63], v44, s[76:79], s82 offen\nqm v,a,v,136,176,224\ns_add_u32 s82, s82, 28672\nqw 12\n"
"qt 152,37,16384\nqm v,a,v,140,184,224\nqt 154,37,20480\nqm v,a,v,140,188,228\nqt 156,37,24576\nbuffer_load_dwordx4 v[168:171], v0, s[64:67], s1 offen offset:0 sc1\n"
"qm v,a,v,124,188,220\nqt 158,37,28672\nqm v,a,v,124,184,216\nqw 12\nqt 160,38,16384\nbuffer_load_dwordx4 v[172:175], v0, s[64:67], s1 offen offset:1024 sc1\n"
"v_readlane_b32 s2, v18, 8\nqm v,a,v,128,192,216\nqt 162,38,20480\nqm v,a,v,128,196,220\ns_add_u32 m0, s0, 0x0\nqt 164,38,24576\n"
"qm v,a,v,144,196,228\nqt 166,38,28672\nqm v,a,v,144,192,224\nqw 12\nqt 168,39,16384\nqd 12,60,2,0\n"
"qm v,a,v,148,200,224\nqt 170,39,20480\nqm v,a,v,148,204,228\nqt 172,39,24576\nqm v,a,v,132,204,220\nqt 174,39,28672\n"
"qm v,a,v,132,200,216\nqw 12\nqv 29\nqt 176,40,16384\nqd 13,60,2,1024\nqm v,a,v,88,144,232\n"
"qt 178,40,20480\nqm v,a,v,88,148,236\nqt 180,40,24576\nqm v,a,v,104,148,244\nqt 182,40,28672\nbuffer_load_dwordx4 v[176:179], v0, s[68:71], s1 offen offset:0 sc1\n"
"qm v,a,v,104,144,240\nqw 12\nqt 184,41,16384\nqm v,a,v,108,152,240\nqt 186,41,20480\nqm v,a,v,108,156,244\n"
"qt 188,41,24576\nbuffer_load_dwordx4 v[180:183], v0, s[68:71], s1 offen offset:1024 sc1\nqm v,a,v,92,156,236\nqt 190,41,28672\nqm v,a,v,92,152,232\nqw 12\n"
"qt 192,42,16384\nqm v,a,v,96,160,232\nqt 194,42,20480\nqm v,a,v,96,164,236\nqt 196,42,24576\nqd 14,60,2,2048\n"
"qm v,a,v,112,164,244\nqt 198,42,28672\nqm v,a,v,112,160,240\nqw 12\nqt 200,43,16384\nqd 15,60,2,3072\n"
"v_readlane_b32 s2, v18, 9\nqm v,a,v,116,168,240\nqt 202,43,20480\nqm v,a,v,116,172,244\nqt 204,43,24576\nqt 206,43,28672\n"
"qv 16\ns_barrier\ns_add_u32 m0, s0, 0x4000\nqm v,a,v,100,172,236\nqt 144,36,32768\nqm v,a,v,100,168,232\n"
"qw 13\nqd 12,60,2,0\nqm v,a,v,120,176,232\nqt 146,36,36864\nqm v,a,v,120,180,236\nqt 148,36,40960\n"
"qm v,a,v,136,180,244\nqt 150,36,45056\nqm v,a,v,136,176,240\nqw 12\nqt 152,37,32768\nqd 13,60,2,1024\n"
"qm v,a,v,140,184,240\nqt 154,37,36864\nv_readlane_b32 s1, v17, 8\nqm v,a,v,140,188,244\nqt 156,37,40960\nqm v,a,v,124,188,236\n"
"qt 158,37,45056\ns_nop 0\nbuffer_load_dwordx4 v[184:187], v0, s[64:67], s1 offen offset:0 sc1\nqm v,a,v,124,184,232\nqw 12\nqt 160,38,32768\n"
"qm v,a,v,128,192,232\nqt 162,38,36864\nqm v,a,v,128,196,236\nqt 164,38,40960\nbuffer_load_dwordx4 v[188:191], v0, s[64:67], s1 offen offset:1024 sc1\nqm v,a,v,144,196,244\n"
"qt 166,38,45056\nqm v,a,v,144,192,240\nqw 12\nqt 168,39,32768\nqm v,a,v,148,200,240\nqt 170,39,36864\n"
"qm v,a,v,148,204,244\nqt 172,39,40960\nqd 14,60,2,2048\nqm v,a,v,132,204,236\nqt 174,39,45056\nqm v,a,v,132,200,232\n"
"qw 12\nqt 176,40,32768\nqm v,a,v,88,144,152\nqt 178,40,36864\nqd 15,60,2,3072\nqm v,a,v,88,148,156\n"
"qt 180,40,40960\nqm v,a,v,104,148,164\nqt 182,40,45056\nqm v,a,v,104,144,160\nqw 12\nqt 184,41,32768\n"
"buffer_load_dwordx4 v[192:195], v0, s[68:71], s1 offen offset:0 sc1\nqm v,a,v,108,152,160\nqt 186,41,36864\nqm v,a,v,108,156,164\nqt 188,41,40960\nqm v,a,v,92,156,156\n"
"qt 190,41,45056\nqm v,a,v,92,152,152\nqw 12\nqt 192,42,32768\nbuffer_load_dwordx4 v[196:199], v0, s[68:71], s1 offen offset:1024 sc1\nqm v,a,v,96,160,152\n"
"qt 194,42,36864\nv_readlane_b32 s1, v17, 9\nqm v,a,v,96,164,156\nqt 196,42,40960\nqm v,a,v,112,164,164\nqt 198,42,45056\n"
"qm v,a,v,112,160,160\nqw 12\nqt 200,43,32768\nqm v,a,v,116,168,160\nqt 202,43,36864\nqm v,a,v,116,172,164\n"
"qt 204,43,40960\nqm v,a,v,100,172,156\nqt 206,43,45056\nqm v,a,v,100,168,152\nqw 12\nqt 144,36,49152\n"
"qm v,a,v,120,176,152\nbuffer_load_dwordx4 v[200:203], v0, s[64:67], s1 offen offset:0 sc1\nqm v,a,v,120,180,156\nqt 146,36,53248\nqm v,a,v,136,180,164\nqt 148,36,57344\n"
"qm v,a,v,136,176,160\nqw 11\nqt 150,36,61440\nv_add_u32 v36, 0xffff0000, v36\nqm v,a,v,140,184,160\nbuffer_load_dwordx4 v[204:207], v0, s[64:67], s1 offen offset:1024 sc1\n"
"qm v,a,v,140,188,164\nqt 152,37,49152\nv_readlane_b32 s2, v18, 10\nqm v,a,v,124,188,156\nqt 154,37,53248\nqm v,a,v,124,184,152\n"
"qw 10\ns_add_u32 m0, s0, 0x8000\nqt 156,37,57344\nqm v,a,v,128,192,152\nqt 158,37,61440\nv_add_u32 v37, 0xffff0000, v37\n"
"qm v,a,v,128,196,156\nqt 160,38,49152\nqm v,a,v,144,196,164\nqd 12,60,2,0\nqm v,a,v,144,192,160\nqw 9\n"
"qt 162,38,53248\nqm v,a,v,148,200,160\nqt 164,38,57344\nqm v,a,v,148,204,164\nqt 166,38,61440\nv_add_u32 v38, 0xffff0000, v38\n"
"qm v,a,v,132,204,156\nqd 13,60,2,1024\nqm v,a,v,132,200,152\nqv 20\nqt 168,39,49152\nqz v,v,a,52,48,0\n"
"qt 170,39,53248\nqm v,v,a,52,48,4\nbuffer_load_dwordx4 v[208:211], v0, s[68:71], s1 offen offset:0 sc1\nqm v,v,a,52,48,8\nqt 172,39,57344\nqm v,v,a,52,48,12\n"
"qt 174,39,61440\nv_add_u32 v39, 0xffff0000, v39\nqm v,v,a,52,48,16\nbuffer_load_dwordx4 v[212:215], v0, s[68:71], s1 offen offset:1024 sc1\nqm v,v,a,52,48,20\nqt 176,40,49152\n"
"qm v,v,a,52,48,24\nqt 178,40,53248\nqm v,v,a,52,48,28\nqt 180,40,57344\nqm v,v,a,52,48,32\nqd 14,60,2,2048\n"
"qm v,v,a,52,48,36\nqt 182,40,61440\nv_add_u32 v40, 0xffff0000, v40\nqm v,v,a,52,48,40\nqt 184,41,49152\nqm v,v,a,52,48,44\n"
"qd 15,60,2,3072\nv_readlane_b32 s2, v18, 11\nqm v,v,a,52,48,48\nqt 186,41,53248\nqm v,v,a,52,48,52\ns_add_u32 m0, s0, 0xc000\n"
"qt 188,41,57344\nqm v,v,a,52,48,56\nqd 12,60,2,0\nqm v,v,a,52,48,60\nqv 19\nqt 190,41,61440\n"
"qm v,a,v,88,144,168\nv_add_u32 v41, 0xffff0000, v41\nqm v,a,v,88,148,172\nqt 192,42,49152\nqm v,a,v,104,148,180\nqd 13,60,2,1024\n"
"qm v,a,v,104,144,176\nqt 194,42,53248\nv_readlane_b32 s1, v17, 10\nqm v,a,v,108,152,176\nqt 196,42,57344\nqm v,a,v,108,156,180\n"
"s_nop 1\nbuffer_load_dwordx4 v[216:219], v0, s[64:67], s1 offen offset:0 sc1\nqm v,a,v,92,156,172\nqt 198,42,61440\nv_add_u32 v42, 0xffff0000, v42\nqm v,a,v,92,152,168\n"
"qt 200,43,49152\nqm v,a,v,96,160,168\ns_mov_b64 exec, 0xffffffff\nds_write_b128 v46, v[52:55]\ns_mov_b64 exec, -1\nqm v,a,v,96,164,172\n"
"buffer_load_dwordx4 v[220:223], v0, s[64:67], s1 offen offset:1024 sc1\nqm v,a,v,112,164,180\nqt 202,43,53248\nqm v,a,v,112,160,176\nqt 204,43,57344\nqm v,a,v,116,168,176\n"
"qt 206,43,61440\nv_add_u32 v43, 0xffff0000, v43\nqm v,a,v,116,172,180\nqv 14\ns_barrier\nqd 14,60,2,2048\n"
"qm v,a,v,100,172,172\nqt 144,36,0\nqm v,a,v,100,168,168\nqw 14\nqt 146,36,4096\nqm v,a,v,120,176,168\n"
"qd 15,60,2,3072\nqm v,a,v,120,180,172\nqt 148,36,8192\nqm v,a,v,136,180,180\nqt 150,36,12288\nqm v,a,v,136,176,176\n"
"qw 13\nqt 152,37,0\nbuffer_load_dwordx4 v[224:227], v0, s[68:71], s1 offen offset:0 sc1\nqm v,a,v,140,184,176\nqt 154,37,4096\nqm v,a,v,140,188,180\n"
"qt 156,37,8192\nqm v,a,v,124,188,172\nqt 158,37,12288\nqm v,a,v,124,184,168\nqw 13\nqt 160,38,0\n"
"buffer_load_dwordx4 v[228:231], v0, s[68:71], s1 offen offset:1024 sc1\nqm v,a,v,128,192,168\nqt 162,38,4096\nv_readlane_b32 s1, v17, 11\nqm v,a,v,128,196,172\nqt 164,38,8192\n"
"qm v,a,v,144,196,180\nqt 166,38,12288\nqm v,a,v,144,192,176\nqw 12\nqt 168,39,0\nqm v,a,v,148,200,176\n"
"qt 170,39,4096\nqm v,a,v,148,204,180\nqt 172,39,8192\nqm v,a,v,132,204,172\nqt 174,39,12288\nqm v,a,v,132,200,168\n"
"qw 14\nqv 16\nqt 176,40,0\nqm v,a,v,88,144,184\nqw 0\nqt 178,40,4096\n"
"qm v,a,v,88,148,188\nqt 180,40,8192\nbuffer_load_dwordx4 v[232:235], v0, s[64:67], s1 offen offset:0 sc1\nqm v,a,v,104,148,196\nqt 182,40,12288\nqm v,a,v,104,144,192\n"
"qt 184,41,0\nqm v,a,v,108,152,192\nqt 186,41,4096\nqm v,a,v,108,156,196\nqt 188,41,8192\nbuffer_load_dwordx4 v[236:239], v0, s[64:67], s1 offen offset:1024 sc1\n"
"qm v,a,v,92,156,188\nqt 190,41,12288\nv_readlane_b32 s2, v18, 12\nqm v,a,v,92,152,184\nqt 192,42,0\nqm v,a,v,96,160,184\n"
"s_add_u32 m0, s0, 0x10000\nqt 194,42,4096\nqm v,a,v,96,164,188\nqt 196,42,8192\nqm v,a,v,112,164,196\nqt 198,42,12288\n"
"qm v,a,v,112,160,192\nqt 200,43,0\nqd 12,60,2,0\nqm v,a,v,116,168,192\nqt 202,43,4096\nqm v,a,v,116,172,196\n"
"qt 204,43,8192\nqm v,a,v,100,172,188\nqt 206,43,12288\nqm v,a,v,100,168,184\nqw 12\nqt 144,36,16384\n"
"qd 13,60,2,1024\nqm v,a,v,120,176,184\nqt 146,36,20480\nqm v,a,v,120,180,188\nqt 148,36,24576\nqm v,a,v,136,180,196\n"
"qt 150,36,28672\nbuffer_load_dwordx4 v[240:243], v0, s[68:71], s1 offen offset:0 sc1\nqm v,a,v,136,176,192\nqw 12\nqt 152,37,16384\nqm v,a,v,140,184,192\n"
"qt 154,37,20480\nqm v,a,v,140,188,196\nqt 156,37,24576\nbuffer_load_dwordx4 v[244:247], v0, s[68:71], s1 offen offset:1024 sc1\nqm v,a,v,124,188,188\nqt 158,37,28672\n"
"qm v,a,v,124,184,184\nqw 12\nqt 160,38,16384\nqm v,a,v,128,192,184\nqt 162,38,20480\nqd 14,60,2,2048\n"
"qm v,a,v,128,196,188\nqt 164,38,24576\nqm v,a,v,144,196,196\nqt 166,38,28672\nqm v,a,v,144,192,192\nqw 12\n"
"qt 168,39,16384\nqd 15,60,2,3072\nv_readlane_b32 s2, v18, 13\nqm v,a,v,148,200,192\nqt 170,39,20480\nqm v,a,v,148,204,196\n"
"s_add_u32 m0, s0, 0x14000\nqt 172,39,24576\nqm v,a,v,132,204,188\nqt 174,39,28672\nqm v,a,v,132,200,184\nqw 12\n"
"qv 18\nqt 176,40,16384\nqd 12,60,2,0\nqm v,a,v,88,144,200\nqt 178,40,20480\nqm v,a,v,88,148,204\n"
"qt 180,40,24576\nqm v,a,v,104,148,212\nqt 182,40,28672\nqd 13,60,2,1024\nqm v,a,v,104,144,208\nqw 12\n"
"qt 184,41,16384\nv_readlane_b32 s1, v17, 12\nqm v,a,v,108,152,208\nqt 186,41,20480\nqm v,a,v,108,156,212\nqt 188,41,24576\n"
"qm v,a,v,92,156,204\nqt 190,41,28672\nbuffer_load_dwordx4 v[152:155], v0, s[64:67], s1 offen offset:0 sc1\nqm v,a,v,92,152,200\nqw 12\nqt 192,42,16384\n"
"qm v,a,v,96,160,200\nqt 194,42,20480\nqm v,a,v,96,164,204\nqt 196,42,24576\nbuffer_load_dwordx4 v[156:159], v0, s[64:67], s1 offen offset:1024 sc1\nqm v,a,v,112,164,212\n"
"qt 198,42,28672\nqm v,a,v,112,160,208\nqw 12\nqt 200,43,16384\nqm v,a,v,116,168,208\nqt 202,43,20480\n"
"qm v,a,v,116,172,212\nqt 204,43,24576\nqt 206,43,28672\nqv 14\ns_barrier\nqd 14,60,2,2048\n"
"qm v,a,v,100,172,204\nqt 144,36,32768\nqm v,a,v,100,168,200\nqw 13\nqt 146,36,36864\nqm v,a,v,120,176,200\n"
"qd 15,60,2,3072\nqm v,a,v,120,180,204\nqt 148,36,40960\nqm v,a,v,136,180,212\nqt 150,36,45056\nqm v,a,v,136,176,208\n"
"qw 12\nqt 152,37,32768\nbuffer_load_dwordx4 v[160:163], v0, s[68:71], s1 offen offset:0 sc1\nqm v,a,v,140,184,208\nqt 154,37,36864\nqm v,a,v,140,188,212\n"
"qt 156,37,40960\nqm v,a,v,124,188,204\nqt 158,37,45056\nqm v,a,v,124,184,200\nqw 12\nqt 160,38,32768\n"
"buffer_load_dwordx4 v[164:167], v0, s[68:71], s1 offen offset:1024 sc1\nqm v,a,v,128,192,200\nqt 162,38,36864\nv_readlane_b32 s1, v17, 13\nqm v,a,v,128,196,204\nqt 164,38,40960\n"
"qm v,a,v,144,196,212\nqt 166,38,45056\nqm v,a,v,144,192,208\nqw 12\nqt 168,39,32768\nqm v,a,v,148,200,208\n"
"ds_read_b128 v[56:59], v45 offset:0\nds_read_b128 v[60:63], v45 offset:512\nds_read_b128 v[64:67], v45 offset:1024\nds_read_b128 v[68:71], v45 offset:1536\nqt 170,39,36864\nqm v,a,v,148,204,212\n"
"qt 172,39,40960\nqm v,a,v,132,204,204\nqt 174,39,45056\nqm v,a,v,132,200,200\nqv 16\nqt 176,40,32768\n"
"qm v,a,v,88,144,216\nqt 178,40,36864\nqm v,a,v,88,148,220\nqt 180,40,40960\nbuffer_load_dwordx4 v[168:171], v0, s[64:67], s1 offen offset:0 sc1\nqm v,a,v,104,148,228\n"
"qt 182,40,45056\nqm v,a,v,104,144,224\nqt 184,41,32768\nqm v,a,v,108,152,224\nqt 186,41,36864\nqm v,a,v,108,156,228\n"
"qt 188,41,40960\nbuffer_load_dwordx4 v[172:175], v0, s[64:67], s1 offen offset:1024 sc1\nqm v,a,v,92,156,220\nqt 190,41,45056\nv_readlane_b32 s2, v18, 14\nqm v,a,v,92,152,216\n"
"qw 13\nqt 192,42,32768\nqm v,a,v,96,160,216\ns_add_u32 m0, s0, 0x18000\nqt 194,42,36864\nv_add_f32 v56, v56, v60\n"
"qm v,a,v,96,164,220\nqt 196,42,40960\nv_add_f32 v57, v57, v61\nqm v,a,v,112,164,228\nqt 198,42,45056\nv_add_f32 v58, v58, v62\n"
"qm v,a,v,112,160,224\nqw 12\nqt 200,43,32768\nqd 12,60,2,0\nqm v,a,v,116,168,224\nqt 202,43,36864\n"
"v_add_f32 v59, v59, v63\nqm v,a,v,116,172,228\nqt 204,43,40960\nv_add_f32 v64, v64, v68\nqm v,a,v,100,172,220\nqt 206,43,45056\n"
"v_add_f32 v65, v65, v69\nqm v,a,v,100,168,216\nqw 12\nqt 144,36,49152\nqd 13,60,2,1024\nqm v,a,v,120,176,216\n"
"qt 146,36,53248\nv_add_f32 v66, v66, v70\nqm v,a,v,120,180,220\nqt 148,36,57344\nv_add_f32 v67, v67, v71\nqm v,a,v,136,180,228\n"
"qt 150,36,61440\nbuffer_load_dwordx4 v[176:179], v0, s[68:71], s1 offen offset:0 sc1\nqm v,a,v,136,176,224\nv_add_u32 v36, 0x10000, v36\nqt 152,37,49152\nqw 13\n"
"qm v,a,v,140,184,224\nqt 154,37,53248\nv_add_f32 v56, v56, v64\nqm v,a,v,140,188,228\nqt 156,37,57344\nbuffer_load_dwordx4 v[180:183], v0, s[68:71], s1 offen offset:1024 sc1\n"
"v_add_f32 v57, v57, v65\nqm v,a,v,124,188,220\nqt 158,37,61440\nv_add_f32 v58, v58, v66\nqm v,a,v,124,184,216\nv_add_u32 v37, 0x10000, v37\n"
"qt 160,38,49152\nqw 13\nqm v,a,v,128,192,216\nqt 162,38,53248\nqd 14,60,2,2048\nv_add_f32 v59, v59, v67\n"
"qm v,a,v,128,196,220\nqt 164,38,57344\nqm v,a,v,144,196,228\nqd 15,60,2,3072\nqt 166,38,61440\nv_readlane_b32 s2, v18, 15\n"
"qm v,a,v,144,192,224\nv_add_u32 v38, 0x10000, v38\nqt 168,39,49152\nqw 13\nqm v,a,v,148,200,224\ns_add_u32 m0, s0, 0x1c000\n"
"qt 170,39,53248\nqm v,a,v,148,204,228\nqt 172,39,57344\nqm v,a,v,132,204,220\nqt 174,39,61440\nv_add_u32 v39, 0x10000, v39\n"
"qm v,a,v,132,200,216\nqw 12\nqv 18\nqt 176,40,49152\nqm v,a,v,88,144,232\nqt 178,40,53248\n"
"s_mov_b64 exec, 0xffffffff\nbuffer_store_dwordx4 v[56:59], v47, s[56:59], s83 offen sc1\ns_mov_b64 exec, -1\nqd 12,60,2,0\nqm v,a,v,88,148,236\nqt 180,40,57344\n"
"qm v,a,v,104,148,244\nqt 182,40,61440\nv_add_u32 v40, 0x10000, v40\nqm v,a,v,104,144,240\nqw 12\nqt 184,41,49152\n"
"qd 13,60,2,1024\nqm v,a,v,108,152,240\nqt 186,41,53248\nv_readlane_b32 s1, v17, 14\nqm v,a,v,108,156,244\nqt 188,41,57344\n"
"qm v,a,v,92,156,236\nqt 190,41,61440\ns_nop 0\nbuffer_load_dwordx4 v[184:187], v0, s[64:67], s1 offen offset:0 sc1\nqm v,a,v,92,152,232\nv_add_u32 v41, 0x10000, v41\n"
"qt 192,42,49152\nqw 13\nqm v,a,v,96,160,232\nqt 194,42,53248\nqm v,a,v,96,164,236\nqt 196,42,57344\n"
"buffer_load_dwordx4 v[188:191], v0, s[64:67], s1 offen offset:1024 sc1\nqm v,a,v,112,164,244\nqt 198,42,61440\nv_add_u32 v42, 0x10000, v42\nqm v,a,v,112,160,240\nqw 12\n"
"qt 200,43,49152\nqd 14,60,2,2048\nqm v,a,v,116,168,240\nqt 202,43,53248\nqm v,a,v,116,172,244\nqt 204,43,57344\n"
"qt 206,43,61440\nv_add_u32 v43, 0x10000, v43\nqv 16\ns_barrier\nqm v,a,v,100,172,236\nqd 15,60,2,3072\n"
"qm v,a,v,100,168,232\nqw 12\nqt 144,36,0\nqm v,a,v,120,176,232\nqt 146,36,4096\nqm v,a,v,120,180,236\n"
"qt 148,36,8192\nbuffer_load_dwordx4 v[192:195], v0, s[68:71], s1 offen offset:0 sc1\nqm v,a,v,136,180,244\nqt 150,36,12288\nv_xor_b32 v19, 8, v19\nqm v,a,v,136,176,240\n"
"qw 12\nqt 152,37,0\nqm v,a,v,140,184,240\nqt 154,37,4096\nqm v,a,v,140,188,244\nqt 156,37,8192\n"
"qm v,a,v,124,188,236\nqt 158,37,12288\nqm v,a,v,124,184,232\nqw 12\nqt 160,38,0\nbuffer_load_dwordx2 v[248:249], v19, s[56:59], s5 offen sc1\n"
"buffer_load_dwordx4 v[196:199], v0, s[68:71], s1 offen offset:1024 sc1\nqm v,a,v,128,192,232\nqt 162,38,4096\nv_readlane_b32 s1, v17, 15\nqm v,a,v,128,196,236\nqt 164,38,8192\n"
"qm v,a,v,144,196,244\nqt 166,38,12288\nqm v,a,v,144,192,240\nqw 12\nqt 168,39,0\nqm v,a,v,148,200,240\n"
"qt 170,39,4096\nqm v,a,v,148,204,244\nqt 172,39,8192\nqm v,a,v,132,204,236\nqt 174,39,12288\nqm v,a,v,132,200,232\n"
"qw 12\nqv 18\nqt 176,40,0\nqm v,a,v,88,144,152\nqt 178,40,4096\nqm v,a,v,88,148,156\n"
"qt 180,40,8192\nbuffer_load_dwordx4 v[200:203], v0, s[64:67], s1 offen offset:0 sc1\nqm v,a,v,104,148,164\nqt 182,40,12288\nqm v,a,v,104,144,160\nqw 12\n"
"qt 184,41,0\nqm v,a,v,108,152,160\nqt 186,41,4096\nqm v,a,v,108,156,164\nqt 188,41,8192\nbuffer_load_dwordx4 v[204:207], v0, s[64:67], s1 offen offset:1024 sc1\n"
"qm v,a,v,92,156,156\nqt 190,41,12288\nv_readlane_b32 s2, v18, 16\nqm v,a,v,92,152,152\nqw 12\nqt 192,42,0\n"
"qm v,a,v,96,160,152\ns_add_u32 m0, s0, 0x0\nqt 194,42,4096\nqm v,a,v,96,164,156\nqt 196,42,8192\nqm v,a,v,112,164,164\n"
"qt 198,42,12288\nqm v,a,v,112,160,160\nqw 12\nqt 200,43,0\nqd 12,60,2,0\nqm v,a,v,116,168,160\n"
"qt 202,43,4096\nqm v,a,v,116,172,164\nqt 204,43,8192\nqm v,a,v,100,172,156\nqt 206,43,12288\nqm v,a,v,100,168,152\n"
"qw 12\nqt 144,36,16384\nqd 13,60,2,1024\nqm v,a,v,120,176,152\nqt 146,36,20480\nqm v,a,v,120,180,156\n"
"qt 148,36,24576\nqm v,a,v,136,180,164\nqt 150,36,28672\nbuffer_load_dwordx4 v[208:211], v0, s[68:71], s1 offen offset:0 sc1\nqm v,a,v,136,176,160\nqw 12\n"
"qt 152,37,16384\nqm v,a,v,140,184,160\nqt 154,37,20480\nqm v,a,v,140,188,164\nqt 156,37,24576\nbuffer_load_dwordx4 v[212:215], v0, s[68:71], s1 offen offset:1024 sc1\n"
"qm v,a,v,124,188,156\nqt 158,37,28672\nqm v,a,v,124,184,152\nqw 12\nqd 14,60,2,2048\nqt 160,38,16384\n"
"qm v,a,v,128,192,152\nqv 8\nqt 162,38,20480\nv_cmp_eq_u64 vcc, s[16:17], v[248:249]\nqm v,a,v,128,196,156\nqt 164,38,24576\n"
"qm v,a,v,144,196,164\nqt 166,38,28672\nqm v,a,v,144,192,160\nqw 12\nqt 168,39,16384\nqm v,a,v,148,200,160\n"
"s_cmp_eq_u64 vcc, exec\ns_cbranch_scc0 PLG2_o%=\nPLG2_r%=:\nqt 170,39,20480\nqm v,a,v,148,204,164\nqd 15,60,2,3072\n"
"qt 172,39,24576\nv_readlane_b32 s2, v18, 17\nqm v,a,v,132,204,156\nqt 174,39,28672\nqm v,a,v,132,200,152\ns_add_u32 m0, s0, 0x4000\n"
"qw 12\nqt 176,40,16384\nqm v,a,v,88,144,168\nqd 12,60,2,0\nqt 178,40,20480\nqm v,a,v,88,148,172\n"
"qt 180,40,24576\nqm v,a,v,104,148,180\nqt 182,40,28672\nqm v,a,v,104,144,176\nqw 12\nqt 184,41,16384\n"
"qm v,a,v,108,152,176\nqt 186,41,20480\ns_mov_b64 exec, 1\nbuffer_store_dwordx2 v[250:251], off, s[56:59], s81 sc1\ns_mov_b64 exec, -1\nqd 13,60,2,1024\n"
"qm v,a,v,108,156,180\nqt 188,41,24576\nv_readlane_b32 s1, v17, 16\nqm v,a,v,92,156,172\nqt 190,41,28672\nqm v,a,v,92,152,168\n"
"qw 12\nqt 192,42,16384\nbuffer_load_dwordx4 v[216:219], v0, s[64:67], s1 offen offset:0 sc1\nqm v,a,v,96,160,168\nqt 194,42,20480\nqm v,a,v,96,164,172\n"
"qt 196,42,24576\nqm v,a,v,112,164,180\nqt 198,42,28672\nbuffer_load_dwordx4 v[220:223], v0, s[64:67], s1 offen offset:1024 sc1\nqm v,a,v,112,160,176\nqw 12\n"
"qt 200,43,16384\nqm v,a,v,116,168,176\nqt 202,43,20480\nqm v,a,v,116,172,180\nqt 204,43,24576\nqt 206,43,28672\n"
"s_barrier\nqd 14,60,2,2048\nqm v,a,v,100,172,172\nqt 144,36,32768\nqm v,a,v,100,168,168\nqw 13\n"
"qt 146,36,36864\nqm v,a,v,120,176,168\nqd 15,60,2,3072\nqm v,a,v,120,180,172\nqt 148,36,40960\nqm v,a,v,136,180,180\n"
"qt 150,36,45056\nqm v,a,v,136,176,176\nqw 12\nqt 152,37,32768\nbuffer_load_dwordx4 v[224:227], v0, s[68:71], s1 offen offset:0 sc1\nqm v,a,v,140,184,176\n"
"qt 154,37,36864\nqm v,a,v,140,188,180\nqt 156,37,40960\nqm v,a,v,124,188,172\nqt 158,37,45056\nqm v,a,v,124,184,168\n"
"qw 12\nqt 160,38,32768\nbuffer_load_dwordx4 v[228:231], v0, s[68:71], s1 offen offset:1024 sc1\nqm v,a,v,128,192,168\nqt 162,38,36864\nv_readlane_b32 s1, v17, 17\n"
"qm v,a,v,128,196,172\nqt 164,38,40960\nqm v,a,v,144,196,180\nqt 166,38,45056\ns_nop 0\nbuffer_load_dwordx4 v[232:235], v0, s[64:67], s1 offen offset:0 sc1\n"
"qm v,a,v,144,192,176\nqw 12\nqt 168,39,32768\nqm v,a,v,148,200,176\nqt 170,39,36864\nbuffer_load_dwordx4 v[236:239], v0, s[64:67], s1 offen offset:1024 sc1\n"
"v_readlane_b32 s2, v18, 18\nqm v,a,v,148,204,180\nqt 172,39,40960\nqm v,a,v,132,204,172\ns_add_u32 m0, s0, 0x8000\nqt 174,39,45056\n"
"qm v,a,v,132,200,168\nqw 12\nqv 19\nqt 176,40,32768\nqm v,a,v,88,144,184\nqt 178,40,36864\n"
"qd 12,60,2,0\nqm v,a,v,88,148,188\nqt 180,40,40960\nqm v,a,v,104,148,196\nqt 182,40,45056\nqm v,a,v,104,144,192\n"
"qw 12\nqt 184,41,32768\nqd 13,60,2,1024\nqm v,a,v,108,152,192\nqt 186,41,36864\nqm v,a,v,108,156,196\n"
"qt 188,41,40960\nqm v,a,v,92,156,188\nqt 190,41,45056\nbuffer_load_dwordx4 v[240:243], v0, s[68:71], s1 offen offset:0 sc1\nqm v,a,v,92,152,184\nqw 12\n"
"qt 192,42,32768\nqm v,a,v,96,160,184\nqt 194,42,36864\nqm v,a,v,96,164,188\nqt 196,42,40960\nbuffer_load_dwordx4 v[244:247], v0, s[68:71], s1 offen offset:1024 sc1\n"
"qm v,a,v,112,164,196\nqt 198,42,45056\nqm v,a,v,112,160,192\nqw 12\nqt 200,43,32768\nqm v,a,v,116,168,192\n"
"qt 202,43,36864\nqd 14,60,2,2048\nqm v,a,v,116,172,196\nqt 204,43,40960\nqm v,a,v,100,172,188\nqt 206,43,45056\n"
"qm v,a,v,100,168,184\nqw 12\nqt 144,36,49152\nqd 15,60,2,3072\nv_readlane_b32 s2, v18, 19\nqm v,a,v,120,176,184\n"
"qt 146,36,53248\nqm v,a,v,120,180,188\ns_add_u32 m0, s0, 0xc000\nqt 148,36,57344\nqm v,a,v,136,180,196\nqt 150,36,61440\n"
"v_add_u32 v36, 0xffff0000, v36\nqm v,a,v,136,176,192\nqw 12\nqt 152,37,49152\nqd 12,60,2,0\nqm v,a,v,140,184,192\n"
"qt 154,37,53248\nqm v,a,v,140,188,196\nqt 156,37,57344\nqm v,a,v,124,188,188\nqt 158,37,61440\nqd 13,60,2,1024\n"
"qm v,a,v,124,184,184\nv_add_u32 v37, 0xffff0000, v37\nqt 160,38,49152\nv_readlane_b32 s1, v17, 18\nqw 13\nqm v,a,v,128,192,184\n"
"qt 162,38,53248\nqm v,a,v,128,196,188\nqt 164,38,57344\nbuffer_load_dwordx4 v[152:155], v0, s[64:67], s1 offen offset:0 sc1\nqm v,a,v,144,196,196\nqt 166,38,61440\n"
"v_add_u32 v38, 0xffff0000, v38\nqm v,a,v,144,192,192\nqw 12\nqt 168,39,49152\nqm v,a,v,148,200,192\nqt 170,39,53248\n"
"buffer_load_dwordx4 v[156:159], v0, s[64:67], s1 offen offset:1024 sc1\nqm v,a,v,148,204,196\nqt 172,39,57344\nqm v,a,v,132,204,188\nqt 174,39,61440\nv_add_u32 v39, 0xffff0000, v39\n"
"qm v,a,v,132,200,184\nqw 12\nqv 23\nqt 176,40,49152\nqd 14,60,2,2048\nqm v,a,v,88,144,200\n"
"qt 178,40,53248\nqm v,a,v,88,148,204\nqt 180,40,57344\nqm v,a,v,104,148,212\nqt 182,40,61440\nqd 15,60,2,3072\n"
"qm v,a,v,104,144,208\nv_add_u32 v40, 0xffff0000, v40\nqt 184,41,49152\nqw 13\nqm v,a,v,108,152,208\nqt 186,41,53248\n"
"qm v,a,v,108,156,212\nqt 188,41,57344\nbuffer_load_dwordx4 v[160:163], v0, s[68:71], s1 offen offset:0 sc1\nqm v,a,v,92,156,204\nqt 190,41,61440\nv_add_u32 v41, 0xffff0000, v41\n"
"qm v,a,v,92,152,200\nqw 12\nqt 192,42,49152\nqm v,a,v,96,160,200\nqt 194,42,53248\nbuffer_load_dwordx4 v[164:167], v0, s[68:71], s1 offen offset:1024 sc1\n"
"qm v,a,v,96,164,204\nqt 196,42,57344\nv_readlane_b32 s1, v17, 19\nqm v,a,v,112,164,212\nqt 198,42,61440\nv_add_u32 v42, 0xffff0000, v42\n"
"qm v,a,v,112,160,208\nqw 12\nqt 200,43,49152\nqm v,a,v,116,168,208\nqt 202,43,53248\nbuffer_load_dwordx4 v[168:171], v0, s[64:67], s1 offen offset:0 sc1\n"
"qm v,a,v,116,172,212\nqt 204,43,57344\nqt 206,43,61440\nv_add_u32 v43, 0xffff0000, v43\nqv 19\ns_barrier\n"
"qm v,a,v,100,172,204\nbuffer_load_dwordx4 v[172:175], v0, s[64:67], s1 offen offset:1024 sc1\nv_readlane_b32 s2, v18, 20\nqm v,a,v,100,168,200\nqw 12\nqt 144,36,0\n"
"qm v,a,v,120,176,200\ns_add_u32 m0, s0, 0x10000\nqt 146,36,4096\nqm v,a,v,120,180,204\nqt 148,36,8192\nqm v,a,v,136,180,212\n"
"qt 150,36,12288\nqd 12,60,2,0\nqm v,a,v,136,176,208\nqw 12\nqt 152,37,0\nqm v,a,v,140,184,208\n"
"qt 154,37,4096\nqm v,a,v,140,188,212\nqt 156,37,8192\nqd 13,60,2,1024\nqm v,a,v,124,188,204\nqt 158,37,12288\n"
"qm v,a,v,124,184,200\nqw 12\nqt 160,38,0\nqm v,a,v,128,192,200\nqt 162,38,4096\nbuffer_load_dwordx4 v[176:179], v0, s[68:71], s1 offen offset:0 sc1\n"
"qm v,a,v,128,196,204\nqt 164,38,8192\nqm v,a,v,144,196,212\nqt 166,38,12288\nqm v,a,v,144,192,208\nqw 12\n"
"qt 168,39,0\nbuffer_load_dwordx4 v[180:183], v0, s[68:71], s1 offen offset:1024 sc1\nqm v,a,v,148,200,208\nqt 170,39,4096\nqm v,a,v,148,204,212\nqt 172,39,8192\n"
"qm v,a,v,132,204,204\nqt 174,39,12288\nqd 14,60,2,2048\nqm v,a,v,132,200,200\nqw 12\nqv 23\n"
"qt 176,40,0\nqm v,a,v,88,144,216\nqt 178,40,4096\nqm v,a,v,88,148,220\nqt 180,40,8192\nqd 15,60,2,3072\n"
"v_readlane_b32 s2, v18, 21\nqm v,a,v,104,148,228\nqt 182,40,12288\nqm v,a,v,104,144,224\ns_add_u32 m0, s0, 0x14000\nqw 12\n"
"qt 184,41,0\nqm v,a,v,108,152,224\nqt 186,41,4096\nqm v,a,v,108,156,228\nqt 188,41,8192\nqd 12,60,2,0\n"
"qm v,a,v,92,156,220\nqt 190,41,12288\nqm v,a,v,92,152,216\nqw 12\nqt 192,42,0\nqm v,a,v,96,160,216\n"
"qt 194,42,4096\nqd 13,60,2,1024\nqm v,a,v,96,164,220\nqt 196,42,8192\nv_readlane_b32 s1, v17, 20\nqm v,a,v,112,164,228\n"
"qt 198,42,12288\nqm v,a,v,112,160,224\nqw 12\nqt 200,43,0\nbuffer_load_dwordx4 v[184:187], v0, s[64:67], s1 offen offset:0 sc1\nqm v,a,v,116,168,224\n"
"qt 202,43,4096\nqm v,a,v,116,172,228\nqt 204,43,8192\nqm v,a,v,100,172,220\nqt 206,43,12288\nbuffer_load_dwordx4 v[188:191], v0, s[64:67], s1 offen offset:1024 sc1\n"
"qm v,a,v,100,168,216\nqw 12\nqt 144,36,16384\nqm v,a,v,120,176,216\nqt 146,36,20480\nqm v,a,v,120,180,220\n"
"qt 148,36,24576\nqd 14,60,2,2048\nqm v,a,v,136,180,228\nqt 150,36,28672\nqm v,a,v,136,176,224\nqw 12\n"
"qt 152,37,16384\nqm v,a,v,140,184,224\nqt 154,37,20480\nqd 15,60,2,3072\nqm v,a,v,140,188,228\nqt 156,37,24576\n"
"qm v,a,v,124,188,220\nqt 158,37,28672\nqm v,a,v,124,184,216\nqw 12\nqt 160,38,16384\nbuffer_load_dwordx4 v[192:195], v0, s[68:71], s1 offen offset:0 sc1\n"
"qm v,a,v,128,192,216\nqt 162,38,20480\nqm v,a,v,128,196,220\nqt 164,38,24576\nqm v,a,v,144,196,228\nqt 166,38,28672\n"
"buffer_load_dwordx4 v[196:199], v0, s[68:71], s1 offen offset:1024 sc1\nqm v,a,v,144,192,224\nqw 12\nqt 168,39,16384\nv_readlane_b32 s1, v17, 21\nqm v,a,v,148,200,224\n"
"qt 170,39,20480\nqm v,a,v,148,204,228\nqt 172,39,24576\nqm v,a,v,132,204,220\nqt 174,39,28672\nbuffer_load_dwordx4 v[200:203], v0, s[64:67], s1 offen offset:0 sc1\n"
"qm v,a,v,132,200,216\nqw 12\nqv 27\nqt 176,40,16384\nqm v,a,v,88,144,232\nqt 178,40,20480\n"
"buffer_load_dwordx4 v[204:207], v0, s[64:67], s1 offen offset:1024 sc1\nv_readlane_b32 s2, v18, 22\nqm v,a,v,88,148,236\nqt 180,40,24576\nqm v,a,v,104,148,244\ns_add_u32 m0, s0, 0x18000\n"
"qt 182,40,28672\nqm v,a,v,104,144,240\nqw 12\nqt 184,41,16384\nqm v,a,v,108,152,240\nqt 186,41,20480\n"
"qd 12,60,2,0\nqm v,a,v,108,156,244\nqt 188,41,24576\nv_mbcnt_lo_u32_b32 v53, -1, 0\nqm v,a,v,92,156,236\nqt 190,41,28672\n"
"v_mbcnt_hi_u32_b32 v53, -1, v53\nqm v,a,v,92,152,232\ns_and_b32 s74, s23, 1\nqw 12\nqt 192,42,16384\nqd 13,60,2,1024\n"
"qm v,a,v,96,160,232\ns_xor_b32 s75, s74, 1\ns_lshl_b32 s75, s75, 8\nqt 194,42,20480\nqm v,a,v,96,164,236\ns_mov_b32 s72, 7168\n"
"qt 196,42,24576\nv_add_u32 v54, 0, v53\nqm v,a,v,112,164,244\nqt 198,42,28672\nbuffer_load_dwordx4 v[208:211], v0, s[68:71], s1 offen offset:0 sc1\nqm v,a,v,112,160,240\n"
"qw 12\nqt 200,43,16384\nv_mul_u32_u24 v55, 0xaaab, v54\nqm v,a,v,116,168,240\nqt 202,43,20480\nv_lshrrev_b32 v55, 19, v55\n"
"qm v,a,v,116,172,244\nqt 204,43,24576\nqt 206,43,28672\nqv 23\ns_barrier\nbuffer_load_dwordx4 v[212:215], v0, s[68:71], s1 offen offset:1024 sc1\n"
"qm v,a,v,100,172,236\nqt 144,36,32768\nv_mul_u32_u24 v248, 12, v55\nqm v,a,v,100,168,232\nqw 13\nqt 146,36,36864\n"
"v_sub_u32 v54, v54, v248\nqm v,a,v,120,176,232\nqd 14,60,2,2048\nqm v,a,v,120,180,236\nqt 148,36,40960\nv_bfe_u32 v248, v55, 2, 1\n"
"qm v,a,v,136,180,244\nqt 150,36,45056\nqd 15,60,2,3072\nv_lshlrev_b32 v248, 1, v248\nv_readlane_b32 s2, v18, 23\nqm v,a,v,136,176,240\n"
"qw 12\nqt 152,37,32768\nv_xor_b32 v54, v54, v248\nqm v,a,v,140,184,240\ns_add_u32 m0, s0, 0x1c000\nqt 154,37,36864\n"
"v_lshl_add_u32 v54, v54, 4, s75\nqm v,a,v,140,188,244\nqt 156,37,40960\nv_mad_u32_u24 v1, v55, s72, v54\nqm v,a,v,124,188,236\nqt 158,37,45056\n"
"qd 12,60,2,0\nqm v,a,v,124,184,232\nqw 12\nqt 160,38,32768\nv_add_u32 v54, 64, v53\nqm v,a,v,128,192,232\n"
"qt 162,38,36864\nv_mul_u32_u24 v55, 0xaaab, v54\nqm v,a,v,128,196,236\nqt 164,38,40960\nv_lshrrev_b32 v55, 19, v55\nqm v,a,v,144,196,244\n"
"qt 166,38,45056\nqd 13,60,2,1024\nv_mul_u32_u24 v248, 12, v55\nqm v,a,v,144,192,240\nqw 12\nqt 168,39,32768\n"
"v_readlane_b32 s1, v17, 22\nqm v,a,v,148,200,240\nqt 170,39,36864\nv_sub_u32 v54, v54, v248\nqm v,a,v,148,204,244\nqt 172,39,40960\n"
"buffer_load_dwordx4 v[216:219], v0, s[64:67], s1 offen offset:0 sc1\nv_bfe_u32 v248, v55, 2, 1\nqm v,a,v,132,204,236\nqt 174,39,45056\nv_lshlrev_b32 v248, 1, v248\nqm v,a,v,132,200,232\n"
"qw 12\nqv 27\nqt 176,40,32768\nv_xor_b32 v54, v54, v248\nqm v,a,v,88,144,152\nqt 178,40,36864\n"
"buffer_load_dwordx4 v[220:223], v0, s[64:67], s1 offen offset:1024 sc1\nv_lshl_add_u32 v54, v54, 4, s75\nqm v,a,v,88,148,156\nqt 180,40,40960\nv_mad_u32_u24 v2, v55, s72, v54\nqm v,a,v,104,148,164\n"
"qt 182,40,45056\nv_subrev_u32 v2, 1024, v2\nqm v,a,v,104,144,160\nqw 12\nqt 184,41,32768\nqd 14,60,2,2048\n"
"qm v,a,v,108,152,160\nqt 186,41,36864\nv_add_u32 v54, 128, v53\nqm v,a,v,108,156,164\nqt 188,41,40960\nv_mul_u32_u24 v55, 0xaaab, v54\n"
"qm v,a,v,92,156,156\nqt 190,41,45056\nqd 15,60,2,3072\nv_lshrrev_b32 v55, 19, v55\nqm v,a,v,92,152,152\nqw 12\n"
"qt 192,42,32768\nv_mul_u32_u24 v248, 12, v55\nqm v,a,v,96,160,152\nqt 194,42,36864\nv_sub_u32 v54, v54, v248\nqm v,a,v,96,164,156\n"
"qt 196,42,40960\nbuffer_load_dwordx4 v[224:227], v0, s[68:71], s1 offen offset:0 sc1\nv_bfe_u32 v248, v55, 2, 1\nqm v,a,v,112,164,164\nqt 198,42,45056\nv_lshlrev_b32 v248, 1, v248\n"
"qm v,a,v,112,160,160\nqw 12\nqt 200,43,32768\nv_xor_b32 v54, v54, v248\nqm v,a,v,116,168,160\nqt 202,43,36864\n"
"buffer_load_dwordx4 v[228:231], v0, s[68:71], s1 offen offset:1024 sc1\nqm v,a,v,116,172,164\nqt 204,43,40960\nv_lshl_add_u32 v54, v54, 4, s75\nv_readlane_b32 s1, v17, 23\nqm v,a,v,100,172,156\n"
"qt 206,43,45056\nv_mad_u32_u24 v3, v55, s72, v54\nqm v,a,v,100,168,152\nqw 12\nqt 144,36,49152\nbuffer_load_dwordx4 v[232:235], v0, s[64:67], s1 offen offset:0 sc1\n"
"v_subrev_u32 v3, 2048, v3\nqm v,a,v,120,176,152\nqt 146,36,53248\nv_lshrrev_b32 v54, 4, v53\nqm v,a,v,120,180,156\nqt 148,36,57344\n"
"buffer_load_dwordx4 v[236:239], v0, s[64:67], s1 offen offset:1024 sc1\nv_and_b32 v55, 15, v53\nv_readlane_b32 s2, v18, 24\nqm v,a,v,136,180,164\ns_mov_b32 s72, 0x1c00\nqt 150,36,61440\n"
"v_add_u32 v36, 0x10000, v36\nqm v,a,v,136,176,160\ns_add_u32 m0, s0, 0x0\nqw 12\nqt 152,37,49152\nqm v,a,v,140,184,160\n"
"qt 154,37,53248\nv_or_b32 v248, 0, v54\nqm v,a,v,140,188,164\nqt 156,37,57344\nqd 12,60,2,0\nv_lshlrev_b32 v248, 1, v248\n"
"qm v,a,v,124,188,156\nqt 158,37,61440\nv_xor_b32 v248, v248, v55\nqm v,a,v,124,184,152\nv_add_u32 v37, 0x10000, v37\nv_lshlrev_b32 v248, 4, v248\n"
"qt 160,38,49152\nqw 13\nqm v,a,v,128,192,152\nqt 162,38,53248\nv_mad_u32_u24 v4, v54, s72, v248\nqm v,a,v,128,196,156\n"
"qt 164,38,57344\nqd 13,60,2,1024\nv_or_b32 v248, 4, v54\nqm v,a,v,144,196,164\nqt 166,38,61440\nv_lshlrev_b32 v248, 1, v248\n"
"qm v,a,v,144,192,160\nv_add_u32 v38, 0x10000, v38\nqt 168,39,49152\nqw 13\nqm v,a,v,148,200,160\nqt 170,39,53248\n"
"buffer_load_dwordx4 v[240:243], v0, s[68:71], s1 offen offset:0 sc1\nv_xor_b32 v248, v248, v55\nqm v,a,v,148,204,164\nqt 172,39,57344\nv_lshlrev_b32 v248, 4, v248\nqm v,a,v,132,204,156\n"
"qt 174,39,61440\nv_mad_u32_u24 v5, v54, s72, v248\nqm v,a,v,132,200,152\nv_add_u32 v39, 0x10000, v39\nv_add_u32 v5, 0x6c00, v5\nqt 176,40,49152\n"
"buffer_load_dwordx4 v[244:247], v0, s[68:71], s1 offen offset:1024 sc1\nqw 13\nqv 32\nqm v,a,v,88,144,168\nqt 178,40,53248\nv_or_b32 v248, 0, v54\n"
"qm v,a,v,88,148,172\nqt 180,40,57344\nv_lshlrev_b32 v248, 1, v248\nqm v,a,v,104,148,180\nqt 182,40,61440\nqd 14,60,2,2048\n"
"v_xor_b32 v248, v248, v55\nqm v,a,v,104,144,176\nv_add_u32 v40, 0x10000, v40\nqt 184,41,49152\nqw 13\nqm v,a,v,108,152,176\n"
"qt 186,41,53248\nv_lshlrev_b32 v248, 4, v248\nqm v,a,v,108,156,180\nqt 188,41,57344\nqd 15,60,2,3072\nv_mad_u32_u24 v6, v54, s72, v248\n"
"qm v,a,v,92,156,172\nqt 190,41,61440\nv_add_u32 v6, 0xd800, v6\nv_readlane_b32 s1, v17, 24\nqm v,a,v,92,152,168\nv_add_u32 v41, 0x10000, v41\n"
"v_or_b32 v248, 4, v54\nqt 192,42,49152\nqw 13\nqm v,a,v,96,160,168\nqt 194,42,53248\nql 0,0,64,1,0\n"
"v_lshlrev_b32 v248, 1, v248\nqm v,a,v,96,164,172\nqt 196,42,57344\nv_xor_b32 v248, v248, v55\nqm v,a,v,112,164,180\nqt 198,42,61440\n"
"ql 4,0,64,1,1024\nv_lshlrev_b32 v248, 4, v248\nv_readlane_b32 s2, v18, 25\nqm v,a,v,112,160,176\nv_add_u32 v42, 0x10000, v42\nqt 200,43,49152\n"
"qw 13\nqm v,a,v,116,168,176\ns_add_u32 m0, s0, 0x4000\nqt 202,43,53248\nv_mad_u32_u24 v7, v54, s72, v248\nqm v,a,v,116,172,180\n"
"v_add_u32 v7, 0x14400, v7\nqt 204,43,57344\nqt 206,43,61440\nv_add_u32 v43, 0x10000, v43\nqv 28\ns_barrier\n"
"qm v,a,v,100,172,172\ns_mul_i32 s73, s74, 192\nqd 12,60,2,0\nqm v,a,v,100,168,168\nqw 12\nqt 144,36,0\n"
"v_add_u32 v4, s73, v4\nqm v,a,v,120,176,168\nqt 146,36,4096\nv_add_u32 v5, s73, v5\nqm v,a,v,120,180,172\nqt 148,36,8192\n"
"qd 13,60,2,1024\nv_add_u32 v6, s73, v6\nqm v,a,v,136,180,180\nqt 150,36,12288\nv_add_u32 v7, s73, v7\nqm v,a,v,136,176,176\n"
"qw 12\ns_mul_i32 s80, s15, 3072\nqt 152,37,0\nqm v,a,v,140,184,176\nqt 154,37,4096\nql 8,0,68,1,0\n"
"qm v,a,v,140,188,180\ns_lshl_b32 s81, s15, 12\ns_and_b32 s72, s22, 3\nqt 156,37,8192\nqm v,a,v,124,188,172\ns_lshl1_add_u32 s72, s72, s21\n"
"s_and_b32 s72, s72, 7\nqt 158,37,12288\nqm v,a,v,124,184,168\nqw 12\nqt 160,38,0\nv_subrev_u32 v54, s72, v53\n"
"qm v,a,v,128,192,168\nqt 162,38,4096\nql 12,0,68,1,1024\nqm v,a,v,128,196,172\nqt 164,38,8192\nv_add_u32 v54, 10, v54\n"
"qm v,a,v,144,196,180\nqt 166,38,12288\nv_and_b32 v54, 7, v54\nqm v,a,v,144,192,176\nqw 12\nqt 168,39,0\n"
"qd 14,60,2,2048\nqm v,a,v,148,200,176\nqt 170,39,4096\nv_sub_u32 v55, v53, v54\nqm v,a,v,148,204,180\nqt 172,39,8192\n"
"v_add_u32 v49, -1, v53\nqm v,a,v,132,204,172\nqt 174,39,12288\nqd 15,60,2,3072\nqm v,a,v,132,200,168\nqw 12\n"
"qv 32\nqt 176,40,0\nv_cmp_lt_u32 vcc, 2, v54\ns_nop 1\nv_cndmask_b32 v49, v49, v53, vcc\nv_readlane_b32 s1, v17, 25\n"
"qm v,a,v,88,144,184\nqt 178,40,4096\nv_cmp_eq_u32 vcc, 0, v54\nv_add_u32 v54, 2, v53\ns_nop 0\nv_cndmask_b32 v49, v49, v54, vcc\n"
"qm v,a,v,88,148,188\nqt 180,40,8192\nql 16,0,64,1,0\nv_cmp_le_u32 vcc, 14, v55\ns_nop 1\nv_cndmask_b32 v49, v49, v53, vcc\n"
"qm v,a,v,104,148,196\nqt 182,40,12288\ns_nop 0\nv_readlane_b32 s72, v49, 0\nv_cmp_eq_u32 vcc, s23, v49\ns_nop 1\n"
"v_mov_b32 v54, s72\nv_cndmask_b32 v49, v49, v54, vcc\nv_writelane_b32 v49, s23, 0\nqm v,a,v,104,144,192\nqw 12\nqt 184,41,0\n"
"ql 20,0,64,1,1024\nv_readlane_b32 s2, v18, 26\nqm v,a,v,108,152,192\ns_mul_i32 s72, s15, 114688\ns_mul_i32 s73, s23, 448\nqt 186,41,4096\n"
"qm v,a,v,108,156,196\ns_add_u32 m0, s0, 0x8000\ns_add_u32 s72, s72, s73\nqt 188,41,8192\nqm v,a,v,92,156,188\nqt 190,41,12288\n"
"v_mul_u32_u24 v48, 458752, v49\nqm v,a,v,92,152,184\nqw 12\nqt 192,42,0\nqd 12,60,2,0\nqm v,a,v,96,160,184\n"
"qt 194,42,4096\nv_add_u32 v48, s72, v48\nqm v,a,v,96,164,188\ns_lshl_b32 s73, s15, 1\ns_lshl3_add_u32 s73, s22, s73\nqt 196,42,8192\n"
"qm v,a,v,112,164,196\nqt 198,42,12288\nqd 13,60,2,1024\nqm v,a,v,112,160,192\nqw 12\ns_lshl_b32 s72, s73, 15\n"
"qt 200,43,0\nqm v,a,v,116,168,192\ns_add_u32 s72, s72, 0x3800000\nqt 202,43,4096\nv_lshl_add_u32 v49, v49, 11, s72\nqm v,a,v,116,172,196\n"
"qt 204,43,8192\nql 24,0,68,1,0\nqm v,a,v,100,172,188\nqt 206,43,12288\nv_add_u32 v50, 0x8000, v0\nqm v,a,v,100,168,184\n"
"qw 12\ns_mul_i32 s6, s73, 114688\nqt 144,36,16384\nqm v,a,v,120,176,184\nqt 146,36,20480\nql 28,0,68,1,1024\n"
"qm v,a,v,120,180,188\ns_mul_i32 s72, s23, 3072\ns_add_u32 s6, s6, s72\nqt 148,36,24576\nqm v,a,v,136,180,196\ns_add_u32 s6, s6, 0x3c00000\n"
"s_add_u32 s7, s6, 114688\nqt 150,36,28672\nqm v,a,v,136,176,192\nqw 12\nqt 152,37,16384\nqd 14,60,2,2048\n"
"qm v,a,v,140,184,192\ns_lshl2_add_u32 s72, s22, s15\ns_lshl_b32 s10, s72, 12\nqt 154,37,20480\nqm v,a,v,140,188,196\ns_add_u32 s10, s10, 0x10000\n"
"s_lshl_b32 s9, s72, 7\nqt 156,37,24576\nqm v,a,v,124,188,188\ns_add_u32 s9, s9, 0x9000\nqt 158,37,28672\nv_and_b32 v51, 15, v53\n"
"qm v,a,v,124,184,184\nqw 12\nqt 160,38,16384\nqd 15,60,2,3072\nqm v,a,v,128,192,184\nqt 162,38,20480\n"
"v_readlane_b32 s1, v17, 26\nqm v,a,v,128,196,188\nqt 164,38,24576\nv_lshlrev_b32 v51, 3, v51\nqm v,a,v,144,196,196\nqt 166,38,28672\n"
"ql 32,0,64,1,0\nqm v,a,v,144,192,192\nqw 12\nqt 168,39,16384\nv_lshrrev_b32 v54, 4, v53\nqm v,a,v,148,200,192\n"
"qt 170,39,20480\nv_bfe_u32 v55, v53, 2, 2\nqm v,a,v,148,204,196\nqt 172,39,24576\nql 36,0,64,1,1024\nqm v,a,v,132,204,188\n"
"qt 174,39,28672\nv_lshl_add_u32 v55, v54, 2, v55\nqm v,a,v,132,200,184\nqw 12\nqv 36\nqt 176,40,16384\n"
"qm v,a,v,88,144,200\nqt 178,40,20480\nv_mul_u32_u24 v55, 192, v55\nqm v,a,v,88,148,204\nqt 180,40,24576\nv_and_b32 v248, 3, v53\n"
"qm v,a,v,104,148,212\nqt 182,40,28672\nql 40,0,68,1,0\nqm v,a,v,104,144,208\nqw 12\nqt 184,41,16384\n"
"v_lshl_add_u32 v55, v248, 3, v55\nqm v,a,v,108,152,208\nqt 186,41,20480\nv_and_b32 v248, 1, v54\nqm v,a,v,108,156,212\nqt 188,41,24576\n"
"v_lshl_add_u32 v44, v248, 5, v55\nqm v,a,v,92,156,204\nqt 190,41,28672\nql 44,0,68,1,1024\nqm v,a,v,92,152,200\nqw 12\n"
"qt 192,42,16384\nv_xor_b32 v248, 1, v248\nqm v,a,v,96,160,200\nqt 194,42,20480\nv_lshl_add_u32 v45, v248, 5, v55\nv_readlane_b32 s2, v18, 27\n"
"qm v,a,v,96,164,204\ns_lshl_b32 s75, s75, 1\ns_add_u32 s75, s75, 0x20000\nqt 196,42,24576\nqm v,a,v,112,164,212\ns_add_u32 m0, s0, 0xc000\n"
"qt 198,42,28672\nv_lshl_add_u32 v46, v54, 4, s75\nqm v,a,v,112,160,208\nqw 12\ns_mul_i32 s75, s74, 384\nqt 200,43,16384\n"
"qm v,a,v,116,168,208\nqt 202,43,20480\nqt 204,43,24576\nqt 206,43,28672\nqv 30\ns_barrier\n"
"qd 12,60,2,0\nqm v,a,v,116,172,212\ns_add_u32 s75, s75, 0x20000\nqt 144,36,32768\nv_lshl_add_u32 v47, v54, 4, s75\nqm v,a,v,100,172,204\n"
"s_mul_i32 s11, s23, 7\ns_lshr_b32 s11, s11, 2\nqt 146,36,36864\nqm v,a,v,100,168,200\nqw 14\ns_min_u32 s11, s11, 25\n"
"qt 148,36,40960\nqm v,a,v,120,176,200\nqd 13,60,2,1024\nqm v,a,v,120,180,204\ns_mul_i32 s11, s11, 32\nqt 150,36,45056\n"
"v_readlane_b32 s1, v17, 27\nqm v,a,v,136,180,212\ns_add_u32 s11, s11, 0x70000\ns_mul_i32 s12, s23, 896\nqt 152,37,32768\nqm v,a,v,136,176,208\n"
"qw 13\nql 48,0,64,1,0\nqm v,a,v,140,184,208\ns_add_u32 s12, s12, 0x50000\ns_lshl_b32 s72, s15, 8\nqt 154,37,36864\n"
"qm v,a,v,140,188,212\nqt 156,37,40960\nv_lshl_add_u32 v52, v53, 2, s72\nqm v,a,v,124,188,204\nqt 158,37,45056\nql 52,0,64,1,1024\n"
"qm v,a,v,124,184,200\nqw 12\nqt 160,38,32768\nv_min_u32 v53, 11, v53\nqm v,a,v,128,192,200\nqt 162,38,36864\n"
"v_lshlrev_b32 v53, 3, v53\nqm v,a,v,128,196,204\nqt 164,38,40960\nqd 14,60,2,2048\nqm v,a,v,144,196,212\nqt 166,38,45056\n"
"qm v,a,v,144,192,208\nqw 12\nqt 168,39,32768\nqm v,a,v,148,200,208\nqt 170,39,36864\nqd 15,60,2,3072\n"
"qm v,a,v,148,204,212\nqt 172,39,40960\nqm v,a,v,132,204,204\nqt 174,39,45056\nqm v,a,v,132,200,200\nqw 12\n"
"qv 34\nqt 176,40,32768\nql 56,0,68,1,0\nqm v,a,v,88,144,216\ns_lshl2_add_u32 s84, s14, s15\ns_lshl3_add_u32 s86, s84, 0x72000\n"
"qt 178,40,36864\nqm v,a,v,88,148,220\ns_xor_b32 s87, s86, 32\ns_lshl_b32 s84, s84, 13\nqt 180,40,40960\nqm v,a,v,104,148,228\n"
"s_add_u32 s84, s84, 0x5000000\ns_add_u32 s85, s84, 0x1000\nqt 182,40,45056\nqm v,a,v,104,144,224\nqw 12\nqt 184,41,32768\n"
"buffer_load_dwordx2 v[248:249], v53, s[56:59], s11 offen sc1\nql 60,0,68,1,1024\nv_readlane_b32 s2, v18, 28\nqm v,a,v,108,152,224\nqt 186,41,36864\nv_mov_b32 v11, 0\n"
"qm v,a,v,108,156,228\ns_add_u32 m0, s0, 0x10000\nqt 188,41,40960\nqm v,a,v,92,156,220\nqt 190,41,45056\nqm v,a,v,92,152,216\n"
"qw 12\nqt 192,42,32768\nqd 12,60,2,0\nqm v,a,v,96,160,216\nqt 194,42,36864\nqm v,a,v,96,164,220\n"
"qt 196,42,40960\nqm v,a,v,112,164,228\nqt 198,42,45056\nqd 13,60,2,1024\nqm v,a,v,112,160,224\nqw 12\n"
"qt 200,43,32768\nqm v,a,v,116,168,224\nqt 202,43,36864\nqm v,a,v,116,172,228\nqt 204,43,40960\nqd 14,60,2,2048\n"
"qm v,a,v,100,172,220\nqt 206,43,45056\nqm v,a,v,100,168,216\nqw 12\nqt 144,36,49152\nqm v,a,v,120,176,216\n"
"qt 146,36,53248\nqd 15,60,2,3072\nqm v,a,v,120,180,220\nqt 148,36,57344\nv_readlane_b32 s1, v17, 28\nqm v,a,v,136,180,228\n"
"qt 150,36,61440\nv_add_u32 v36, 0xffff0000, v36\nqm v,a,v,136,176,224\nqw 12\nqt 152,37,49152\nql 64,0,64,1,0\n"
"qm v,a,v,140,184,224\nqt 154,37,53248\nqm v,a,v,140,188,228\nqt 156,37,57344\nqm v,a,v,124,188,220\nqt 158,37,61440\n"
"ql 68,0,64,1,1024\nqm v,a,v,124,184,216\nv_add_u32 v37, 0xffff0000, v37\nqt 160,38,49152\nqw 13\nqm v,a,v,128,192,216\n"
"ql 72,0,68,1,0\nqv 8\nv_cmp_eq_u64 vcc, s[16:17], v[248:249]\nqt 162,38,53248\nqm v,a,v,128,196,220\nqt 164,38,57344\n"
"qm v,a,v,144,196,228\nqt 166,38,61440\nv_add_u32 v38, 0xffff0000, v38\nqm v,a,v,144,192,224\nqw 12\nqt 168,39,49152\n"
"qm v,a,v,148,200,224\ns_cmp_eq_u64 vcc, exec\ns_cbranch_scc0 PCS_o%=\nPCS_r%=:\nqt 170,39,53248\nqm v,a,v,148,204,228\n"
"ql 76,0,68,1,1024\nqt 172,39,57344\nv_readlane_b32 s1, v17, 29\nqm v,a,v,132,204,220\nqt 174,39,61440\nv_add_u32 v39, 0xffff0000, v39\n"
"qm v,a,v,132,200,216\nqw 12\nqt 176,40,49152\nql 80,0,64,1,0\nqm v,a,v,88,144,232\nqt 178,40,53248\n"
"qm v,a,v,88,148,236\nqt 180,40,57344\nbuffer_load_dword v56, v52, s[56:59], s12 offen sc1\nqm v,a,v,104,148,244\ns_add_u32 s12, s12, 14336\nqt 182,40,61440\n"
"v_add_u32 v40, 0xffff0000, v40\nqm v,a,v,104,144,240\nqw 12\nqt 184,41,49152\nqm v,a,v,108,152,240\nqt 186,41,53248\n"
"ql 84,0,64,1,1024\nqm v,a,v,108,156,244\nqt 188,41,57344\nqm v,a,v,92,156,236\nqt 190,41,61440\nv_add_u32 v41, 0xffff0000, v41\n"
"qm v,a,v,92,152,232\nqw 12\nqt 192,42,49152\nbuffer_load_dword v57, v52, s[56:59], s12 offen sc1\nqm v,a,v,96,160,232\ns_add_u32 s12, s12, 14336\n"
"qt 194,42,53248\nqm v,a,v,96,164,236\nqt 196,42,57344\nqm v,a,v,112,164,244\nqt 198,42,61440\nbuffer_load_dword v58, v52, s[56:59], s12 offen sc1\n"
"qm v,a,v,112,160,240\nv_add_u32 v42, 0xffff0000, v42\nqt 200,43,49152\ns_add_u32 s12, s12, 14336\nqw 13\nqm v,a,v,116,168,240\n"
"qt 202,43,53248\nqm v,a,v,116,172,244\nqt 204,43,57344\nqt 206,43,61440\nv_add_u32 v43, 0xffff0000, v43\ns_barrier\n"
"ql 88,0,68,1,0\nqm v,a,v,100,172,236\nqt 144,40,0\nqm v,a,v,100,168,232\nqw 13\nqt 146,40,4096\n"
"qm v,a,v,120,176,232\nbuffer_load_dword v59, v52, s[56:59], s12 offen sc1\nqm v,a,v,120,180,236\ns_add_u32 s12, s12, 14336\nqt 148,40,8192\nqm v,a,v,136,180,244\n"
"qt 150,40,12288\nql 92,0,68,1,1024\nv_readlane_b32 s2, v18, 29\nqm v,a,v,136,176,240\nqw 12\nqt 152,41,0\n"
"qm v,a,v,140,184,240\ns_add_u32 m0, s0, 0x14000\nqt 154,41,4096\nqm v,a,v,140,188,244\nqt 156,41,8192\nqm v,a,v,124,188,236\n"
"qt 158,41,12288\nqd 12,60,2,0\nqm v,a,v,124,184,232\nqw 12\nqt 160,42,0\nqm v,a,v,128,192,232\n"
"qt 162,42,4096\nqm v,a,v,128,196,236\nqt 164,42,8192\nqd 13,60,2,1024\nqm v,a,v,144,196,244\nqt 166,42,12288\n"
"v_readlane_b32 s1, v17, 30\nqm v,a,v,144,192,240\nqw 12\nqt 168,43,0\nqm v,a,v,148,200,240\nqt 170,43,4096\n"
"ql 96,0,64,1,0\nqm v,a,v,148,204,244\nqt 172,43,8192\nqm v,a,v,132,204,236\nqt 174,43,12288\nqm v,a,v,132,200,232\n"
"qw 12\nqt 176,40,16384\nql 100,0,64,1,1024\nqm v,a,a,120,144,0\nqt 178,40,20480\nqm v,a,a,120,148,4\n"
"qt 180,40,24576\nqm v,a,a,136,148,12\nqt 182,40,28672\nqd 14,60,2,2048\nqm v,a,a,136,144,8\nqw 12\n"
"qt 184,41,16384\nqm v,a,a,140,152,8\nqt 186,41,20480\nqd 15,60,2,3072\nv_readlane_b32 s2, v18, 30\nqm v,a,a,140,156,12\n"
"qt 188,41,24576\nqm v,a,a,124,156,4\ns_add_u32 m0, s0, 0x18000\nqt 190,41,28672\nqm v,a,a,124,152,0\nqw 12\n"
"qt 192,42,16384\nqm v,a,a,128,160,0\nqt 194,42,20480\nqd 12,60,2,0\nqm v,a,a,128,164,4\nqt 196,42,24576\n"
"qm v,a,a,144,164,12\nqt 198,42,28672\nqm v,a,a,144,160,8\nqw 12\nqt 200,43,16384\nqd 13,60,2,1024\n"
"qm v,a,a,148,168,8\nqt 202,43,20480\nqm v,a,a,148,172,12\nqt 204,43,24576\nqt 206,43,28672\ns_barrier\n"
"qm v,a,a,132,172,4\nql 104,0,68,1,0\nqm v,a,a,132,168,0\nqw 12\nqt 144,40,32768\nqm v,a,a,120,176,16\n"
"qt 146,40,36864\nqm v,a,a,120,180,20\nqt 148,40,40960\nql 108,0,68,1,1024\nqm v,a,a,136,180,28\nqt 150,40,45056\n"
"qm v,a,a,136,176,24\nqw 12\nqt 152,41,32768\nqm v,a,a,140,184,24\nqt 154,41,36864\nqd 14,60,2,2048\n"
"qm v,a,a,140,188,28\nqt 156,41,40960\nqm v,a,a,124,188,20\nqt 158,41,45056\nqd 15,60,2,3072\nv_readlane_b32 s2, v18, 31\n"
"qm v,a,a,124,184,16\nqw 12\nqt 160,42,32768\nqm v,a,a,128,192,16\ns_add_u32 m0, s0, 0x1c000\nqt 162,42,36864\n"
"qm v,a,a,128,196,20\nqt 164,42,40960\nqm v,a,a,144,196,28\nqt 166,42,45056\nqd 12,60,2,0\nqm v,a,a,144,192,24\n"
"qw 12\nqt 168,43,32768\nqm v,a,a,148,200,24\nqt 170,43,36864\nqm v,a,a,148,204,28\nqt 172,43,40960\n"
"qd 13,60,2,1024\nqm v,a,a,132,204,20\nqt 174,43,45056\nv_readlane_b32 s1, v17, 31\nqm v,a,a,132,200,16\nqw 12\n"
"qt 176,40,49152\nqm v,a,a,120,144,32\nqt 178,40,53248\nql 112,0,64,1,0\nqm v,a,a,120,148,36\nqt 180,40,57344\n"
"qm v,a,a,136,148,44\nqt 182,40,61440\nv_add_u32 v40, 0x10000, v40\nqm v,a,a,136,144,40\nqw 12\nqt 184,41,49152\n"
"ql 116,0,64,1,1024\nqm v,a,a,140,152,40\nqt 186,41,53248\nqm v,a,a,140,156,44\nqt 188,41,57344\nqm v,a,a,124,156,36\n"
"qt 190,41,61440\nqd 14,60,2,2048\nqm v,a,a,124,152,32\nv_add_u32 v41, 0x10000, v41\nqt 192,42,49152\nqw 13\n"
"qm v,a,a,128,160,32\nqt 194,42,53248\nqm v,a,a,128,164,36\nqt 196,42,57344\nqd 15,60,2,3072\nqm v,a,a,144,164,44\n"
"qt 198,42,61440\nv_add_u32 v42, 0x10000, v42\nqm v,a,a,144,160,40\nqw 12\nqt 200,43,49152\nqm v,a,a,148,168,40\n"
"qt 202,43,53248\nql 120,0,68,1,0\nqm v,a,a,148,172,44\nqt 204,43,57344\nqt 206,43,61440\nv_add_u32 v43, 0x10000, v43\n"
"qv 13\ns_barrier\nqm v,a,a,132,172,36\nqt 144,40,0\nqm v,a,a,132,168,32\nqw 13\n"
"ql 124,0,68,1,1024\nqm v,a,a,120,176,48\nqt 146,40,4096\nqm v,a,a,120,180,52\nqt 148,40,8192\nqm v,a,a,136,180,60\n"
"qt 150,40,12288\nqm v,a,a,136,176,56\nqw 12\nqt 152,41,0\nqm v,a,a,140,184,56\nqt 154,41,4096\n"
"qm v,a,a,140,188,60\nqt 156,41,8192\nqm v,a,a,124,188,52\nqt 158,41,12288\nqm v,a,a,124,184,48\nqw 12\n"
"qt 160,42,0\nqm v,a,a,128,192,48\nqt 162,42,4096\nqm v,a,a,128,196,52\nqt 164,42,8192\nqm v,a,a,144,196,60\n"
"qt 166,42,12288\nqm v,a,a,144,192,56\nqw 12\nqt 168,43,0\nqm v,a,a,148,200,56\nqt 170,43,4096\n"
"qm v,a,a,148,204,60\nqt 172,43,8192\nqm v,a,a,132,204,52\nqt 174,43,12288\nqm v,a,a,132,200,48\nqw 12\n"
"qt 176,40,16384\nqm v,a,a,120,144,64\nqt 178,40,20480\nqm v,a,a,120,148,68\nqt 180,40,24576\nqm v,a,a,136,148,76\n"
"qt 182,40,28672\nqm v,a,a,136,144,72\nqw 12\nqt 184,41,16384\nqm v,a,a,140,152,72\nqt 186,41,20480\n"
"qm v,a,a,140,156,76\nqt 188,41,24576\nqm v,a,a,124,156,68\nqt 190,41,28672\nqm v,a,a,124,152,64\nqw 12\n"
"qt 192,42,16384\nqm v,a,a,128,160,64\nqt 194,42,20480\nqm v,a,a,128,164,68\nqt 196,42,24576\nqm v,a,a,144,164,76\n"
"qt 198,42,28672\nqm v,a,a,144,160,72\nqw 12\nqt 200,43,16384\nqm v,a,a,148,168,72\nqt 202,43,20480\n"
"qm v,a,a,148,172,76\nqt 204,43,24576\nqt 206,43,28672\nqv 2\ns_barrier\nqm v,a,a,132,172,68\n"
"qt 144,40,32768\nqm v,a,a,132,168,64\nqw 13\nqt 146,40,36864\nqm v,a,a,120,176,80\nqt 148,40,40960\n"
"qm v,a,a,120,180,84\nqt 150,40,45056\nqm v,a,a,136,180,92\nqt 152,41,32768\nqm v,a,a,136,176,88\nqw 13\n"
"qt 154,41,36864\nqm v,a,a,140,184,88\nqt 156,41,40960\nqm v,a,a,140,188,92\nqt 158,41,45056\nqm v,a,a,124,188,84\n"
"qt 160,42,32768\nqm v,a,a,124,184,80\nqw 13\nqt 162,42,36864\nqm v,a,a,128,192,80\nqt 164,42,40960\n"
"qm v,a,a,128,196,84\nqt 166,42,45056\nqm v,a,a,144,196,92\nqt 168,43,32768\nqm v,a,a,144,192,88\nqw 13\n"
"qt 170,43,36864\nqm v,a,a,148,200,88\nqt 172,43,40960\nqm v,a,a,148,204,92\nqt 174,43,45056\nqm v,a,a,132,204,84\n"
"qt 176,40,49152\nqm v,a,a,132,200,80\nqw 13\nqt 178,40,53248\nqm v,a,a,120,144,96\nqt 180,40,57344\n"
"v_add_f32 v56, v56, v57\nqm v,a,a,120,148,100\nqt 182,40,61440\nv_add_u32 v40, 0xffff0000, v40\nqm v,a,a,136,148,108\nqt 184,41,49152\n"
"v_add_f32 v58, v58, v59\nqm v,a,a,136,144,104\nqw 13\nqt 186,41,53248\nv_add_f32 v56, v56, v58\nqm v,a,a,140,152,104\n"
"qt 188,41,57344\nv_add_u32 v57, 0x20000, v52\nqm v,a,a,140,156,108\nqt 190,41,61440\nv_add_u32 v41, 0xffff0000, v41\nqm v,a,a,124,156,100\n"
"qt 192,42,49152\nqm v,a,a,124,152,96\nqw 13\nqt 194,42,53248\nqm v,a,a,128,160,96\nqt 196,42,57344\n"
"qm v,a,a,128,164,100\nqt 198,42,61440\nv_add_u32 v42, 0xffff0000, v42\nqm v,a,a,144,164,108\nqt 200,43,49152\nqm v,a,a,144,160,104\n"
"qw 13\nqt 202,43,53248\nqm v,a,a,148,168,104\nqt 204,43,57344\nqm v,a,a,148,172,108\nqt 206,43,61440\n"
"v_add_u32 v43, 0xffff0000, v43\nqm v,a,a,132,172,100\nqt 144,36,0\nqm v,a,a,132,168,96\nqw 13\nqv 0\n"
"qt 146,36,4096\nqm v,a,a,120,176,112\nds_write_b32 v57, v56\nqm v,a,a,120,180,116\nqt 148,36,8192\nqm v,a,a,136,180,124\n"
"qt 150,36,12288\nqm v,a,a,136,176,120\nqw 13\nqt 152,37,0\nqm v,a,a,140,184,120\nqt 154,37,4096\n"
"qm v,a,a,140,188,124\nqt 156,37,8192\nqm v,a,a,124,188,116\nqt 158,37,12288\nqm v,a,a,124,184,112\nqw 13\n"
"qt 160,38,0\nqm v,a,a,128,192,112\nqt 162,38,4096\nqm v,a,a,128,196,116\nqt 164,38,8192\nqm v,a,a,144,196,124\n"
"qt 166,38,12288\nqm v,a,a,144,192,120\nqw 13\nqt 168,39,0\nqm v,a,a,148,200,120\nqt 170,39,4096\n"
"qm v,a,a,148,204,124\nqt 172,39,8192\nqm v,a,a,132,204,116\nqt 174,39,12288\nqm v,a,a,132,200,112\nqw 12\n"
"qt 176,36,16384\nqm v,a,a,88,144,0\nqt 178,36,20480\nqm v,a,a,88,148,4\nqt 180,36,24576\nqm v,a,a,104,148,12\n"
"qt 182,36,28672\nqm v,a,a,104,144,8\nqw 12\nqt 184,37,16384\nqm v,a,a,108,152,8\nqt 186,37,20480\n"
"qm v,a,a,108,156,12\nqt 188,37,24576\nqc 120,120\nqm v,a,a,92,156,4\nqt 190,37,28672\nqc 121,122\n"
"qm v,a,a,92,152,0\nqw 12\nqt 192,38,16384\nqc 122,124\nqm v,a,a,96,160,0\nqt 194,38,20480\n"
"qc 123,126\nqm v,a,a,96,164,4\nqt 196,38,24576\nqc 128,128\nqm v,a,a,112,164,12\nqt 198,38,28672\n"
"buffer_store_dwordx4 v[120:123], v0, s[56:59], s84 offen offset:0 sc1\nqc 129,130\nqm v,a,a,112,160,8\nqw 12\nqt 200,39,16384\nqc 130,132\n"
"qm v,a,a,116,168,8\nqt 202,39,20480\nqc 131,134\nqm v,a,a,116,172,12\nqt 204,39,24576\nqc 136,136\n"
"qm v,a,a,100,172,4\nqt 206,39,28672\nbuffer_store_dwordx4 v[128:131], v0, s[56:59], s84 offen offset:1024 sc1\nqc 137,138\nqm v,a,a,100,168,0\nqw 12\n"
"qt 144,36,32768\nqc 138,140\nqm v,a,a,88,176,16\nqt 146,36,36864\nqc 139,142\nqm v,a,a,88,180,20\n"
"qt 148,36,40960\nqc 144,144\nqm v,a,a,104,180,28\nqt 150,36,45056\nqc 145,146\nqm v,a,a,104,176,24\n"
"qw 12\nqt 152,37,32768\nbuffer_store_dwordx4 v[136:139], v0, s[56:59], s84 offen offset:2048 sc1\nqc 146,148\nqm v,a,a,108,184,24\nqt 154,37,36864\n"
"qc 147,150\nqm v,a,a,108,188,28\nqt 156,37,40960\nqm v,a,a,92,188,20\nqt 158,37,45056\nqm v,a,a,92,184,16\n"
"qw 12\nqt 160,38,32768\nqm v,a,a,96,192,16\nqt 162,38,36864\nbuffer_store_dwordx4 v[144:147], v0, s[56:59], s84 offen offset:3072 sc1\nqm v,a,a,96,196,20\n"
"qt 164,38,40960\nqm v,a,a,112,196,28\nqt 166,38,45056\nqm v,a,a,112,192,24\nqw 12\nqt 168,39,32768\n"
"qm v,a,a,116,200,24\nqt 170,39,36864\nqm v,a,a,116,204,28\nqt 172,39,40960\nqm v,a,a,100,204,20\nqt 174,39,45056\n"
"qm v,a,a,100,200,16\nqw 12\nqt 176,36,49152\nqm v,a,a,88,144,32\nqt 178,36,53248\nqm v,a,a,88,148,36\n"
"qt 180,36,57344\nqm v,a,a,104,148,44\nqt 182,36,61440\nv_add_u32 v36, 0x10000, v36\nqm v,a,a,104,144,40\nqw 12\n"
"qt 184,37,49152\nqm v,a,a,108,152,40\nqt 186,37,53248\nqm v,a,a,108,156,44\nqt 188,37,57344\nqm v,a,a,92,156,36\n"
"qt 190,37,61440\nv_add_u32 v37, 0x10000, v37\nqm v,a,a,92,152,32\nqw 12\nqt 192,38,49152\nqm v,a,a,96,160,32\n"
"qt 194,38,53248\nqm v,a,a,96,164,36\nqt 196,38,57344\nqm v,a,a,112,164,44\nqt 198,38,61440\nv_add_u32 v38, 0x10000, v38\n"
"qm v,a,a,112,160,40\nqw 12\nqt 200,39,49152\nqm v,a,a,116,168,40\nqt 202,39,53248\nqm v,a,a,116,172,44\n"
"qt 204,39,57344\nqm v,a,a,100,172,36\nqt 206,39,61440\nv_add_u32 v39, 0x10000, v39\nqm v,a,a,100,168,32\nqw 12\n"
"qt 144,36,0\nqm v,a,a,88,176,48\nqt 146,36,4096\nqm v,a,a,88,180,52\nqt 148,36,8192\nqm v,a,a,104,180,60\n"
"qt 150,36,12288\nqm v,a,a,104,176,56\nqw 12\nqt 152,37,0\nqm v,a,a,108,184,56\nqt 154,37,4096\n"
"qm v,a,a,108,188,60\nqt 156,37,8192\nqm v,a,a,92,188,52\nqt 158,37,12288\nqm v,a,a,92,184,48\nqw 12\n"
"qt 160,38,0\nqm v,a,a,96,192,48\nqt 162,38,4096\nqm v,a,a,96,196,52\nqt 164,38,8192\nqm v,a,a,112,196,60\n"
"qt 166,38,12288\nqm v,a,a,112,192,56\nqw 12\nqv 0\nqt 168,39,0\nqm v,a,a,116,200,56\n"
"qt 170,39,4096\nqm v,a,a,116,204,60\nqt 172,39,8192\nqm v,a,a,100,204,52\nqt 174,39,12288\nqm v,a,a,100,200,48\n"
"qw 12\nqt 176,36,16384\nqm v,a,a,88,144,64\nqt 178,36,20480\nqm v,a,a,88,148,68\ns_barrier\n"
"qt 180,36,24576\ns_mov_b64 exec, 1\nbuffer_store_dwordx2 v[250:251], off, s[56:59], s86 sc1\ns_mov_b64 exec, -1\nqm v,a,a,104,148,76\ns_xor_b32 s84, s84, 0x8000\n"
"s_xor_b32 s85, s85, 0x8000\nqt 182,36,28672\nqm v,a,a,104,144,72\nqw 12\nqt 184,37,16384\nv_readlane_b32 s82, v48, 0\n"
"qm v,a,a,108,152,72\nqt 186,37,20480\nqm v,a,a,108,156,76\ns_add_u32 m0, s81, 0xc000\nqt 188,37,24576\nqm v,a,a,92,156,68\n"
"qt 190,37,28672\nqm v,a,a,92,152,64\nqw 12\nqt 192,38,16384\nqd 4,76,82,0\nqm v,a,a,96,160,64\n"
"qt 194,38,20480\nqm v,a,a,96,164,68\nqt 196,38,24576\nqd 5,76,82,1024\nqm v,a,a,112,164,76\nqt 198,38,28672\n"
"qm v,a,a,112,160,72\nqw 12\nqt 200,39,16384\nqd 6,76,82,2048\nqm v,a,a,116,168,72\nqt 202,39,20480\n"
"qm v,a,a,116,172,76\nqt 204,39,24576\nqd 7,76,82,3072\nqm v,a,a,100,172,68\nqt 206,39,28672\nv_readlane_b32 s82, v48, 0\n"
"qm v,a,a,100,168,64\nqw 12\nqt 144,36,32768\nqm v,a,a,88,176,80\ns_add_u32 m0, s80, 0x0\nqt 146,36,36864\n"
"qm v,a,a,88,180,84\nqt 148,36,40960\nqm v,a,a,104,180,92\nqt 150,36,45056\nqm v,a,a,104,176,88\nqw 12\n"
"qt 152,37,32768\nqd 1,76,82,0\nqm v,a,a,108,184,88\nqt 154,37,36864\nqm v,a,a,108,188,92\nqt 156,37,40960\n"
"qd 2,76,82,1024\nqm v,a,a,92,188,84\nqt 158,37,45056\nqm v,a,a,92,184,80\nqw 12\nqt 160,38,32768\n"
"qd 3,76,82,2048\nqm v,a,a,96,192,80\nqt 162,38,36864\nv_readlane_b32 s82, v48, 1\nqm v,a,a,96,196,84\nqt 164,38,40960\n"
"qm v,a,a,112,196,92\ns_add_u32 m0, s80, 0x4000\nqt 166,38,45056\nqm v,a,a,112,192,88\nqw 12\nqt 168,39,32768\n"
"qm v,a,a,116,200,88\nqd 1,76,82,0\nqt 170,39,36864\nqm v,a,a,116,204,92\nqt 172,39,40960\nqm v,a,a,100,204,84\n"
"qd 2,76,82,1024\nqt 174,39,45056\nqm v,a,a,100,200,80\nqw 12\nqt 176,36,49152\nqm v,a,a,88,144,96\n"
"qd 3,76,82,2048\nbuffer_load_dwordx2 v[248:249], v11, s[56:59], s87 offen sc1\nqt 178,36,53248\nqm v,a,a,88,148,100\nqt 180,36,57344\nqm v,a,a,104,148,108\n"
"qt 182,36,61440\nv_add_u32 v36, 0xffff0000, v36\nqm v,a,a,104,144,104\nqw 12\nqt 184,37,49152\nqm v,a,a,108,152,104\n"
"qt 186,37,53248\nqm v,a,a,108,156,108\nqt 188,37,57344\nqm v,a,a,92,156,100\nqt 190,37,61440\nv_add_u32 v37, 0xffff0000, v37\n"
"qm v,a,a,92,152,96\nqw 12\nqt 192,38,49152\nqm v,a,a,96,160,96\nqt 194,38,53248\nqm v,a,a,96,164,100\n"
"qt 196,38,57344\nqm v,a,a,112,164,108\nqv 0\nqt 198,38,61440\nv_add_u32 v38, 0xffff0000, v38\nqm v,a,a,112,160,104\n"
"qw 12\nqt 200,39,49152\nv_cmp_eq_u64 vcc, s[16:17], v[248:249]\nqm v,a,a,116,168,104\nqt 202,39,53248\nqm v,a,a,116,172,108\n"
"qt 204,39,57344\nqm v,a,a,100,172,100\ns_cmp_eq_u64 vcc, exec\ns_cbranch_scc0 PXG2_o%=\nPXG2_r%=:\nqt 206,39,61440\n"
"qm v,a,a,100,168,96\nqw 12\nv_add_u32 v39, 0xffff0000, v39\nqm v,a,a,88,176,112\nbuffer_load_dwordx4 v[184:187], v0, s[56:59], s84 offen offset:0 sc1\nqm v,a,a,88,180,116\n"
"qm v,a,a,104,180,124\nqm v,a,a,104,176,120\nqw 8\nqm v,a,a,108,184,120\nbuffer_load_dwordx4 v[188:191], v0, s[56:59], s84 offen offset:1024 sc1\nqm v,a,a,108,188,124\n"
"qm v,a,a,92,188,116\nbuffer_load_dwordx4 v[192:195], v0, s[56:59], s84 offen offset:2048 sc1\nqm v,a,a,92,184,112\nqw 4\nqm v,a,a,96,192,112\nqm v,a,a,96,196,116\n"
"qm v,a,a,112,196,124\nbuffer_load_dwordx4 v[196:199], v0, s[56:59], s84 offen offset:3072 sc1\nqm v,a,a,112,192,120\nqw 0\nqm v,a,a,116,200,120\nqm v,a,a,116,204,124\n"
"qm v,a,a,100,204,116\nqm v,a,a,100,200,112\ns_lshl_b32 s73, s15, 1\ns_lshl3_add_u32 s73, s22, s73\ns_lshl_b32 s74, s73, 15\ns_lshl_b32 s75, s23, 11\n"
"s_add_u32 s74, s74, s75\ns_add_u32 s74, s74, 0x3800000\ns_add_u32 s75, s74, 0x8000\nqv 2\nv_lshlrev_b32 v152, 16, v184\nv_and_b32 v153, 0xffff0000, v184\n"
"v_lshlrev_b32 v154, 16, v185\nv_and_b32 v155, 0xffff0000, v185\nv_lshlrev_b32 v156, 16, v186\nv_and_b32 v157, 0xffff0000, v186\nv_lshlrev_b32 v158, 16, v187\nv_and_b32 v159, 0xffff0000, v187\n"
"v_lshlrev_b32 v160, 16, v188\nv_and_b32 v161, 0xffff0000, v188\nv_lshlrev_b32 v162, 16, v189\nv_and_b32 v163, 0xffff0000, v189\nv_lshlrev_b32 v164, 16, v190\nv_and_b32 v165, 0xffff0000, v190\n"
"v_lshlrev_b32 v166, 16, v191\nv_and_b32 v167, 0xffff0000, v191\nv_pk_add_f32 v[88:89], v[88:89], v[152:153]\nv_pk_add_f32 v[90:91], v[90:91], v[154:155]\nv_pk_add_f32 v[92:93], v[92:93], v[156:157]\nv_pk_add_f32 v[94:95], v[94:95], v[158:159]\n"
"v_pk_add_f32 v[96:97], v[96:97], v[160:161]\nv_pk_add_f32 v[98:99], v[98:99], v[162:163]\nv_pk_add_f32 v[100:101], v[100:101], v[164:165]\nv_pk_add_f32 v[102:103], v[102:103], v[166:167]\nqc 20,88\nqc 21,90\n"
"qc 22,92\nqc 23,94\nqc 24,96\nqc 25,98\nqc 26,100\nqc 27,102\n"
"buffer_store_dwordx4 v[20:23], v0, s[56:59], s74 offen offset:0 sc1\nbuffer_store_dwordx4 v[24:27], v0, s[56:59], s74 offen offset:1024 sc1\nqv 2\nv_lshlrev_b32 v168, 16, v192\nv_and_b32 v169, 0xffff0000, v192\nv_lshlrev_b32 v170, 16, v193\n"
"v_and_b32 v171, 0xffff0000, v193\nv_lshlrev_b32 v172, 16, v194\nv_and_b32 v173, 0xffff0000, v194\nv_lshlrev_b32 v174, 16, v195\nv_and_b32 v175, 0xffff0000, v195\nv_lshlrev_b32 v176, 16, v196\n"
"v_and_b32 v177, 0xffff0000, v196\nv_lshlrev_b32 v178, 16, v197\nv_and_b32 v179, 0xffff0000, v197\nv_lshlrev_b32 v180, 16, v198\nv_and_b32 v181, 0xffff0000, v198\nv_lshlrev_b32 v182, 16, v199\n"
"v_and_b32 v183, 0xffff0000, v199\nv_pk_add_f32 v[104:105], v[104:105], v[168:169]\nv_pk_add_f32 v[106:107], v[106:107], v[170:171]\nv_pk_add_f32 v[108:109], v[108:109], v[172:173]\nv_pk_add_f32 v[110:111], v[110:111], v[174:175]\nv_pk_add_f32 v[112:113], v[112:113], v[176:177]\n"
"v_pk_add_f32 v[114:115], v[114:115], v[178:179]\nv_pk_add_f32 v[116:117], v[116:117], v[180:181]\nv_pk_add_f32 v[118:119], v[118:119], v[182:183]\nqc 28,104\nqc 29,106\nqc 30,108\n"
"qc 31,110\nqc 32,112\nqc 33,114\nqc 34,116\nqc 35,118\nbuffer_store_dwordx4 v[28:31], v0, s[56:59], s75 offen offset:0 sc1\n"
"buffer_store_dwordx4 v[32:35], v0, s[56:59], s75 offen offset:1024 sc1\nv_pk_add_f32 v[152:153], v[88:89], v[90:91]\nv_pk_mul_f32 v[154:155], v[88:89], v[88:89]\nv_pk_fma_f32 v[154:155], v[90:91], v[90:91], v[154:155]\nv_pk_add_f32 v[152:153], v[152:153], v[92:93]\nv_pk_fma_f32 v[154:155], v[92:93], v[92:93], v[154:155]\n"
"v_pk_add_f32 v[152:153], v[152:153], v[94:95]\nv_pk_fma_f32 v[154:155], v[94:95], v[94:95], v[154:155]\nv_pk_add_f32 v[152:153], v[152:153], v[96:97]\nv_pk_fma_f32 v[154:155], v[96:97], v[96:97], v[154:155]\nv_pk_add_f32 v[152:153], v[152:153], v[98:99]\nv_pk_fma_f32 v[154:155], v[98:99], v[98:99], v[154:155]\n"
"v_pk_add_f32 v[152:153], v[152:153], v[100:101]\nv_pk_fma_f32 v[154:155], v[100:101], v[100:101], v[154:155]\nv_pk_add_f32 v[152:153], v[152:153], v[102:103]\nv_pk_fma_f32 v[154:155], v[102:103], v[102:103], v[154:155]\nv_add_f32 v152, v152, v153\nv_add_f32 v153, v154, v155\n"
"v_pk_add_f32 v[160:161], v[104:105], v[106:107]\nv_pk_mul_f32 v[162:163], v[104:105], v[104:105]\nv_pk_fma_f32 v[162:163], v[106:107], v[106:107], v[162:163]\nv_pk_add_f32 v[160:161], v[160:161], v[108:109]\nv_pk_fma_f32 v[162:163], v[108:109], v[108:109], v[162:163]\nv_pk_add_f32 v[160:161], v[160:161], v[110:111]\n"
"v_pk_fma_f32 v[162:163], v[110:111], v[110:111], v[162:163]\nv_pk_add_f32 v[160:161], v[160:161], v[112:113]\nv_pk_fma_f32 v[162:163], v[112:113], v[112:113], v[162:163]\nv_pk_add_f32 v[160:161], v[160:161], v[114:115]\nv_pk_fma_f32 v[162:163], v[114:115], v[114:115], v[162:163]\nv_pk_add_f32 v[160:161], v[160:161], v[116:117]\n"
"v_pk_fma_f32 v[162:163], v[116:117], v[116:117], v[162:163]\nv_pk_add_f32 v[160:161], v[160:161], v[118:119]\nv_pk_fma_f32 v[162:163], v[118:119], v[118:119], v[162:163]\nv_add_f32 v160, v160, v161\nv_add_f32 v161, v162, v163\ns_nop 0\n"
"v_permlane16_swap_b32 v152, v160\nv_permlane16_swap_b32 v153, v161\nv_pk_add_f32 v[152:153], v[152:153], v[160:161]\nv_mov_b32 v154, v152\nv_mov_b32 v155, v153\ns_nop 0\n"
"v_permlane32_swap_b32 v154, v152\nv_permlane32_swap_b32 v155, v153\nv_and_b32 v54, 0xff, v0\nv_bfe_u32 v55, v0, 8, 1\nv_lshl_add_u32 v54, v55, 3, v54\nv_pk_add_f32 v[152:153], v[152:153], v[154:155]\n"
"s_lshl_b32 s72, s23, 8\ns_add_u32 s72, s72, s10\ns_mov_b64 exec, 0xffffffff\nbuffer_store_dwordx2 v[152:153], v54, s[56:59], s72 offen sc1\ns_mov_b64 exec, -1\ns_or_b32 s72, s14, s15\n"
"s_cmp_eq_u32 s72, 0\ns_cselect_b32 s72, 0, 0x6000000\ns_mov_b64 exec, 1\nbuffer_store_dwordx2 v[250:251], off, s[56:59], s72 sc1\ns_mov_b64 exec, -1\nqb 240,v20\n"
"qb 241,v21\nqb 242,v22\nqb 243,v23\nqb 244,v24\nqb 245,v25\nqb 246,v26\n"
"qb 247,v27\nqb 248,v28\nqb 249,v29\nqb 250,v30\nqb 251,v31\nqb 252,v32\n"
"qb 253,v33\nqb 254,v34\nqb 255,v35\ns_lshl3_add_u32 s8, s23, s9\nqv 1\ns_mov_b64 exec, 1\n"
"buffer_store_dwordx2 v[250:251], off, s[56:59], s8 sc1\ns_mov_b64 exec, -1\ns_getpc_b64 s[72:73]\nCT9%=:\ns_add_u32 s72, s72, G3C%=-CT9%=\ns_addc_u32 s73, s73, 0\n"
"s_lshl2_add_u32 s74, s19, s15\ns_mul_i32 s74, s74, 384\nv_lshl_add_u32 v54, v0, 3, s74\ns_mov_b32 s74, FINISH%=-G3C%=\ns_mov_b32 s75, 0x20000\ns_bfm_b64 exec, 3, 0\n"
"buffer_load_dword v55, v54, s[72:75], 0 offen\ns_mov_b64 exec, -1\nqw 0\ns_barrier\nds_read_b64_tr_b16 v[224:225], v36 offset:49152\nds_read_b64_tr_b16 v[226:227], v36 offset:53248\n"
"ds_read_b64_tr_b16 v[228:229], v36 offset:57344\nds_read_b64_tr_b16 v[230:231], v36 offset:61440\nds_read_b64_tr_b16 v[232:233], v37 offset:49152\nds_read_b64_tr_b16 v[234:235], v37 offset:53248\nds_read_b64_tr_b16 v[236:237], v37 offset:57344\nds_read_b64_tr_b16 v[238:239], v37 offset:61440\n"
"qw 4\nqz v,v,a,136,224,240\nds_read_b64_tr_b16 v[56:57], v38 offset:49152\nds_read_b64_tr_b16 v[58:59], v38 offset:53248\nds_read_b64_tr_b16 v[60:61], v38 offset:57344\nds_read_b64_tr_b16 v[62:63], v38 offset:61440\n"
"v_readlane_b32 s82, v48, 2\nqm v,v,a,136,228,244\nds_read_b64_tr_b16 v[64:65], v39 offset:49152\nqz v,v,a,168,228,252\ns_add_u32 m0, s80, 0x8000\nds_read_b64_tr_b16 v[66:67], v39 offset:53248\n"
"qm v,v,a,168,224,248\nqw 6\nqd 1,76,82,0\nqz v,v,a,172,232,248\nds_read_b64_tr_b16 v[68:69], v39 offset:57344\nds_read_b64_tr_b16 v[70:71], v39 offset:61440\n"
"qm v,v,a,172,236,252\nds_read_b64_tr_b16 v[72:73], v40 offset:49152\nqz v,v,a,140,236,244\nqd 2,76,82,1024\nqm v,v,a,140,232,240\nqw 5\n"
"ds_read_b64_tr_b16 v[74:75], v40 offset:53248\nqz v,v,a,144,56,240\nds_read_b64_tr_b16 v[76:77], v40 offset:57344\nds_read_b64_tr_b16 v[78:79], v40 offset:61440\nqm v,v,a,144,60,244\nds_read_b64_tr_b16 v[80:81], v41 offset:49152\n"
"qz v,v,a,176,60,252\nds_read_b64_tr_b16 v[82:83], v41 offset:53248\nqm v,v,a,176,56,248\nqw 6\nds_read_b64_tr_b16 v[84:85], v41 offset:57344\nqz v,v,a,180,64,248\n"
"ds_read_b64_tr_b16 v[86:87], v41 offset:61440\nqm v,v,a,180,68,252\nqd 3,76,82,2048\nqz v,v,a,148,68,244\nds_read_b64_tr_b16 v[224:225], v42 offset:49152\nv_readlane_b32 s82, v48, 4\n"
"qm v,v,a,148,64,240\nqw 5\nds_read_b64_tr_b16 v[226:227], v42 offset:53248\nqz v,v,a,152,72,240\nds_read_b64_tr_b16 v[228:229], v42 offset:57344\nds_read_b64_tr_b16 v[230:231], v42 offset:61440\n"
"qm v,v,a,152,76,244\ns_add_u32 m0, s80, 0x10000\nds_read_b64_tr_b16 v[232:233], v43 offset:49152\nqz v,v,a,184,76,252\nqd 1,76,82,0\nqm v,v,a,184,72,248\n"
"qw 5\nds_read_b64_tr_b16 v[234:235], v43 offset:53248\nqz v,v,a,188,80,248\nds_read_b64_tr_b16 v[236:237], v43 offset:57344\nds_read_b64_tr_b16 v[238:239], v43 offset:61440\nqm v,v,a,188,84,252\n"
"ds_read_b64_tr_b16 v[56:57], v44 offset:0\nqz v,v,a,156,84,244\nds_read_b64_tr_b16 v[58:59], v44 offset:3072\nqm v,v,a,156,80,240\nqw 6\nds_read_b64_tr_b16 v[60:61], v44 offset:6144\n"
"qz v,v,a,160,224,240\nqd 2,76,82,1024\nqm v,v,a,160,228,244\nds_read_b64_tr_b16 v[62:63], v44 offset:9216\nqz v,v,a,192,228,252\nds_read_b64_tr_b16 v[64:65], v45 offset:0\n"
"qm v,v,a,192,224,248\nqw 5\nds_read_b64_tr_b16 v[66:67], v45 offset:3072\nqz v,v,a,196,232,248\nds_read_b64_tr_b16 v[68:69], v45 offset:6144\nqm v,v,a,196,236,252\n"
"ds_read_b64_tr_b16 v[70:71], v45 offset:9216\nqz v,v,a,164,236,244\nqd 3,76,82,2048\nqm v,v,a,164,232,240\ns_barrier\nv_readlane_b32 s82, v48, 3\n"
"s_add_u32 m0, s80, 0xc000\ns_nop 3\nqd 1,76,82,0\nqd 2,76,82,1024\nqd 3,76,82,2048\ns_sleep 0\n"
"s_mov_b32 s35, 200000\nPH%=:\nbuffer_load_dwordx2 v[248:249], v51, s[56:59], s9 offen offset:0 sc1\nqv 0\nv_cmp_eq_u64 vcc, s[16:17], v[248:249]\ns_nop 3\n"
"s_cmp_eq_u64 vcc, exec\ns_cbranch_scc1 PHx%=\ns_sleep 3\ns_sub_u32 s35, s35, 1\ns_cmp_lg_u32 s35, 0\ns_cbranch_scc1 PH%=\n"
"PHx%=:\nv_readlane_b32 s83, v49, 1\ns_nop 4\nql 0,0,56,83,0\nql 4,0,56,83,1024\nql 8,50,56,83,0\n"
"ql 12,50,56,83,1024\nv_readlane_b32 s83, v49, 2\ns_nop 4\nql 16,0,56,83,0\nql 20,0,56,83,1024\nql 24,50,56,83,0\n"
"ql 28,50,56,83,1024\nv_readlane_b32 s83, v49, 3\ns_nop 4\nql 32,0,56,83,0\nql 36,0,56,83,1024\nql 40,50,56,83,0\n"
"ql 44,50,56,83,1024\nqw 4\nqz v,v,a,88,56,240\nds_read_b64_tr_b16 v[72:73], v44 offset:64\nv_readlane_b32 s82, v48, 5\nqm v,v,a,88,60,244\n"
"ds_read_b64_tr_b16 v[74:75], v44 offset:3136\nqz v,v,a,112,60,252\ns_add_u32 m0, s80, 0x14000\nds_read_b64_tr_b16 v[76:77], v44 offset:6208\nds_read_b64_tr_b16 v[78:79], v44 offset:9280\nqm v,v,a,112,56,248\n"
"qw 4\nqd 1,76,82,0\nqz v,v,a,116,64,248\nds_read_b64_tr_b16 v[80:81], v45 offset:64\nqm v,v,a,116,68,252\nds_read_b64_tr_b16 v[82:83], v45 offset:3136\n"
"qz v,v,a,92,68,244\nds_read_b64_tr_b16 v[84:85], v45 offset:6208\nds_read_b64_tr_b16 v[86:87], v45 offset:9280\nqm v,v,a,92,64,240\nqw 4\nds_read_b64_tr_b16 v[224:225], v44 offset:128\n"
"qz v,v,a,96,72,240\nds_read_b64_tr_b16 v[226:227], v44 offset:3200\nqm v,v,a,96,76,244\nds_read_b64_tr_b16 v[228:229], v44 offset:6272\nqz v,v,a,120,76,252\nds_read_b64_tr_b16 v[230:231], v44 offset:9344\n"
"qm v,v,a,120,72,248\nqw 4\nds_read_b64_tr_b16 v[232:233], v45 offset:128\nqz v,v,a,124,80,248\nqd 2,76,82,1024\nqm v,v,a,124,84,252\n"
"ds_read_b64_tr_b16 v[234:235], v45 offset:3200\nqz v,v,a,100,84,244\nds_read_b64_tr_b16 v[236:237], v45 offset:6272\nds_read_b64_tr_b16 v[238:239], v45 offset:9344\nqm v,v,a,100,80,240\nqw 4\n"
"ds_read_b64_tr_b16 v[56:57], v44 offset:16384\nqz v,v,a,104,224,240\nds_read_b64_tr_b16 v[58:59], v44 offset:19456\nqm v,v,a,104,228,244\nds_read_b64_tr_b16 v[60:61], v44 offset:22528\nqz v,v,a,128,228,252\n"
"ds_read_b64_tr_b16 v[62:63], v44 offset:25600\nqm v,v,a,128,224,248\nqw 4\nds_read_b64_tr_b16 v[64:65], v45 offset:16384\nqz v,v,a,132,232,248\nds_read_b64_tr_b16 v[66:67], v45 offset:19456\n"
"qm v,v,a,132,236,252\nds_read_b64_tr_b16 v[68:69], v45 offset:22528\nqz v,v,a,108,236,244\nds_read_b64_tr_b16 v[70:71], v45 offset:25600\nqm v,v,a,108,232,240\nds_read_b64_tr_b16 v[72:73], v44 offset:16448\n"
"ds_read_b64_tr_b16 v[74:75], v44 offset:19520\nds_read_b64_tr_b16 v[76:77], v44 offset:22592\nds_read_b64_tr_b16 v[78:79], v44 offset:25664\nds_read_b64_tr_b16 v[80:81], v45 offset:16448\nds_read_b64_tr_b16 v[82:83], v45 offset:19520\nds_read_b64_tr_b16 v[84:85], v45 offset:22592\n"
"ds_read_b64_tr_b16 v[86:87], v45 offset:25664\nqd 3,76,82,2048\nG3C%=:\nqw 12\nqv 11\nqm v,v,a,88,56,0\n"
"ds_read_b64_tr_b16 v[224:225], v44 offset:16512\nv_readlane_b32 s83, v49, 4\nqm v,v,a,88,60,4\nds_read_b64_tr_b16 v[226:227], v44 offset:19584\nqm v,v,a,112,60,12\ns_nop 1\n"
"ql 48,0,56,83,0\nqm v,v,a,112,56,8\nqw 10\nds_read_b64_tr_b16 v[228:229], v44 offset:22656\nqm v,v,a,116,64,8\nql 52,0,56,83,1024\n"
"qm v,v,a,116,68,12\nds_read_b64_tr_b16 v[230:231], v44 offset:25728\nqm v,v,a,92,68,4\nds_read_b64_tr_b16 v[232:233], v45 offset:16512\nqm v,v,a,92,64,0\nqw 9\n"
"ds_read_b64_tr_b16 v[234:235], v45 offset:19584\nqm v,v,a,96,72,0\nql 56,50,56,83,0\nqm v,v,a,96,76,4\nds_read_b64_tr_b16 v[236:237], v45 offset:22656\nds_read_b64_tr_b16 v[238:239], v45 offset:25728\n"
"s_barrier\nqm v,v,a,120,76,12\nds_read_b64_tr_b16 v[56:57], v44 offset:32768\nqm v,v,a,120,72,8\nqw 9\nql 60,50,56,83,1024\n"
"qm v,v,a,124,80,8\nds_read_b64_tr_b16 v[58:59], v44 offset:35840\nqm v,v,a,124,84,12\nds_read_b64_tr_b16 v[60:61], v44 offset:38912\nqm v,v,a,100,84,4\nds_read_b64_tr_b16 v[62:63], v44 offset:41984\n"
"qm v,v,a,100,80,0\nqw 8\nds_read_b64_tr_b16 v[64:65], v45 offset:32768\nbuffer_load_dwordx4 v[200:203], v0, s[56:59], s10 offen offset:0 sc1\nqm v,v,a,104,224,0\nds_read_b64_tr_b16 v[66:67], v45 offset:35840\n"
"qm v,v,a,104,228,4\nds_read_b64_tr_b16 v[68:69], v45 offset:38912\nqm v,v,a,128,228,12\nds_read_b64_tr_b16 v[70:71], v45 offset:41984\nqm v,v,a,128,224,8\nqw 8\n"
"ds_read_b64_tr_b16 v[72:73], v44 offset:32832\nbuffer_load_dwordx4 v[204:207], v0, s[56:59], s10 offen offset:1024 sc1\nqm v,v,a,132,232,8\nds_read_b64_tr_b16 v[74:75], v44 offset:35904\nqm v,v,a,132,236,12\nds_read_b64_tr_b16 v[76:77], v44 offset:38976\n"
"qm v,v,a,108,236,4\nds_read_b64_tr_b16 v[78:79], v44 offset:42048\nbuffer_load_dwordx4 v[208:211], v0, s[56:59], s10 offen offset:2048 sc1\nqm v,v,a,108,232,0\nqw 8\nqv 14\n"
"ds_read_b64_tr_b16 v[80:81], v45 offset:32832\nqm v,v,a,88,56,16\nds_read_b64_tr_b16 v[82:83], v45 offset:35904\nqm v,v,a,88,60,20\nds_read_b64_tr_b16 v[84:85], v45 offset:38976\nqm v,v,a,112,60,28\n"
"ds_read_b64_tr_b16 v[86:87], v45 offset:42048\nbuffer_load_dwordx4 v[212:215], v0, s[56:59], s10 offen offset:3072 sc1\nqm v,v,a,112,56,24\nqw 8\nds_read_b64_tr_b16 v[224:225], v44 offset:32896\nv_readlane_b32 s83, v49, 5\n"
"qm v,v,a,116,64,24\nds_read_b64_tr_b16 v[226:227], v44 offset:35968\nqm v,v,a,116,68,28\nds_read_b64_tr_b16 v[228:229], v44 offset:39040\ns_nop 0\nql 64,0,56,83,0\n"
"qm v,v,a,92,68,20\nds_read_b64_tr_b16 v[230:231], v44 offset:42112\nqm v,v,a,92,64,16\nqw 8\nds_read_b64_tr_b16 v[232:233], v45 offset:32896\nqm v,v,a,96,72,16\n"
"ds_read_b64_tr_b16 v[234:235], v45 offset:35968\nqm v,v,a,96,76,20\nds_read_b64_tr_b16 v[236:237], v45 offset:39040\nds_read_b64_tr_b16 v[238:239], v45 offset:42112\ns_barrier\nql 68,0,56,83,1024\n"
"qm v,v,a,120,76,28\nds_read_b64_tr_b16 v[56:57], v44 offset:49152\nqm v,v,a,120,72,24\nqw 9\nds_read_b64_tr_b16 v[58:59], v44 offset:52224\nqm v,v,a,124,80,24\n"
"ds_read_b64_tr_b16 v[60:61], v44 offset:55296\nqm v,v,a,124,84,28\nql 72,50,56,83,0\nqm v,v,a,100,84,20\nds_read_b64_tr_b16 v[62:63], v44 offset:58368\nqm v,v,a,100,80,16\n"
"qw 8\nds_read_b64_tr_b16 v[64:65], v45 offset:49152\nqm v,v,a,104,224,16\nds_read_b64_tr_b16 v[66:67], v45 offset:52224\nqm v,v,a,104,228,20\nds_read_b64_tr_b16 v[68:69], v45 offset:55296\n"
"ql 76,50,56,83,1024\nqm v,v,a,128,228,28\nds_read_b64_tr_b16 v[70:71], v45 offset:58368\nqm v,v,a,128,224,24\nqw 8\nds_read_b64_tr_b16 v[72:73], v44 offset:49216\n"
"qm v,v,a,132,232,24\nds_read_b64_tr_b16 v[74:75], v44 offset:52288\nqm v,v,a,132,236,28\nds_read_b64_tr_b16 v[76:77], v44 offset:55360\nqm v,v,a,108,236,20\nds_read_b64_tr_b16 v[78:79], v44 offset:58432\n"
"qm v,v,a,108,232,16\nqw 8\nqv 15\nds_read_b64_tr_b16 v[80:81], v45 offset:49216\nqm v,v,a,88,56,32\nds_read_b64_tr_b16 v[82:83], v45 offset:52288\n"
"v_readlane_b32 s83, v49, 6\nqm v,v,a,88,60,36\nds_read_b64_tr_b16 v[84:85], v45 offset:55360\nqm v,v,a,112,60,44\ns_nop 1\nql 80,0,56,83,0\n"
"ds_read_b64_tr_b16 v[86:87], v45 offset:58432\nqm v,v,a,112,56,40\nqw 8\nds_read_b64_tr_b16 v[224:225], v44 offset:49280\nqm v,v,a,116,64,40\nds_read_b64_tr_b16 v[226:227], v44 offset:52352\n"
"ql 84,0,56,83,1024\nqm v,v,a,116,68,44\nds_read_b64_tr_b16 v[228:229], v44 offset:55424\nqm v,v,a,92,68,36\nds_read_b64_tr_b16 v[230:231], v44 offset:58496\nv_readlane_b32 s82, v48, 6\n"
"qm v,v,a,92,64,32\nv_add_u32 v44, 0x10000, v44\nds_read_b64_tr_b16 v[232:233], v45 offset:49280\nqw 9\nqm v,v,a,96,72,32\ns_add_u32 m0, s80, 0x18000\n"
"ds_read_b64_tr_b16 v[234:235], v45 offset:52352\nqm v,v,a,96,76,36\nqd 1,76,82,0\nds_read_b64_tr_b16 v[236:237], v45 offset:55424\nds_read_b64_tr_b16 v[238:239], v45 offset:58496\nv_add_u32 v45, 0x10000, v45\n"
"s_barrier\nqm v,v,a,120,76,44\nds_read_b64_tr_b16 v[56:57], v44 offset:0\nqm v,v,a,120,72,40\nqw 9\nqd 2,76,82,1024\n"
"qm v,v,a,124,80,40\nds_read_b64_tr_b16 v[58:59], v44 offset:3072\nqm v,v,a,124,84,44\nds_read_b64_tr_b16 v[60:61], v44 offset:6144\nqm v,v,a,100,84,36\nds_read_b64_tr_b16 v[62:63], v44 offset:9216\n"
"qm v,v,a,100,80,32\nqw 8\nds_read_b64_tr_b16 v[64:65], v45 offset:0\nql 88,50,56,83,0\nqm v,v,a,104,224,32\nds_read_b64_tr_b16 v[66:67], v45 offset:3072\n"
"qm v,v,a,104,228,36\nds_read_b64_tr_b16 v[68:69], v45 offset:6144\nqm v,v,a,128,228,44\nds_read_b64_tr_b16 v[70:71], v45 offset:9216\nql 92,50,56,83,1024\nqm v,v,a,128,224,40\n"
"qw 8\nds_read_b64_tr_b16 v[72:73], v44 offset:64\nqm v,v,a,132,232,40\nds_read_b64_tr_b16 v[74:75], v44 offset:3136\nqm v,v,a,132,236,44\nds_read_b64_tr_b16 v[76:77], v44 offset:6208\n"
"qm v,v,a,108,236,36\nds_read_b64_tr_b16 v[78:79], v44 offset:9280\nqd 3,76,82,2048\nqm v,v,a,108,232,32\nqw 8\nqv 15\n"
"ds_read_b64_tr_b16 v[80:81], v45 offset:64\nqm v,v,a,88,56,48\nds_read_b64_tr_b16 v[82:83], v45 offset:3136\nv_readlane_b32 s83, v49, 7\nqm v,v,a,88,60,52\nds_read_b64_tr_b16 v[84:85], v45 offset:6208\n"
"qm v,v,a,112,60,60\ns_nop 1\nql 96,0,56,83,0\nds_read_b64_tr_b16 v[86:87], v45 offset:9280\nqm v,v,a,112,56,56\nqw 8\n"
"ds_read_b64_tr_b16 v[224:225], v44 offset:128\nqm v,v,a,116,64,56\nds_read_b64_tr_b16 v[226:227], v44 offset:3200\nql 100,0,56,83,1024\nqm v,v,a,116,68,60\nds_read_b64_tr_b16 v[228:229], v44 offset:6272\n"
"qm v,v,a,92,68,52\nds_read_b64_tr_b16 v[230:231], v44 offset:9344\nv_readlane_b32 s82, v48, 7\nqm v,v,a,92,64,48\nqw 8\nds_read_b64_tr_b16 v[232:233], v45 offset:128\n"
"qm v,v,a,96,72,48\ns_add_u32 m0, s80, 0x1c000\nds_read_b64_tr_b16 v[234:235], v45 offset:3200\nqm v,v,a,96,76,52\nqd 1,76,82,0\nds_read_b64_tr_b16 v[236:237], v45 offset:6272\n"
"ds_read_b64_tr_b16 v[238:239], v45 offset:9344\ns_barrier\nqm v,v,a,120,76,60\nds_read_b64_tr_b16 v[56:57], v44 offset:16384\nqm v,v,a,120,72,56\nqw 9\n"
"ds_read_b64_tr_b16 v[58:59], v44 offset:19456\nqm v,v,a,124,80,56\nqd 2,76,82,1024\nqm v,v,a,124,84,60\nds_read_b64_tr_b16 v[60:61], v44 offset:22528\nqm v,v,a,100,84,52\n"
"ds_read_b64_tr_b16 v[62:63], v44 offset:25600\nqm v,v,a,100,80,48\nqw 8\nds_read_b64_tr_b16 v[64:65], v45 offset:16384\nql 104,50,56,83,0\nqm v,v,a,104,224,48\n"
"ds_read_b64_tr_b16 v[66:67], v45 offset:19456\nqm v,v,a,104,228,52\nds_read_b64_tr_b16 v[68:69], v45 offset:22528\nqm v,v,a,128,228,60\nds_read_b64_tr_b16 v[70:71], v45 offset:25600\nqm v,v,a,128,224,56\n"
"qw 8\nds_read_b64_tr_b16 v[72:73], v44 offset:16448\nql 108,50,56,83,1024\nqm v,v,a,132,232,56\nds_read_b64_tr_b16 v[74:75], v44 offset:19520\nqm v,v,a,132,236,60\n"
"ds_read_b64_tr_b16 v[76:77], v44 offset:22592\nqm v,v,a,108,236,52\nds_read_b64_tr_b16 v[78:79], v44 offset:25664\nqd 3,76,82,2048\nqm v,v,a,108,232,48\nqw 8\n"
"qv 14\nds_read_b64_tr_b16 v[80:81], v45 offset:16448\nqm v,v,a,88,56,64\nds_read_b64_tr_b16 v[82:83], v45 offset:19520\nv_readlane_b32 s83, v49, 8\nqm v,v,a,88,60,68\n"
"ds_read_b64_tr_b16 v[84:85], v45 offset:22592\nv_add_f32 v200, v200, v204\nqm v,v,a,112,60,76\nds_read_b64_tr_b16 v[86:87], v45 offset:25664\nql 112,0,56,83,0\nqm v,v,a,112,56,72\n"
"qw 8\nds_read_b64_tr_b16 v[224:225], v44 offset:16512\nv_add_f32 v201, v201, v205\nqm v,v,a,116,64,72\nds_read_b64_tr_b16 v[226:227], v44 offset:19584\nv_add_f32 v202, v202, v206\n"
"qm v,v,a,116,68,76\nds_read_b64_tr_b16 v[228:229], v44 offset:22656\nql 116,0,56,83,1024\nqm v,v,a,92,68,68\nds_read_b64_tr_b16 v[230:231], v44 offset:25728\nv_readlane_b32 s82, v48, 8\n"
"qm v,v,a,92,64,64\nqw 8\nds_read_b64_tr_b16 v[232:233], v45 offset:16512\nv_add_f32 v203, v203, v207\nqm v,v,a,96,72,64\ns_add_u32 m0, s80, 0x0\n"
"ds_read_b64_tr_b16 v[234:235], v45 offset:19584\nv_add_f32 v208, v208, v212\nqm v,v,a,96,76,68\nds_read_b64_tr_b16 v[236:237], v45 offset:22656\nds_read_b64_tr_b16 v[238:239], v45 offset:25728\nqv 9\n"
"s_barrier\nqd 1,76,82,0\nqm v,v,a,120,76,76\nds_read_b64_tr_b16 v[56:57], v44 offset:32768\nv_add_f32 v209, v209, v213\nqm v,v,a,120,72,72\n"
"qw 9\nds_read_b64_tr_b16 v[58:59], v44 offset:35840\nv_add_f32 v210, v210, v214\nqm v,v,a,124,80,72\nds_read_b64_tr_b16 v[60:61], v44 offset:38912\nv_add_f32 v211, v211, v215\n"
"qm v,v,a,124,84,76\nqd 2,76,82,1024\nqm v,v,a,100,84,68\nds_read_b64_tr_b16 v[62:63], v44 offset:41984\nv_add_f32 v200, v200, v208\nqm v,v,a,100,80,64\n"
"qw 8\nds_read_b64_tr_b16 v[64:65], v45 offset:32768\nv_add_f32 v201, v201, v209\nqm v,v,a,104,224,64\nds_read_b64_tr_b16 v[66:67], v45 offset:35840\nql 120,50,56,83,0\n"
"qm v,v,a,104,228,68\nds_read_b64_tr_b16 v[68:69], v45 offset:38912\nv_add_f32 v202, v202, v210\nqm v,v,a,128,228,76\nds_read_b64_tr_b16 v[70:71], v45 offset:41984\nv_add_f32 v203, v203, v211\n"
"qm v,v,a,128,224,72\nqw 8\nds_read_b64_tr_b16 v[72:73], v44 offset:32832\nv_mov_b32 v204, v200\nqm v,v,a,132,232,72\nds_read_b64_tr_b16 v[74:75], v44 offset:35904\n"
"ql 124,50,56,83,1024\nqm v,v,a,132,236,76\nds_read_b64_tr_b16 v[76:77], v44 offset:38976\nv_mov_b32 v205, v201\nqm v,v,a,108,236,68\nds_read_b64_tr_b16 v[78:79], v44 offset:42048\n"
"v_mov_b32 v206, v202\nqm v,v,a,108,232,64\nqw 8\nds_read_b64_tr_b16 v[80:81], v45 offset:32832\nqd 3,76,82,2048\nqm v,v,a,88,56,80\n"
"ds_read_b64_tr_b16 v[82:83], v45 offset:35904\nv_readlane_b32 s83, v49, 9\nqm v,v,a,88,60,84\nds_read_b64_tr_b16 v[84:85], v45 offset:38976\nv_mov_b32 v207, v203\nqm v,v,a,112,60,92\n"
"ds_read_b64_tr_b16 v[86:87], v45 offset:42048\nqm v,v,a,112,56,88\nqw 8\nds_read_b64_tr_b16 v[224:225], v44 offset:32896\nql 128,0,56,83,0\nqm v,v,a,116,64,88\n"
"ds_read_b64_tr_b16 v[226:227], v44 offset:35968\nqm v,v,a,116,68,92\nds_read_b64_tr_b16 v[228:229], v44 offset:39040\nqm v,v,a,92,68,84\nds_read_b64_tr_b16 v[230:231], v44 offset:42112\nql 132,0,56,83,1024\n"
"v_readlane_b32 s82, v48, 9\nqm v,v,a,92,64,80\nqw 8\nds_read_b64_tr_b16 v[232:233], v45 offset:32896\nqm v,v,a,96,72,80\ns_add_u32 m0, s80, 0x4000\n"
"ds_read_b64_tr_b16 v[234:235], v45 offset:35968\nqm v,v,a,96,76,84\nds_read_b64_tr_b16 v[236:237], v45 offset:39040\nds_read_b64_tr_b16 v[238:239], v45 offset:42112\nqv 9\ns_barrier\n"
"qm v,v,a,120,76,92\nqd 1,76,82,0\nqm v,v,a,120,72,88\nqw 8\nds_read_b64_tr_b16 v[56:57], v44 offset:49152\nqm v,v,a,124,80,88\n"
"ds_read_b64_tr_b16 v[58:59], v44 offset:52224\nv_permlane16_swap_b32 v204, v200\nqm v,v,a,124,84,92\nds_read_b64_tr_b16 v[60:61], v44 offset:55296\nqd 2,76,82,1024\nqm v,v,a,100,84,84\n"
"ds_read_b64_tr_b16 v[62:63], v44 offset:58368\nv_permlane16_swap_b32 v205, v201\nqm v,v,a,100,80,80\nqw 8\nds_read_b64_tr_b16 v[64:65], v45 offset:49152\nqm v,v,a,104,224,80\n"
"ds_read_b64_tr_b16 v[66:67], v45 offset:52224\nv_permlane16_swap_b32 v206, v202\nqm v,v,a,104,228,84\nds_read_b64_tr_b16 v[68:69], v45 offset:55296\nql 136,50,56,83,0\nqm v,v,a,128,228,92\n"
"ds_read_b64_tr_b16 v[70:71], v45 offset:58368\nv_permlane16_swap_b32 v207, v203\nqm v,v,a,128,224,88\nqw 8\nds_read_b64_tr_b16 v[72:73], v44 offset:49216\nv_add_f32 v200, v200, v204\n"
"qm v,v,a,132,232,88\nds_read_b64_tr_b16 v[74:75], v44 offset:52288\nql 140,50,56,83,1024\nqm v,v,a,132,236,92\nds_read_b64_tr_b16 v[76:77], v44 offset:55360\nv_add_f32 v201, v201, v205\n"
"qm v,v,a,108,236,84\nds_read_b64_tr_b16 v[78:79], v44 offset:58432\nv_add_f32 v202, v202, v206\nqm v,v,a,108,232,80\nqw 8\nds_read_b64_tr_b16 v[80:81], v45 offset:49216\n"
"qm v,v,a,88,56,96\nds_read_b64_tr_b16 v[82:83], v45 offset:52288\nqd 3,76,82,2048\nqm v,v,a,88,60,100\nds_read_b64_tr_b16 v[84:85], v45 offset:55360\nv_readlane_b32 s83, v49, 10\n"
"qm v,v,a,112,60,108\nds_read_b64_tr_b16 v[86:87], v45 offset:58432\nv_add_f32 v203, v203, v207\nqm v,v,a,112,56,104\nqw 8\nds_read_b64_tr_b16 v[224:225], v44 offset:49280\n"
"v_mov_b32 v204, v200\nqm v,v,a,116,64,104\nds_read_b64_tr_b16 v[226:227], v44 offset:52352\nql 144,0,56,83,0\nqm v,v,a,116,68,108\nds_read_b64_tr_b16 v[228:229], v44 offset:55424\n"
"v_mov_b32 v205, v201\nqm v,v,a,92,68,100\nds_read_b64_tr_b16 v[230:231], v44 offset:58496\nv_add_u32 v44, 0xffff0000, v44\nqm v,v,a,92,64,96\nqw 8\n"
"ds_read_b64_tr_b16 v[232:233], v45 offset:49280\nql 148,0,56,83,1024\nv_readlane_b32 s82, v48, 10\nqm v,v,a,96,72,96\nds_read_b64_tr_b16 v[234:235], v45 offset:52352\nv_mov_b32 v206, v202\n"
"qm v,v,a,96,76,100\nds_read_b64_tr_b16 v[236:237], v45 offset:55424\nds_read_b64_tr_b16 v[238:239], v45 offset:58496\nv_add_u32 v45, 0xffff0000, v45\nqv 9\ns_barrier\n"
"s_add_u32 m0, s80, 0x8000\nqm v,v,a,120,76,108\nds_read_b64_tr_b16 v[56:57], v44 offset:0\nv_mov_b32 v207, v203\nqm v,v,a,120,72,104\nqw 9\n"
"qd 1,76,82,0\nqm v,v,a,124,80,104\nds_read_b64_tr_b16 v[58:59], v44 offset:3072\nqm v,v,a,124,84,108\nds_read_b64_tr_b16 v[60:61], v44 offset:6144\nqm v,v,a,100,84,100\n"
"ds_read_b64_tr_b16 v[62:63], v44 offset:9216\nqd 2,76,82,1024\nv_permlane32_swap_b32 v204, v200\nqm v,v,a,100,80,96\nqw 8\nds_read_b64_tr_b16 v[64:65], v45 offset:0\n"
"qm v,v,a,104,224,96\nds_read_b64_tr_b16 v[66:67], v45 offset:3072\nv_permlane32_swap_b32 v205, v201\nqm v,v,a,104,228,100\nds_read_b64_tr_b16 v[68:69], v45 offset:6144\nqm v,v,a,128,228,108\n"
"ds_read_b64_tr_b16 v[70:71], v45 offset:9216\nql 152,50,56,83,0\nv_permlane32_swap_b32 v206, v202\nqm v,v,a,128,224,104\nqw 8\nds_read_b64_tr_b16 v[72:73], v44 offset:64\n"
"qm v,v,a,132,232,104\nds_read_b64_tr_b16 v[74:75], v44 offset:3136\nv_permlane32_swap_b32 v207, v203\nqm v,v,a,132,236,108\nds_read_b64_tr_b16 v[76:77], v44 offset:6208\nql 156,50,56,83,1024\n"
"qm v,v,a,108,236,100\nds_read_b64_tr_b16 v[78:79], v44 offset:9280\nv_add_f32 v200, v200, v204\nqm v,v,a,108,232,96\nqw 8\nds_read_b64_tr_b16 v[80:81], v45 offset:64\n"
"qm v,v,a,88,56,112\nds_read_b64_tr_b16 v[82:83], v45 offset:3136\nv_add_f32 v201, v201, v205\nqm v,v,a,88,60,116\nds_read_b64_tr_b16 v[84:85], v45 offset:6208\nqd 3,76,82,2048\n"
"qm v,v,a,112,60,124\nds_read_b64_tr_b16 v[86:87], v45 offset:9280\nv_readlane_b32 s83, v49, 11\nqm v,v,a,112,56,120\nqw 8\nds_read_b64_tr_b16 v[224:225], v44 offset:128\n"
"v_add_f32 v202, v202, v206\nqm v,v,a,116,64,120\nds_read_b64_tr_b16 v[226:227], v44 offset:3200\nql 160,0,56,83,0\nqm v,v,a,116,68,124\nds_read_b64_tr_b16 v[228:229], v44 offset:6272\n"
"v_add_f32 v203, v203, v207\nqm v,v,a,92,68,116\nds_read_b64_tr_b16 v[230:231], v44 offset:9344\nv_mul_f32 v20, 0xba800000, v200\nqm v,v,a,92,64,112\nqw 8\n"
"ds_read_b64_tr_b16 v[232:233], v45 offset:128\nql 164,0,56,83,1024\nv_readlane_b32 s82, v48, 11\nqm v,v,a,96,72,112\nds_read_b64_tr_b16 v[234:235], v45 offset:3200\nv_mul_f32 v24, 0xba800000, v202\n"
"qm v,v,a,96,76,116\nds_read_b64_tr_b16 v[236:237], v45 offset:6272\nds_read_b64_tr_b16 v[238:239], v45 offset:9344\nqv 9\ns_barrier\ns_add_u32 m0, s80, 0xc000\n"
"qm v,v,a,120,76,124\nds_read_b64_tr_b16 v[56:57], v44 offset:16384\nv_mov_b32 v204, 0x3727c5ac\nqm v,v,a,120,72,120\nqw 9\nqd 1,76,82,0\n"
"qm v,v,a,124,80,120\nds_read_b64_tr_b16 v[58:59], v44 offset:19456\nv_mov_b32 v206, 0x3727c5ac\nqm v,v,a,124,84,124\nds_read_b64_tr_b16 v[60:61], v44 offset:22528\nv_fmac_f32 v204, 0x3a800000, v201\n"
"qm v,v,a,100,84,116\nds_read_b64_tr_b16 v[62:63], v44 offset:25600\nv_fmac_f32 v206, 0x3a800000, v203\nqm v,v,a,100,80,112\nqw 8\nds_read_b64_tr_b16 v[64:65], v45 offset:16384\n"
"qd 2,76,82,1024\nqm v,v,a,104,224,112\nds_read_b64_tr_b16 v[66:67], v45 offset:19456\nv_fma_f32 v204, -v20, v20, v204\nqm v,v,a,104,228,116\nds_read_b64_tr_b16 v[68:69], v45 offset:22528\n"
"v_fma_f32 v206, -v24, v24, v206\nqm v,v,a,128,228,124\nds_read_b64_tr_b16 v[70:71], v45 offset:25600\nql 168,50,56,83,0\nqm v,v,a,128,224,120\nqw 8\n"
"ds_read_b64_tr_b16 v[72:73], v44 offset:16448\nv_max_f32 v204, 0x3727c5ac, v204\nqm v,v,a,132,232,120\nds_read_b64_tr_b16 v[74:75], v44 offset:19520\nv_max_f32 v206, 0x3727c5ac, v206\nqm v,v,a,132,236,124\n"
"ds_read_b64_tr_b16 v[76:77], v44 offset:22592\nqm v,v,a,108,236,116\nds_read_b64_tr_b16 v[78:79], v44 offset:25664\nql 172,50,56,83,1024\nqm v,v,a,108,232,112\nqw 8\n"
"ds_read_b64_tr_b16 v[80:81], v45 offset:16448\nqm v,v,a,88,56,128\nds_read_b64_tr_b16 v[82:83], v45 offset:19520\nqm v,v,a,88,60,132\nds_read_b64_tr_b16 v[84:85], v45 offset:22592\nqd 3,76,82,2048\n"
"qm v,v,a,112,60,140\nds_read_b64_tr_b16 v[86:87], v45 offset:25664\nv_readlane_b32 s83, v49, 12\nqm v,v,a,112,56,136\nqw 8\nds_read_b64_tr_b16 v[224:225], v44 offset:16512\n"
"qm v,v,a,116,64,136\nds_read_b64_tr_b16 v[226:227], v44 offset:19584\nv_rsq_f32 v205, v204\nqm v,v,a,116,68,140\nds_read_b64_tr_b16 v[228:229], v44 offset:22656\nql 176,0,56,83,0\n"
"qm v,v,a,92,68,132\nds_read_b64_tr_b16 v[230:231], v44 offset:25728\nv_rsq_f32 v207, v206\nqm v,v,a,92,64,128\nqw 8\nds_read_b64_tr_b16 v[232:233], v45 offset:16512\n"
"qm v,v,a,96,72,128\nds_read_b64_tr_b16 v[234:235], v45 offset:19584\nql 180,0,56,83,1024\nv_readlane_b32 s82, v48, 12\nqm v,v,a,96,76,132\nv_log_f32 v22, v204\n"
"ds_read_b64_tr_b16 v[236:237], v45 offset:22656\nds_read_b64_tr_b16 v[238:239], v45 offset:25728\nqv 9\ns_barrier\nqm v,v,a,120,76,140\ns_add_u32 m0, s80, 0x10000\n"
"v_log_f32 v26, v206\nqm v,v,a,120,72,136\nqw 8\nds_read_b64_tr_b16 v[56:57], v44 offset:32768\nv_mul_f32 v21, 0xbfb8aa3b, v205\nqm v,v,a,124,80,136\n"
"ds_read_b64_tr_b16 v[58:59], v44 offset:35840\nqd 1,76,82,0\nqm v,v,a,124,84,140\nds_read_b64_tr_b16 v[60:61], v44 offset:38912\nv_mul_f32 v25, 0xbfb8aa3b, v207\nqm v,v,a,100,84,132\n"
"ds_read_b64_tr_b16 v[62:63], v44 offset:41984\nv_mul_f32 v22, 0.5, v22\nqm v,v,a,100,80,128\nqw 8\nds_read_b64_tr_b16 v[64:65], v45 offset:32768\nv_mul_f32 v26, 0.5, v26\n"
"qm v,v,a,104,224,128\nds_read_b64_tr_b16 v[66:67], v45 offset:35840\nqd 2,76,82,1024\nqm v,v,a,104,228,132\nds_read_b64_tr_b16 v[68:69], v45 offset:38912\nv_mul_f32 v23, v204, v205\n"
"qm v,v,a,128,228,140\nds_read_b64_tr_b16 v[70:71], v45 offset:41984\nv_mul_f32 v27, v206, v207\nqm v,v,a,128,224,136\nqw 8\nds_read_b64_tr_b16 v[72:73], v44 offset:32832\n"
"ql 184,50,56,83,0\nqm v,v,a,132,232,136\nds_read_b64_tr_b16 v[74:75], v44 offset:35904\nqm v,v,a,132,236,140\nds_read_b64_tr_b16 v[76:77], v44 offset:38976\nqm v,v,a,108,236,132\n"
"ds_read_b64_tr_b16 v[78:79], v44 offset:42048\nqm v,v,a,108,232,128\nqw 8\nds_read_b64_tr_b16 v[80:81], v45 offset:32832\nql 188,50,56,83,1024\nqm v,v,a,88,56,144\n"
"ds_read_b64_tr_b16 v[82:83], v45 offset:35904\nv_mbcnt_lo_u32_b32 v53, -1, 0\nqm v,v,a,88,60,148\nds_read_b64_tr_b16 v[84:85], v45 offset:38976\nv_mbcnt_hi_u32_b32 v53, -1, v53\nqm v,v,a,112,60,156\n"
"ds_read_b64_tr_b16 v[86:87], v45 offset:42048\nqd 3,76,82,2048\nqm v,v,a,112,56,152\nqw 8\nds_read_b64_tr_b16 v[224:225], v44 offset:32896\nv_readlane_b32 s83, v49, 13\n"
"qm v,v,a,116,64,152\ns_mov_b32 s60, s50\ns_and_b32 s61, s51, 0xffff\nds_read_b64_tr_b16 v[226:227], v44 offset:35968\nqm v,v,a,116,68,156\ns_mov_b32 s62, 0x700000\n"
"s_mov_b32 s63, 0x20000\nds_read_b64_tr_b16 v[228:229], v44 offset:39040\nqm v,v,a,92,68,148\nds_read_b64_tr_b16 v[230:231], v44 offset:42112\nql 192,0,56,83,0\nqm v,v,a,92,64,144\n"
"qw 8\ns_lshl_b32 s73, s15, 1\nds_read_b64_tr_b16 v[232:233], v45 offset:32896\nqm v,v,a,96,72,144\ns_lshl3_add_u32 s73, s22, s73\ns_add_u32 s72, s73, 0\n"
"ds_read_b64_tr_b16 v[234:235], v45 offset:35968\nqm v,v,a,96,76,148\nds_read_b64_tr_b16 v[236:237], v45 offset:39040\nds_read_b64_tr_b16 v[238:239], v45 offset:42112\nqv 8\ns_barrier\n"
"ql 196,0,56,83,1024\nv_readlane_b32 s82, v48, 13\nqm v,v,a,120,76,156\ns_mul_i32 s72, s72, 114688\ns_add_u32 s72, s72, 0x3c00000\nds_read_b64_tr_b16 v[56:57], v44 offset:49152\n"
"qm v,v,a,120,72,152\ns_add_u32 m0, s80, 0x14000\nqw 9\nds_read_b64_tr_b16 v[58:59], v44 offset:52224\nqm v,v,a,124,80,152\ns_add_u32 s64, s56, s72\n"
"s_addc_u32 s65, s57, 0\nds_read_b64_tr_b16 v[60:61], v44 offset:55296\nqm v,v,a,124,84,156\nqd 1,76,82,0\nqm v,v,a,100,84,148\ns_mov_b32 s66, 114688\n"
"s_mov_b32 s67, 0x20000\nds_read_b64_tr_b16 v[62:63], v44 offset:58368\nqm v,v,a,100,80,144\nqw 8\ns_add_u32 s72, s73, 1\nds_read_b64_tr_b16 v[64:65], v45 offset:49152\n"
"qm v,v,a,104,224,144\nds_read_b64_tr_b16 v[66:67], v45 offset:52224\nqd 2,76,82,1024\nqm v,v,a,104,228,148\ns_mul_i32 s72, s72, 114688\ns_add_u32 s72, s72, 0x3c00000\n"
"ds_read_b64_tr_b16 v[68:69], v45 offset:55296\nqm v,v,a,128,228,156\ns_add_u32 s68, s56, s72\ns_addc_u32 s69, s57, 0\nds_read_b64_tr_b16 v[70:71], v45 offset:58368\nqm v,v,a,128,224,152\n"
"qw 8\ns_mov_b32 s70, 114688\nds_read_b64_tr_b16 v[72:73], v44 offset:49216\nqm v,v,a,132,232,152\nds_read_b64_tr_b16 v[74:75], v44 offset:52288\nql 200,50,56,83,0\n"
"qm v,v,a,132,236,156\ns_mov_b32 s71, 0x20000\ns_lshl2_add_u32 s5, s22, s15\nds_read_b64_tr_b16 v[76:77], v44 offset:55360\nqm v,v,a,108,236,148\ns_lshl_b32 s5, s5, 8\n"
"s_add_u32 s5, s5, 0xb000\nds_read_b64_tr_b16 v[78:79], v44 offset:58432\nqm v,v,a,108,232,144\nqw 8\nds_read_b64_tr_b16 v[80:81], v45 offset:49216\nql 204,50,56,83,1024\n"
"qm v,v,a,88,56,160\ns_lshr_b32 s72, s23, 1\ns_add_u32 s72, s72, s22\nds_read_b64_tr_b16 v[82:83], v45 offset:52288\nqm v,v,a,88,60,164\ns_and_b32 s72, s72, 3\n"
"s_and_b32 s74, s23, 1\nds_read_b64_tr_b16 v[84:85], v45 offset:55360\nqm v,v,a,112,60,172\ns_lshl_b32 s75, s74, 3\nds_read_b64_tr_b16 v[86:87], v45 offset:58432\nqm v,v,a,112,56,168\n"
"qw 8\nds_read_b64_tr_b16 v[224:225], v44 offset:49280\nqd 3,76,82,2048\nqm v,v,a,116,64,168\nds_read_b64_tr_b16 v[226:227], v44 offset:52352\nv_readlane_b32 s83, v49, 14\n"
"qm v,v,a,116,68,172\nds_read_b64_tr_b16 v[228:229], v44 offset:55424\nqm v,v,a,92,68,164\nds_read_b64_tr_b16 v[230:231], v44 offset:58496\nv_add_u32 v44, 0x10000, v44\nqm v,v,a,92,64,160\n"
"qw 8\nds_read_b64_tr_b16 v[232:233], v45 offset:49280\nql 208,0,56,83,0\nqm v,v,a,96,72,160\nds_read_b64_tr_b16 v[234:235], v45 offset:52352\nqm v,v,a,96,76,164\n"
"ds_read_b64_tr_b16 v[236:237], v45 offset:55424\nds_read_b64_tr_b16 v[238:239], v45 offset:58496\nv_add_u32 v45, 0x10000, v45\nqv 8\ns_barrier\nqm v,v,a,120,76,172\n"
"ql 212,0,56,83,1024\nv_readlane_b32 s82, v48, 14\nqm v,v,a,120,72,168\nqw 8\nds_read_b64_tr_b16 v[56:57], v44 offset:0\nqm v,v,a,124,80,168\n"
"s_add_u32 m0, s80, 0x18000\nds_read_b64_tr_b16 v[58:59], v44 offset:3072\nqm v,v,a,124,84,172\nds_read_b64_tr_b16 v[60:61], v44 offset:6144\nqm v,v,a,100,84,164\nds_read_b64_tr_b16 v[62:63], v44 offset:9216\n"
"qd 1,76,82,0\nqm v,v,a,100,80,160\nqw 8\nds_read_b64_tr_b16 v[64:65], v45 offset:0\nqm v,v,a,104,224,160\nds_read_b64_tr_b16 v[66:67], v45 offset:3072\n"
"qm v,v,a,104,228,164\nds_read_b64_tr_b16 v[68:69], v45 offset:6144\nqd 2,76,82,1024\nqm v,v,a,128,228,172\nds_read_b64_tr_b16 v[70:71], v45 offset:9216\nqm v,v,a,128,224,168\n"
"qw 8\nds_read_b64_tr_b16 v[72:73], v44 offset:64\nqm v,v,a,132,232,168\nds_read_b64_tr_b16 v[74:75], v44 offset:3136\nqm v,v,a,132,236,172\nds_read_b64_tr_b16 v[76:77], v44 offset:6208\n"
"ql 216,50,56,83,0\nqm v,v,a,108,236,164\nds_read_b64_tr_b16 v[78:79], v44 offset:9280\nqm v,v,a,108,232,160\nqw 8\nds_read_b64_tr_b16 v[80:81], v45 offset:64\n"
"qm v,v,a,88,56,176\nds_read_b64_tr_b16 v[82:83], v45 offset:3136\nql 220,50,56,83,1024\nqm v,v,a,88,60,180\nds_read_b64_tr_b16 v[84:85], v45 offset:6208\nqm v,v,a,112,60,188\n"
"ds_read_b64_tr_b16 v[86:87], v45 offset:9280\nqm v,v,a,112,56,184\nqw 8\nds_read_b64_tr_b16 v[224:225], v44 offset:128\nqm v,v,a,116,64,184\nds_read_b64_tr_b16 v[226:227], v44 offset:3200\n"
"qd 3,76,82,2048\nv_cmp_gt_u32 vcc, 12, v53\nv_add_u32 v18, -12, v53\ns_nop 0\nv_cndmask_b32 v18, v18, v53, vcc\nv_cndmask_b32 v248, 15, 11, vcc\n"
"qm v,v,a,116,68,188\nds_read_b64_tr_b16 v[228:229], v44 offset:6272\nv_readlane_b32 s83, v49, 15\nqm v,v,a,92,68,180\nds_read_b64_tr_b16 v[230:231], v44 offset:9344\nv_subrev_u32 v54, s72, v18\n"
"qm v,v,a,92,64,176\nqw 8\nds_read_b64_tr_b16 v[232:233], v45 offset:128\nql 224,0,56,83,0\nqm v,v,a,96,72,176\nds_read_b64_tr_b16 v[234:235], v45 offset:3200\n"
"v_add_u32 v54, 5, v54\nqm v,v,a,96,76,180\nds_read_b64_tr_b16 v[236:237], v45 offset:6272\nds_read_b64_tr_b16 v[238:239], v45 offset:9344\nqv 8\ns_barrier\n"
"qm v,v,a,120,76,188\nql 228,0,56,83,1024\nv_and_b32 v54, 3, v54\nv_readlane_b32 s82, v48, 15\nqm v,v,a,120,72,184\nqw 8\n"
"ds_read_b64_tr_b16 v[56:57], v44 offset:16384\nv_sub_u32 v55, v18, v54\nqm v,v,a,124,80,184\ns_add_u32 m0, s80, 0x1c000\nds_read_b64_tr_b16 v[58:59], v44 offset:19456\nv_add_u32 v17, -1, v18\n"
"qm v,v,a,124,84,188\nds_read_b64_tr_b16 v[60:61], v44 offset:22528\nqm v,v,a,100,84,180\nds_read_b64_tr_b16 v[62:63], v44 offset:25600\nqd 1,76,82,0\nqm v,v,a,100,80,176\n"
"qw 8\nds_read_b64_tr_b16 v[64:65], v45 offset:16384\nv_cmp_lt_u32 vcc, 1, v54\ns_nop 1\nv_cndmask_b32 v17, v17, v18, vcc\nqm v,v,a,104,224,176\n"
"ds_read_b64_tr_b16 v[66:67], v45 offset:19456\nqm v,v,a,104,228,180\nds_read_b64_tr_b16 v[68:69], v45 offset:22528\nv_cmp_eq_u32 vcc, 0, v54\nv_add_u32 v54, 1, v18\ns_nop 0\n"
"v_cndmask_b32 v17, v17, v54, vcc\nqm v,v,a,128,228,188\nds_read_b64_tr_b16 v[70:71], v45 offset:25600\nqd 2,76,82,1024\nqm v,v,a,128,224,184\nqw 8\n"
"ds_read_b64_tr_b16 v[72:73], v44 offset:16448\nv_cmp_le_u32 vcc, v248, v55\ns_nop 1\nv_cndmask_b32 v17, v17, v18, vcc\nqm v,v,a,132,232,184\ns_mul_i32 s72, s74, 12\n"
"s_lshr_b32 s73, s15, 1\nds_read_b64_tr_b16 v[74:75], v44 offset:19520\nqm v,v,a,132,236,188\nds_read_b64_tr_b16 v[76:77], v44 offset:22592\nql 232,50,56,83,0\nqm v,v,a,108,236,180\n"
"ds_read_b64_tr_b16 v[78:79], v44 offset:25664\nv_add_u32 v248, s72, v17\nqm v,v,a,108,232,176\nqw 8\nds_read_b64_tr_b16 v[80:81], v45 offset:16448\nqm v,v,a,88,56,192\n"
"ds_read_b64_tr_b16 v[82:83], v45 offset:19520\nv_lshl_add_u32 v54, v248, 1, s73\nqm v,v,a,88,60,196\nds_read_b64_tr_b16 v[84:85], v45 offset:22592\nql 236,50,56,83,1024\nqm v,v,a,112,60,204\n"
"ds_read_b64_tr_b16 v[86:87], v45 offset:25664\nv_mul_u32_u24 v55, 43, v54\nqm v,v,a,112,56,200\nqw 8\nds_read_b64_tr_b16 v[224:225], v44 offset:16512\nv_lshrrev_b32 v55, 8, v55\n"
"qm v,v,a,116,64,200\nds_read_b64_tr_b16 v[226:227], v44 offset:19584\nv_lshl_add_u32 v54, v55, 3, v54\nqm v,v,a,116,68,204\nds_read_b64_tr_b16 v[228:229], v44 offset:22656\nqd 3,76,82,2048\n"
"qm v,v,a,92,68,196\nds_read_b64_tr_b16 v[230:231], v44 offset:25728\nv_readlane_b32 s82, v48, 1\nqm v,v,a,92,64,192\nqw 8\nds_read_b64_tr_b16 v[232:233], v45 offset:16512\n"
"v_add_u32 v54, 4, v54\nqm v,v,a,96,72,192\ns_add_u32 m0, s81, 0x4000\ns_lshl_b32 s72, s74, 4\nds_read_b64_tr_b16 v[234:235], v45 offset:19584\nqm v,v,a,96,76,196\n"
"ds_read_b64_tr_b16 v[236:237], v45 offset:22656\nds_read_b64_tr_b16 v[238:239], v45 offset:25728\nqv 7\ns_barrier\nqm v,v,a,120,76,204\nqd 4,76,82,0\n"
"qm v,v,a,120,72,200\nqw 8\nds_read_b64_tr_b16 v[56:57], v44 offset:32768\nv_add_u32 v17, s72, v17\nqm v,v,a,124,80,200\nds_read_b64_tr_b16 v[58:59], v44 offset:35840\n"
"v_lshl_add_u32 v18, v17, 1, s73\nqm v,v,a,124,84,204\nds_read_b64_tr_b16 v[60:61], v44 offset:38912\nv_add_u32 v17, 24, v17\nqm v,v,a,100,84,196\nds_read_b64_tr_b16 v[62:63], v44 offset:41984\n"
"v_bfe_u32 v55, v18, 2, 1\nqm v,v,a,100,80,192\nqw 8\nds_read_b64_tr_b16 v[64:65], v45 offset:32768\nqd 5,76,82,1024\nqm v,v,a,104,224,192\n"
"ds_read_b64_tr_b16 v[66:67], v45 offset:35840\nv_mul_u32_u24 v55, 3, v55\nqm v,v,a,104,228,196\nds_read_b64_tr_b16 v[68:69], v45 offset:38912\nv_and_b32 v19, 3, v18\nqm v,v,a,128,228,204\n"
"ds_read_b64_tr_b16 v[70:71], v45 offset:41984\nv_add_u32 v55, v19, v55\nqm v,v,a,128,224,200\nqw 8\nds_read_b64_tr_b16 v[72:73], v44 offset:32832\nv_lshrrev_b32 v18, 2, v18\n"
"qm v,v,a,132,232,200\nds_read_b64_tr_b16 v[74:75], v44 offset:35904\nv_mad_u32_u24 v55, v18, 7, v55\nqm v,v,a,132,236,204\nds_read_b64_tr_b16 v[76:77], v44 offset:38976\nqd 6,76,82,2048\n"
"qm v,v,a,108,236,196\nds_read_b64_tr_b16 v[78:79], v44 offset:42048\nqm v,v,a,108,232,192\nqw 8\nds_read_b64_tr_b16 v[80:81], v45 offset:32832\nqm v,v,a,88,56,208\n"
"ds_read_b64_tr_b16 v[82:83], v45 offset:35904\nqm v,v,a,88,60,212\nds_read_b64_tr_b16 v[84:85], v45 offset:38976\nqm v,v,a,112,60,220\nds_read_b64_tr_b16 v[86:87], v45 offset:42048\nqd 7,76,82,3072\n"
"qm v,v,a,112,56,216\nqw 8\nds_read_b64_tr_b16 v[224:225], v44 offset:32896\nv_readlane_b32 s82, v48, 2\nqm v,v,a,116,64,216\nds_read_b64_tr_b16 v[226:227], v44 offset:35968\n"
"qm v,v,a,116,68,220\nds_read_b64_tr_b16 v[228:229], v44 offset:39040\nv_cmp_gt_u32 vcc, 12, v53\ns_nop 1\nv_cndmask_b32 v18, v55, v54, vcc\nv_cndmask_b32 v17, v17, v248, vcc\n"
"qm v,v,a,92,68,212\ns_add_u32 m0, s81, 0x8000\ns_and_b32 s72, s15, 1\nds_read_b64_tr_b16 v[230:231], v44 offset:42112\nqm v,v,a,92,64,208\nqw 8\n"
"s_lshl_b32 s72, s72, 15\nds_read_b64_tr_b16 v[232:233], v45 offset:32896\nqm v,v,a,96,72,208\nds_read_b64_tr_b16 v[234:235], v45 offset:35968\nqd 4,76,82,0\nqm v,v,a,96,76,212\n"
"ds_read_b64_tr_b16 v[236:237], v45 offset:39040\nds_read_b64_tr_b16 v[238:239], v45 offset:42112\nqv 5\ns_barrier\nqm v,v,a,120,76,220\ns_lshr_b32 s73, s23, 1\n"
"s_lshl_b32 s73, s73, 8\nds_read_b64_tr_b16 v[56:57], v44 offset:49152\nqm v,v,a,120,72,216\nqw 9\ns_add_u32 s72, s72, s73\nds_read_b64_tr_b16 v[58:59], v44 offset:52224\n"
"qm v,v,a,124,80,216\nds_read_b64_tr_b16 v[60:61], v44 offset:55296\nv_lshl_add_u32 v18, v18, 16, s72\nqm v,v,a,124,84,220\nqd 5,76,82,1024\nqm v,v,a,100,84,212\n"
"ds_read_b64_tr_b16 v[62:63], v44 offset:58368\nv_lshlrev_b32 v17, 11, v17\nqm v,v,a,100,80,208\nqw 8\nds_read_b64_tr_b16 v[64:65], v45 offset:49152\nv_and_b32 v54, 7, v53\n"
"qm v,v,a,104,224,208\nds_read_b64_tr_b16 v[66:67], v45 offset:52224\nv_add_u32 v54, s75, v54\nqm v,v,a,104,228,212\nds_read_b64_tr_b16 v[68:69], v45 offset:55296\nv_lshlrev_b32 v19, 3, v54\n"
"qm v,v,a,128,228,220\nds_read_b64_tr_b16 v[70:71], v45 offset:58368\nqm v,v,a,128,224,216\nqw 8\nds_read_b64_tr_b16 v[72:73], v44 offset:49216\nqd 6,76,82,2048\n"
"qm v,v,a,132,232,216\nds_read_b64_tr_b16 v[74:75], v44 offset:52288\nqm v,v,a,132,236,220\nds_read_b64_tr_b16 v[76:77], v44 offset:55360\nqm v,v,a,108,236,212\nds_read_b64_tr_b16 v[78:79], v44 offset:58432\n"
"qm v,v,a,108,232,208\nqw 8\nds_read_b64_tr_b16 v[80:81], v45 offset:49216\nqm v,v,a,88,56,224\nds_read_b64_tr_b16 v[82:83], v45 offset:52288\nqd 7,76,82,3072\n"
"qm v,v,a,88,60,228\nds_read_b64_tr_b16 v[84:85], v45 offset:55360\nv_readlane_b32 s82, v48, 3\nqm v,v,a,112,60,236\nds_read_b64_tr_b16 v[86:87], v45 offset:58432\nqm v,v,a,112,56,232\n"
"qw 8\ns_add_u32 m0, s81, 0xc000\nds_read_b64_tr_b16 v[224:225], v44 offset:49280\nqm v,v,a,116,64,232\nds_read_b64_tr_b16 v[226:227], v44 offset:52352\nqm v,v,a,116,68,236\n"
"ds_read_b64_tr_b16 v[228:229], v44 offset:55424\nqm v,v,a,92,68,228\nds_read_b64_tr_b16 v[230:231], v44 offset:58496\nqd 4,76,82,0\nqm v,v,a,92,64,224\nqw 8\n"
"ds_read_b64_tr_b16 v[232:233], v45 offset:49280\nqm v,v,a,96,72,224\nds_read_b64_tr_b16 v[234:235], v45 offset:52352\nqm v,v,a,96,76,228\nds_read_b64_tr_b16 v[236:237], v45 offset:55424\nds_read_b64_tr_b16 v[238:239], v45 offset:58496\n"
"qv 5\ns_barrier\nqm v,v,a,120,76,236\nds_read_b64_tr_b16 v[56:57], v36 offset:16384\nqm v,v,a,120,72,232\nqw 9\n"
"qd 5,76,82,1024\nqm v,v,a,124,80,232\nds_read_b64_tr_b16 v[58:59], v36 offset:20480\nqm v,v,a,124,84,236\nds_read_b64_tr_b16 v[60:61], v36 offset:24576\nqm v,v,a,100,84,228\n"
"ds_read_b64_tr_b16 v[62:63], v36 offset:28672\nqm v,v,a,100,80,224\nqw 8\nds_read_b64_tr_b16 v[64:65], v37 offset:16384\nqm v,v,a,104,224,224\nds_read_b64_tr_b16 v[66:67], v37 offset:20480\n"
"qm v,v,a,104,228,228\nds_read_b64_tr_b16 v[68:69], v37 offset:24576\nqd 6,76,82,2048\nqm v,v,a,128,228,236\nds_read_b128 v[200:203], v46 offset:0\nds_read_b128 v[204:207], v46 offset:64\n"
"ds_read_b64_tr_b16 v[70:71], v37 offset:28672\nqm v,v,a,128,224,232\nqw 10\nds_read_b64_tr_b16 v[72:73], v38 offset:16384\nqm v,v,a,132,232,232\nds_read_b64_tr_b16 v[74:75], v38 offset:20480\n"
"qm v,v,a,132,236,236\nds_read_b64_tr_b16 v[76:77], v38 offset:24576\nqm v,v,a,108,236,228\nds_read_b64_tr_b16 v[78:79], v38 offset:28672\nqd 7,76,82,3072\nqm v,v,a,108,232,224\n"
"qw 10\nds_read_b64_tr_b16 v[80:81], v39 offset:16384\nqm v,v,a,136,56,0\ns_lshl3_add_u32 s8, s23, s5\nds_read_b64_tr_b16 v[82:83], v39 offset:20480\nv_readlane_b32 s82, v48, 4\n"
"qm v,v,a,136,60,4\nds_read_b64_tr_b16 v[84:85], v39 offset:24576\nqm v,v,a,168,60,12\ns_add_u32 m0, s81, 0x10000\nds_read_b64_tr_b16 v[86:87], v39 offset:28672\nqm v,v,a,168,56,8\n"
"qw 8\nds_read_b64_tr_b16 v[224:225], v40 offset:16384\nqm v,v,a,172,64,8\nds_read_b64_tr_b16 v[226:227], v40 offset:20480\nqd 4,76,82,0\nv_fmac_f32 v88, v200, v20\n"
"qm v,v,a,172,68,12\nds_read_b64_tr_b16 v[228:229], v40 offset:24576\nv_fmac_f32 v89, v201, v20\nqm v,v,a,140,68,4\nds_read_b64_tr_b16 v[230:231], v40 offset:28672\nv_fmac_f32 v90, v202, v20\n"
"qm v,v,a,140,64,0\nqw 8\nds_read_b64_tr_b16 v[232:233], v41 offset:16384\nv_fmac_f32 v91, v203, v20\nqm v,v,a,144,72,0\nds_read_b64_tr_b16 v[234:235], v41 offset:20480\n"
"v_fmac_f32 v92, v204, v20\nqm v,v,a,144,76,4\nds_read_b64_tr_b16 v[236:237], v41 offset:24576\nqd 5,76,82,1024\nv_fmac_f32 v93, v205, v20\nqm v,v,a,176,76,12\n"
"ds_read_b64_tr_b16 v[238:239], v41 offset:28672\nv_fmac_f32 v94, v206, v20\nqm v,v,a,176,72,8\nqw 8\nds_read_b64_tr_b16 v[56:57], v42 offset:16384\nv_fmac_f32 v95, v207, v20\n"
"qm v,v,a,180,80,8\nds_read_b64_tr_b16 v[58:59], v42 offset:20480\nv_fma_f32 v28, v88, v21, v22\nqm v,v,a,180,84,12\nds_read_b64_tr_b16 v[60:61], v42 offset:24576\nv_fma_f32 v29, v89, v21, v22\n"
"qm v,v,a,148,84,4\nds_read_b64_tr_b16 v[62:63], v42 offset:28672\nv_fma_f32 v30, v90, v21, v22\nv_fma_f32 v31, v91, v21, v22\nqm v,v,a,148,80,0\nqw 8\n"
"ds_read_b64_tr_b16 v[64:65], v43 offset:16384\nv_fma_f32 v32, v92, v21, v22\nqm v,v,a,152,224,0\nds_read_b64_tr_b16 v[66:67], v43 offset:20480\nqd 6,76,82,2048\nv_fma_f32 v33, v93, v21, v22\n"
"qm v,v,a,152,228,4\nv_fma_f32 v34, v94, v21, v22\nds_read_b64_tr_b16 v[68:69], v43 offset:24576\nds_read_b64_tr_b16 v[70:71], v43 offset:28672\nqv 7\ns_barrier\n"
"qm v,v,a,184,228,12\nds_read_b64_tr_b16 v[72:73], v36 offset:32768\nv_fma_f32 v35, v95, v21, v22\nqm v,v,a,184,224,8\nqw 9\nqx 28\n"
"qm v,v,a,188,232,8\nds_read_b64_tr_b16 v[74:75], v36 offset:36864\nqx 29\nqm v,v,a,188,236,12\nds_read_b64_tr_b16 v[76:77], v36 offset:40960\nqx 30\n"
"qm v,v,a,156,236,4\nds_read_b64_tr_b16 v[78:79], v36 offset:45056\nqx 31\nqm v,v,a,156,232,0\nqw 8\nds_read_b64_tr_b16 v[80:81], v37 offset:32768\n"
"qd 7,76,82,3072\nqx 32\nqm v,v,a,160,56,0\nds_read_b64_tr_b16 v[82:83], v37 offset:36864\nqx 33\nqx 34\n"
"qm v,v,a,160,60,4\nds_read_b64_tr_b16 v[84:85], v37 offset:40960\nqx 35\nqm v,v,a,192,60,12\nds_read_b64_tr_b16 v[86:87], v37 offset:45056\nv_add_f32 v28, v28, v23\n"
"qm v,v,a,192,56,8\nqw 8\nds_read_b64_tr_b16 v[224:225], v38 offset:32768\nv_add_f32 v29, v29, v23\nqm v,v,a,196,64,8\nds_read_b64_tr_b16 v[226:227], v38 offset:36864\n"
"v_add_f32 v30, v30, v23\nv_fmac_f32 v112, v200, v24\nqm v,v,a,196,68,12\nds_read_b64_tr_b16 v[228:229], v38 offset:40960\nv_add_f32 v31, v31, v23\nv_fmac_f32 v113, v201, v24\n"
"qm v,v,a,164,68,4\nds_read_b64_tr_b16 v[230:231], v38 offset:45056\nv_add_f32 v32, v32, v23\nv_fmac_f32 v114, v202, v24\nqm v,v,a,164,64,0\nqw 8\n"
"ds_read_b64_tr_b16 v[232:233], v39 offset:32768\nv_add_f32 v33, v33, v23\nv_fmac_f32 v115, v203, v24\nqm v,v,a,136,72,16\nds_read_b64_tr_b16 v[234:235], v39 offset:36864\nv_readlane_b32 s82, v48, 5\n"
"v_add_f32 v34, v34, v23\nv_fmac_f32 v116, v204, v24\nqm v,v,a,136,76,20\nds_read_b64_tr_b16 v[236:237], v39 offset:40960\nv_add_f32 v35, v35, v23\nv_fmac_f32 v117, v205, v24\n"
"qm v,v,a,168,76,28\ns_add_u32 m0, s81, 0x14000\nds_read_b64_tr_b16 v[238:239], v39 offset:45056\nqp 28\nv_fmac_f32 v118, v206, v24\nqm v,v,a,168,72,24\n"
"qw 8\nqd 4,76,82,0\nds_read_b64_tr_b16 v[56:57], v40 offset:32768\nqp 29\nqp 30\nv_fmac_f32 v119, v207, v24\n"
"v_fma_f32 v216, v112, v25, v26\nqm v,v,a,172,80,24\nds_read_b64_tr_b16 v[58:59], v40 offset:36864\nqp 31\nv_fma_f32 v217, v113, v25, v26\nqm v,v,a,172,84,28\n"
"ds_read_b64_tr_b16 v[60:61], v40 offset:40960\nqp 32\nv_fma_f32 v218, v114, v25, v26\nqm v,v,a,140,84,20\nds_read_b64_tr_b16 v[62:63], v40 offset:45056\nqp 33\n"
"v_fma_f32 v219, v115, v25, v26\nqm v,v,a,140,80,16\nqw 8\nds_read_b64_tr_b16 v[64:65], v41 offset:32768\nqp 34\nv_fma_f32 v220, v116, v25, v26\n"
"qm v,v,a,144,224,16\nds_read_b64_tr_b16 v[66:67], v41 offset:36864\nqd 5,76,82,1024\nqp 35\nv_fma_f32 v221, v117, v25, v26\nqm v,v,a,144,228,20\n"
"ds_read_b128 v[208:211], v46 offset:128\nds_read_b128 v[212:215], v46 offset:192\nds_read_b64_tr_b16 v[68:69], v41 offset:40960\nv_mul_f32 v88, v88, v28\nv_fma_f32 v222, v118, v25, v26\nqm v,v,a,176,228,28\n"
"ds_read_b64_tr_b16 v[70:71], v41 offset:45056\nv_mul_f32 v89, v89, v29\nv_fma_f32 v223, v119, v25, v26\nqm v,v,a,176,224,24\nqw 10\nds_read_b64_tr_b16 v[72:73], v42 offset:32768\n"
"v_mul_f32 v90, v90, v30\nqx 216\nqm v,v,a,180,232,24\nds_read_b64_tr_b16 v[74:75], v42 offset:36864\nv_mul_f32 v91, v91, v31\nqx 217\n"
"qm v,v,a,180,236,28\nds_read_b64_tr_b16 v[76:77], v42 offset:40960\nv_mul_f32 v92, v92, v32\nv_mul_f32 v93, v93, v33\nqx 218\nqx 219\n"
"qm v,v,a,148,236,20\nds_read_b64_tr_b16 v[78:79], v42 offset:45056\nv_mul_f32 v94, v94, v34\nqx 220\nqm v,v,a,148,232,16\nqw 10\n"
"ds_read_b64_tr_b16 v[80:81], v43 offset:32768\nv_mul_f32 v95, v95, v35\nqx 221\nqm v,v,a,152,56,16\nds_read_b64_tr_b16 v[82:83], v43 offset:36864\nqd 6,76,82,2048\n"
"qc 88,88\nqx 222\nqm v,v,a,152,60,20\nqc 89,90\nqx 223\nds_read_b64_tr_b16 v[84:85], v43 offset:40960\n"
"ds_read_b64_tr_b16 v[86:87], v43 offset:45056\nqv 7\ns_barrier\nqm v,v,a,184,60,28\nqc 90,92\nv_add_f32 v216, v216, v27\n"
"qm v,v,a,184,56,24\nqw 8\nds_read_b64_tr_b16 v[224:225], v36 offset:49152\nqc 91,94\nv_add_f32 v217, v217, v27\nqm v,v,a,188,64,24\n"
"ds_read_b64_tr_b16 v[226:227], v36 offset:53248\nv_add_f32 v218, v218, v27\nv_fmac_f32 v96, v208, v20\nqm v,v,a,188,68,28\nds_read_b64_tr_b16 v[228:229], v36 offset:57344\nv_add_f32 v219, v219, v27\n"
"v_fmac_f32 v97, v209, v20\nqm v,v,a,156,68,20\nds_read_b64_tr_b16 v[230:231], v36 offset:61440\nv_add_f32 v220, v220, v27\nv_fmac_f32 v98, v210, v20\nqm v,v,a,156,64,16\n"
"v_add_u32 v36, 0x10000, v36\nv_add_f32 v221, v221, v27\nv_add_f32 v222, v222, v27\nv_fmac_f32 v99, v211, v20\nv_fmac_f32 v100, v212, v20\nds_read_b64_tr_b16 v[232:233], v37 offset:49152\n"
"buffer_store_dwordx4 v[88:91], v0, s[56:59], s6 offen offset:0 sc1\nqw 9\nqm v,v,a,160,72,16\nds_read_b64_tr_b16 v[234:235], v37 offset:53248\nv_add_f32 v223, v223, v27\nv_fmac_f32 v101, v213, v20\n"
"qm v,v,a,160,76,20\nds_read_b64_tr_b16 v[236:237], v37 offset:57344\nqp 216\nv_fmac_f32 v102, v214, v20\nqm v,v,a,192,76,28\nds_read_b64_tr_b16 v[238:239], v37 offset:61440\n"
"qp 217\nv_fmac_f32 v103, v215, v20\nqm v,v,a,192,72,24\nv_add_u32 v37, 0x10000, v37\nqp 218\nv_fma_f32 v28, v96, v21, v22\n"
"ds_read_b64_tr_b16 v[56:57], v38 offset:49152\nqw 9\nqm v,v,a,196,80,24\nds_read_b64_tr_b16 v[58:59], v38 offset:53248\nqp 219\nv_fma_f32 v29, v97, v21, v22\n"
"qm v,v,a,196,84,28\nds_read_b64_tr_b16 v[60:61], v38 offset:57344\nqd 7,76,82,3072\nqp 220\nv_fma_f32 v30, v98, v21, v22\nqm v,v,a,164,84,20\n"
"ds_read_b64_tr_b16 v[62:63], v38 offset:61440\nqp 221\nv_fma_f32 v31, v99, v21, v22\nqm v,v,a,164,80,16\nv_add_u32 v38, 0x10000, v38\nqp 222\n"
"v_fma_f32 v32, v100, v21, v22\nds_read_b64_tr_b16 v[64:65], v39 offset:49152\nqw 9\nqm v,v,a,136,224,32\nds_read_b64_tr_b16 v[66:67], v39 offset:53248\nv_readlane_b32 s82, v48, 6\n"
"qp 223\nv_fma_f32 v33, v101, v21, v22\nqm v,v,a,136,228,36\nds_read_b64_tr_b16 v[68:69], v39 offset:57344\nv_mul_f32 v112, v112, v216\nv_fma_f32 v34, v102, v21, v22\n"
"qm v,v,a,168,228,44\ns_add_u32 m0, s81, 0x18000\nds_read_b64_tr_b16 v[70:71], v39 offset:61440\nv_mul_f32 v113, v113, v217\nv_mul_f32 v114, v114, v218\nv_fma_f32 v35, v103, v21, v22\n"
"qx 28\nqm v,v,a,168,224,40\nqd 4,76,82,0\nv_add_u32 v39, 0x10000, v39\nv_mul_f32 v115, v115, v219\nqx 29\n"
"ds_read_b64_tr_b16 v[72:73], v40 offset:49152\nqw 9\nqm v,v,a,172,232,40\nds_read_b64_tr_b16 v[74:75], v40 offset:53248\nv_mul_f32 v116, v116, v220\nqx 30\n"
"qm v,v,a,172,236,44\nds_read_b64_tr_b16 v[76:77], v40 offset:57344\nv_mul_f32 v117, v117, v221\nqx 31\nqm v,v,a,140,236,36\nds_read_b64_tr_b16 v[78:79], v40 offset:61440\n"
"v_mul_f32 v118, v118, v222\nqx 32\nqm v,v,a,140,232,32\nv_add_u32 v40, 0x10000, v40\nv_mul_f32 v119, v119, v223\nqx 33\n"
"ds_read_b64_tr_b16 v[80:81], v41 offset:49152\nqw 9\nqm v,v,a,144,56,32\nds_read_b64_tr_b16 v[82:83], v41 offset:53248\nqd 5,76,82,1024\nqc 112,112\n"
"qx 34\nqm v,v,a,144,60,36\nds_read_b64_tr_b16 v[84:85], v41 offset:57344\nqc 113,114\nqx 35\nqm v,v,a,176,60,44\n"
"ds_read_b64_tr_b16 v[86:87], v41 offset:61440\nqc 114,116\nv_add_f32 v28, v28, v23\nqm v,v,a,176,56,40\nv_add_u32 v41, 0x10000, v41\nqc 115,118\n"
"v_add_f32 v29, v29, v23\nds_read_b64_tr_b16 v[224:225], v42 offset:49152\nqw 9\nqm v,v,a,180,64,40\nds_read_b64_tr_b16 v[226:227], v42 offset:53248\nv_add_f32 v30, v30, v23\n"
"v_add_f32 v31, v31, v23\nv_fmac_f32 v120, v208, v24\nv_fmac_f32 v121, v209, v24\nqm v,v,a,180,68,44\nds_read_b64_tr_b16 v[228:229], v42 offset:57344\nv_add_f32 v32, v32, v23\n"
"v_fmac_f32 v122, v210, v24\nqm v,v,a,148,68,36\nds_read_b64_tr_b16 v[230:231], v42 offset:61440\nv_add_f32 v33, v33, v23\nv_fmac_f32 v123, v211, v24\nqm v,v,a,148,64,32\n"
"v_add_u32 v42, 0x10000, v42\nv_add_f32 v34, v34, v23\nv_fmac_f32 v124, v212, v24\nds_read_b64_tr_b16 v[232:233], v43 offset:49152\nbuffer_store_dwordx4 v[112:115], v0, s[56:59], s7 offen offset:0 sc1\nqw 9\n"
"qm v,v,a,152,72,32\nds_read_b64_tr_b16 v[234:235], v43 offset:53248\nv_add_f32 v35, v35, v23\nv_fmac_f32 v125, v213, v24\nqm v,v,a,152,76,36\nqp 28\n"
"v_fmac_f32 v126, v214, v24\nds_read_b64_tr_b16 v[236:237], v43 offset:57344\nds_read_b64_tr_b16 v[238:239], v43 offset:61440\nv_add_u32 v43, 0x10000, v43\nqv 8\ns_barrier\n"
"qm v,v,a,184,76,44\nqp 29\nv_fmac_f32 v127, v215, v24\nqm v,v,a,184,72,40\nqw 8\nds_read_b64_tr_b16 v[56:57], v36 offset:0\n"
"qp 30\nv_fma_f32 v216, v120, v25, v26\nqm v,v,a,188,80,40\nds_read_b64_tr_b16 v[58:59], v36 offset:4096\nqp 31\nv_fma_f32 v217, v121, v25, v26\n"
"qm v,v,a,188,84,44\nds_read_b64_tr_b16 v[60:61], v36 offset:8192\nqd 6,76,82,2048\nqp 32\nv_fma_f32 v218, v122, v25, v26\nqm v,v,a,156,84,36\n"
"ds_read_b64_tr_b16 v[62:63], v36 offset:12288\nqp 33\nv_fma_f32 v219, v123, v25, v26\nqm v,v,a,156,80,32\nqw 8\nds_read_b64_tr_b16 v[64:65], v37 offset:0\n"
"qp 34\nqp 35\nv_fma_f32 v220, v124, v25, v26\nv_fma_f32 v221, v125, v25, v26\nqm v,v,a,160,224,32\nds_read_b128 v[200:203], v46 offset:256\n"
"ds_read_b128 v[204:207], v46 offset:320\nds_read_b64_tr_b16 v[66:67], v37 offset:4096\nv_mul_f32 v96, v96, v28\nv_fma_f32 v222, v126, v25, v26\nqm v,v,a,160,228,36\nds_read_b64_tr_b16 v[68:69], v37 offset:8192\n"
"v_mul_f32 v97, v97, v29\nv_fma_f32 v223, v127, v25, v26\nqm v,v,a,192,228,44\nds_read_b64_tr_b16 v[70:71], v37 offset:12288\nqd 7,76,82,3072\nv_mul_f32 v98, v98, v30\n"
"qx 216\nqm v,v,a,192,224,40\nqw 10\nds_read_b64_tr_b16 v[72:73], v38 offset:0\nv_mul_f32 v99, v99, v31\nqx 217\n"
"qm v,v,a,196,232,40\nds_read_b64_tr_b16 v[74:75], v38 offset:4096\nv_mul_f32 v100, v100, v32\nqx 218\nqm v,v,a,196,236,44\nds_read_b64_tr_b16 v[76:77], v38 offset:8192\n"
"v_mul_f32 v101, v101, v33\nqx 219\nqm v,v,a,164,236,36\nds_read_b64_tr_b16 v[78:79], v38 offset:12288\nv_mul_f32 v102, v102, v34\nqx 220\n"
"qm v,v,a,164,232,32\nqw 10\nds_read_b64_tr_b16 v[80:81], v39 offset:0\nv_mul_f32 v103, v103, v35\nqx 221\nqm v,v,a,136,56,48\n"
"ds_read_b64_tr_b16 v[82:83], v39 offset:4096\nv_readlane_b32 s82, v48, 7\nqc 96,96\nqx 222\nqm v,v,a,136,60,52\nds_read_b64_tr_b16 v[84:85], v39 offset:8192\n"
"qc 97,98\nqc 98,100\nqx 223\nv_add_f32 v216, v216, v27\nqm v,v,a,168,60,60\ns_add_u32 m0, s81, 0x1c000\n"
"ds_read_b64_tr_b16 v[86:87], v39 offset:12288\nqc 99,102\nv_add_f32 v217, v217, v27\nqm v,v,a,168,56,56\nqw 8\nqd 4,76,82,0\n"
"ds_read_b64_tr_b16 v[224:225], v40 offset:0\nv_add_f32 v218, v218, v27\nv_fmac_f32 v104, v200, v20\nqm v,v,a,172,64,56\nds_read_b64_tr_b16 v[226:227], v40 offset:4096\nv_add_f32 v219, v219, v27\n"
"v_fmac_f32 v105, v201, v20\nqm v,v,a,172,68,60\nds_read_b64_tr_b16 v[228:229], v40 offset:8192\nv_add_f32 v220, v220, v27\nv_fmac_f32 v106, v202, v20\nqm v,v,a,140,68,52\n"
"ds_read_b64_tr_b16 v[230:231], v40 offset:12288\nv_add_f32 v221, v221, v27\nv_fmac_f32 v107, v203, v20\nqm v,v,a,140,64,48\nqw 8\nds_read_b64_tr_b16 v[232:233], v41 offset:0\n"
"v_add_f32 v222, v222, v27\nv_fmac_f32 v108, v204, v20\nqm v,v,a,144,72,48\nds_read_b64_tr_b16 v[234:235], v41 offset:4096\nbuffer_store_dwordx4 v[96:99], v0, s[56:59], s6 offen offset:1024 sc1\nv_add_f32 v223, v223, v27\n"
"v_fmac_f32 v109, v205, v20\nqm v,v,a,144,76,52\nds_read_b64_tr_b16 v[236:237], v41 offset:8192\nqp 216\nv_fmac_f32 v110, v206, v20\nqm v,v,a,176,76,60\n"
"ds_read_b64_tr_b16 v[238:239], v41 offset:12288\nqp 217\nv_fmac_f32 v111, v207, v20\nqm v,v,a,176,72,56\nqw 8\nds_read_b64_tr_b16 v[56:57], v42 offset:0\n"
"qp 218\nqp 219\nv_fma_f32 v28, v104, v21, v22\nv_fma_f32 v29, v105, v21, v22\nqm v,v,a,180,80,56\nds_read_b64_tr_b16 v[58:59], v42 offset:4096\n"
"qp 220\nv_fma_f32 v30, v106, v21, v22\nqm v,v,a,180,84,60\nds_read_b64_tr_b16 v[60:61], v42 offset:8192\nqp 221\nv_fma_f32 v31, v107, v21, v22\n"
"qm v,v,a,148,84,52\nds_read_b64_tr_b16 v[62:63], v42 offset:12288\nqd 5,76,82,1024\nqp 222\nv_fma_f32 v32, v108, v21, v22\nqm v,v,a,148,80,48\n"
"qw 8\nds_read_b64_tr_b16 v[64:65], v43 offset:0\nqp 223\nv_fma_f32 v33, v109, v21, v22\nqm v,v,a,152,224,48\nds_read_b64_tr_b16 v[66:67], v43 offset:4096\n"
"v_mul_f32 v120, v120, v216\nv_fma_f32 v34, v110, v21, v22\nqm v,v,a,152,228,52\nv_mul_f32 v121, v121, v217\nv_fma_f32 v35, v111, v21, v22\nds_read_b64_tr_b16 v[68:69], v43 offset:8192\n"
"ds_read_b64_tr_b16 v[70:71], v43 offset:12288\nqv 8\ns_barrier\nqm v,v,a,184,228,60\nv_mul_f32 v122, v122, v218\nqx 28\n"
"qm v,v,a,184,224,56\nqw 8\nds_read_b64_tr_b16 v[72:73], v36 offset:16384\nqd 6,76,82,2048\nv_mul_f32 v123, v123, v219\nqx 29\n"
"qm v,v,a,188,232,56\nds_read_b64_tr_b16 v[74:75], v36 offset:20480\nv_mul_f32 v124, v124, v220\nqx 30\nqm v,v,a,188,236,60\nds_read_b64_tr_b16 v[76:77], v36 offset:24576\n"
"v_mul_f32 v125, v125, v221\nqx 31\nqm v,v,a,156,236,52\nds_read_b64_tr_b16 v[78:79], v36 offset:28672\nv_mul_f32 v126, v126, v222\nv_mul_f32 v127, v127, v223\n"
"qx 32\nqx 33\nqm v,v,a,156,232,48\nqw 8\nds_read_b64_tr_b16 v[80:81], v37 offset:16384\nqc 120,120\n"
"qx 34\nqm v,v,a,160,56,48\nds_read_b64_tr_b16 v[82:83], v37 offset:20480\nqd 7,76,82,3072\nqc 121,122\nqx 35\n"
"qm v,v,a,160,60,52\nds_read_b64_tr_b16 v[84:85], v37 offset:24576\nqc 122,124\nv_add_f32 v28, v28, v23\nqm v,v,a,192,60,60\nds_read_b64_tr_b16 v[86:87], v37 offset:28672\n"
"qc 123,126\nv_add_f32 v29, v29, v23\nqm v,v,a,192,56,56\nqw 8\nds_read_b64_tr_b16 v[224:225], v38 offset:16384\nv_add_f32 v30, v30, v23\n"
"v_fmac_f32 v128, v200, v24\nqm v,v,a,196,64,56\nds_read_b64_tr_b16 v[226:227], v38 offset:20480\nv_add_f32 v31, v31, v23\nv_fmac_f32 v129, v201, v24\nqm v,v,a,196,68,60\n"
"ds_read_b64_tr_b16 v[228:229], v38 offset:24576\nv_add_f32 v32, v32, v23\nv_fmac_f32 v130, v202, v24\nqm v,v,a,164,68,52\nds_read_b64_tr_b16 v[230:231], v38 offset:28672\nv_add_f32 v33, v33, v23\n"
"v_fmac_f32 v131, v203, v24\nqm v,v,a,164,64,48\nqw 8\nds_read_b64_tr_b16 v[232:233], v39 offset:16384\nv_add_f32 v34, v34, v23\nv_fmac_f32 v132, v204, v24\n"
"qm v,v,a,136,72,64\nds_read_b64_tr_b16 v[234:235], v39 offset:20480\nbuffer_store_dwordx4 v[120:123], v0, s[56:59], s7 offen offset:1024 sc1\nv_add_f32 v35, v35, v23\nqp 28\nv_fmac_f32 v133, v205, v24\n"
"v_fmac_f32 v134, v206, v24\nqm v,v,a,136,76,68\nds_read_b64_tr_b16 v[236:237], v39 offset:24576\nqp 29\nv_fmac_f32 v135, v207, v24\nqm v,v,a,168,76,76\n"
"ds_read_b64_tr_b16 v[238:239], v39 offset:28672\nqp 30\nv_fma_f32 v216, v128, v25, v26\nv_readlane_b32 s82, v48, 8\nqm v,v,a,168,72,72\nqw 8\n"
"ds_read_b64_tr_b16 v[56:57], v40 offset:16384\nqp 31\nv_fma_f32 v217, v129, v25, v26\nqm v,v,a,172,80,72\ns_add_u32 m0, s81, 0x0\nds_read_b64_tr_b16 v[58:59], v40 offset:20480\n"
"qp 32\nv_fma_f32 v218, v130, v25, v26\nqm v,v,a,172,84,76\nds_read_b64_tr_b16 v[60:61], v40 offset:24576\nqp 33\nv_fma_f32 v219, v131, v25, v26\n"
"qm v,v,a,140,84,68\nds_read_b64_tr_b16 v[62:63], v40 offset:28672\nqd 4,76,82,0\nqp 34\nv_fma_f32 v220, v132, v25, v26\nqm v,v,a,140,80,64\n"
"qw 8\nds_read_b64_tr_b16 v[64:65], v41 offset:16384\nqp 35\nv_fma_f32 v221, v133, v25, v26\nqm v,v,a,144,224,64\nds_read_b64_tr_b16 v[66:67], v41 offset:20480\n"
"v_mul_f32 v104, v104, v28\nv_fma_f32 v222, v134, v25, v26\nqm v,v,a,144,228,68\nds_read_b64_tr_b16 v[68:69], v41 offset:24576\nv_mul_f32 v105, v105, v29\nv_fma_f32 v223, v135, v25, v26\n"
"qm v,v,a,176,228,76\nds_read_b64_tr_b16 v[70:71], v41 offset:28672\nv_mul_f32 v106, v106, v30\nv_mul_f32 v107, v107, v31\nqx 216\nqx 217\n"
"qm v,v,a,176,224,72\nqw 8\nds_read_b64_tr_b16 v[72:73], v42 offset:16384\nqd 5,76,82,1024\nv_mul_f32 v108, v108, v32\nqx 218\n"
"qm v,v,a,180,232,72\nds_read_b64_tr_b16 v[74:75], v42 offset:20480\nv_mul_f32 v109, v109, v33\nqx 219\nqm v,v,a,180,236,76\nds_read_b64_tr_b16 v[76:77], v42 offset:24576\n"
"v_mul_f32 v110, v110, v34\nqx 220\nqm v,v,a,148,236,68\nds_read_b64_tr_b16 v[78:79], v42 offset:28672\nv_mul_f32 v111, v111, v35\nqx 221\n"
"qm v,v,a,148,232,64\nqw 8\nds_read_b64_tr_b16 v[80:81], v43 offset:16384\nqc 104,104\nqx 222\nqm v,v,a,152,56,64\n"
"ds_read_b64_tr_b16 v[82:83], v43 offset:20480\nqd 6,76,82,2048\nqc 105,106\nqx 223\nqm v,v,a,152,60,68\nqc 106,108\n"
"v_add_f32 v216, v216, v27\nds_read_b64_tr_b16 v[84:85], v43 offset:24576\nds_read_b64_tr_b16 v[86:87], v43 offset:28672\nqv 9\ns_barrier\nqm v,v,a,184,60,76\n"
"qc 107,110\nv_add_f32 v217, v217, v27\nqm v,v,a,184,56,72\nqw 8\nds_read_b64_tr_b16 v[224:225], v36 offset:32768\nv_add_f32 v218, v218, v27\n"
"qm v,v,a,188,64,72\nds_read_b64_tr_b16 v[226:227], v36 offset:36864\nv_add_f32 v219, v219, v27\nqm v,v,a,188,68,76\nds_read_b64_tr_b16 v[228:229], v36 offset:40960\nv_add_f32 v220, v220, v27\n"
"v_add_f32 v221, v221, v27\nqm v,v,a,156,68,68\nds_read_b64_tr_b16 v[230:231], v36 offset:45056\nv_add_f32 v222, v222, v27\nqm v,v,a,156,64,64\nqw 8\n"
"ds_read_b64_tr_b16 v[232:233], v37 offset:32768\nv_add_f32 v223, v223, v27\nqm v,v,a,160,72,64\nds_read_b64_tr_b16 v[234:235], v37 offset:36864\nbuffer_store_dwordx4 v[104:107], v0, s[56:59], s6 offen offset:2048 sc1\nqp 216\n"
"qm v,v,a,160,76,68\nds_read_b64_tr_b16 v[236:237], v37 offset:40960\nqp 217\nqm v,v,a,192,76,76\nds_read_b64_tr_b16 v[238:239], v37 offset:45056\nqp 218\n"
"qm v,v,a,192,72,72\nqw 8\nds_read_b64_tr_b16 v[56:57], v38 offset:32768\nqp 219\nqm v,v,a,196,80,72\nds_read_b64_tr_b16 v[58:59], v38 offset:36864\n"
"qp 220\nqm v,v,a,196,84,76\nds_read_b64_tr_b16 v[60:61], v38 offset:40960\nqp 221\nqm v,v,a,164,84,68\nds_read_b64_tr_b16 v[62:63], v38 offset:45056\n"
"qd 7,76,82,3072\nqp 222\nqm v,v,a,164,80,64\nqw 8\nds_read_b64_tr_b16 v[64:65], v39 offset:32768\nqp 223\n"
"v_mul_f32 v128, v128, v216\nqm v,v,a,136,224,80\nds_read_b64_tr_b16 v[66:67], v39 offset:36864\nv_readlane_b32 s82, v48, 9\nv_mul_f32 v129, v129, v217\nqm v,v,a,136,228,84\n"
"ds_read_b64_tr_b16 v[68:69], v39 offset:40960\nv_mul_f32 v130, v130, v218\nqm v,v,a,168,228,92\ns_add_u32 m0, s81, 0x4000\nds_read_b64_tr_b16 v[70:71], v39 offset:45056\nv_mul_f32 v131, v131, v219\n"
"qm v,v,a,168,224,88\nqw 8\nds_read_b64_tr_b16 v[72:73], v40 offset:32768\nqd 4,76,82,0\nv_mul_f32 v132, v132, v220\nqm v,v,a,172,232,88\n"
"ds_read_b64_tr_b16 v[74:75], v40 offset:36864\nv_mul_f32 v133, v133, v221\nqm v,v,a,172,236,92\nds_read_b64_tr_b16 v[76:77], v40 offset:40960\nv_mul_f32 v134, v134, v222\nqm v,v,a,140,236,84\n"
"ds_read_b64_tr_b16 v[78:79], v40 offset:45056\nv_mul_f32 v135, v135, v223\nqm v,v,a,140,232,80\nqw 8\nds_read_b64_tr_b16 v[80:81], v41 offset:32768\nqc 128,128\n"
"qm v,v,a,144,56,80\nds_read_b64_tr_b16 v[82:83], v41 offset:36864\nqd 5,76,82,1024\nqc 129,130\nqm v,v,a,144,60,84\nds_read_b64_tr_b16 v[84:85], v41 offset:40960\n"
"qc 130,132\nqm v,v,a,176,60,92\nds_read_b64_tr_b16 v[86:87], v41 offset:45056\nqc 131,134\nqm v,v,a,176,56,88\nqw 8\n"
"ds_read_b64_tr_b16 v[224:225], v42 offset:32768\nqm v,v,a,180,64,88\nds_read_b64_tr_b16 v[226:227], v42 offset:36864\nqm v,v,a,180,68,92\nds_read_b64_tr_b16 v[228:229], v42 offset:40960\nqm v,v,a,148,68,84\n"
"ds_read_b64_tr_b16 v[230:231], v42 offset:45056\nqm v,v,a,148,64,80\nqw 8\nds_read_b64_tr_b16 v[232:233], v43 offset:32768\nqm v,v,a,152,72,80\nds_read_b64_tr_b16 v[234:235], v43 offset:36864\n"
"buffer_store_dwordx4 v[128:131], v0, s[56:59], s7 offen offset:2048 sc1\nqm v,v,a,152,76,84\nds_read_b64_tr_b16 v[236:237], v43 offset:40960\nds_read_b64_tr_b16 v[238:239], v43 offset:45056\nqv 9\ns_barrier\n"
"qm v,v,a,184,76,92\nds_read_b64_tr_b16 v[56:57], v36 offset:49152\nqm v,v,a,184,72,88\nqw 9\nds_read_b64_tr_b16 v[58:59], v36 offset:53248\nqm v,v,a,188,80,88\n"
"ds_read_b64_tr_b16 v[60:61], v36 offset:57344\nqm v,v,a,188,84,92\nqd 6,76,82,2048\nqm v,v,a,156,84,84\nds_read_b64_tr_b16 v[62:63], v36 offset:61440\nv_add_u32 v36, 0xffff0000, v36\n"
"qm v,v,a,156,80,80\nqw 8\nds_read_b64_tr_b16 v[64:65], v37 offset:49152\nqm v,v,a,160,224,80\nds_read_b64_tr_b16 v[66:67], v37 offset:53248\nqm v,v,a,160,228,84\n"
"ds_read_b64_tr_b16 v[68:69], v37 offset:57344\nqm v,v,a,192,228,92\nds_read_b64_tr_b16 v[70:71], v37 offset:61440\nv_add_u32 v37, 0xffff0000, v37\nqm v,v,a,192,224,88\nqw 8\n"
"ds_read_b64_tr_b16 v[72:73], v38 offset:49152\nqd 7,76,82,3072\nqm v,v,a,196,232,88\nds_read_b64_tr_b16 v[74:75], v38 offset:53248\nqm v,v,a,196,236,92\nds_read_b64_tr_b16 v[76:77], v38 offset:57344\n"
"qm v,v,a,164,236,84\nds_read_b64_tr_b16 v[78:79], v38 offset:61440\nv_add_u32 v38, 0xffff0000, v38\nqm v,v,a,164,232,80\nqw 8\nds_read_b64_tr_b16 v[80:81], v39 offset:49152\n"
"qm v,v,a,136,56,96\nds_read_b64_tr_b16 v[82:83], v39 offset:53248\nv_readlane_b32 s82, v48, 10\nqm v,v,a,136,60,100\nds_read_b64_tr_b16 v[84:85], v39 offset:57344\nqm v,v,a,168,60,108\n"
"s_add_u32 m0, s81, 0x8000\nds_read_b64_tr_b16 v[86:87], v39 offset:61440\nv_add_u32 v39, 0xffff0000, v39\nqm v,v,a,168,56,104\nqw 8\nqd 4,76,82,0\n"
"ds_read_b64_tr_b16 v[224:225], v40 offset:49152\nqm v,v,a,172,64,104\nds_read_b64_tr_b16 v[226:227], v40 offset:53248\nqm v,v,a,172,68,108\nds_read_b64_tr_b16 v[228:229], v40 offset:57344\nqm v,v,a,140,68,100\n"
"ds_read_b64_tr_b16 v[230:231], v40 offset:61440\nv_add_u32 v40, 0xffff0000, v40\nqm v,v,a,140,64,96\nqw 8\nds_read_b64_tr_b16 v[232:233], v41 offset:49152\nqm v,v,a,144,72,96\n"
"ds_read_b64_tr_b16 v[234:235], v41 offset:53248\nqd 5,76,82,1024\nqm v,v,a,144,76,100\nds_read_b64_tr_b16 v[236:237], v41 offset:57344\nqm v,v,a,176,76,108\nds_read_b64_tr_b16 v[238:239], v41 offset:61440\n"
"v_add_u32 v41, 0xffff0000, v41\nqm v,v,a,176,72,104\nqw 8\nds_read_b64_tr_b16 v[56:57], v42 offset:49152\nqm v,v,a,180,80,104\nds_read_b64_tr_b16 v[58:59], v42 offset:53248\n"
"qm v,v,a,180,84,108\nds_read_b64_tr_b16 v[60:61], v42 offset:57344\nqm v,v,a,148,84,100\nds_read_b64_tr_b16 v[62:63], v42 offset:61440\nv_add_u32 v42, 0xffff0000, v42\nqm v,v,a,148,80,96\n"
"qw 8\nds_read_b64_tr_b16 v[64:65], v43 offset:49152\nqm v,v,a,152,224,96\nds_read_b64_tr_b16 v[66:67], v43 offset:53248\nqd 6,76,82,2048\nqm v,v,a,152,228,100\n"
"ds_read_b64_tr_b16 v[68:69], v43 offset:57344\nds_read_b64_tr_b16 v[70:71], v43 offset:61440\nv_add_u32 v43, 0xffff0000, v43\nqv 8\ns_barrier\nqm v,v,a,184,228,108\n"
"ds_read_b64_tr_b16 v[72:73], v36 offset:0\nqm v,v,a,184,224,104\nqw 9\nds_read_b64_tr_b16 v[74:75], v36 offset:4096\nqm v,v,a,188,232,104\nds_read_b64_tr_b16 v[76:77], v36 offset:8192\n"
"qm v,v,a,188,236,108\nds_read_b64_tr_b16 v[78:79], v36 offset:12288\nqm v,v,a,156,236,100\nds_read_b64_tr_b16 v[80:81], v37 offset:0\nqm v,v,a,156,232,96\nqw 9\n"
"qd 7,76,82,3072\nqm v,v,a,160,56,96\nds_read_b64_tr_b16 v[82:83], v37 offset:4096\nqm v,v,a,160,60,100\nds_read_b64_tr_b16 v[84:85], v37 offset:8192\nqm v,v,a,192,60,108\n"
"ds_read_b64_tr_b16 v[86:87], v37 offset:12288\nqm v,v,a,192,56,104\nqw 8\nds_read_b64_tr_b16 v[224:225], v38 offset:0\nqm v,v,a,196,64,104\nds_read_b64_tr_b16 v[226:227], v38 offset:4096\n"
"qm v,v,a,196,68,108\nds_read_b64_tr_b16 v[228:229], v38 offset:8192\nqm v,v,a,164,68,100\nds_read_b64_tr_b16 v[230:231], v38 offset:12288\nqm v,v,a,164,64,96\nqw 8\n"
"ds_read_b64_tr_b16 v[232:233], v39 offset:0\nqm v,v,a,136,72,112\nds_read_b64_tr_b16 v[234:235], v39 offset:4096\nv_readlane_b32 s82, v48, 11\nqm v,v,a,136,76,116\nds_read_b64_tr_b16 v[236:237], v39 offset:8192\n"
"qm v,v,a,168,76,124\ns_add_u32 m0, s81, 0xc000\nds_read_b64_tr_b16 v[238:239], v39 offset:12288\nqm v,v,a,168,72,120\nqw 8\nqd 4,76,82,0\n"
"ds_read_b64_tr_b16 v[56:57], v40 offset:0\nqm v,v,a,172,80,120\nds_read_b64_tr_b16 v[58:59], v40 offset:4096\nqm v,v,a,172,84,124\nds_read_b64_tr_b16 v[60:61], v40 offset:8192\nqm v,v,a,140,84,116\n"
"ds_read_b64_tr_b16 v[62:63], v40 offset:12288\nqm v,v,a,140,80,112\nqw 8\nds_read_b64_tr_b16 v[64:65], v41 offset:0\nqm v,v,a,144,224,112\nds_read_b64_tr_b16 v[66:67], v41 offset:4096\n"
"qd 5,76,82,1024\nqm v,v,a,144,228,116\nds_read_b64_tr_b16 v[68:69], v41 offset:8192\nqm v,v,a,176,228,124\nds_read_b64_tr_b16 v[70:71], v41 offset:12288\nqm v,v,a,176,224,120\n"
"qw 8\nds_read_b64_tr_b16 v[72:73], v42 offset:0\nqm v,v,a,180,232,120\nds_read_b64_tr_b16 v[74:75], v42 offset:4096\nqm v,v,a,180,236,124\nds_read_b64_tr_b16 v[76:77], v42 offset:8192\n"
"qm v,v,a,148,236,116\nds_read_b64_tr_b16 v[78:79], v42 offset:12288\nqm v,v,a,148,232,112\nqw 8\nds_read_b64_tr_b16 v[80:81], v43 offset:0\nqm v,v,a,152,56,112\n"
"ds_read_b64_tr_b16 v[82:83], v43 offset:4096\nqd 6,76,82,2048\nqv 9\nqm v,v,a,152,60,116\nds_read_b64_tr_b16 v[84:85], v43 offset:8192\nds_read_b64_tr_b16 v[86:87], v43 offset:12288\n"
"qv 7\ns_barrier\nqm v,v,a,184,60,124\nds_read_b64_tr_b16 v[224:225], v36 offset:16384\nqm v,v,a,184,56,120\nqw 9\n"
"ds_read_b64_tr_b16 v[226:227], v36 offset:20480\nqm v,v,a,188,64,120\nds_read_b64_tr_b16 v[228:229], v36 offset:24576\nqm v,v,a,188,68,124\ns_mov_b64 exec, 1\nbuffer_store_dwordx2 v[250:251], off, s[56:59], s8 sc1\n"
"s_mov_b64 exec, -1\ns_add_u32 s8, s8, 128\ns_lshl_b32 s72, s23, 10\ns_add_u32 s72, s72, 49152\ns_add_u32 s6, s6, s72\ns_add_u32 s7, s7, s72\n"
"qm v,v,a,156,68,116\nds_read_b64_tr_b16 v[230:231], v36 offset:28672\nqm v,v,a,156,64,112\nqw 8\nds_read_b64_tr_b16 v[232:233], v37 offset:16384\nqd 7,76,82,3072\n"
"qm v,v,a,160,72,112\nds_read_b64_tr_b16 v[234:235], v37 offset:20480\nqm v,v,a,160,76,116\nds_read_b64_tr_b16 v[236:237], v37 offset:24576\nqm v,v,a,192,76,124\nds_read_b64_tr_b16 v[238:239], v37 offset:28672\n"
"qm v,v,a,192,72,120\nqw 8\nds_read_b64_tr_b16 v[56:57], v38 offset:16384\nqm v,v,a,196,80,120\nds_read_b64_tr_b16 v[58:59], v38 offset:20480\nqm v,v,a,196,84,124\n"
"ds_read_b64_tr_b16 v[60:61], v38 offset:24576\nqm v,v,a,164,84,116\nds_read_b64_tr_b16 v[62:63], v38 offset:28672\nqm v,v,a,164,80,112\nqw 8\nds_read_b64_tr_b16 v[64:65], v39 offset:16384\n"
"qm v,v,a,136,224,128\nds_read_b64_tr_b16 v[66:67], v39 offset:20480\nv_readlane_b32 s82, v48, 12\nqm v,v,a,136,228,132\nds_read_b64_tr_b16 v[68:69], v39 offset:24576\nqm v,v,a,168,228,140\n"
"s_add_u32 m0, s81, 0x10000\nds_read_b64_tr_b16 v[70:71], v39 offset:28672\nqm v,v,a,168,224,136\nqw 8\nqd 4,76,82,0\nds_read_b64_tr_b16 v[72:73], v40 offset:16384\n"
"qm v,v,a,172,232,136\nds_read_b64_tr_b16 v[74:75], v40 offset:20480\nqm v,v,a,172,236,140\nds_read_b64_tr_b16 v[76:77], v40 offset:24576\nqm v,v,a,140,236,132\nds_read_b64_tr_b16 v[78:79], v40 offset:28672\n"
"qm v,v,a,140,232,128\nqw 8\nds_read_b64_tr_b16 v[80:81], v41 offset:16384\nqm v,v,a,144,56,128\nds_read_b64_tr_b16 v[82:83], v41 offset:20480\nqd 5,76,82,1024\n"
"qm v,v,a,144,60,132\nds_read_b64_tr_b16 v[84:85], v41 offset:24576\nqm v,v,a,176,60,140\nds_read_b64_tr_b16 v[86:87], v41 offset:28672\nqm v,v,a,176,56,136\nqw 8\n"
"ds_read_b64_tr_b16 v[224:225], v42 offset:16384\nqm v,v,a,180,64,136\nds_read_b64_tr_b16 v[226:227], v42 offset:20480\nqm v,v,a,180,68,140\nds_read_b64_tr_b16 v[228:229], v42 offset:24576\nqm v,v,a,148,68,132\n"
"ds_read_b64_tr_b16 v[230:231], v42 offset:28672\nqm v,v,a,148,64,128\nqw 8\nds_read_b64_tr_b16 v[232:233], v43 offset:16384\nqm v,v,a,152,72,128\nds_read_b64_tr_b16 v[234:235], v43 offset:20480\n"
"qd 6,76,82,2048\nqm v,v,a,152,76,132\nds_read_b64_tr_b16 v[236:237], v43 offset:24576\nds_read_b64_tr_b16 v[238:239], v43 offset:28672\nqv 8\ns_barrier\n"
"qm v,v,a,184,76,140\nds_read_b64_tr_b16 v[56:57], v36 offset:32768\nqm v,v,a,184,72,136\nqw 9\nds_read_b64_tr_b16 v[58:59], v36 offset:36864\nqm v,v,a,188,80,136\n"
"ds_read_b64_tr_b16 v[60:61], v36 offset:40960\nqm v,v,a,188,84,140\nds_read_b64_tr_b16 v[62:63], v36 offset:45056\nqm v,v,a,156,84,132\nds_read_b64_tr_b16 v[64:65], v37 offset:32768\nqm v,v,a,156,80,128\n"
"qw 9\nqd 7,76,82,3072\nqm v,v,a,160,224,128\nds_read_b64_tr_b16 v[66:67], v37 offset:36864\nqm v,v,a,160,228,132\nds_read_b64_tr_b16 v[68:69], v37 offset:40960\n"
"qm v,v,a,192,228,140\nds_read_b64_tr_b16 v[70:71], v37 offset:45056\nqm v,v,a,192,224,136\nqw 8\nds_read_b64_tr_b16 v[72:73], v38 offset:32768\nqm v,v,a,196,232,136\n"
"ds_read_b64_tr_b16 v[74:75], v38 offset:36864\nqm v,v,a,196,236,140\nds_read_b64_tr_b16 v[76:77], v38 offset:40960\nqm v,v,a,164,236,132\nds_read_b64_tr_b16 v[78:79], v38 offset:45056\nqm v,v,a,164,232,128\n"
"qw 8\nds_read_b64_tr_b16 v[80:81], v39 offset:32768\nqm v,v,a,136,56,144\nds_read_b64_tr_b16 v[82:83], v39 offset:36864\nv_readlane_b32 s82, v48, 13\nqm v,v,a,136,60,148\n"
"ds_read_b64_tr_b16 v[84:85], v39 offset:40960\nqm v,v,a,168,60,156\ns_add_u32 m0, s81, 0x14000\nds_read_b64_tr_b16 v[86:87], v39 offset:45056\nqm v,v,a,168,56,152\nqw 8\n"
"qd 4,76,82,0\nds_read_b64_tr_b16 v[224:225], v40 offset:32768\nqm v,v,a,172,64,152\nds_read_b64_tr_b16 v[226:227], v40 offset:36864\nqm v,v,a,172,68,156\nds_read_b64_tr_b16 v[228:229], v40 offset:40960\n"
"qd 5,76,82,1024\nqm v,v,a,140,68,148\nds_read_b64_tr_b16 v[230:231], v40 offset:45056\nqm v,v,a,140,64,144\nqw 8\nds_read_b64_tr_b16 v[232:233], v41 offset:32768\n"
"qm v,v,a,144,72,144\nds_read_b64_tr_b16 v[234:235], v41 offset:36864\nqm v,v,a,144,76,148\nds_read_b64_tr_b16 v[236:237], v41 offset:40960\nqd 6,76,82,2048\nqm v,v,a,176,76,156\n"
"ds_read_b64_tr_b16 v[238:239], v41 offset:45056\nqm v,v,a,176,72,152\nqw 8\nds_read_b64_tr_b16 v[56:57], v42 offset:32768\nqm v,v,a,180,80,152\nds_read_b64_tr_b16 v[58:59], v42 offset:36864\n"
"qm v,v,a,180,84,156\nds_read_b64_tr_b16 v[60:61], v42 offset:40960\nqd 7,76,82,3072\nqm v,v,a,148,84,148\nds_read_b64_tr_b16 v[62:63], v42 offset:45056\nqm v,v,a,148,80,144\n"
"qw 8\nds_read_b64_tr_b16 v[64:65], v43 offset:32768\nv_readlane_b32 s2, v18, 0\nqm v,v,a,152,224,144\nds_read_b64_tr_b16 v[66:67], v43 offset:36864\nqm v,v,a,152,228,148\n"
"s_add_u32 m0, s0, 0x0\nds_read_b64_tr_b16 v[68:69], v43 offset:40960\nds_read_b64_tr_b16 v[70:71], v43 offset:45056\nqv 8\ns_barrier\nqm v,v,a,184,228,156\n"
"qd 12,60,2,0\nqm v,v,a,184,224,152\nqw 8\nds_read_b64_tr_b16 v[72:73], v36 offset:49152\nqm v,v,a,188,232,152\nds_read_b64_tr_b16 v[74:75], v36 offset:53248\n"
"qm v,v,a,188,236,156\nds_read_b64_tr_b16 v[76:77], v36 offset:57344\nqd 13,60,2,1024\nqm v,v,a,156,236,148\nds_read_b64_tr_b16 v[78:79], v36 offset:61440\nv_add_u32 v36, 0x10000, v36\n"
"qm v,v,a,156,232,144\nqw 8\nds_read_b64_tr_b16 v[80:81], v37 offset:49152\nqm v,v,a,160,56,144\nds_read_b64_tr_b16 v[82:83], v37 offset:53248\nqm v,v,a,160,60,148\n"
"ds_read_b64_tr_b16 v[84:85], v37 offset:57344\nqd 14,60,2,2048\nqm v,v,a,192,60,156\nds_read_b64_tr_b16 v[86:87], v37 offset:61440\nv_add_u32 v37, 0x10000, v37\nqm v,v,a,192,56,152\n"
"qw 8\nds_read_b64_tr_b16 v[224:225], v38 offset:49152\nqm v,v,a,196,64,152\nds_read_b64_tr_b16 v[226:227], v38 offset:53248\nqm v,v,a,196,68,156\nds_read_b64_tr_b16 v[228:229], v38 offset:57344\n"
"qd 15,60,2,3072\nqm v,v,a,164,68,148\nds_read_b64_tr_b16 v[230:231], v38 offset:61440\nv_add_u32 v38, 0x10000, v38\nqm v,v,a,164,64,144\nqw 8\n"
"ds_read_b64_tr_b16 v[232:233], v39 offset:49152\nqm v,v,a,136,72,160\nds_read_b64_tr_b16 v[234:235], v39 offset:53248\nv_readlane_b32 s82, v48, 14\nqm v,v,a,136,76,164\nds_read_b64_tr_b16 v[236:237], v39 offset:57344\n"
"qm v,v,a,168,76,172\ns_add_u32 m0, s81, 0x18000\nds_read_b64_tr_b16 v[238:239], v39 offset:61440\nv_add_u32 v39, 0x10000, v39\nqm v,v,a,168,72,168\nqw 8\n"
"qd 4,76,82,0\nds_read_b64_tr_b16 v[56:57], v40 offset:49152\nqm v,v,a,172,80,168\nds_read_b64_tr_b16 v[58:59], v40 offset:53248\nqm v,v,a,172,84,172\nds_read_b64_tr_b16 v[60:61], v40 offset:57344\n"
"qm v,v,a,140,84,164\nds_read_b64_tr_b16 v[62:63], v40 offset:61440\nqd 5,76,82,1024\nqm v,v,a,140,80,160\nv_add_u32 v40, 0x10000, v40\nds_read_b64_tr_b16 v[64:65], v41 offset:49152\n"
"qw 9\nqm v,v,a,144,224,160\nds_read_b64_tr_b16 v[66:67], v41 offset:53248\nqm v,v,a,144,228,164\nds_read_b64_tr_b16 v[68:69], v41 offset:57344\nqm v,v,a,176,228,172\n"
"ds_read_b64_tr_b16 v[70:71], v41 offset:61440\nqd 6,76,82,2048\nqm v,v,a,176,224,168\nv_add_u32 v41, 0x10000, v41\nds_read_b64_tr_b16 v[72:73], v42 offset:49152\nqw 9\n"
"qm v,v,a,180,232,168\nds_read_b64_tr_b16 v[74:75], v42 offset:53248\nqm v,v,a,180,236,172\nds_read_b64_tr_b16 v[76:77], v42 offset:57344\nqm v,v,a,148,236,164\nds_read_b64_tr_b16 v[78:79], v42 offset:61440\n"
"qd 7,76,82,3072\nqm v,v,a,148,232,160\nv_readlane_b32 s2, v18, 1\nv_add_u32 v42, 0x10000, v42\nds_read_b64_tr_b16 v[80:81], v43 offset:49152\nqw 9\n"
"qm v,v,a,152,56,160\nds_read_b64_tr_b16 v[82:83], v43 offset:53248\nqm v,v,a,152,60,164\ns_add_u32 m0, s0, 0x4000\nds_read_b64_tr_b16 v[84:85], v43 offset:57344\nds_read_b64_tr_b16 v[86:87], v43 offset:61440\n"
"v_add_u32 v43, 0x10000, v43\nqv 12\ns_barrier\nqm v,v,a,184,60,172\nqd 12,60,2,0\nqm v,v,a,184,56,168\n"
"qw 8\nds_read_b64_tr_b16 v[224:225], v36 offset:0\nqm v,v,a,188,64,168\nds_read_b64_tr_b16 v[226:227], v36 offset:4096\nqm v,v,a,188,68,172\nds_read_b64_tr_b16 v[228:229], v36 offset:8192\n"
"qm v,v,a,156,68,164\nds_read_b64_tr_b16 v[230:231], v36 offset:12288\nqd 13,60,2,1024\nqm v,v,a,156,64,160\nqw 8\nds_read_b64_tr_b16 v[232:233], v37 offset:0\n"
"qm v,v,a,160,72,160\nds_read_b64_tr_b16 v[234:235], v37 offset:4096\nqm v,v,a,160,76,164\nds_read_b64_tr_b16 v[236:237], v37 offset:8192\nqm v,v,a,192,76,172\nds_read_b64_tr_b16 v[238:239], v37 offset:12288\n"
"buffer_load_dwordx2 v[248:249], v19, s[56:59], s5 offen sc1\nqm v,v,a,192,72,168\nqw 8\nds_read_b64_tr_b16 v[56:57], v38 offset:0\nqm v,v,a,196,80,168\nds_read_b64_tr_b16 v[58:59], v38 offset:4096\n"
"qm v,v,a,196,84,172\nds_read_b64_tr_b16 v[60:61], v38 offset:8192\nqm v,v,a,164,84,164\nds_read_b64_tr_b16 v[62:63], v38 offset:12288\nqm v,v,a,164,80,160\nqw 8\n"
"ds_read_b64_tr_b16 v[64:65], v39 offset:0\nqd 14,60,2,2048\nqm v,v,a,136,224,176\nds_read_b64_tr_b16 v[66:67], v39 offset:4096\nqa 240,240\nqm v,v,a,136,228,180\n"
"ds_read_b64_tr_b16 v[68:69], v39 offset:8192\nv_lshlrev_b32 v88, 16, v240\nqm v,v,a,168,228,188\nds_read_b64_tr_b16 v[70:71], v39 offset:12288\nv_and_b32 v89, 0xffff0000, v240\nqm v,v,a,168,224,184\n"
"qw 8\nds_read_b64_tr_b16 v[72:73], v40 offset:0\nqd 15,60,2,3072\nv_readlane_b32 s82, v48, 15\nqm v,v,a,172,232,184\nds_read_b64_tr_b16 v[74:75], v40 offset:4096\n"
"qa 241,241\nqm v,v,a,172,236,188\ns_add_u32 m0, s81, 0x1c000\nds_read_b64_tr_b16 v[76:77], v40 offset:8192\nv_lshlrev_b32 v90, 16, v241\nqm v,v,a,140,236,180\n"
"ds_read_b64_tr_b16 v[78:79], v40 offset:12288\nv_and_b32 v91, 0xffff0000, v241\nqm v,v,a,140,232,176\nqw 8\nds_read_b64_tr_b16 v[80:81], v41 offset:0\nqd 4,76,82,0\n"
"qm v,v,a,144,56,176\nds_read_b64_tr_b16 v[82:83], v41 offset:4096\nqa 242,242\nqm v,v,a,144,60,180\nds_read_b64_tr_b16 v[84:85], v41 offset:8192\nv_lshlrev_b32 v92, 16, v242\n"
"qm v,v,a,176,60,188\nds_read_b64_tr_b16 v[86:87], v41 offset:12288\nv_and_b32 v93, 0xffff0000, v242\nqm v,v,a,176,56,184\nqw 8\nds_read_b64_tr_b16 v[224:225], v42 offset:0\n"
"qd 5,76,82,1024\nqm v,v,a,180,64,184\nds_read_b64_tr_b16 v[226:227], v42 offset:4096\nqa 243,243\nqm v,v,a,180,68,188\nds_read_b64_tr_b16 v[228:229], v42 offset:8192\n"
"v_lshlrev_b32 v94, 16, v243\nqm v,v,a,148,68,180\nds_read_b64_tr_b16 v[230:231], v42 offset:12288\nv_and_b32 v95, 0xffff0000, v243\nqm v,v,a,148,64,176\nqw 8\n"
"ds_read_b64_tr_b16 v[232:233], v43 offset:0\nqa 240,244\nqm v,v,a,152,72,176\nds_read_b64_tr_b16 v[234:235], v43 offset:4096\nqd 6,76,82,2048\nqm v,v,a,152,76,180\n"
"ds_read_b64_tr_b16 v[236:237], v43 offset:8192\nds_read_b64_tr_b16 v[238:239], v43 offset:12288\nqv 16\ns_barrier\nqm v,v,a,184,76,188\nds_read_b64_tr_b16 v[56:57], v36 offset:16384\n"
"v_lshlrev_b32 v96, 16, v240\nqm v,v,a,184,72,184\nqw 9\nds_read_b64_tr_b16 v[58:59], v36 offset:20480\nv_and_b32 v97, 0xffff0000, v240\nqm v,v,a,188,80,184\n"
"qd 7,76,82,3072\nv_readlane_b32 s2, v18, 2\nqm v,v,a,188,84,188\nds_read_b64_tr_b16 v[60:61], v36 offset:24576\nqa 241,245\nqm v,v,a,156,84,180\n"
"s_add_u32 m0, s0, 0x8000\nds_read_b64_tr_b16 v[62:63], v36 offset:28672\nv_lshlrev_b32 v98, 16, v241\nqm v,v,a,156,80,176\nqw 8\nds_read_b64_tr_b16 v[64:65], v37 offset:16384\n"
"v_and_b32 v99, 0xffff0000, v241\nqm v,v,a,160,224,176\nds_read_b64_tr_b16 v[66:67], v37 offset:20480\nqd 12,60,2,0\nqm v,v,a,160,228,180\nds_read_b64_tr_b16 v[68:69], v37 offset:24576\n"
"qa 242,246\nqm v,v,a,192,228,188\nds_read_b64_tr_b16 v[70:71], v37 offset:28672\nv_lshlrev_b32 v100, 16, v242\nqm v,v,a,192,224,184\nqw 8\n"
"qd 13,60,2,1024\nds_read_b64_tr_b16 v[72:73], v38 offset:16384\nqm v,v,a,196,232,184\nds_read_b64_tr_b16 v[74:75], v38 offset:20480\nv_and_b32 v101, 0xffff0000, v242\nqm v,v,a,196,236,188\n"
"ds_read_b64_tr_b16 v[76:77], v38 offset:24576\nqa 243,247\nqm v,v,a,164,236,180\nds_read_b64_tr_b16 v[78:79], v38 offset:28672\nv_lshlrev_b32 v102, 16, v243\nqm v,v,a,164,232,176\n"
"qw 8\nqd 14,60,2,2048\nds_read_b64_tr_b16 v[80:81], v39 offset:16384\nqm v,v,a,136,56,192\nds_read_b64_tr_b16 v[82:83], v39 offset:20480\nv_and_b32 v103, 0xffff0000, v243\n"
"qm v,v,a,136,60,196\nds_read_b64_tr_b16 v[84:85], v39 offset:24576\nqa 240,248\nqm v,v,a,168,60,204\nds_read_b64_tr_b16 v[86:87], v39 offset:28672\nv_lshlrev_b32 v104, 16, v240\n"
"qm v,v,a,168,56,200\nqw 8\nds_read_b64_tr_b16 v[224:225], v40 offset:16384\nv_and_b32 v105, 0xffff0000, v240\nqm v,v,a,172,64,200\nqd 15,60,2,3072\n"
"qv 10\nv_cmp_eq_u64 vcc, s[16:17], v[248:249]\nds_read_b64_tr_b16 v[226:227], v40 offset:20480\nqm v,v,a,172,68,204\nds_read_b64_tr_b16 v[228:229], v40 offset:24576\nqa 241,249\n"
"qm v,v,a,140,68,196\nds_read_b64_tr_b16 v[230:231], v40 offset:28672\nv_lshlrev_b32 v106, 16, v241\nqm v,v,a,140,64,192\ns_cmp_eq_u64 vcc, exec\ns_cbranch_scc0 PEG4_o%=\n"
"PEG4_r%=:\nqw 8\nds_read_b64_tr_b16 v[232:233], v41 offset:16384\nqm v,v,a,144,72,192\nds_read_b64_tr_b16 v[234:235], v41 offset:20480\nv_readlane_b32 s1, v17, 0\n"
"qm v,v,a,144,76,196\nds_read_b64_tr_b16 v[236:237], v41 offset:24576\nv_and_b32 v107, 0xffff0000, v241\nqm v,v,a,176,76,204\nds_read_b64_tr_b16 v[238:239], v41 offset:28672\nqa 242,250\n"
"qm v,v,a,176,72,200\nqw 8\nds_read_b64_tr_b16 v[56:57], v42 offset:16384\nv_lshlrev_b32 v108, 16, v242\nqm v,v,a,180,80,200\nds_read_b64_tr_b16 v[58:59], v42 offset:20480\n"
"ql 64,0,64,1,0\nqm v,v,a,180,84,204\nds_read_b64_tr_b16 v[60:61], v42 offset:24576\nv_and_b32 v109, 0xffff0000, v242\nqm v,v,a,148,84,196\nds_read_b64_tr_b16 v[62:63], v42 offset:28672\n"
"qa 243,251\nqm v,v,a,148,80,192\nqw 8\nds_read_b64_tr_b16 v[64:65], v43 offset:16384\nv_lshlrev_b32 v110, 16, v243\nqm v,v,a,152,224,192\n"
"ds_read_b64_tr_b16 v[66:67], v43 offset:20480\nv_and_b32 v111, 0xffff0000, v243\nqm v,v,a,152,228,196\nds_read_b64_tr_b16 v[68:69], v43 offset:24576\nds_read_b64_tr_b16 v[70:71], v43 offset:28672\ns_barrier\n"
"qm v,v,a,184,228,204\nds_read_b64_tr_b16 v[72:73], v36 offset:32768\nqa 240,252\nqm v,v,a,184,224,200\nqw 9\nds_read_b64_tr_b16 v[74:75], v36 offset:36864\n"
"v_lshlrev_b32 v112, 16, v240\nqm v,v,a,188,232,200\nql 68,0,64,1,1024\nqm v,v,a,188,236,204\nds_read_b64_tr_b16 v[76:77], v36 offset:40960\nv_and_b32 v113, 0xffff0000, v240\n"
"qm v,v,a,156,236,196\nds_read_b64_tr_b16 v[78:79], v36 offset:45056\nqa 241,253\nqm v,v,a,156,232,192\nqw 8\nds_read_b64_tr_b16 v[80:81], v37 offset:32768\n"
"v_lshlrev_b32 v114, 16, v241\nqm v,v,a,160,56,192\nds_read_b64_tr_b16 v[82:83], v37 offset:36864\nv_and_b32 v115, 0xffff0000, v241\nqm v,v,a,160,60,196\nds_read_b64_tr_b16 v[84:85], v37 offset:40960\n"
"qa 242,254\nqm v,v,a,192,60,204\nds_read_b64_tr_b16 v[86:87], v37 offset:45056\nv_lshlrev_b32 v116, 16, v242\nqm v,v,a,192,56,200\nqw 8\n"
"ds_read_b64_tr_b16 v[224:225], v38 offset:32768\nql 72,0,68,1,0\nqm v,v,a,196,64,200\nds_read_b64_tr_b16 v[226:227], v38 offset:36864\nv_and_b32 v117, 0xffff0000, v242\nqm v,v,a,196,68,204\n"
"ds_read_b64_tr_b16 v[228:229], v38 offset:40960\nqa 243,255\nqm v,v,a,164,68,196\nds_read_b64_tr_b16 v[230:231], v38 offset:45056\nv_lshlrev_b32 v118, 16, v243\nqm v,v,a,164,64,192\n"
"qw 8\nds_read_b64_tr_b16 v[232:233], v39 offset:32768\nv_and_b32 v119, 0xffff0000, v243\nqm v,v,a,136,72,208\nds_read_b64_tr_b16 v[234:235], v39 offset:36864\nqm v,v,a,136,76,212\n"
"ds_read_b64_tr_b16 v[236:237], v39 offset:40960\nqm v,v,a,168,76,220\nds_read_b64_tr_b16 v[238:239], v39 offset:45056\nqm v,v,a,168,72,216\nqw 8\nds_read_b64_tr_b16 v[56:57], v40 offset:32768\n"
"ql 76,0,68,1,1024\nqm v,v,a,172,80,216\nds_read_b64_tr_b16 v[58:59], v40 offset:36864\nv_readlane_b32 s1, v17, 1\nqm v,v,a,172,84,220\nds_read_b64_tr_b16 v[60:61], v40 offset:40960\n"
"qm v,v,a,140,84,212\nds_read_b64_tr_b16 v[62:63], v40 offset:45056\nqm v,v,a,140,80,208\nqw 8\nds_read_b64_tr_b16 v[64:65], v41 offset:32768\nqm v,v,a,144,224,208\n"
"ds_read_b64_tr_b16 v[66:67], v41 offset:36864\nqm v,v,a,144,228,212\nds_read_b64_tr_b16 v[68:69], v41 offset:40960\nqm v,v,a,176,228,220\nds_read_b64_tr_b16 v[70:71], v41 offset:45056\nql 80,0,64,1,0\n"
"qm v,v,a,176,224,216\nqw 8\nds_read_b64_tr_b16 v[72:73], v42 offset:32768\nqm v,v,a,180,232,216\nds_read_b64_tr_b16 v[74:75], v42 offset:36864\nqm v,v,a,180,236,220\n"
"ds_read_b64_tr_b16 v[76:77], v42 offset:40960\nqm v,v,a,148,236,212\nds_read_b64_tr_b16 v[78:79], v42 offset:45056\nqm v,v,a,148,232,208\nqw 8\nds_read_b64_tr_b16 v[80:81], v43 offset:32768\n"
"qm v,v,a,152,56,208\nds_read_b64_tr_b16 v[82:83], v43 offset:36864\nqm v,v,a,152,60,212\nds_read_b64_tr_b16 v[84:85], v43 offset:40960\nds_read_b64_tr_b16 v[86:87], v43 offset:45056\nqv 9\n"
"s_barrier\nqm v,v,a,184,60,220\nql 84,0,64,1,1024\nqm v,v,a,184,56,216\nqw 8\nds_read_b64_tr_b16 v[224:225], v36 offset:49152\n"
"qm v,v,a,188,64,216\nds_read_b64_tr_b16 v[226:227], v36 offset:53248\nqm v,v,a,188,68,220\nds_read_b64_tr_b16 v[228:229], v36 offset:57344\nqm v,v,a,156,68,212\nds_read_b64_tr_b16 v[230:231], v36 offset:61440\n"
"v_add_u32 v36, 0xffff0000, v36\nqm v,v,a,156,64,208\nqw 8\nds_read_b64_tr_b16 v[232:233], v37 offset:49152\nqm v,v,a,160,72,208\nds_read_b64_tr_b16 v[234:235], v37 offset:53248\n"
"qm v,v,a,160,76,212\nds_read_b64_tr_b16 v[236:237], v37 offset:57344\nql 88,0,68,1,0\nqm v,v,a,192,76,220\nds_read_b64_tr_b16 v[238:239], v37 offset:61440\nv_add_u32 v37, 0xffff0000, v37\n"
"qm v,v,a,192,72,216\nqw 8\nds_read_b64_tr_b16 v[56:57], v38 offset:49152\nqm v,v,a,196,80,216\nds_read_b64_tr_b16 v[58:59], v38 offset:53248\nqm v,v,a,196,84,220\n"
"ds_read_b64_tr_b16 v[60:61], v38 offset:57344\nqm v,v,a,164,84,212\nds_read_b64_tr_b16 v[62:63], v38 offset:61440\nv_add_u32 v38, 0xffff0000, v38\nqm v,v,a,164,80,208\nqw 8\n"
"ds_read_b64_tr_b16 v[64:65], v39 offset:49152\nqm v,v,a,136,224,224\nds_read_b64_tr_b16 v[66:67], v39 offset:53248\nqm v,v,a,136,228,228\nds_read_b64_tr_b16 v[68:69], v39 offset:57344\nql 92,0,68,1,1024\n"
"qm v,v,a,168,228,236\nds_read_b64_tr_b16 v[70:71], v39 offset:61440\nv_add_u32 v39, 0xffff0000, v39\nqm v,v,a,168,224,232\nqw 8\nds_read_b64_tr_b16 v[72:73], v40 offset:49152\n"
"v_readlane_b32 s1, v17, 2\nqm v,v,a,172,232,232\nds_read_b64_tr_b16 v[74:75], v40 offset:53248\nqm v,v,a,172,236,236\nds_read_b64_tr_b16 v[76:77], v40 offset:57344\nqm v,v,a,140,236,228\n"
"ds_read_b64_tr_b16 v[78:79], v40 offset:61440\nv_add_u32 v40, 0xffff0000, v40\nqm v,v,a,140,232,224\nqw 8\nds_read_b64_tr_b16 v[80:81], v41 offset:49152\nqm v,v,a,144,56,224\n"
"ds_read_b64_tr_b16 v[82:83], v41 offset:53248\nql 96,0,64,1,0\nqm v,v,a,144,60,228\nds_read_b64_tr_b16 v[84:85], v41 offset:57344\nqm v,v,a,176,60,236\nds_read_b64_tr_b16 v[86:87], v41 offset:61440\n"
"v_add_u32 v41, 0xffff0000, v41\nqm v,v,a,176,56,232\nqw 8\nds_read_b64_tr_b16 v[224:225], v42 offset:49152\nqm v,v,a,180,64,232\nds_read_b64_tr_b16 v[226:227], v42 offset:53248\n"
"qm v,v,a,180,68,236\nds_read_b64_tr_b16 v[228:229], v42 offset:57344\nqm v,v,a,148,68,228\nds_read_b64_tr_b16 v[230:231], v42 offset:61440\nv_add_u32 v42, 0xffff0000, v42\nqm v,v,a,148,64,224\n"
"qw 8\nds_read_b64_tr_b16 v[232:233], v43 offset:49152\nqm v,v,a,152,72,224\nds_read_b64_tr_b16 v[234:235], v43 offset:53248\nql 100,0,64,1,1024\nqm v,v,a,152,76,228\n"
"ds_read_b64_tr_b16 v[236:237], v43 offset:57344\nqm v,v,a,184,76,236\nds_read_b64_tr_b16 v[238:239], v43 offset:61440\nv_add_u32 v43, 0xffff0000, v43\nqm v,v,a,184,72,232\nqw 8\n"
"ds_read_b128 v[200:203], v47 offset:0\nqm v,v,a,188,80,232\ns_barrier\nds_read_b128 v[204:207], v47 offset:64\nds_read_b128 v[208:211], v47 offset:128\nds_read_b128 v[212:215], v47 offset:192\n"
"qt 0,36,0\nqm v,v,a,188,84,236\nqt 2,36,4096\nqm v,v,a,156,84,228\nqt 4,36,8192\nqm v,v,a,156,80,224\n"
"qw 11\nqt 6,36,12288\nql 104,0,68,1,0\nqm v,v,a,160,224,224\nqt 8,37,0\nqm v,v,a,160,228,228\n"
"qt 10,37,4096\nqm v,v,a,192,228,236\nqt 12,37,8192\nqm v,v,a,192,224,232\nqw 11\nqt 14,37,12288\n"
"qm v,v,a,196,232,232\nqt 16,38,0\nqm v,v,a,196,236,236\nqt 18,38,4096\nqm v,v,a,164,236,228\nqt 20,38,8192\n"
"qm v,v,a,164,232,224\nqt 22,38,12288\nql 108,0,68,1,1024\nG4C%=:\nqw 14\nv_pk_fma_f32 v[136:137], v[200:201], v[20:21], v[136:137] op_sel_hi:[1,0,1]\n"
"v_pk_fma_f32 v[138:139], v[202:203], v[20:21], v[138:139] op_sel_hi:[1,0,1]\nv_pk_fma_f32 v[140:141], v[204:205], v[20:21], v[140:141] op_sel_hi:[1,0,1]\nv_pk_fma_f32 v[142:143], v[206:207], v[20:21], v[142:143] op_sel_hi:[1,0,1]\nv_pk_fma_f32 v[28:29], v[136:137], v[20:21], v[22:23] op_sel:[0,1,0] op_sel_hi:[1,1,0]\nv_pk_fma_f32 v[30:31], v[138:139], v[20:21], v[22:23] op_sel:[0,1,0] op_sel_hi:[1,1,0]\nv_pk_fma_f32 v[32:33], v[140:141], v[20:21], v[22:23] op_sel:[0,1,0] op_sel_hi:[1,1,0]\n"
"v_pk_fma_f32 v[34:35], v[142:143], v[20:21], v[22:23] op_sel:[0,1,0] op_sel_hi:[1,1,0]\nv_readlane_b32 s2, v18, 3\ns_add_u32 m0, s0, 0xc000\ns_nop 3\nqd 12,60,2,0\nqx 28\n"
"v_pk_fma_f32 v[168:169], v[200:201], v[24:25], v[168:169] op_sel_hi:[1,0,1]\nqx 29\nv_pk_fma_f32 v[170:171], v[202:203], v[24:25], v[170:171] op_sel_hi:[1,0,1]\nqx 30\nv_pk_fma_f32 v[172:173], v[204:205], v[24:25], v[172:173] op_sel_hi:[1,0,1]\nqx 31\n"
"v_pk_fma_f32 v[174:175], v[206:207], v[24:25], v[174:175] op_sel_hi:[1,0,1]\nds_read_b128 v[200:203], v47 offset:256\nqx 32\nds_read_b128 v[204:207], v47 offset:320\nqx 33\nv_pk_fma_f32 v[216:217], v[168:169], v[24:25], v[26:27] op_sel:[0,1,0] op_sel_hi:[1,1,0]\n"
"qx 34\nv_pk_fma_f32 v[218:219], v[170:171], v[24:25], v[26:27] op_sel:[0,1,0] op_sel_hi:[1,1,0]\nqx 35\nv_pk_fma_f32 v[220:221], v[172:173], v[24:25], v[26:27] op_sel:[0,1,0] op_sel_hi:[1,1,0]\nv_pk_fma_f32 v[222:223], v[174:175], v[24:25], v[26:27] op_sel:[0,1,0] op_sel_hi:[1,1,0]\nqd 13,60,2,1024\n"
"qx 216\nqx 217\nv_pk_add_f32 v[28:29], v[28:29], v[22:23] op_sel:[0,1] op_sel_hi:[1,1]\nqx 218\nqx 219\nv_pk_add_f32 v[30:31], v[30:31], v[22:23] op_sel:[0,1] op_sel_hi:[1,1]\n"
"qx 220\nqx 221\nv_pk_add_f32 v[32:33], v[32:33], v[22:23] op_sel:[0,1] op_sel_hi:[1,1]\nqx 222\nqx 223\nv_pk_add_f32 v[34:35], v[34:35], v[22:23] op_sel:[0,1] op_sel_hi:[1,1]\n"
"qd 14,60,2,2048\nqp 28\nqp 29\nv_pk_add_f32 v[216:217], v[216:217], v[26:27] op_sel:[0,1] op_sel_hi:[1,1]\nqp 30\nqp 31\n"
"v_pk_add_f32 v[218:219], v[218:219], v[26:27] op_sel:[0,1] op_sel_hi:[1,1]\nqp 32\nqp 33\nv_pk_add_f32 v[220:221], v[220:221], v[26:27] op_sel:[0,1] op_sel_hi:[1,1]\nqp 34\nqp 35\n"
"v_pk_add_f32 v[222:223], v[222:223], v[26:27] op_sel:[0,1] op_sel_hi:[1,1]\nqd 15,60,2,3072\nv_readlane_b32 s1, v17, 3\nqp 216\nv_pk_mul_f32 v[136:137], v[136:137], v[28:29]\nv_pk_mul_f32 v[138:139], v[138:139], v[30:31]\n"
"qp 217\nv_pk_mul_f32 v[140:141], v[140:141], v[32:33]\nv_pk_mul_f32 v[142:143], v[142:143], v[34:35]\nqp 218\nqc 136,136\nqc 137,138\n"
"qp 219\nqc 138,140\nqc 139,142\nbuffer_store_dwordx4 v[136:139], v0, s[56:59], s6 offen offset:0 sc1\nqp 220\nqw 14\n"
"v_pk_fma_f32 v[144:145], v[208:209], v[20:21], v[144:145] op_sel_hi:[1,0,1]\nqp 221\nv_pk_fma_f32 v[146:147], v[210:211], v[20:21], v[146:147] op_sel_hi:[1,0,1]\nv_pk_fma_f32 v[148:149], v[212:213], v[20:21], v[148:149] op_sel_hi:[1,0,1]\nqp 222\nv_pk_fma_f32 v[150:151], v[214:215], v[20:21], v[150:151] op_sel_hi:[1,0,1]\n"
"v_pk_fma_f32 v[28:29], v[144:145], v[20:21], v[22:23] op_sel:[0,1,0] op_sel_hi:[1,1,0]\nqp 223\nv_pk_fma_f32 v[30:31], v[146:147], v[20:21], v[22:23] op_sel:[0,1,0] op_sel_hi:[1,1,0]\nv_pk_fma_f32 v[32:33], v[148:149], v[20:21], v[22:23] op_sel:[0,1,0] op_sel_hi:[1,1,0]\nv_pk_fma_f32 v[34:35], v[150:151], v[20:21], v[22:23] op_sel:[0,1,0] op_sel_hi:[1,1,0]\nql 112,0,64,1,0\n"
"qx 28\nv_pk_mul_f32 v[168:169], v[168:169], v[216:217]\nv_pk_mul_f32 v[170:171], v[170:171], v[218:219]\nqx 29\nv_pk_mul_f32 v[172:173], v[172:173], v[220:221]\nv_pk_mul_f32 v[174:175], v[174:175], v[222:223]\n"
"qx 30\nqc 168,168\nqc 169,170\nqc 170,172\nqx 31\nqc 171,174\n"
"buffer_store_dwordx4 v[168:171], v0, s[56:59], s7 offen offset:0 sc1\nqx 32\nv_pk_fma_f32 v[176:177], v[208:209], v[24:25], v[176:177] op_sel_hi:[1,0,1]\nv_pk_fma_f32 v[178:179], v[210:211], v[24:25], v[178:179] op_sel_hi:[1,0,1]\nqx 33\nv_pk_fma_f32 v[180:181], v[212:213], v[24:25], v[180:181] op_sel_hi:[1,0,1]\n"
"v_pk_fma_f32 v[182:183], v[214:215], v[24:25], v[182:183] op_sel_hi:[1,0,1]\nds_read_b128 v[208:211], v47 offset:384\nqx 34\nds_read_b128 v[212:215], v47 offset:448\nv_pk_fma_f32 v[216:217], v[176:177], v[24:25], v[26:27] op_sel:[0,1,0] op_sel_hi:[1,1,0]\nqx 35\n"
"v_pk_fma_f32 v[218:219], v[178:179], v[24:25], v[26:27] op_sel:[0,1,0] op_sel_hi:[1,1,0]\nv_pk_fma_f32 v[220:221], v[180:181], v[24:25], v[26:27] op_sel:[0,1,0] op_sel_hi:[1,1,0]\nv_pk_fma_f32 v[222:223], v[182:183], v[24:25], v[26:27] op_sel:[0,1,0] op_sel_hi:[1,1,0]\nql 116,0,64,1,1024\nqx 216\nqx 217\n"
"v_pk_add_f32 v[28:29], v[28:29], v[22:23] op_sel:[0,1] op_sel_hi:[1,1]\nqx 218\nqx 219\nv_pk_add_f32 v[30:31], v[30:31], v[22:23] op_sel:[0,1] op_sel_hi:[1,1]\nqx 220\nqx 221\n"
"v_pk_add_f32 v[32:33], v[32:33], v[22:23] op_sel:[0,1] op_sel_hi:[1,1]\nqx 222\nqx 223\nv_pk_add_f32 v[34:35], v[34:35], v[22:23] op_sel:[0,1] op_sel_hi:[1,1]\nql 120,0,68,1,0\nqp 28\n"
"qp 29\nv_pk_add_f32 v[216:217], v[216:217], v[26:27] op_sel:[0,1] op_sel_hi:[1,1]\nqp 30\nqp 31\nv_pk_add_f32 v[218:219], v[218:219], v[26:27] op_sel:[0,1] op_sel_hi:[1,1]\nqp 32\n"
"qp 33\nv_pk_add_f32 v[220:221], v[220:221], v[26:27] op_sel:[0,1] op_sel_hi:[1,1]\nqp 34\nqp 35\nv_pk_add_f32 v[222:223], v[222:223], v[26:27] op_sel:[0,1] op_sel_hi:[1,1]\nql 124,0,68,1,1024\n"
"v_readlane_b32 s2, v18, 4\ns_add_u32 m0, s0, 0x10000\nqp 216\nv_pk_mul_f32 v[144:145], v[144:145], v[28:29]\nv_pk_mul_f32 v[146:147], v[146:147], v[30:31]\nqp 217\n"
"v_pk_mul_f32 v[148:149], v[148:149], v[32:33]\nv_pk_mul_f32 v[150:151], v[150:151], v[34:35]\nqp 218\nqc 144,144\nqc 145,146\nqp 219\n"
"qc 146,148\nqc 147,150\nbuffer_store_dwordx4 v[144:147], v0, s[56:59], s6 offen offset:1024 sc1\nqp 220\nqw 2\nv_pk_fma_f32 v[152:153], v[200:201], v[20:21], v[152:153] op_sel_hi:[1,0,1]\n"
"qp 221\nv_pk_fma_f32 v[154:155], v[202:203], v[20:21], v[154:155] op_sel_hi:[1,0,1]\nv_pk_fma_f32 v[156:157], v[204:205], v[20:21], v[156:157] op_sel_hi:[1,0,1]\nqp 222\nv_pk_fma_f32 v[158:159], v[206:207], v[20:21], v[158:159] op_sel_hi:[1,0,1]\nv_pk_fma_f32 v[28:29], v[152:153], v[20:21], v[22:23] op_sel:[0,1,0] op_sel_hi:[1,1,0]\n"
"qp 223\nv_pk_fma_f32 v[30:31], v[154:155], v[20:21], v[22:23] op_sel:[0,1,0] op_sel_hi:[1,1,0]\nv_pk_fma_f32 v[32:33], v[156:157], v[20:21], v[22:23] op_sel:[0,1,0] op_sel_hi:[1,1,0]\nv_pk_fma_f32 v[34:35], v[158:159], v[20:21], v[22:23] op_sel:[0,1,0] op_sel_hi:[1,1,0]\nqx 28\nv_pk_mul_f32 v[176:177], v[176:177], v[216:217]\n"
"v_pk_mul_f32 v[178:179], v[178:179], v[218:219]\nqx 29\nv_pk_mul_f32 v[180:181], v[180:181], v[220:221]\nv_pk_mul_f32 v[182:183], v[182:183], v[222:223]\nqx 30\nqc 176,176\n"
"qc 177,178\nqx 31\nqc 178,180\nqc 179,182\nqx 32\nbuffer_store_dwordx4 v[176:179], v0, s[56:59], s7 offen offset:1024 sc1\n"
"v_pk_fma_f32 v[184:185], v[200:201], v[24:25], v[184:185] op_sel_hi:[1,0,1]\nqx 33\nv_pk_fma_f32 v[186:187], v[202:203], v[24:25], v[186:187] op_sel_hi:[1,0,1]\nv_pk_fma_f32 v[188:189], v[204:205], v[24:25], v[188:189] op_sel_hi:[1,0,1]\nqx 34\nv_pk_fma_f32 v[190:191], v[206:207], v[24:25], v[190:191] op_sel_hi:[1,0,1]\n"
"v_pk_fma_f32 v[216:217], v[184:185], v[24:25], v[26:27] op_sel:[0,1,0] op_sel_hi:[1,1,0]\nqx 35\nv_pk_fma_f32 v[218:219], v[186:187], v[24:25], v[26:27] op_sel:[0,1,0] op_sel_hi:[1,1,0]\nv_pk_fma_f32 v[220:221], v[188:189], v[24:25], v[26:27] op_sel:[0,1,0] op_sel_hi:[1,1,0]\nv_pk_fma_f32 v[222:223], v[190:191], v[24:25], v[26:27] op_sel:[0,1,0] op_sel_hi:[1,1,0]\nqd 12,60,2,0\n"
"qx 216\nqx 217\nv_pk_add_f32 v[28:29], v[28:29], v[22:23] op_sel:[0,1] op_sel_hi:[1,1]\nqx 218\nqx 219\nv_pk_add_f32 v[30:31], v[30:31], v[22:23] op_sel:[0,1] op_sel_hi:[1,1]\n"
"qx 220\nqx 221\nv_pk_add_f32 v[32:33], v[32:33], v[22:23] op_sel:[0,1] op_sel_hi:[1,1]\nqx 222\nqx 223\nv_pk_add_f32 v[34:35], v[34:35], v[22:23] op_sel:[0,1] op_sel_hi:[1,1]\n"
"qd 13,60,2,1024\nqp 28\nqp 29\nv_pk_add_f32 v[216:217], v[216:217], v[26:27] op_sel:[0,1] op_sel_hi:[1,1]\nqp 30\nqp 31\n"
"v_pk_add_f32 v[218:219], v[218:219], v[26:27] op_sel:[0,1] op_sel_hi:[1,1]\nqp 32\nqp 33\nv_pk_add_f32 v[220:221], v[220:221], v[26:27] op_sel:[0,1] op_sel_hi:[1,1]\nqp 34\nqp 35\n"
"v_pk_add_f32 v[222:223], v[222:223], v[26:27] op_sel:[0,1] op_sel_hi:[1,1]\nqd 14,60,2,2048\nqp 216\nv_pk_mul_f32 v[152:153], v[152:153], v[28:29]\nv_pk_mul_f32 v[154:155], v[154:155], v[30:31]\nqp 217\n"
"v_pk_mul_f32 v[156:157], v[156:157], v[32:33]\nv_pk_mul_f32 v[158:159], v[158:159], v[34:35]\nqp 218\nqc 152,152\nqc 153,154\nqp 219\n"
"qc 154,156\nqc 155,158\nbuffer_store_dwordx4 v[152:155], v0, s[56:59], s6 offen offset:2048 sc1\nqp 220\nqw 0\nv_pk_fma_f32 v[160:161], v[208:209], v[20:21], v[160:161] op_sel_hi:[1,0,1]\n"
"qp 221\nv_pk_fma_f32 v[162:163], v[210:211], v[20:21], v[162:163] op_sel_hi:[1,0,1]\nv_pk_fma_f32 v[164:165], v[212:213], v[20:21], v[164:165] op_sel_hi:[1,0,1]\nqp 222\nv_pk_fma_f32 v[166:167], v[214:215], v[20:21], v[166:167] op_sel_hi:[1,0,1]\nv_pk_fma_f32 v[28:29], v[160:161], v[20:21], v[22:23] op_sel:[0,1,0] op_sel_hi:[1,1,0]\n"
"qp 223\nv_pk_fma_f32 v[30:31], v[162:163], v[20:21], v[22:23] op_sel:[0,1,0] op_sel_hi:[1,1,0]\nv_pk_fma_f32 v[32:33], v[164:165], v[20:21], v[22:23] op_sel:[0,1,0] op_sel_hi:[1,1,0]\nv_pk_fma_f32 v[34:35], v[166:167], v[20:21], v[22:23] op_sel:[0,1,0] op_sel_hi:[1,1,0]\nqd 15,60,2,3072\nv_readlane_b32 s1, v17, 4\n"
"qx 28\nv_pk_mul_f32 v[184:185], v[184:185], v[216:217]\nv_pk_mul_f32 v[186:187], v[186:187], v[218:219]\nqx 29\nv_pk_mul_f32 v[188:189], v[188:189], v[220:221]\nv_pk_mul_f32 v[190:191], v[190:191], v[222:223]\n"
"qx 30\nqc 184,184\nqc 185,186\nqx 31\nqc 186,188\nqc 187,190\n"
"qx 32\nbuffer_store_dwordx4 v[184:187], v0, s[56:59], s7 offen offset:2048 sc1\nv_pk_fma_f32 v[192:193], v[208:209], v[24:25], v[192:193] op_sel_hi:[1,0,1]\nqx 33\nv_pk_fma_f32 v[194:195], v[210:211], v[24:25], v[194:195] op_sel_hi:[1,0,1]\nv_pk_fma_f32 v[196:197], v[212:213], v[24:25], v[196:197] op_sel_hi:[1,0,1]\n"
"qx 34\nv_pk_fma_f32 v[198:199], v[214:215], v[24:25], v[198:199] op_sel_hi:[1,0,1]\nv_pk_fma_f32 v[216:217], v[192:193], v[24:25], v[26:27] op_sel:[0,1,0] op_sel_hi:[1,1,0]\nqx 35\nv_pk_fma_f32 v[218:219], v[194:195], v[24:25], v[26:27] op_sel:[0,1,0] op_sel_hi:[1,1,0]\nv_pk_fma_f32 v[220:221], v[196:197], v[24:25], v[26:27] op_sel:[0,1,0] op_sel_hi:[1,1,0]\n"
"v_pk_fma_f32 v[222:223], v[198:199], v[24:25], v[26:27] op_sel:[0,1,0] op_sel_hi:[1,1,0]\nql 128,0,64,1,0\nqx 216\nqx 217\nv_pk_add_f32 v[28:29], v[28:29], v[22:23] op_sel:[0,1] op_sel_hi:[1,1]\nqx 218\n"
"qx 219\nv_pk_add_f32 v[30:31], v[30:31], v[22:23] op_sel:[0,1] op_sel_hi:[1,1]\nqx 220\nqx 221\nv_pk_add_f32 v[32:33], v[32:33], v[22:23] op_sel:[0,1] op_sel_hi:[1,1]\nqx 222\n"
"qx 223\nv_pk_add_f32 v[34:35], v[34:35], v[22:23] op_sel:[0,1] op_sel_hi:[1,1]\nql 132,0,64,1,1024\nqp 28\nqp 29\nv_pk_add_f32 v[216:217], v[216:217], v[26:27] op_sel:[0,1] op_sel_hi:[1,1]\n"
"qp 30\nqp 31\nv_pk_add_f32 v[218:219], v[218:219], v[26:27] op_sel:[0,1] op_sel_hi:[1,1]\nqp 32\nqp 33\nv_pk_add_f32 v[220:221], v[220:221], v[26:27] op_sel:[0,1] op_sel_hi:[1,1]\n"
"qp 34\nqp 35\nv_pk_add_f32 v[222:223], v[222:223], v[26:27] op_sel:[0,1] op_sel_hi:[1,1]\nql 136,0,68,1,0\nqp 216\nv_pk_mul_f32 v[160:161], v[160:161], v[28:29]\n"
"qp 217\nv_pk_mul_f32 v[162:163], v[162:163], v[30:31]\nqp 218\nv_pk_mul_f32 v[164:165], v[164:165], v[32:33]\nqp 219\nv_pk_mul_f32 v[166:167], v[166:167], v[34:35]\n"
"qp 220\nqc 160,160\nqp 221\nqc 161,162\nqp 222\nqc 162,164\n"
"qp 223\nqc 163,166\nbuffer_store_dwordx4 v[160:163], v0, s[56:59], s6 offen offset:3072 sc1\nql 140,0,68,1,1024\nv_pk_mul_f32 v[192:193], v[192:193], v[216:217]\nv_pk_mul_f32 v[194:195], v[194:195], v[218:219]\n"
"v_pk_mul_f32 v[196:197], v[196:197], v[220:221]\nv_pk_mul_f32 v[198:199], v[198:199], v[222:223]\nqc 192,192\nqc 193,194\nqc 194,196\nqc 195,198\n"
"buffer_store_dwordx4 v[192:195], v0, s[56:59], s7 offen offset:3072 sc1\nqv 32\nqm v,a,a,88,0,64\nqt 24,39,0\nqt 26,39,4096\nqm v,a,a,88,4,68\n"
"qt 28,39,8192\nqt 30,39,12288\nqm v,a,a,104,4,76\nqt 32,40,0\nqt 34,40,4096\nqm v,a,a,104,0,72\n"
"qt 36,40,8192\nqt 38,40,12288\nqm v,a,a,108,8,72\nqt 40,41,0\nqt 42,41,4096\nqm v,a,a,108,12,76\n"
"qt 44,41,8192\nv_readlane_b32 s2, v18, 5\nqm v,a,a,92,12,68\nqt 46,41,12288\nqm v,a,a,92,8,64\ns_add_u32 m0, s0, 0x14000\n"
"qt 48,42,0\nqm v,a,a,96,16,64\nqt 50,42,4096\nqm v,a,a,96,20,68\nqt 52,42,8192\nqm v,a,a,112,20,76\n"
"qt 54,42,12288\nqm v,a,a,112,16,72\nqw 12\nqt 56,43,0\nqm v,a,a,116,24,72\nqt 58,43,4096\n"
"qm v,a,a,116,28,76\nqt 60,43,8192\nqt 62,43,12288\ns_barrier\nqm v,a,a,100,28,68\nqt 0,36,16384\n"
"qm v,a,a,100,24,64\nqw 13\nqt 2,36,20480\nqz v,a,a,56,32,64\nqt 4,36,24576\nqm v,a,a,56,36,68\n"
"qt 6,36,28672\nqz v,a,a,72,36,76\nqt 8,37,16384\nqm v,a,a,72,32,72\nqw 13\nqt 10,37,20480\n"
"qz v,a,a,76,40,72\nqt 12,37,24576\nqm v,a,a,76,44,76\nqt 14,37,28672\nqz v,a,a,60,44,68\nqt 16,38,16384\n"
"qm v,a,a,60,40,64\nqw 13\nqt 18,38,20480\nqz v,a,a,64,48,64\nqt 20,38,24576\nqm v,a,a,64,52,68\n"
"qt 22,38,28672\nqz v,a,a,80,52,76\nqt 24,39,16384\nqm v,a,a,80,48,72\nqw 13\nqd 12,60,2,0\n"
"qz v,a,a,84,56,72\nqt 26,39,20480\nqm v,a,a,84,60,76\nqt 28,39,24576\nqz v,a,a,68,60,68\nqt 30,39,28672\n"
"qm v,a,a,68,56,64\nqw 12\nqv 29\nqt 32,40,16384\nqd 13,60,2,1024\nqm v,a,a,88,0,80\n"
"qt 34,40,20480\nqm v,a,a,88,4,84\nqt 36,40,24576\nqm v,a,a,104,4,92\nqt 38,40,28672\nqm v,a,a,104,0,88\n"
"qw 12\nqt 40,41,16384\nqm v,a,a,108,8,88\nqt 42,41,20480\nqm v,a,a,108,12,92\nqt 44,41,24576\n"
"qm v,a,a,92,12,84\nqt 46,41,28672\nqm v,a,a,92,8,80\nqw 12\nqt 48,42,16384\nqd 14,60,2,2048\n"
"qm v,a,a,96,16,80\nqt 50,42,20480\nqm v,a,a,96,20,84\nqt 52,42,24576\nqm v,a,a,112,20,92\nqt 54,42,28672\n"
"qm v,a,a,112,16,88\nqw 12\nqt 56,43,16384\nqd 15,60,2,3072\nqm v,a,a,116,24,88\nqt 58,43,20480\n"
"v_readlane_b32 s1, v17, 5\nqm v,a,a,116,28,92\nqt 60,43,24576\nqt 62,43,28672\nqv 24\ns_barrier\n"
"qm v,a,a,100,28,84\nqt 0,36,32768\nqm v,a,a,100,24,80\nqw 13\nqt 2,36,36864\nqm v,a,a,56,32,80\n"
"qt 4,36,40960\nqm v,a,a,56,36,84\nqt 6,36,45056\nqm v,a,a,72,36,92\nqt 8,37,32768\nqm v,a,a,72,32,88\n"
"qw 13\nqt 10,37,36864\nqm v,a,a,76,40,88\nqt 12,37,40960\nqm v,a,a,76,44,92\nqt 14,37,45056\n"
"qm v,a,a,60,44,84\nqt 16,38,32768\nqm v,a,a,60,40,80\nqw 13\nqt 18,38,36864\nqm v,a,a,64,48,80\n"
"qt 20,38,40960\nqm v,a,a,64,52,84\nqt 22,38,45056\nqm v,a,a,80,52,92\nqt 24,39,32768\nqm v,a,a,80,48,88\n"
"qw 13\nqt 26,39,36864\nqm v,a,a,84,56,88\nqt 28,39,40960\nqm v,a,a,84,60,92\nqt 30,39,45056\n"
"qm v,a,a,68,60,84\nqt 32,40,32768\nqm v,a,a,68,56,80\nqw 13\nqt 34,40,36864\nqm v,a,a,88,0,96\n"
"qv 4\nqt 36,40,40960\nqm v,a,a,88,4,100\nqt 38,40,45056\nqm v,a,a,104,4,108\nqt 40,41,32768\n"
"qm v,a,a,104,0,104\nqw 13\nqt 42,41,36864\nqm v,a,a,108,8,104\ns_mov_b64 exec, 1\nbuffer_store_dwordx2 v[250:251], off, s[56:59], s8 sc1\n"
"s_mov_b64 exec, -1\nql 144,0,64,1,0\nqm v,a,a,108,12,108\nqt 44,41,40960\nqm v,a,a,92,12,100\nqt 46,41,45056\n"
"qm v,a,a,92,8,96\nqw 12\nqt 48,42,32768\nql 148,0,64,1,1024\nv_readlane_b32 s2, v18, 6\nqm v,a,a,96,16,96\n"
"qt 50,42,36864\nqm v,a,a,96,20,100\ns_add_u32 m0, s0, 0x18000\nqt 52,42,40960\nqm v,a,a,112,20,108\nqt 54,42,45056\n"
"qm v,a,a,112,16,104\nqw 12\nqt 56,43,32768\nqd 12,60,2,0\nqm v,a,a,116,24,104\nqt 58,43,36864\n"
"qm v,a,a,116,28,108\nqt 60,43,40960\nqm v,a,a,100,28,100\nqt 62,43,45056\nqm v,a,a,100,24,96\nqw 12\n"
"qt 0,36,49152\nqd 13,60,2,1024\nqm v,a,a,56,32,96\nqt 2,36,53248\nqm v,a,a,56,36,100\nqt 4,36,57344\n"
"qm v,a,a,72,36,108\nqt 6,36,61440\nql 152,0,68,1,0\nqm v,a,a,72,32,104\nv_add_u32 v36, 0x10000, v36\nqt 8,37,49152\n"
"qw 13\nqm v,a,a,76,40,104\nqt 10,37,53248\nqm v,a,a,76,44,108\nqt 12,37,57344\nql 156,0,68,1,1024\n"
"qm v,a,a,60,44,100\nqt 14,37,61440\nv_add_u32 v37, 0x10000, v37\nqm v,a,a,60,40,96\nqw 12\nqt 16,38,49152\n"
"qm v,a,a,64,48,96\nqt 18,38,53248\nqd 14,60,2,2048\nqm v,a,a,64,52,100\nqt 20,38,57344\nqm v,a,a,80,52,108\n"
"qt 22,38,61440\nv_add_u32 v38, 0x10000, v38\nqm v,a,a,80,48,104\nqw 12\nqt 24,39,49152\nqd 15,60,2,3072\n"
"v_readlane_b32 s2, v18, 7\nqm v,a,a,84,56,104\nqt 26,39,53248\nqm v,a,a,84,60,108\ns_add_u32 m0, s0, 0x1c000\nqt 28,39,57344\n"
"qm v,a,a,68,60,100\nqt 30,39,61440\nv_add_u32 v39, 0x10000, v39\nqm v,a,a,68,56,96\nqw 12\nqt 32,40,49152\n"
"qd 12,60,2,0\nqm v,a,a,88,0,112\nqt 34,40,53248\nqm v,a,a,88,4,116\nqt 36,40,57344\nqm v,a,a,104,4,124\n"
"qt 38,40,61440\nqd 13,60,2,1024\nqm v,a,a,104,0,120\nv_add_u32 v40, 0x10000, v40\nqt 40,41,49152\nqw 13\n"
"qm v,a,a,108,8,120\nqt 42,41,53248\nv_readlane_b32 s1, v17, 6\nqm v,a,a,108,12,124\nqt 44,41,57344\nqm v,a,a,92,12,116\n"
"qt 46,41,61440\ns_nop 0\nql 160,0,64,1,0\nqm v,a,a,92,8,112\nv_add_u32 v41, 0x10000, v41\nqt 48,42,49152\n"
"qw 13\nqm v,a,a,96,16,112\nqt 50,42,53248\nqm v,a,a,96,20,116\nqt 52,42,57344\nql 164,0,64,1,1024\n"
"qm v,a,a,112,20,124\nqt 54,42,61440\nv_add_u32 v42, 0x10000, v42\nqm v,a,a,112,16,120\nqw 12\nqt 56,43,49152\n"
"qm v,a,a,116,24,120\nqt 58,43,53248\nqm v,a,a,116,28,124\nqt 60,43,57344\nqt 62,43,61440\nv_add_u32 v43, 0x10000, v43\n"
"qv 13\ns_barrier\nqd 14,60,2,2048\nqm v,a,a,100,28,116\nqt 0,36,0\nqm v,a,a,100,24,112\n"
"qw 13\nqt 2,36,4096\nqm v,a,a,56,32,112\nqd 15,60,2,3072\nqm v,a,a,56,36,116\nqt 4,36,8192\n"
"qm v,a,a,72,36,124\nqt 6,36,12288\nqm v,a,a,72,32,120\nqw 12\nqt 8,37,0\nql 168,0,68,1,0\n"
"qm v,a,a,76,40,120\nqt 10,37,4096\nqm v,a,a,76,44,124\nqt 12,37,8192\nqm v,a,a,60,44,116\nqt 14,37,12288\n"
"qm v,a,a,60,40,112\nqw 12\nqt 16,38,0\nql 172,0,68,1,1024\nqm v,a,a,64,48,112\nqt 18,38,4096\n"
"v_readlane_b32 s1, v17, 7\nqm v,a,a,64,52,116\nqt 20,38,8192\nqm v,a,a,80,52,124\nqt 22,38,12288\nqm v,a,a,80,48,120\n"
"qw 12\nqt 24,39,0\nqm v,a,a,84,56,120\nqt 26,39,4096\nqm v,a,a,84,60,124\nqt 28,39,8192\n"
"qm v,a,a,68,60,116\nqt 30,39,12288\nqm v,a,a,68,56,112\nqw 12\nqt 32,40,0\nqm v,a,a,88,0,128\n"
"qt 34,40,4096\nqm v,a,a,88,4,132\nqt 36,40,8192\nql 176,0,64,1,0\nqm v,a,a,104,4,140\nqt 38,40,12288\n"
"qm v,a,a,104,0,136\nqw 12\nqt 40,41,0\nqm v,a,a,108,8,136\nqt 42,41,4096\nqm v,a,a,108,12,140\n"
"qt 44,41,8192\nql 180,0,64,1,1024\nqm v,a,a,92,12,132\nqt 46,41,12288\nv_readlane_b32 s2, v18, 8\nqm v,a,a,92,8,128\n"
"qw 12\nqt 48,42,0\nqm v,a,a,96,16,128\ns_add_u32 m0, s0, 0x0\nqt 50,42,4096\nqm v,a,a,96,20,132\n"
"qt 52,42,8192\nqm v,a,a,112,20,140\nqt 54,42,12288\nqm v,a,a,112,16,136\nqw 12\nqt 56,43,0\n"
"qd 12,60,2,0\nqm v,a,a,116,24,136\nqt 58,43,4096\nqm v,a,a,116,28,140\nqt 60,43,8192\nqm v,a,a,100,28,132\n"
"qt 62,43,12288\nqm v,a,a,100,24,128\nqw 12\nqt 0,36,16384\nqd 13,60,2,1024\nqm v,a,a,56,32,128\n"
"qt 2,36,20480\nqm v,a,a,56,36,132\nqt 4,36,24576\nqm v,a,a,72,36,140\nqt 6,36,28672\nql 184,0,68,1,0\n"
"qm v,a,a,72,32,136\nqw 12\nqt 8,37,16384\nqm v,a,a,76,40,136\nqt 10,37,20480\nqm v,a,a,76,44,140\n"
"qt 12,37,24576\nql 188,0,68,1,1024\nqm v,a,a,60,44,132\nqt 14,37,28672\nqm v,a,a,60,40,128\nqw 12\n"
"qt 16,38,16384\nqm v,a,a,64,48,128\nqt 18,38,20480\nqm v,a,a,64,52,132\nqt 20,38,24576\nqd 14,60,2,2048\n"
"qm v,a,a,80,52,140\nqt 22,38,28672\nqm v,a,a,80,48,136\nqw 12\nqt 24,39,16384\nqd 15,60,2,3072\n"
"v_readlane_b32 s2, v18, 9\nqm v,a,a,84,56,136\nqt 26,39,20480\nqm v,a,a,84,60,140\ns_add_u32 m0, s0, 0x4000\nqt 28,39,24576\n"
"qm v,a,a,68,60,132\nqt 30,39,28672\nqm v,a,a,68,56,128\nqw 12\nqv 18\nqt 32,40,16384\n"
"qd 12,60,2,0\nqm v,a,a,88,0,144\nqt 34,40,20480\nqm v,a,a,88,4,148\nqt 36,40,24576\nqm v,a,a,104,4,156\n"
"qt 38,40,28672\nqm v,a,a,104,0,152\nqw 12\nqt 40,41,16384\nqd 13,60,2,1024\nqm v,a,a,108,8,152\n"
"qt 42,41,20480\nv_readlane_b32 s1, v17, 8\nqm v,a,a,108,12,156\nqt 44,41,24576\nqm v,a,a,92,12,148\nqt 46,41,28672\n"
"s_nop 0\nql 192,0,64,1,0\nqm v,a,a,92,8,144\nqw 12\nqt 48,42,16384\nqm v,a,a,96,16,144\n"
"qt 50,42,20480\nqm v,a,a,96,20,148\nqt 52,42,24576\nql 196,0,64,1,1024\nqm v,a,a,112,20,156\nqt 54,42,28672\n"
"qm v,a,a,112,16,152\nqw 12\nqt 56,43,16384\nqm v,a,a,116,24,152\nqt 58,43,20480\nqm v,a,a,116,28,156\n"
"qt 60,43,24576\nqt 62,43,28672\nqv 14\ns_barrier\nqd 14,60,2,2048\nqm v,a,a,100,28,148\n"
"qt 0,36,32768\nqm v,a,a,100,24,144\nqw 13\nqt 2,36,36864\nqm v,a,a,56,32,144\nqd 15,60,2,3072\n"
"qm v,a,a,56,36,148\nqt 4,36,40960\nqm v,a,a,72,36,156\nqt 6,36,45056\nqm v,a,a,72,32,152\nqw 12\n"
"qt 8,37,32768\nql 200,0,68,1,0\nqm v,a,a,76,40,152\nqt 10,37,36864\nqm v,a,a,76,44,156\nqt 12,37,40960\n"
"qm v,a,a,60,44,148\nqt 14,37,45056\nqm v,a,a,60,40,144\nqw 12\nqt 16,38,32768\nql 204,0,68,1,1024\n"
"qm v,a,a,64,48,144\nqt 18,38,36864\nv_readlane_b32 s1, v17, 9\nqm v,a,a,64,52,148\nqt 20,38,40960\nqm v,a,a,80,52,156\n"
"qt 22,38,45056\nqm v,a,a,80,48,152\nqw 12\nqt 24,39,32768\nqm v,a,a,84,56,152\nqt 26,39,36864\n"
"qm v,a,a,84,60,156\nqt 28,39,40960\nqm v,a,a,68,60,148\nqt 30,39,45056\nqm v,a,a,68,56,144\nqw 12\n"
"qv 16\nqt 32,40,32768\nqm v,a,a,88,0,160\nqt 34,40,36864\nqm v,a,a,88,4,164\nqt 36,40,40960\n"
"ql 208,0,64,1,0\nqm v,a,a,104,4,172\nqt 38,40,45056\nqm v,a,a,104,0,168\nqw 12\nqt 40,41,32768\n"
"qm v,a,a,108,8,168\nqt 42,41,36864\nqm v,a,a,108,12,172\nqt 44,41,40960\nql 212,0,64,1,1024\nqm v,a,a,92,12,164\n"
"qt 46,41,45056\nv_readlane_b32 s2, v18, 10\nqm v,a,a,92,8,160\nqw 12\nqt 48,42,32768\nqm v,a,a,96,16,160\n"
"s_add_u32 m0, s0, 0x8000\nqt 50,42,36864\nqm v,a,a,96,20,164\nqt 52,42,40960\nqm v,a,a,112,20,172\nqt 54,42,45056\n"
"qm v,a,a,112,16,168\nqw 12\nqt 56,43,32768\nqd 12,60,2,0\nqm v,a,a,116,24,168\nqt 58,43,36864\n"
"qm v,a,a,116,28,172\nqt 60,43,40960\nqm v,a,a,100,28,164\nqt 62,43,45056\nqm v,a,a,100,24,160\nqw 12\n"
"qt 0,36,49152\nqd 13,60,2,1024\nqm v,a,a,56,32,160\nqt 2,36,53248\nqm v,a,a,56,36,164\nqt 4,36,57344\n"
"qm v,a,a,72,36,172\nqt 6,36,61440\nql 216,0,68,1,0\nqm v,a,a,72,32,168\nv_add_u32 v36, 0xffff0000, v36\nqt 8,37,49152\n"
"qw 13\nqm v,a,a,76,40,168\nqt 10,37,53248\nqm v,a,a,76,44,172\nqt 12,37,57344\nql 220,0,68,1,1024\n"
"qm v,a,a,60,44,164\nqt 14,37,61440\nv_add_u32 v37, 0xffff0000, v37\nqm v,a,a,60,40,160\nqw 12\nqt 16,38,49152\n"
"qm v,a,a,64,48,160\nqt 18,38,53248\nqd 14,60,2,2048\nqm v,a,a,64,52,164\nqt 20,38,57344\nqm v,a,a,80,52,172\n"
"qt 22,38,61440\nqd 15,60,2,3072\nv_readlane_b32 s2, v18, 11\nqm v,a,a,80,48,168\nv_add_u32 v38, 0xffff0000, v38\nqt 24,39,49152\n"
"qw 13\nqm v,a,a,84,56,168\ns_add_u32 m0, s0, 0xc000\nqt 26,39,53248\nqm v,a,a,84,60,172\nqt 28,39,57344\n"
"qm v,a,a,68,60,164\nqt 30,39,61440\nqd 12,60,2,0\nqm v,a,a,68,56,160\nv_add_u32 v39, 0xffff0000, v39\nqt 32,40,49152\n"
"qw 13\nqv 19\nqm v,a,a,88,0,176\nqt 34,40,53248\nqm v,a,a,88,4,180\nqt 36,40,57344\n"
"qd 13,60,2,1024\nqm v,a,a,104,4,188\nqt 38,40,61440\nv_readlane_b32 s1, v17, 10\nqm v,a,a,104,0,184\nv_add_u32 v40, 0xffff0000, v40\n"
"qt 40,41,49152\nqw 13\nqm v,a,a,108,8,184\nqt 42,41,53248\nql 224,0,64,1,0\nqm v,a,a,108,12,188\n"
"qt 44,41,57344\nqm v,a,a,92,12,180\nqt 46,41,61440\nv_add_u32 v41, 0xffff0000, v41\nqm v,a,a,92,8,176\nqw 12\n"
"qt 48,42,49152\nql 228,0,64,1,1024\nqm v,a,a,96,16,176\nqt 50,42,53248\nqm v,a,a,96,20,180\nqt 52,42,57344\n"
"qm v,a,a,112,20,188\nqt 54,42,61440\nqd 14,60,2,2048\nqm v,a,a,112,16,184\nv_add_u32 v42, 0xffff0000, v42\nqt 56,43,49152\n"
"qw 13\nqm v,a,a,116,24,184\nqt 58,43,53248\nqm v,a,a,116,28,188\nqt 60,43,57344\nqt 62,43,61440\n"
"v_add_u32 v43, 0xffff0000, v43\nqv 15\ns_barrier\nqd 15,60,2,3072\nqm v,a,a,100,28,180\nqt 0,36,0\n"
"qm v,a,a,100,24,176\nqw 13\nqt 2,36,4096\nqm v,a,a,56,32,176\nql 232,0,68,1,0\nqm v,a,a,56,36,180\n"
"s_add_u32 s5, s5, 128\nqt 4,36,8192\nqm v,a,a,72,36,188\nqt 6,36,12288\nqm v,a,a,72,32,184\nqw 12\n"
"qt 8,37,0\nqm v,a,a,76,40,184\nqt 10,37,4096\nqm v,a,a,76,44,188\nqt 12,37,8192\nqm v,a,a,60,44,180\n"
"qt 14,37,12288\nqm v,a,a,60,40,176\nqw 12\nqt 16,38,0\nbuffer_load_dwordx2 v[248:249], v19, s[56:59], s5 offen sc1\nql 236,0,68,1,1024\n"
"qm v,a,a,64,48,176\nqt 18,38,4096\nv_readlane_b32 s1, v17, 11\nqm v,a,a,64,52,180\nqt 20,38,8192\nqm v,a,a,80,52,188\n"
"qt 22,38,12288\nqm v,a,a,80,48,184\nqw 12\nqt 24,39,0\nqm v,a,a,84,56,184\nqt 26,39,4096\n"
"qm v,a,a,84,60,188\nqt 28,39,8192\nqm v,a,a,68,60,180\nqt 30,39,12288\nqm v,a,a,68,56,176\nqw 12\n"
"qv 17\nqt 32,40,0\nqm v,a,a,88,0,192\nqt 34,40,4096\nqm v,a,a,88,4,196\nqt 36,40,8192\n"
"ql 64,0,64,1,0\nqm v,a,a,104,4,204\nqt 38,40,12288\nqm v,a,a,104,0,200\nqw 12\nqt 40,41,0\n"
"qm v,a,a,108,8,200\nqt 42,41,4096\nqm v,a,a,108,12,204\nqt 44,41,8192\nql 68,0,64,1,1024\nqm v,a,a,92,12,196\n"
"qt 46,41,12288\nv_readlane_b32 s2, v18, 12\nqm v,a,a,92,8,192\nqw 12\nqt 48,42,0\nqm v,a,a,96,16,192\n"
"s_add_u32 m0, s0, 0x10000\nqt 50,42,4096\nqm v,a,a,96,20,196\nqt 52,42,8192\nqm v,a,a,112,20,204\nqt 54,42,12288\n"
"qm v,a,a,112,16,200\nqw 12\nqt 56,43,0\nqd 12,60,2,0\nqm v,a,a,116,24,200\nqt 58,43,4096\n"
"qm v,a,a,116,28,204\nqt 60,43,8192\nqm v,a,a,100,28,196\nqt 62,43,12288\nqm v,a,a,100,24,192\nqw 12\n"
"qt 0,36,16384\nqd 13,60,2,1024\nqm v,a,a,56,32,192\nqt 2,36,20480\nqm v,a,a,56,36,196\nqt 4,36,24576\n"
"qm v,a,a,72,36,204\nqt 6,36,28672\nql 72,0,68,1,0\nqm v,a,a,72,32,200\nqw 12\nqt 8,37,16384\n"
"qm v,a,a,76,40,200\nqt 10,37,20480\nqm v,a,a,76,44,204\nqt 12,37,24576\nql 76,0,68,1,1024\nqm v,a,a,60,44,196\n"
"qt 14,37,28672\nqm v,a,a,60,40,192\nqw 12\nqd 14,60,2,2048\nqt 16,38,16384\nqm v,a,a,64,48,192\n"
"qv 8\nqt 18,38,20480\nv_cmp_eq_u64 vcc, s[16:17], v[248:249]\nqm v,a,a,64,52,196\nqt 20,38,24576\nqm v,a,a,80,52,204\n"
"qt 22,38,28672\nqm v,a,a,80,48,200\nqw 12\nqt 24,39,16384\nqm v,a,a,84,56,200\ns_cmp_eq_u64 vcc, exec\n"
"s_cbranch_scc0 PLG4_o%=\nPLG4_r%=:\nqt 26,39,20480\nqm v,a,a,84,60,204\nqd 15,60,2,3072\nqt 28,39,24576\n"
"v_readlane_b32 s2, v18, 13\nqm v,a,a,68,60,196\nqt 30,39,28672\nqm v,a,a,68,56,192\ns_add_u32 m0, s0, 0x14000\nqw 12\n"
"qt 32,40,16384\nqm v,a,a,88,0,208\nqd 12,60,2,0\nqt 34,40,20480\nqm v,a,a,88,4,212\nqt 36,40,24576\n"
"qd 13,60,2,1024\nqm v,a,a,104,4,220\nqt 38,40,28672\nv_readlane_b32 s1, v17, 12\nqm v,a,a,104,0,216\nqw 12\n"
"qt 40,41,16384\nqm v,a,a,108,8,216\nqt 42,41,20480\nqm v,a,a,108,12,220\nqt 44,41,24576\nql 80,0,64,1,0\n"
"qm v,a,a,92,12,212\nqt 46,41,28672\nqm v,a,a,92,8,208\nqw 12\nqt 48,42,16384\nqm v,a,a,96,16,208\n"
"qt 50,42,20480\nql 84,0,64,1,1024\nqm v,a,a,96,20,212\nqt 52,42,24576\nqm v,a,a,112,20,220\nqt 54,42,28672\n"
"qm v,a,a,112,16,216\nqw 12\nqt 56,43,16384\nqd 14,60,2,2048\nqm v,a,a,116,24,216\nqt 58,43,20480\n"
"qm v,a,a,116,28,220\nqt 60,43,24576\nqt 62,43,28672\ns_barrier\nqm v,a,a,100,28,212\nqd 15,60,2,3072\n"
"qm v,a,a,100,24,208\nqw 12\nqt 0,36,32768\nqm v,a,a,56,32,208\nqt 2,36,36864\nqm v,a,a,56,36,212\n"
"qt 4,36,40960\nql 88,0,68,1,0\nqm v,a,a,72,36,220\nqt 6,36,45056\nqm v,a,a,72,32,216\nqw 12\n"
"qt 8,37,32768\nqm v,a,a,76,40,216\nqt 10,37,36864\nql 92,0,68,1,1024\nqm v,a,a,76,44,220\nqt 12,37,40960\n"
"v_readlane_b32 s1, v17, 13\nqm v,a,a,60,44,212\nqt 14,37,45056\nqm v,a,a,60,40,208\nqw 12\nqt 16,38,32768\n"
"qm v,a,a,64,48,208\nqt 18,38,36864\nqm v,a,a,64,52,212\nqt 20,38,40960\nql 96,0,64,1,0\nqm v,a,a,80,52,220\n"
"qt 22,38,45056\nqm v,a,a,80,48,216\nqw 12\nqt 24,39,32768\nqm v,a,a,84,56,216\nqt 26,39,36864\n"
"ql 100,0,64,1,1024\nv_readlane_b32 s2, v18, 14\nqm v,a,a,84,60,220\nqt 28,39,40960\nqm v,a,a,68,60,212\ns_add_u32 m0, s0, 0x18000\n"
"qt 30,39,45056\nqm v,a,a,68,56,208\nqw 12\nqv 18\nqt 32,40,32768\nqm v,a,a,88,0,224\n"
"qt 34,40,36864\nqd 12,60,2,0\nqm v,a,a,88,4,228\nqt 36,40,40960\nqm v,a,a,104,4,236\nqt 38,40,45056\n"
"qm v,a,a,104,0,232\nqw 12\nqt 40,41,32768\nqd 13,60,2,1024\nqm v,a,a,108,8,232\nqt 42,41,36864\n"
"qm v,a,a,108,12,236\nqt 44,41,40960\nqm v,a,a,92,12,228\nqt 46,41,45056\nql 104,0,68,1,0\nqm v,a,a,92,8,224\n"
"qw 12\nqt 48,42,32768\nqm v,a,a,96,16,224\nqt 50,42,36864\nqm v,a,a,96,20,228\nqt 52,42,40960\n"
"ql 108,0,68,1,1024\nqm v,a,a,112,20,236\nqt 54,42,45056\nqm v,a,a,112,16,232\nqw 12\nqt 56,43,32768\n"
"qm v,a,a,116,24,232\nqt 58,43,36864\nqd 14,60,2,2048\nqm v,a,a,116,28,236\nqt 60,43,40960\nqm v,a,a,100,28,228\n"
"qt 62,43,45056\nqd 15,60,2,3072\nv_readlane_b32 s2, v18, 15\nqm v,a,a,100,24,224\nqw 12\nqt 0,36,49152\n"
"qm v,a,a,56,32,224\ns_add_u32 m0, s0, 0x1c000\nqt 2,36,53248\nqm v,a,a,56,36,228\nqt 4,36,57344\nqm v,a,a,72,36,236\n"
"qt 6,36,61440\nqd 12,60,2,0\nqm v,a,a,72,32,232\nv_add_u32 v36, 0x10000, v36\nqt 8,37,49152\nqw 13\n"
"qm v,a,a,76,40,232\nqt 10,37,53248\nqm v,a,a,76,44,236\nqt 12,37,57344\nqm v,a,a,60,44,228\nqt 14,37,61440\n"
"qd 13,60,2,1024\nqm v,a,a,60,40,224\nv_add_u32 v37, 0x10000, v37\nqt 16,38,49152\nv_readlane_b32 s1, v17, 14\nqw 13\n"
"qm v,a,a,64,48,224\nqt 18,38,53248\nqm v,a,a,64,52,228\nqt 20,38,57344\nql 112,0,64,1,0\nqm v,a,a,80,52,236\n"
"qt 22,38,61440\nv_add_u32 v38, 0x10000, v38\nqm v,a,a,80,48,232\nqw 12\nqt 24,39,49152\nqm v,a,a,84,56,232\n"
"qt 26,39,53248\nql 116,0,64,1,1024\nqm v,a,a,84,60,236\nqt 28,39,57344\nqm v,a,a,68,60,228\nqt 30,39,61440\n"
"v_add_u32 v39, 0x10000, v39\nqm v,a,a,68,56,224\nqw 12\nqv 22\nqt 32,40,49152\nqd 14,60,2,2048\n"
"qm v,a,a,88,0,64\nqt 34,40,53248\nqm v,a,a,88,4,68\nqt 36,40,57344\nqm v,a,a,104,4,76\nqt 38,40,61440\n"
"qd 15,60,2,3072\nqm v,a,a,104,0,72\nv_add_u32 v40, 0x10000, v40\nqt 40,41,49152\nqw 13\nqm v,a,a,108,8,72\n"
"qt 42,41,53248\nqm v,a,a,108,12,76\nqt 44,41,57344\nql 120,0,68,1,0\nqm v,a,a,92,12,68\nqt 46,41,61440\n"
"v_add_u32 v41, 0x10000, v41\nqm v,a,a,92,8,64\nqw 12\nqt 48,42,49152\nqm v,a,a,96,16,64\nqt 50,42,53248\n"
"ql 124,0,68,1,1024\nqm v,a,a,96,20,68\nqt 52,42,57344\nv_readlane_b32 s1, v17, 15\nqm v,a,a,112,20,76\nqt 54,42,61440\n"
"v_add_u32 v42, 0x10000, v42\nqm v,a,a,112,16,72\nqw 12\nqt 56,43,49152\nql 128,0,64,1,0\nqm v,a,a,116,24,72\n"
"qt 58,43,53248\nqm v,a,a,116,28,76\nqt 60,43,57344\nqt 62,43,61440\nv_add_u32 v43, 0x10000, v43\nqv 19\n"
"s_barrier\nqm v,a,a,100,28,68\nql 132,0,64,1,1024\nv_readlane_b32 s2, v18, 16\nqm v,a,a,100,24,64\nqw 12\n"
"qt 0,36,0\nqm v,a,a,56,32,64\ns_add_u32 m0, s0, 0x0\nqt 2,36,4096\nqm v,a,a,56,36,68\nqt 4,36,8192\n"
"qm v,a,a,72,36,76\nqt 6,36,12288\nqd 12,60,2,0\nqm v,a,a,72,32,72\nqw 12\nqt 8,37,0\n"
"qm v,a,a,76,40,72\nqt 10,37,4096\nqm v,a,a,76,44,76\nqt 12,37,8192\nqd 13,60,2,1024\nqm v,a,a,60,44,68\n"
"qt 14,37,12288\nqm v,a,a,60,40,64\nqw 12\nqt 16,38,0\nqm v,a,a,64,48,64\nqt 18,38,4096\n"
"ql 136,0,68,1,0\nqm v,a,a,64,52,68\nqt 20,38,8192\nqm v,a,a,80,52,76\nqt 22,38,12288\nqm v,a,a,80,48,72\n"
"qw 12\nqt 24,39,0\nql 140,0,68,1,1024\nqm v,a,a,84,56,72\nqt 26,39,4096\nqm v,a,a,84,60,76\n"
"qt 28,39,8192\nqm v,a,a,68,60,68\nqt 30,39,12288\nqd 14,60,2,2048\nqm v,a,a,68,56,64\nqw 12\n"
"qv 23\nqt 32,40,0\nqm v,a,a,88,0,80\nqt 34,40,4096\nqd 15,60,2,3072\nv_readlane_b32 s2, v18, 17\n"
"qm v,a,a,88,4,84\nqt 36,40,8192\nqm v,a,a,104,4,92\ns_add_u32 m0, s0, 0x4000\nqt 38,40,12288\nqm v,a,a,104,0,88\n"
"qw 12\nqt 40,41,0\nqm v,a,a,108,8,88\nqt 42,41,4096\nqd 12,60,2,0\nqm v,a,a,108,12,92\n"
"qt 44,41,8192\nqm v,a,a,92,12,84\nqt 46,41,12288\nqm v,a,a,92,8,80\nqw 12\nqt 48,42,0\n"
"qm v,a,a,96,16,80\nqt 50,42,4096\nqd 13,60,2,1024\nqm v,a,a,96,20,84\nqt 52,42,8192\nv_readlane_b32 s1, v17, 16\n"
"qm v,a,a,112,20,92\nqt 54,42,12288\nqm v,a,a,112,16,88\nqw 12\nqt 56,43,0\nql 144,0,64,1,0\n"
"qm v,a,a,116,24,88\nqt 58,43,4096\nqm v,a,a,116,28,92\nqt 60,43,8192\nqm v,a,a,100,28,84\nqt 62,43,12288\n"
"ql 148,0,64,1,1024\nqm v,a,a,100,24,80\nqw 12\nqt 0,36,16384\nqm v,a,a,56,32,80\nqt 2,36,20480\n"
"qm v,a,a,56,36,84\nqt 4,36,24576\nqd 14,60,2,2048\nqm v,a,a,72,36,92\nqt 6,36,28672\nqm v,a,a,72,32,88\n"
"qw 12\nqt 8,37,16384\nqm v,a,a,76,40,88\nqt 10,37,20480\nqd 15,60,2,3072\nqm v,a,a,76,44,92\n"
"qt 12,37,24576\nqm v,a,a,60,44,84\nqt 14,37,28672\nqm v,a,a,60,40,80\nqw 12\nqt 16,38,16384\n"
"ql 152,0,68,1,0\nqm v,a,a,64,48,80\nqt 18,38,20480\nqm v,a,a,64,52,84\nqt 20,38,24576\nqm v,a,a,80,52,92\n"
"qt 22,38,28672\nql 156,0,68,1,1024\nqm v,a,a,80,48,88\nqw 12\nqt 24,39,16384\nv_readlane_b32 s1, v17, 17\n"
"qm v,a,a,84,56,88\nqt 26,39,20480\nqm v,a,a,84,60,92\nqt 28,39,24576\ns_nop 0\nql 160,0,64,1,0\n"
"qm v,a,a,68,60,84\nqt 30,39,28672\nqm v,a,a,68,56,80\nqw 12\nqv 27\nqt 32,40,16384\n"
"qm v,a,a,88,0,96\nqt 34,40,20480\nql 164,0,64,1,1024\nv_readlane_b32 s2, v18, 18\nqm v,a,a,88,4,100\nqt 36,40,24576\n"
"qm v,a,a,104,4,108\ns_add_u32 m0, s0, 0x8000\nqt 38,40,28672\nqm v,a,a,104,0,104\nqw 12\nqt 40,41,16384\n"
"qm v,a,a,108,8,104\nqt 42,41,20480\nqd 12,60,2,0\nqm v,a,a,108,12,108\nqt 44,41,24576\nqm v,a,a,92,12,100\n"
"qt 46,41,28672\nqm v,a,a,92,8,96\nqw 12\nqt 48,42,16384\nqd 13,60,2,1024\nqm v,a,a,96,16,96\n"
"qt 50,42,20480\nqm v,a,a,96,20,100\nqt 52,42,24576\nqm v,a,a,112,20,108\nqt 54,42,28672\nql 168,0,68,1,0\n"
"qm v,a,a,112,16,104\nqw 12\nqt 56,43,16384\nqm v,a,a,116,24,104\nqt 58,43,20480\nqm v,a,a,116,28,108\n"
"qt 60,43,24576\nqt 62,43,28672\nqv 23\ns_barrier\nql 172,0,68,1,1024\nqm v,a,a,100,28,100\n"
"qt 0,36,32768\nqm v,a,a,100,24,96\nqw 13\nqt 2,36,36864\nqm v,a,a,56,32,96\nqd 14,60,2,2048\n"
"qm v,a,a,56,36,100\nqt 4,36,40960\nqm v,a,a,72,36,108\nqt 6,36,45056\nqd 15,60,2,3072\nv_readlane_b32 s2, v18, 19\n"
"qm v,a,a,72,32,104\nqw 12\nqt 8,37,32768\nqm v,a,a,76,40,104\ns_add_u32 m0, s0, 0xc000\nqt 10,37,36864\n"
"qm v,a,a,76,44,108\nqt 12,37,40960\nqm v,a,a,60,44,100\nqt 14,37,45056\nqd 12,60,2,0\nqm v,a,a,60,40,96\n"
"qw 12\nqt 16,38,32768\nqm v,a,a,64,48,96\nqt 18,38,36864\nqm v,a,a,64,52,100\nqt 20,38,40960\n"
"qm v,a,a,80,52,108\nqt 22,38,45056\nqd 13,60,2,1024\nqm v,a,a,80,48,104\nqw 12\nqt 24,39,32768\n"
"v_readlane_b32 s1, v17, 18\nqm v,a,a,84,56,104\nqt 26,39,36864\nqm v,a,a,84,60,108\nqt 28,39,40960\ns_nop 0\n"
"ql 176,0,64,1,0\nqm v,a,a,68,60,100\nqt 30,39,45056\nqm v,a,a,68,56,96\nqw 12\nqv 27\n"
"qt 32,40,32768\nqm v,a,a,88,0,112\nqt 34,40,36864\nql 180,0,64,1,1024\nqm v,a,a,88,4,116\ns_mov_b32 s72, s52\n"
"s_and_b32 s73, s53, 0xffff\nqt 36,40,40960\nqm v,a,a,104,4,124\ns_mov_b32 s74, 0x400000\ns_mov_b32 s75, 0x20000\nqt 38,40,45056\n"
"qm v,a,a,104,0,120\nqw 12\nqt 40,41,32768\nqd 14,60,2,2048\nqm v,a,a,108,8,120\nqt 42,41,36864\n"
"qm v,a,a,108,12,124\nqt 44,41,40960\nqm v,a,a,92,12,116\nqt 46,41,45056\nqd 15,60,2,3072\nqm v,a,a,92,8,112\n"
"qw 12\nqt 48,42,32768\nqm v,a,a,96,16,112\nqt 50,42,36864\nqm v,a,a,96,20,116\nqt 52,42,40960\n"
"ql 184,0,68,1,0\nqm v,a,a,112,20,124\nqt 54,42,45056\nqm v,a,a,112,16,120\nqw 12\nqt 56,43,32768\n"
"qm v,a,a,116,24,120\nqt 58,43,36864\nql 188,0,68,1,1024\nqm v,a,a,116,28,124\nqt 60,43,40960\nv_readlane_b32 s1, v17, 19\n"
"qm v,a,a,100,28,116\nqt 62,43,45056\nqm v,a,a,100,24,112\nqw 12\nqt 0,36,49152\nql 192,0,64,1,0\n"
"qm v,a,a,56,32,112\nqt 2,36,53248\nqm v,a,a,56,36,116\nqt 4,36,57344\nqm v,a,a,72,36,124\nqt 6,36,61440\n"
"ql 196,0,64,1,1024\nv_readlane_b32 s2, v18, 20\nqm v,a,a,72,32,120\nv_add_u32 v36, 0xffff0000, v36\nqt 8,37,49152\nqw 13\n"
"qm v,a,a,76,40,120\ns_add_u32 m0, s0, 0x10000\nqt 10,37,53248\nqm v,a,a,76,44,124\nqt 12,37,57344\nqm v,a,a,60,44,116\n"
"qt 14,37,61440\nqd 12,60,2,0\nqm v,a,a,60,40,112\nv_add_u32 v37, 0xffff0000, v37\nqt 16,38,49152\nqw 13\n"
"qm v,a,a,64,48,112\nqt 18,38,53248\nqm v,a,a,64,52,116\nqt 20,38,57344\nqd 13,60,2,1024\nqm v,a,a,80,52,124\n"
"qt 22,38,61440\nv_add_u32 v38, 0xffff0000, v38\nqm v,a,a,80,48,120\nqw 12\nqt 24,39,49152\nqm v,a,a,84,56,120\n"
"qt 26,39,53248\nql 200,0,68,1,0\nqm v,a,a,84,60,124\nqt 28,39,57344\nqm v,a,a,68,60,116\nqt 30,39,61440\n"
"v_add_u32 v39, 0xffff0000, v39\nqm v,a,a,68,56,112\nqw 12\nqv 31\nqt 32,40,49152\nql 204,0,68,1,1024\n"
"qm v,a,a,88,0,128\nqt 34,40,53248\nqm v,a,a,88,4,132\nqt 36,40,57344\nqm v,a,a,104,4,140\nqt 38,40,61440\n"
"qd 14,60,2,2048\nqm v,a,a,104,0,136\nv_add_u32 v40, 0xffff0000, v40\nqt 40,41,49152\nqw 13\nqm v,a,a,108,8,136\n"
"qt 42,41,53248\nqm v,a,a,108,12,140\nqt 44,41,57344\nqd 15,60,2,3072\nqm v,a,a,92,12,132\nqt 46,41,61440\n"
"v_readlane_b32 s1, v17, 20\nqm v,a,a,92,8,128\nv_add_u32 v41, 0xffff0000, v41\nqt 48,42,49152\nqw 13\nqm v,a,a,96,16,128\n"
"qt 50,42,53248\nql 208,0,64,1,0\nqm v,a,a,96,20,132\nqt 52,42,57344\nqm v,a,a,112,20,140\nqt 54,42,61440\n"
"v_add_u32 v42, 0xffff0000, v42\nqm v,a,a,112,16,136\nqw 12\nqt 56,43,49152\nql 212,0,64,1,1024\nv_readlane_b32 s2, v18, 21\n"
"qm v,a,a,116,24,136\nqt 58,43,53248\nqm v,a,a,116,28,140\nqt 60,43,57344\nqt 62,43,61440\nv_add_u32 v43, 0xffff0000, v43\n"
"s_add_u32 m0, s0, 0x14000\nqv 28\ns_barrier\nqm v,a,a,100,28,132\nqt 0,36,0\nqm v,a,a,100,24,128\n"
"qw 13\nqd 12,60,2,0\nqm v,a,a,56,32,128\nqt 2,36,4096\nqm v,a,a,56,36,132\nqt 4,36,8192\n"
"qm v,a,a,72,36,140\nqt 6,36,12288\nqd 13,60,2,1024\nqm v,a,a,72,32,136\nqw 12\nqt 8,37,0\n"
"qm v,a,a,76,40,136\nqt 10,37,4096\nqm v,a,a,76,44,140\nqt 12,37,8192\nql 216,0,68,1,0\nqm v,a,a,60,44,132\n"
"qt 14,37,12288\nqm v,a,a,60,40,128\nqw 12\nqt 16,38,0\nqm v,a,a,64,48,128\nqt 18,38,4096\n"
"ql 220,0,68,1,1024\nqm v,a,a,64,52,132\nqt 20,38,8192\nqm v,a,a,80,52,140\nqt 22,38,12288\nqm v,a,a,80,48,136\n"
"qw 12\nqt 24,39,0\nqd 14,60,2,2048\nqm v,a,a,84,56,136\nqt 26,39,4096\nqm v,a,a,84,60,140\n"
"qt 28,39,8192\nqm v,a,a,68,60,132\nqt 30,39,12288\nqd 15,60,2,3072\nqm v,a,a,68,56,128\nqw 12\n"
"qv 32\nqt 32,40,0\nv_readlane_b32 s1, v17, 21\nqm v,a,a,88,0,144\nqt 34,40,4096\nqm v,a,a,88,4,148\n"
"qt 36,40,8192\ns_nop 0\nql 224,0,64,1,0\nqm v,a,a,104,4,156\nqt 38,40,12288\nqm v,a,a,104,0,152\n"
"qw 12\nqt 40,41,0\nqm v,a,a,108,8,152\nqt 42,41,4096\nql 228,0,64,1,1024\nv_readlane_b32 s2, v18, 22\n"
"qm v,a,a,108,12,156\nqt 44,41,8192\nqm v,a,a,92,12,148\ns_add_u32 m0, s0, 0x18000\nqt 46,41,12288\nqm v,a,a,92,8,144\n"
"qw 12\nqt 48,42,0\nqm v,a,a,96,16,144\nqt 50,42,4096\nqd 12,60,2,0\nqm v,a,a,96,20,148\n"
"qt 52,42,8192\nqm v,a,a,112,20,156\nqt 54,42,12288\nqm v,a,a,112,16,152\nqw 12\nqt 56,43,0\n"
"qd 13,60,2,1024\nqm v,a,a,116,24,152\nqt 58,43,4096\nqm v,a,a,116,28,156\nqt 60,43,8192\nqm v,a,a,100,28,148\n"
"qt 62,43,12288\nql 232,0,68,1,0\nqm v,a,a,100,24,144\nqw 12\nqt 0,36,16384\nqm v,a,a,56,32,144\n"
"qt 2,36,20480\nqm v,a,a,56,36,148\nqt 4,36,24576\nql 236,0,68,1,1024\nqm v,a,a,72,36,156\nqt 6,36,28672\n"
"qm v,a,a,72,32,152\nqw 12\nqt 8,37,16384\nqm v,a,a,76,40,152\nqt 10,37,20480\nqd 14,60,2,2048\n"
"qm v,a,a,76,44,156\nqt 12,37,24576\nqm v,a,a,60,44,148\nqt 14,37,28672\nqm v,a,a,60,40,144\nqw 12\n"
"qt 16,38,16384\nqd 15,60,2,3072\nqm v,a,a,64,48,144\nqt 18,38,20480\nv_readlane_b32 s1, v17, 22\nqm v,a,a,64,52,148\n"
"qt 20,38,24576\nqm v,a,a,80,52,156\nqt 22,38,28672\ns_nop 0\nql 64,0,64,1,0\nqm v,a,a,80,48,152\n"
"qw 12\nqt 24,39,16384\nqm v,a,a,84,56,152\nqt 26,39,20480\nqm v,a,a,84,60,156\nqt 28,39,24576\n"
"qm v,a,a,68,60,148\nqt 30,39,28672\nql 68,0,64,1,1024\nqm v,a,a,68,56,144\nqw 12\nqv 36\n"
"qt 32,40,16384\nqm v,a,a,88,0,160\nqt 34,40,20480\nqm v,a,a,88,4,164\nqt 36,40,24576\nqm v,a,a,104,4,172\n"
"qt 38,40,28672\nql 72,0,68,1,0\nqm v,a,a,104,0,168\nqw 12\nqt 40,41,16384\nqm v,a,a,108,8,168\n"
"qt 42,41,20480\nqm v,a,a,108,12,172\nqt 44,41,24576\nqm v,a,a,92,12,164\nqt 46,41,28672\nql 76,0,68,1,1024\n"
"qm v,a,a,92,8,160\nqw 12\nqt 48,42,16384\nv_readlane_b32 s2, v18, 23\nqm v,a,a,96,16,160\nqt 50,42,20480\n"
"qm v,a,a,96,20,164\ns_add_u32 m0, s0, 0x1c000\nqt 52,42,24576\nqm v,a,a,112,20,172\nqt 54,42,28672\nqm v,a,a,112,16,168\n"
"qw 12\nqt 56,43,16384\nqm v,a,a,116,24,168\nqt 58,43,20480\nqt 60,43,24576\nqt 62,43,28672\n"
"qv 30\ns_barrier\nqd 12,60,2,0\nqm v,a,a,116,28,172\nqt 0,36,32768\nqm v,a,a,100,28,164\n"
"qt 2,36,36864\nqm v,a,a,100,24,160\nqw 14\nqt 4,36,40960\nqm v,a,a,56,32,160\nqd 13,60,2,1024\n"
"qm v,a,a,56,36,164\nqt 6,36,45056\nv_readlane_b32 s1, v17, 23\nqm v,a,a,72,36,172\nqt 8,37,32768\nqm v,a,a,72,32,168\n"
"qw 13\ns_nop 0\nql 80,0,64,1,0\nqm v,a,a,76,40,168\nqt 10,37,36864\nqm v,a,a,76,44,172\n"
"qt 12,37,40960\nqm v,a,a,60,44,164\nqt 14,37,45056\nql 84,0,64,1,1024\nqm v,a,a,60,40,160\nqw 12\n"
"qt 16,38,32768\nqm v,a,a,64,48,160\nqt 18,38,36864\nqm v,a,a,64,52,164\nqt 20,38,40960\nqd 14,60,2,2048\n"
"qm v,a,a,80,52,172\nqt 22,38,45056\nqm v,a,a,80,48,168\nqw 12\nqt 24,39,32768\nqm v,a,a,84,56,168\n"
"qt 26,39,36864\nqm v,a,a,84,60,172\nqt 28,39,40960\nqd 15,60,2,3072\nqm v,a,a,68,60,164\nqt 30,39,45056\n"
"qm v,a,a,68,56,160\nqw 12\nqv 34\nqt 32,40,32768\nqm v,a,a,88,0,176\nqt 34,40,36864\n"
"ql 88,0,68,1,0\nqm v,a,a,88,4,180\ns_lshl2_add_u32 s84, s14, s15\ns_lshl3_add_u32 s86, s84, 0x74000\nqt 36,40,40960\nqm v,a,a,104,4,188\n"
"s_xor_b32 s87, s86, 32\ns_lshl_b32 s84, s84, 13\nqt 38,40,45056\nqm v,a,a,104,0,184\nqw 12\nqt 40,41,32768\n"
"ql 92,0,68,1,1024\nv_readlane_b32 s2, v18, 24\nqm v,a,a,108,8,184\ns_add_u32 s84, s84, 0x5800000\ns_add_u32 s85, s84, 0x1000\nqt 42,41,36864\n"
"qm v,a,a,108,12,188\ns_add_u32 m0, s0, 0x0\nqt 44,41,40960\nv_mov_b32 v11, 0\nqm v,a,a,92,12,180\nqt 46,41,45056\n"
"qm v,a,a,92,8,176\nqw 12\nqt 48,42,32768\nqd 12,60,2,0\nqm v,a,a,96,16,176\nqt 50,42,36864\n"
"qm v,a,a,96,20,180\nqt 52,42,40960\nqm v,a,a,112,20,188\nqt 54,42,45056\nqd 13,60,2,1024\nqm v,a,a,112,16,184\n"
"qw 12\nqt 56,43,32768\nqm v,a,a,116,24,184\nqt 58,43,36864\nqm v,a,a,116,28,188\nqt 60,43,40960\n"
"qd 14,60,2,2048\nqm v,a,a,100,28,180\nqt 62,43,45056\nqm v,a,a,100,24,176\nqw 12\nqt 0,36,49152\n"
"qm v,a,a,56,32,176\nqt 2,36,53248\nqd 15,60,2,3072\nqm v,a,a,56,36,180\nqt 4,36,57344\nv_readlane_b32 s1, v17, 24\n"
"qm v,a,a,72,36,188\nqt 6,36,61440\nv_add_u32 v36, 0x10000, v36\nqm v,a,a,72,32,184\nqw 12\nqt 8,37,49152\n"
"qm v,a,a,76,40,184\nqt 10,37,53248\nql 96,0,64,1,0\nqm v,a,a,76,44,188\nqt 12,37,57344\nqm v,a,a,60,44,180\n"
"qt 14,37,61440\nv_add_u32 v37, 0x10000, v37\nqm v,a,a,60,40,176\nqw 12\nqt 16,38,49152\nql 100,0,64,1,1024\n"
"qm v,a,a,64,48,176\nqt 18,38,53248\nqm v,a,a,64,52,180\nqt 20,38,57344\nqm v,a,a,80,52,188\nqt 22,38,61440\n"
"ql 104,0,68,1,0\nqm v,a,a,80,48,184\nv_add_u32 v38, 0x10000, v38\nqt 24,39,49152\nqw 13\nqm v,a,a,84,56,184\n"
"qt 26,39,53248\nqm v,a,a,84,60,188\nqt 28,39,57344\nqm v,a,a,68,60,180\nqt 30,39,61440\nql 108,0,68,1,1024\n"
"qm v,a,a,68,56,176\nv_add_u32 v39, 0x10000, v39\nqt 32,40,49152\nv_readlane_b32 s1, v17, 25\nqw 13\nqv 38\n"
"qm v,a,a,88,0,192\nqt 34,40,53248\nqm v,a,a,88,4,196\nqt 36,40,57344\nql 112,0,64,1,0\nqm v,a,a,104,4,204\n"
"qt 38,40,61440\nv_add_u32 v40, 0x10000, v40\nqm v,a,a,104,0,200\nqw 12\nqt 40,41,49152\nqm v,a,a,108,8,200\n"
"qt 42,41,53248\nql 116,0,64,1,1024\nqm v,a,a,108,12,204\nqt 44,41,57344\nqm v,a,a,92,12,196\nqt 46,41,61440\n"
"v_add_u32 v41, 0x10000, v41\nqm v,a,a,92,8,192\nqw 12\nqt 48,42,49152\nqm v,a,a,96,16,192\nqt 50,42,53248\n"
"qm v,a,a,96,20,196\nqt 52,42,57344\nqm v,a,a,112,20,204\nqt 54,42,61440\nv_add_u32 v42, 0x10000, v42\nqm v,a,a,112,16,200\n"
"qw 12\nqt 56,43,49152\nql 120,0,68,1,0\nqm v,a,a,116,24,200\nqt 58,43,53248\nqm v,a,a,116,28,204\n"
"qt 60,43,57344\nqt 62,43,61440\nv_add_u32 v43, 0x10000, v43\nqv 31\ns_barrier\nqm v,a,a,100,28,196\n"
"ql 124,0,68,1,1024\nv_readlane_b32 s2, v18, 25\nqm v,a,a,100,24,192\nqw 12\nqt 0,40,0\nqm v,a,a,56,32,192\n"
"s_add_u32 m0, s0, 0x4000\nqt 2,40,4096\nqm v,a,a,56,36,196\nqt 4,40,8192\nqm v,a,a,72,36,204\nqt 6,40,12288\n"
"qd 12,60,2,0\nqm v,a,a,72,32,200\nqw 12\nqt 8,41,0\nqm v,a,a,76,40,200\nqt 10,41,4096\n"
"qm v,a,a,76,44,204\nqt 12,41,8192\nqd 13,60,2,1024\nqm v,a,a,60,44,196\nqt 14,41,12288\nv_readlane_b32 s1, v17, 26\n"
"qm v,a,a,60,40,192\nqw 12\nqt 16,42,0\nqm v,a,a,64,48,192\nqt 18,42,4096\nql 128,0,64,1,0\n"
"qm v,a,a,64,52,196\nqt 20,42,8192\nqm v,a,a,80,52,204\nqt 22,42,12288\nqm v,a,a,80,48,200\nqw 12\n"
"qt 24,43,0\nqm v,a,a,84,56,200\nqt 26,43,4096\nql 132,0,64,1,1024\nqm v,a,a,84,60,204\nqt 28,43,8192\n"
"qm v,a,a,68,60,196\nqt 30,43,12288\nqm v,a,a,68,56,192\nqw 12\nqt 32,40,16384\nqd 14,60,2,2048\n"
"qm v,a,a,56,0,208\nqt 34,40,20480\nqm v,a,a,56,4,212\nqt 36,40,24576\nqd 15,60,2,3072\nv_readlane_b32 s2, v18, 26\n"
"qm v,a,a,72,4,220\nqt 38,40,28672\nqm v,a,a,72,0,216\ns_add_u32 m0, s0, 0x8000\nqw 12\nqt 40,41,16384\n"
"qm v,a,a,76,8,216\nqt 42,41,20480\nqm v,a,a,76,12,220\nqt 44,41,24576\nqd 12,60,2,0\nqm v,a,a,60,12,212\n"
"qt 46,41,28672\nqm v,a,a,60,8,208\nqw 12\nqt 48,42,16384\nqm v,a,a,64,16,208\nqt 50,42,20480\n"
"qd 13,60,2,1024\nqm v,a,a,64,20,212\nqt 52,42,24576\nqm v,a,a,80,20,220\nqt 54,42,28672\nqm v,a,a,80,16,216\n"
"qw 12\nqt 56,43,16384\nql 136,0,68,1,0\nqm v,a,a,84,24,216\nqt 58,43,20480\nqm v,a,a,84,28,220\n"
"qt 60,43,24576\nqt 62,43,28672\nqv 23\ns_barrier\nqm v,a,a,68,28,212\nqt 0,40,32768\n"
"qm v,a,a,68,24,208\nqw 13\nql 140,0,68,1,1024\nqm v,a,a,56,32,224\nqt 2,40,36864\nqm v,a,a,56,36,228\n"
"qt 4,40,40960\nqm v,a,a,72,36,236\nqt 6,40,45056\nqd 14,60,2,2048\nqm v,a,a,72,32,232\nqw 12\n"
"qt 8,41,32768\nqm v,a,a,76,40,232\nqt 10,41,36864\nqd 15,60,2,3072\nv_readlane_b32 s2, v18, 27\nqm v,a,a,76,44,236\n"
"qt 12,41,40960\nqm v,a,a,60,44,228\ns_add_u32 m0, s0, 0xc000\nqt 14,41,45056\nqm v,a,a,60,40,224\nqw 12\n"
"qt 16,42,32768\nqm v,a,a,64,48,224\nqt 18,42,36864\nqd 12,60,2,0\nqm v,a,a,64,52,228\nqt 20,42,40960\n"
"qm v,a,a,80,52,236\nqt 22,42,45056\nqm v,a,a,80,48,232\nqw 12\nqt 24,43,32768\nqd 13,60,2,1024\n"
"qm v,a,a,84,56,232\nqt 26,43,36864\nv_readlane_b32 s1, v17, 27\nqm v,a,a,84,60,236\nqt 28,43,40960\nqm v,a,a,68,60,228\n"
"qt 30,43,45056\nqm v,a,a,68,56,224\nqw 12\nqt 32,40,49152\nql 144,0,64,1,0\nqm v,a,a,56,0,64\n"
"qt 34,40,53248\nqm v,a,a,56,4,68\nqt 36,40,57344\nqm v,a,a,72,4,76\nqt 38,40,61440\nql 148,0,64,1,1024\n"
"qm v,a,a,72,0,72\nv_add_u32 v40, 0xffff0000, v40\nqt 40,41,49152\nqw 13\nqm v,a,a,76,8,72\nqt 42,41,53248\n"
"qm v,a,a,76,12,76\nqt 44,41,57344\nqd 14,60,2,2048\nqm v,a,a,60,12,68\nqt 46,41,61440\nv_add_u32 v41, 0xffff0000, v41\n"
"qm v,a,a,60,8,64\nqw 12\nqt 48,42,49152\nqm v,a,a,64,16,64\nqt 50,42,53248\nqd 15,60,2,3072\n"
"qm v,a,a,64,20,68\nqt 52,42,57344\nqm v,a,a,80,20,76\nqt 54,42,61440\nv_add_u32 v42, 0xffff0000, v42\nqm v,a,a,80,16,72\n"
"qw 12\nqt 56,43,49152\nql 152,0,68,1,0\nqm v,a,a,84,24,72\nqt 58,43,53248\nqm v,a,a,84,28,76\n"
"qt 60,43,57344\nqt 62,43,61440\nv_add_u32 v43, 0xffff0000, v43\nqv 13\ns_barrier\nqm v,a,a,68,28,68\n"
"qt 0,40,0\nqm v,a,a,68,24,64\nqw 13\nql 156,0,68,1,1024\nqm v,a,a,56,32,80\nqt 2,40,4096\n"
"qm v,a,a,56,36,84\nqt 4,40,8192\nqm v,a,a,72,36,92\nqt 6,40,12288\nqm v,a,a,72,32,88\nqw 12\n"
"qt 8,41,0\nqm v,a,a,76,40,88\nqt 10,41,4096\nqm v,a,a,76,44,92\nqt 12,41,8192\nqm v,a,a,60,44,84\n"
"qt 14,41,12288\nqm v,a,a,60,40,80\nqw 12\nqt 16,42,0\nqm v,a,a,64,48,80\nqt 18,42,4096\n"
"qm v,a,a,64,52,84\nqt 20,42,8192\nqm v,a,a,80,52,92\nqt 22,42,12288\nqm v,a,a,80,48,88\nqw 12\n"
"qt 24,43,0\nqm v,a,a,84,56,88\nqt 26,43,4096\nqm v,a,a,84,60,92\nqt 28,43,8192\nqm v,a,a,68,60,84\n"
"qt 30,43,12288\nqm v,a,a,68,56,80\nqw 12\nqt 32,40,16384\nqm v,a,a,56,0,96\nqt 34,40,20480\n"
"qm v,a,a,56,4,100\nqt 36,40,24576\nqm v,a,a,72,4,108\nqt 38,40,28672\nqm v,a,a,72,0,104\nqw 12\n"
"qt 40,41,16384\nqm v,a,a,76,8,104\nqt 42,41,20480\nqm v,a,a,76,12,108\nqt 44,41,24576\nqm v,a,a,60,12,100\n"
"qt 46,41,28672\nqm v,a,a,60,8,96\nqw 12\nqt 48,42,16384\nqm v,a,a,64,16,96\nqt 50,42,20480\n"
"qm v,a,a,64,20,100\nqt 52,42,24576\nqm v,a,a,80,20,108\nqt 54,42,28672\nqm v,a,a,80,16,104\nqw 12\n"
"qt 56,43,16384\nqm v,a,a,84,24,104\nqt 58,43,20480\nqm v,a,a,84,28,108\nqt 60,43,24576\nqt 62,43,28672\n"
"qv 2\ns_barrier\nqm v,a,a,68,28,100\nqt 0,40,32768\nqm v,a,a,68,24,96\nqw 13\n"
"qt 2,40,36864\nqm v,a,a,56,32,112\nqt 4,40,40960\nqm v,a,a,56,36,116\nqt 6,40,45056\nqm v,a,a,72,36,124\n"
"qt 8,41,32768\nqm v,a,a,72,32,120\nqw 13\nqt 10,41,36864\nqm v,a,a,76,40,120\nqt 12,41,40960\n"
"qm v,a,a,76,44,124\nqt 14,41,45056\nqm v,a,a,60,44,116\nqt 16,42,32768\nqm v,a,a,60,40,112\nqw 13\n"
"qt 18,42,36864\nqm v,a,a,64,48,112\nqt 20,42,40960\nqm v,a,a,64,52,116\nqt 22,42,45056\nqm v,a,a,80,52,124\n"
"qt 24,43,32768\nqm v,a,a,80,48,120\nqw 13\nqt 26,43,36864\nqm v,a,a,84,56,120\nqt 28,43,40960\n"
"qm v,a,a,84,60,124\nqt 30,43,45056\nqm v,a,a,68,60,116\nqt 32,40,49152\nqm v,a,a,68,56,112\nqw 13\n"
"qt 34,40,53248\nqm v,a,a,56,0,128\nqt 36,40,57344\nqm v,a,a,56,4,132\nqt 38,40,61440\nqm v,a,a,72,4,140\n"
"qt 40,41,49152\nqm v,a,a,72,0,136\nqw 13\nqt 42,41,53248\nqm v,a,a,76,8,136\nqt 44,41,57344\n"
"qm v,a,a,76,12,140\nqt 46,41,61440\nqm v,a,a,60,12,132\nqt 48,42,49152\nqm v,a,a,60,8,128\nqw 13\n"
"qt 50,42,53248\nqm v,a,a,64,16,128\nqt 52,42,57344\nqm v,a,a,64,20,132\nqt 54,42,61440\nqm v,a,a,80,20,140\n"
"qt 56,43,49152\nqm v,a,a,80,16,136\nqw 13\nqt 58,43,53248\nqm v,a,a,84,24,136\nqt 60,43,57344\n"
"qm v,a,a,84,28,140\nqt 62,43,61440\nqm v,a,a,68,28,132\nqt 0,36,0\nqm v,a,a,68,24,128\nqw 13\n"
"qv 0\nqt 2,36,4096\nqm v,a,a,56,32,144\nqt 4,36,8192\nqm v,a,a,56,36,148\nqt 6,36,12288\n"
"qm v,a,a,72,36,156\nqt 8,37,0\nqm v,a,a,72,32,152\nqw 13\nqt 10,37,4096\nqm v,a,a,76,40,152\n"
"qt 12,37,8192\nqm v,a,a,76,44,156\nqt 14,37,12288\nqm v,a,a,60,44,148\nqt 16,38,0\nqm v,a,a,60,40,144\n"
"qw 13\nqt 18,38,4096\nqm v,a,a,64,48,144\nqt 20,38,8192\nqm v,a,a,64,52,148\nqt 22,38,12288\n"
"qm v,a,a,80,52,156\nqt 24,39,0\nqm v,a,a,80,48,152\nqw 13\nqt 26,39,4096\nqm v,a,a,84,56,152\n"
"qt 28,39,8192\nqm v,a,a,84,60,156\nqt 30,39,12288\nqm v,a,a,68,60,148\nqt 32,36,16384\nqm v,a,a,68,56,144\n"
"qw 13\nqt 34,36,20480\nqm v,a,a,88,0,208\nqt 36,36,24576\nqm v,a,a,88,4,212\nqt 38,36,28672\n"
"qm v,a,a,104,4,220\nqt 40,37,16384\nqm v,a,a,104,0,216\nqw 13\nqt 42,37,20480\nqm v,a,a,108,8,216\n"
"qt 44,37,24576\nqm v,a,a,108,12,220\nqt 46,37,28672\nqc 56,56\nqm v,a,a,92,12,212\nqt 48,38,16384\n"
"qc 57,58\nqm v,a,a,92,8,208\nqw 13\nqt 50,38,20480\nqc 58,60\nqm v,a,a,96,16,208\n"
"qt 52,38,24576\nqc 59,62\nqm v,a,a,96,20,212\nqt 54,38,28672\nqc 64,64\nqm v,a,a,112,20,220\n"
"buffer_store_dwordx4 v[56:59], v0, s[56:59], s84 offen offset:0 sc1\nqc 65,66\nqm v,a,a,112,16,216\nqw 12\nqt 56,39,16384\nqc 66,68\n"
"qm v,a,a,116,24,216\nqt 58,39,20480\nqc 67,70\nqm v,a,a,116,28,220\nqt 60,39,24576\nqc 72,72\n"
"qm v,a,a,100,28,212\nqt 62,39,28672\nbuffer_store_dwordx4 v[64:67], v0, s[56:59], s84 offen offset:1024 sc1\nqc 73,74\nqm v,a,a,100,24,208\nqw 12\n"
"qt 0,36,32768\nqc 74,76\nqm v,a,a,88,32,224\nqt 2,36,36864\nqc 75,78\nqm v,a,a,88,36,228\n"
"qt 4,36,40960\nqc 80,80\nqm v,a,a,104,36,236\nqt 6,36,45056\nqc 81,82\nqm v,a,a,104,32,232\n"
"qw 12\nqt 8,37,32768\nbuffer_store_dwordx4 v[72:75], v0, s[56:59], s84 offen offset:2048 sc1\nqc 82,84\nqm v,a,a,108,40,232\nqt 10,37,36864\n"
"qc 83,86\nqm v,a,a,108,44,236\nqt 12,37,40960\nqm v,a,a,92,44,228\nqt 14,37,45056\nqm v,a,a,92,40,224\n"
"qw 12\nqt 16,38,32768\nqm v,a,a,96,48,224\nqt 18,38,36864\nbuffer_store_dwordx4 v[80:83], v0, s[56:59], s84 offen offset:3072 sc1\nqm v,a,a,96,52,228\n"
"qt 20,38,40960\nqm v,a,a,112,52,236\nqt 22,38,45056\nqm v,a,a,112,48,232\nqw 12\nqt 24,39,32768\n"
"qm v,a,a,116,56,232\nqt 26,39,36864\nqm v,a,a,116,60,236\nqt 28,39,40960\nqm v,a,a,100,60,228\nqt 30,39,45056\n"
"qm v,a,a,100,56,224\nqw 12\nqt 32,36,49152\nqm v,a,a,88,0,64\nqt 34,36,53248\nqm v,a,a,88,4,68\n"
"qt 36,36,57344\nqm v,a,a,104,4,76\nqt 38,36,61440\nv_add_u32 v36, 0xffff0000, v36\nqm v,a,a,104,0,72\nqw 12\n"
"qt 40,37,49152\nqm v,a,a,108,8,72\nqt 42,37,53248\nqm v,a,a,108,12,76\nqt 44,37,57344\nqm v,a,a,92,12,68\n"
"qt 46,37,61440\nv_add_u32 v37, 0xffff0000, v37\nqm v,a,a,92,8,64\nqw 12\nqt 48,38,49152\nqm v,a,a,96,16,64\n"
"qt 50,38,53248\nqm v,a,a,96,20,68\nqt 52,38,57344\nqm v,a,a,112,20,76\nqt 54,38,61440\nv_add_u32 v38, 0xffff0000, v38\n"
"qm v,a,a,112,16,72\nqw 12\nqt 56,39,49152\nqm v,a,a,116,24,72\nqt 58,39,53248\nqm v,a,a,116,28,76\n"
"qt 60,39,57344\nqm v,a,a,100,28,68\nqt 62,39,61440\nv_add_u32 v39, 0xffff0000, v39\nqm v,a,a,100,24,64\nqw 12\n"
"qt 0,36,0\nqm v,a,a,88,32,80\nqt 2,36,4096\nqm v,a,a,88,36,84\nqt 4,36,8192\nqm v,a,a,104,36,92\n"
"qt 6,36,12288\nqm v,a,a,104,32,88\nqw 12\nqt 8,37,0\nqm v,a,a,108,40,88\nqt 10,37,4096\n"
"qm v,a,a,108,44,92\nqt 12,37,8192\nqm v,a,a,92,44,84\nqt 14,37,12288\nqm v,a,a,92,40,80\nqw 12\n"
"qt 16,38,0\nqm v,a,a,96,48,80\nqt 18,38,4096\nqm v,a,a,96,52,84\nqt 20,38,8192\nqm v,a,a,112,52,92\n"
"qt 22,38,12288\nqm v,a,a,112,48,88\nqw 12\nqv 0\nqt 24,39,0\nqm v,a,a,116,56,88\n"
"qt 26,39,4096\nqm v,a,a,116,60,92\nqt 28,39,8192\nqm v,a,a,100,60,84\nqt 30,39,12288\nqm v,a,a,100,56,80\n"
"qw 12\nqt 32,36,16384\nqm v,a,a,88,0,96\nqt 34,36,20480\nqm v,a,a,88,4,100\nqt 36,36,24576\n"
"s_mov_b64 exec, 1\nbuffer_store_dwordx2 v[250:251], off, s[56:59], s86 sc1\ns_mov_b64 exec, -1\nqm v,a,a,104,4,108\ns_xor_b32 s84, s84, 0x8000\ns_xor_b32 s85, s85, 0x8000\n"
"qt 38,36,28672\nqm v,a,a,104,0,104\nqw 12\nqt 40,37,16384\nqm v,a,a,108,8,104\nqt 42,37,20480\n"
"qm v,a,a,108,12,108\nqt 44,37,24576\nqm v,a,a,92,12,100\nqt 46,37,28672\nqm v,a,a,92,8,96\nqw 12\n"
"qt 48,38,16384\nqm v,a,a,96,16,96\nqt 50,38,20480\nqm v,a,a,96,20,100\nqt 52,38,24576\nqm v,a,a,112,20,108\n"
"qt 54,38,28672\nqm v,a,a,112,16,104\nqw 12\nqt 56,39,16384\nqm v,a,a,116,24,104\nqt 58,39,20480\n"
"qm v,a,a,116,28,108\nqt 60,39,24576\nqm v,a,a,100,28,100\nqt 62,39,28672\nqm v,a,a,100,24,96\nqw 12\n"
"qt 0,36,32768\nqm v,a,a,88,32,112\nqt 2,36,36864\nqm v,a,a,88,36,116\nqt 4,36,40960\nqm v,a,a,104,36,124\n"
"qt 6,36,45056\nqm v,a,a,104,32,120\nqw 12\nqt 8,37,32768\nqm v,a,a,108,40,120\nqt 10,37,36864\n"
"qm v,a,a,108,44,124\nqt 12,37,40960\nqm v,a,a,92,44,116\nqt 14,37,45056\nqm v,a,a,92,40,112\nqw 12\n"
"qt 16,38,32768\nqm v,a,a,96,48,112\nqt 18,38,36864\nqm v,a,a,96,52,116\nqt 20,38,40960\nqm v,a,a,112,52,124\n"
"qt 22,38,45056\nbuffer_load_dwordx2 v[248:249], v11, s[56:59], s87 offen sc1\nqm v,a,a,112,48,120\nqw 12\nqt 24,39,32768\nqm v,a,a,116,56,120\n"
"qt 26,39,36864\nqm v,a,a,116,60,124\nqt 28,39,40960\nqm v,a,a,100,60,116\nqt 30,39,45056\nqm v,a,a,100,56,112\n"
"qw 12\nqt 32,36,49152\nqm v,a,a,88,0,128\nqt 34,36,53248\nqm v,a,a,88,4,132\nqt 36,36,57344\n"
"qm v,a,a,104,4,140\nqt 38,36,61440\nqm v,a,a,104,0,136\nqw 12\nqt 40,37,49152\nqm v,a,a,108,8,136\n"
"qt 42,37,53248\nqm v,a,a,108,12,140\nqt 44,37,57344\nqm v,a,a,92,12,132\nqt 46,37,61440\nqm v,a,a,92,8,128\n"
"qw 12\nqt 48,38,49152\nqm v,a,a,96,16,128\nqt 50,38,53248\nqm v,a,a,96,20,132\nqt 52,38,57344\n"
"qm v,a,a,112,20,140\nqv 0\nqt 54,38,61440\nv_cmp_eq_u64 vcc, s[16:17], v[248:249]\nqm v,a,a,112,16,136\nqw 12\n"
"qt 56,39,49152\nqm v,a,a,116,24,136\nqt 58,39,53248\nqm v,a,a,116,28,140\nqt 60,39,57344\nqm v,a,a,100,28,132\n"
"s_cmp_eq_u64 vcc, exec\ns_cbranch_scc0 PXG4_o%=\nPXG4_r%=:\nqt 62,39,61440\nqm v,a,a,100,24,128\nqw 12\n"
"qm v,a,a,88,32,144\nbuffer_load_dwordx4 v[152:155], v0, s[56:59], s84 offen offset:0 sc1\nqm v,a,a,88,36,148\nqm v,a,a,104,36,156\nqm v,a,a,104,32,152\nqw 8\n"
"qm v,a,a,108,40,152\nbuffer_load_dwordx4 v[156:159], v0, s[56:59], s84 offen offset:1024 sc1\nqm v,a,a,108,44,156\nqm v,a,a,92,44,148\nbuffer_load_dwordx4 v[160:163], v0, s[56:59], s84 offen offset:2048 sc1\nqm v,a,a,92,40,144\n"
"qw 4\nqm v,a,a,96,48,144\nqm v,a,a,96,52,148\nqm v,a,a,112,52,156\nbuffer_load_dwordx4 v[164:167], v0, s[56:59], s84 offen offset:3072 sc1\nqm v,a,a,112,48,152\n"
"qw 0\nqm v,a,a,116,56,152\nqm v,a,a,116,60,156\nqm v,a,a,100,60,148\nqm v,a,a,100,56,144\nqv 2\n"
"v_lshlrev_b32 v120, 16, v152\nv_and_b32 v121, 0xffff0000, v152\nv_lshlrev_b32 v122, 16, v153\nv_and_b32 v123, 0xffff0000, v153\nv_lshlrev_b32 v124, 16, v154\nv_and_b32 v125, 0xffff0000, v154\n"
"v_lshlrev_b32 v126, 16, v155\nv_and_b32 v127, 0xffff0000, v155\nv_lshlrev_b32 v128, 16, v156\nv_and_b32 v129, 0xffff0000, v156\nv_lshlrev_b32 v130, 16, v157\nv_and_b32 v131, 0xffff0000, v157\n"
"v_lshlrev_b32 v132, 16, v158\nv_and_b32 v133, 0xffff0000, v158\nv_lshlrev_b32 v134, 16, v159\nv_and_b32 v135, 0xffff0000, v159\nv_pk_add_f32 v[88:89], v[88:89], v[120:121]\nv_pk_add_f32 v[90:91], v[90:91], v[122:123]\n"
"v_pk_add_f32 v[92:93], v[92:93], v[124:125]\nv_pk_add_f32 v[94:95], v[94:95], v[126:127]\nv_pk_add_f32 v[96:97], v[96:97], v[128:129]\nv_pk_add_f32 v[98:99], v[98:99], v[130:131]\nv_pk_add_f32 v[100:101], v[100:101], v[132:133]\nv_pk_add_f32 v[102:103], v[102:103], v[134:135]\n"
"qc 20,88\nqc 21,90\nqc 22,92\nqc 23,94\nqc 24,96\nqc 25,98\n"
"qc 26,100\nqc 27,102\nv_permlane16_swap_b32 v20, v22\nv_permlane16_swap_b32 v21, v23\nv_permlane16_swap_b32 v24, v26\nv_permlane16_swap_b32 v25, v27\n"
"buffer_store_dwordx4 v[20:23], v16, s[72:75], s3 offen offset:0\nbuffer_store_dwordx4 v[24:27], v16, s[72:75], s3 offen offset:64\nqv 2\nv_lshlrev_b32 v136, 16, v160\nv_and_b32 v137, 0xffff0000, v160\nv_lshlrev_b32 v138, 16, v161\n"
"v_and_b32 v139, 0xffff0000, v161\nv_lshlrev_b32 v140, 16, v162\nv_and_b32 v141, 0xffff0000, v162\nv_lshlrev_b32 v142, 16, v163\nv_and_b32 v143, 0xffff0000, v163\nv_lshlrev_b32 v144, 16, v164\n"
"v_and_b32 v145, 0xffff0000, v164\nv_lshlrev_b32 v146, 16, v165\nv_and_b32 v147, 0xffff0000, v165\nv_lshlrev_b32 v148, 16, v166\nv_and_b32 v149, 0xffff0000, v166\nv_lshlrev_b32 v150, 16, v167\n"
"v_and_b32 v151, 0xffff0000, v167\nv_pk_add_f32 v[104:105], v[104:105], v[136:137]\nv_pk_add_f32 v[106:107], v[106:107], v[138:139]\nv_pk_add_f32 v[108:109], v[108:109], v[140:141]\nv_pk_add_f32 v[110:111], v[110:111], v[142:143]\nv_pk_add_f32 v[112:113], v[112:113], v[144:145]\n"
"v_pk_add_f32 v[114:115], v[114:115], v[146:147]\nv_pk_add_f32 v[116:117], v[116:117], v[148:149]\nv_pk_add_f32 v[118:119], v[118:119], v[150:151]\nqc 28,104\nqc 29,106\nqc 30,108\n"
"qc 31,110\nqc 32,112\nqc 33,114\nqc 34,116\nqc 35,118\nv_permlane16_swap_b32 v28, v30\n"
"v_permlane16_swap_b32 v29, v31\nv_permlane16_swap_b32 v32, v34\nv_permlane16_swap_b32 v33, v35\nbuffer_store_dwordx4 v[28:31], v16, s[72:75], s4 offen offset:0\nbuffer_store_dwordx4 v[32:35], v16, s[72:75], s4 offen offset:64\nEND%=:\n"
"s_waitcnt vmcnt(0) lgkmcnt(0)\ns_branch FINISH%=\nPEFINE_o%=:\ns_mov_b32 s35, 200000\nPEFINE_s%=:\nbuffer_load_dwordx2 v[248:249], v19, s[56:59], s5 offen offset:0 sc1\n"
"qv 0\nv_cmp_eq_u64 vcc, s[16:17], v[248:249]\ns_nop 3\ns_cmp_eq_u64 vcc, exec\ns_cbranch_scc1 PEFINE_sx%=\ns_sleep 3\n"
"s_sub_u32 s35, s35, 1\ns_cmp_lg_u32 s35, 0\ns_cbranch_scc1 PEFINE_s%=\nPEFINE_sx%=:\ns_branch PEFINE_r%=\nPEFIN_o%=:\n"
"s_mov_b32 s35, 200000\nPEFIN_s%=:\nbuffer_load_dwordx2 v[248:249], v19, s[56:59], s5 offen offset:0 sc1\nqv 0\nv_cmp_eq_u64 vcc, s[16:17], v[248:249]\ns_nop 3\n"
"s_cmp_eq_u64 vcc, exec\ns_cbranch_scc1 PEFIN_sx%=\ns_sleep 3\ns_sub_u32 s35, s35, 1\ns_cmp_lg_u32 s35, 0\ns_cbranch_scc1 PEFIN_s%=\n"
"PEFIN_sx%=:\ns_branch PEFIN_r%=\nSLOW%=:\ns_waitcnt vmcnt(0) lgkmcnt(0)\nqb 0,0\nqb 1,0\n"
"qb 2,0\nqb 3,0\nqb 4,0\nqb 5,0\nqb 6,0\nqb 7,0\n"
"qb 8,0\nqb 9,0\nqb 10,0\nqb 11,0\nqb 12,0\nqb 13,0\n"
"qb 14,0\nqb 15,0\nqb 16,0\nqb 17,0\nqb 18,0\nqb 19,0\n"
"qb 20,0\nqb 21,0\nqb 22,0\nqb 23,0\nqb 24,0\nqb 25,0\n"
"qb 26,0\nqb 27,0\nqb 28,0\nqb 29,0\nqb 30,0\nqb 31,0\n"
"qb 32,0\nqb 33,0\nqb 34,0\nqb 35,0\nqb 36,0\nqb 37,0\n"
"qb 38,0\nqb 39,0\nqb 40,0\nqb 41,0\nqb 42,0\nqb 43,0\n"
"qb 44,0\nqb 45,0\nqb 46,0\nqb 47,0\nqb 48,0\nqb 49,0\n"
"qb 50,0\nqb 51,0\nqb 52,0\nqb 53,0\nqb 54,0\nqb 55,0\n"
"qb 56,0\nqb 57,0\nqb 58,0\nqb 59,0\nqb 60,0\nqb 61,0\n"
"qb 62,0\nqb 63,0\nqb 64,0\nqb 65,0\nqb 66,0\nqb 67,0\n"
"qb 68,0\nqb 69,0\nqb 70,0\nqb 71,0\nqb 72,0\nqb 73,0\n"
"qb 74,0\nqb 75,0\nqb 76,0\nqb 77,0\nqb 78,0\nqb 79,0\n"
"qb 80,0\nqb 81,0\nqb 82,0\nqb 83,0\nqb 84,0\nqb 85,0\n"
"qb 86,0\nqb 87,0\nqb 88,0\nqb 89,0\nqb 90,0\nqb 91,0\n"
"qb 92,0\nqb 93,0\nqb 94,0\nqb 95,0\nqb 96,0\nqb 97,0\n"
"qb 98,0\nqb 99,0\nqb 100,0\nqb 101,0\nqb 102,0\nqb 103,0\n"
"qb 104,0\nqb 105,0\nqb 106,0\nqb 107,0\nqb 108,0\nqb 109,0\n"
"qb 110,0\nqb 111,0\nqb 112,0\nqb 113,0\nqb 114,0\nqb 115,0\n"
"qb 116,0\nqb 117,0\nqb 118,0\nqb 119,0\nqb 120,0\nqb 121,0\n"
"qb 122,0\nqb 123,0\nqb 124,0\nqb 125,0\nqb 126,0\nqb 127,0\n"
"qb 128,0\nqb 129,0\nqb 130,0\nqb 131,0\nqb 132,0\nqb 133,0\n"
"qb 134,0\nqb 135,0\nqb 136,0\nqb 137,0\nqb 138,0\nqb 139,0\n"
"qb 140,0\nqb 141,0\nqb 142,0\nqb 143,0\nv_mbcnt_lo_u32_b32 v33, -1, 0\nv_mbcnt_hi_u32_b32 v33, -1, v33\n"
"v_and_b32 v34, 15, v33\nv_lshrrev_b32 v33, 4, v33\nv_lshlrev_b32 v33, 2, v33\nv_sub_u32 v33, v34, v33\ns_xor_b32 s72, s88, 0\ns_lshl_b32 s72, s72, 4\n"
"v_add_u32 v20, s72, v33\nv_mov_b32 v24, 0xf149f2ca\ns_xor_b32 s72, s88, 7\ns_lshl_b32 s72, s72, 4\nv_add_u32 v21, s72, v33\nv_mov_b32 v25, 0xf149f2ca\n"
"s_xor_b32 s72, s97, 0\ns_lshl_b32 s72, s72, 4\nv_add_u32 v22, s72, v33\nv_mov_b32 v26, 0xf149f2ca\ns_xor_b32 s72, s97, 7\ns_lshl_b32 s72, s72, 4\n"
"v_add_u32 v23, s72, v33\nv_mov_b32 v27, 0xf149f2ca\nv_mov_b32 v32, 0xff800000\ns_lshl_b32 s13, s15, 12\ns_mov_b32 s76, s86\ns_add_u32 s77, s86, 0x1000000\n"
"s_sub_u32 s84, 16, s21\ns_lshl_b32 s84, s84, 1\nSLOWL%=:\ns_barrier\ns_mov_b32 m0, s13\ns_nop 0\n"
"qe 0,56,76,0\nqe 0,56,76,1024\nqe 0,56,76,2048\nqe 0,56,76,3072\ns_or_b32 m0, s13, 0x10000\ns_nop 0\n"
"qe 0,56,77,0\nqe 0,56,77,1024\nqe 0,56,77,2048\nqe 0,56,77,3072\ns_add_u32 s76, s76, 0x4000\ns_add_u32 s77, s77, 0x4000\n"
"qv 0\ns_barrier\nqr 144,0,0\nqr 148,0,1024\nqr 152,0,2048\nqr 156,0,3072\n"
"qr 160,0,4096\nqr 164,0,5120\nqr 168,0,6144\nqr 172,0,7168\nqw 0\nqr 176,0,8192\n"
"qr 180,0,9216\nqr 184,0,10240\nqr 188,0,11264\nqr 192,0,12288\nqr 196,0,13312\nqr 200,0,14336\n"
"qr 204,0,15360\nqw 0\nqz v,a,v,124,144,120\nqm v,a,v,124,148,128\nqm v,a,v,124,152,136\nqm v,a,v,124,156,144\n"
"qz v,a,v,132,144,152\nqm v,a,v,132,148,160\nqm v,a,v,132,152,168\nqm v,a,v,132,156,176\nqz v,a,v,140,144,184\nqm v,a,v,140,148,192\n"
"qm v,a,v,140,152,200\nqm v,a,v,140,156,208\nqz v,a,v,148,144,216\nqm v,a,v,148,148,224\nqm v,a,v,148,152,232\nqm v,a,v,148,156,240\n"
"qz v,a,v,156,160,120\nqm v,a,v,156,164,128\nqm v,a,v,156,168,136\nqm v,a,v,156,172,144\nqz v,a,v,164,160,152\nqm v,a,v,164,164,160\n"
"qm v,a,v,164,168,168\nqm v,a,v,164,172,176\nqz v,a,v,172,160,184\nqm v,a,v,172,164,192\nqm v,a,v,172,168,200\nqm v,a,v,172,172,208\n"
"qz v,a,v,180,160,216\nqm v,a,v,180,164,224\nqm v,a,v,180,168,232\nqm v,a,v,180,172,240\nqz v,a,v,188,176,120\nqm v,a,v,188,180,128\n"
"qm v,a,v,188,184,136\nqm v,a,v,188,188,144\nqz v,a,v,196,176,152\nqm v,a,v,196,180,160\nqm v,a,v,196,184,168\nqm v,a,v,196,188,176\n"
"qz v,a,v,204,176,184\nqm v,a,v,204,180,192\nqm v,a,v,204,184,200\nqm v,a,v,204,188,208\nqz v,a,v,212,176,216\nqm v,a,v,212,180,224\n"
"qm v,a,v,212,184,232\nqm v,a,v,212,188,240\nqz v,a,v,220,192,120\nqm v,a,v,220,196,128\nqm v,a,v,220,200,136\nqm v,a,v,220,204,144\n"
"qz v,a,v,228,192,152\nqm v,a,v,228,196,160\nqm v,a,v,228,200,168\nqm v,a,v,228,204,176\nqz v,a,v,236,192,184\nqm v,a,v,236,196,192\n"
"qm v,a,v,236,200,200\nqm v,a,v,236,204,208\nqz v,a,v,244,192,216\nqm v,a,v,244,196,224\nqm v,a,v,244,200,232\nqm v,a,v,244,204,240\n"
"qr 144,3,0\nqr 148,3,8192\nqr 152,3,1024\nqr 156,3,9216\nqr 160,3,2048\nqr 164,3,10240\n"
"qr 168,3,3072\nqr 172,3,11264\nqw 0\nqr 176,3,4096\nqr 180,3,12288\nqr 184,3,5120\n"
"qr 188,3,13312\nqr 192,3,6144\nqr 196,3,14336\nqr 200,3,7168\nqr 204,3,15360\nqw 0\n"
"v_cmp_gt_i32 vcc, 0, v20\ns_nop 1\nv_cndmask_b32 v124, v124, v32, vcc\nv_cmp_gt_i32 vcc, 1, v20\ns_nop 1\nv_cndmask_b32 v125, v125, v32, vcc\n"
"v_cmp_gt_i32 vcc, 2, v20\ns_nop 1\nv_cndmask_b32 v126, v126, v32, vcc\nv_cmp_gt_i32 vcc, 3, v20\ns_nop 1\nv_cndmask_b32 v127, v127, v32, vcc\n"
"v_cmp_gt_i32 vcc, 16, v20\ns_nop 1\nv_cndmask_b32 v156, v156, v32, vcc\nv_cmp_gt_i32 vcc, 17, v20\ns_nop 1\nv_cndmask_b32 v157, v157, v32, vcc\n"
"v_cmp_gt_i32 vcc, 18, v20\ns_nop 1\nv_cndmask_b32 v158, v158, v32, vcc\nv_cmp_gt_i32 vcc, 19, v20\ns_nop 1\nv_cndmask_b32 v159, v159, v32, vcc\n"
"v_cmp_gt_i32 vcc, 32, v20\ns_nop 1\nv_cndmask_b32 v188, v188, v32, vcc\nv_cmp_gt_i32 vcc, 33, v20\ns_nop 1\nv_cndmask_b32 v189, v189, v32, vcc\n"
"v_cmp_gt_i32 vcc, 34, v20\ns_nop 1\nv_cndmask_b32 v190, v190, v32, vcc\nv_cmp_gt_i32 vcc, 35, v20\ns_nop 1\nv_cndmask_b32 v191, v191, v32, vcc\n"
"v_cmp_gt_i32 vcc, 48, v20\ns_nop 1\nv_cndmask_b32 v220, v220, v32, vcc\nv_cmp_gt_i32 vcc, 49, v20\ns_nop 1\nv_cndmask_b32 v221, v221, v32, vcc\n"
"v_cmp_gt_i32 vcc, 50, v20\ns_nop 1\nv_cndmask_b32 v222, v222, v32, vcc\nv_cmp_gt_i32 vcc, 51, v20\ns_nop 1\nv_cndmask_b32 v223, v223, v32, vcc\n"
"v_subrev_u32 v20, 64, v20\nv_max3_f32 v33, v24, v124, v125\nv_max3_f32 v33, v33, v126, v127\nv_max3_f32 v33, v33, v156, v157\nv_max3_f32 v33, v33, v158, v159\nv_max3_f32 v33, v33, v188, v189\n"
"v_max3_f32 v33, v33, v190, v191\nv_max3_f32 v33, v33, v220, v221\nv_max3_f32 v33, v33, v222, v223\nv_mov_b32 v34, v33\ns_nop 1\nv_permlane16_swap_b32 v34, v33\n"
"v_max_f32 v33, v33, v34\nv_mov_b32 v34, v33\ns_nop 1\nv_permlane32_swap_b32 v34, v33\nv_max_f32 v33, v33, v34\nv_sub_f32 v28, v24, v33\n"
"qx 28\nv_mov_b32 v24, v33\nv_sub_f32 v124, v124, v33\nv_sub_f32 v125, v125, v33\nv_sub_f32 v126, v126, v33\nv_sub_f32 v127, v127, v33\n"
"v_sub_f32 v156, v156, v33\nv_sub_f32 v157, v157, v33\nv_sub_f32 v158, v158, v33\nv_sub_f32 v159, v159, v33\nv_sub_f32 v188, v188, v33\nv_sub_f32 v189, v189, v33\n"
"v_sub_f32 v190, v190, v33\nv_sub_f32 v191, v191, v33\nv_sub_f32 v220, v220, v33\nv_sub_f32 v221, v221, v33\nv_sub_f32 v222, v222, v33\nv_sub_f32 v223, v223, v33\n"
"qx 124\nqx 125\nqx 126\nqx 127\nqx 156\nqx 157\n"
"qx 158\nqx 159\nqx 188\nqx 189\nqx 190\nqx 191\n"
"qx 220\nqx 221\nqx 222\nqx 223\nqc 56,124\nqc 57,126\n"
"qc 58,156\nqc 59,158\nqc 60,188\nqc 61,190\nqc 62,220\nqc 63,222\n"
"qa 35,0\nqa 36,1\nqa 37,2\nqa 38,3\nv_mul_f32 v35, v28, v35\nv_mul_f32 v36, v28, v36\n"
"v_mul_f32 v37, v28, v37\nv_mul_f32 v38, v28, v38\nqb 0,v35\nqb 1,v36\nqb 2,v37\nqb 3,v38\n"
"qa 35,8\nqa 36,9\nqa 37,10\nqa 38,11\nv_mul_f32 v35, v28, v35\nv_mul_f32 v36, v28, v36\n"
"v_mul_f32 v37, v28, v37\nv_mul_f32 v38, v28, v38\nqb 8,v35\nqb 9,v36\nqb 10,v37\nqb 11,v38\n"
"qa 35,16\nqa 36,17\nqa 37,18\nqa 38,19\nv_mul_f32 v35, v28, v35\nv_mul_f32 v36, v28, v36\n"
"v_mul_f32 v37, v28, v37\nv_mul_f32 v38, v28, v38\nqb 16,v35\nqb 17,v36\nqb 18,v37\nqb 19,v38\n"
"qa 35,24\nqa 36,25\nqa 37,26\nqa 38,27\nv_mul_f32 v35, v28, v35\nv_mul_f32 v36, v28, v36\n"
"v_mul_f32 v37, v28, v37\nv_mul_f32 v38, v28, v38\nqb 24,v35\nqb 25,v36\nqb 26,v37\nqb 27,v38\n"
"qa 35,32\nqa 36,33\nqa 37,34\nqa 38,35\nv_mul_f32 v35, v28, v35\nv_mul_f32 v36, v28, v36\n"
"v_mul_f32 v37, v28, v37\nv_mul_f32 v38, v28, v38\nqb 32,v35\nqb 33,v36\nqb 34,v37\nqb 35,v38\n"
"qa 35,40\nqa 36,41\nqa 37,42\nqa 38,43\nv_mul_f32 v35, v28, v35\nv_mul_f32 v36, v28, v36\n"
"v_mul_f32 v37, v28, v37\nv_mul_f32 v38, v28, v38\nqb 40,v35\nqb 41,v36\nqb 42,v37\nqb 43,v38\n"
"qa 35,48\nqa 36,49\nqa 37,50\nqa 38,51\nv_mul_f32 v35, v28, v35\nv_mul_f32 v36, v28, v36\n"
"v_mul_f32 v37, v28, v37\nv_mul_f32 v38, v28, v38\nqb 48,v35\nqb 49,v36\nqb 50,v37\nqb 51,v38\n"
"qa 35,56\nqa 36,57\nqa 37,58\nqa 38,59\nv_mul_f32 v35, v28, v35\nv_mul_f32 v36, v28, v36\n"
"v_mul_f32 v37, v28, v37\nv_mul_f32 v38, v28, v38\nqb 56,v35\nqb 57,v36\nqb 58,v37\nqb 59,v38\n"
"qa 35,64\nqa 36,65\nqa 37,66\nqa 38,67\nv_mul_f32 v35, v28, v35\nv_mul_f32 v36, v28, v36\n"
"v_mul_f32 v37, v28, v37\nv_mul_f32 v38, v28, v38\nqb 64,v35\nqb 65,v36\nqb 66,v37\nqb 67,v38\n"
"v_cmp_gt_i32 vcc, 0, v21\ns_nop 1\nv_cndmask_b32 v132, v132, v32, vcc\nv_cmp_gt_i32 vcc, 1, v21\ns_nop 1\nv_cndmask_b32 v133, v133, v32, vcc\n"
"v_cmp_gt_i32 vcc, 2, v21\ns_nop 1\nv_cndmask_b32 v134, v134, v32, vcc\nv_cmp_gt_i32 vcc, 3, v21\ns_nop 1\nv_cndmask_b32 v135, v135, v32, vcc\n"
"v_cmp_gt_i32 vcc, 16, v21\ns_nop 1\nv_cndmask_b32 v164, v164, v32, vcc\nv_cmp_gt_i32 vcc, 17, v21\ns_nop 1\nv_cndmask_b32 v165, v165, v32, vcc\n"
"v_cmp_gt_i32 vcc, 18, v21\ns_nop 1\nv_cndmask_b32 v166, v166, v32, vcc\nv_cmp_gt_i32 vcc, 19, v21\ns_nop 1\nv_cndmask_b32 v167, v167, v32, vcc\n"
"v_cmp_gt_i32 vcc, 32, v21\ns_nop 1\nv_cndmask_b32 v196, v196, v32, vcc\nv_cmp_gt_i32 vcc, 33, v21\ns_nop 1\nv_cndmask_b32 v197, v197, v32, vcc\n"
"v_cmp_gt_i32 vcc, 34, v21\ns_nop 1\nv_cndmask_b32 v198, v198, v32, vcc\nv_cmp_gt_i32 vcc, 35, v21\ns_nop 1\nv_cndmask_b32 v199, v199, v32, vcc\n"
"v_cmp_gt_i32 vcc, 48, v21\ns_nop 1\nv_cndmask_b32 v228, v228, v32, vcc\nv_cmp_gt_i32 vcc, 49, v21\ns_nop 1\nv_cndmask_b32 v229, v229, v32, vcc\n"
"v_cmp_gt_i32 vcc, 50, v21\ns_nop 1\nv_cndmask_b32 v230, v230, v32, vcc\nv_cmp_gt_i32 vcc, 51, v21\ns_nop 1\nv_cndmask_b32 v231, v231, v32, vcc\n"
"v_subrev_u32 v21, 64, v21\nv_max3_f32 v33, v25, v132, v133\nv_max3_f32 v33, v33, v134, v135\nv_max3_f32 v33, v33, v164, v165\nv_max3_f32 v33, v33, v166, v167\nv_max3_f32 v33, v33, v196, v197\n"
"v_max3_f32 v33, v33, v198, v199\nv_max3_f32 v33, v33, v228, v229\nv_max3_f32 v33, v33, v230, v231\nv_mov_b32 v34, v33\ns_nop 1\nv_permlane16_swap_b32 v34, v33\n"
"v_max_f32 v33, v33, v34\nv_mov_b32 v34, v33\ns_nop 1\nv_permlane32_swap_b32 v34, v33\nv_max_f32 v33, v33, v34\nv_sub_f32 v29, v25, v33\n"
"qx 29\nv_mov_b32 v25, v33\nv_sub_f32 v132, v132, v33\nv_sub_f32 v133, v133, v33\nv_sub_f32 v134, v134, v33\nv_sub_f32 v135, v135, v33\n"
"v_sub_f32 v164, v164, v33\nv_sub_f32 v165, v165, v33\nv_sub_f32 v166, v166, v33\nv_sub_f32 v167, v167, v33\nv_sub_f32 v196, v196, v33\nv_sub_f32 v197, v197, v33\n"
"v_sub_f32 v198, v198, v33\nv_sub_f32 v199, v199, v33\nv_sub_f32 v228, v228, v33\nv_sub_f32 v229, v229, v33\nv_sub_f32 v230, v230, v33\nv_sub_f32 v231, v231, v33\n"
"qx 132\nqx 133\nqx 134\nqx 135\nqx 164\nqx 165\n"
"qx 166\nqx 167\nqx 196\nqx 197\nqx 198\nqx 199\n"
"qx 228\nqx 229\nqx 230\nqx 231\nqc 64,132\nqc 65,134\n"
"qc 66,164\nqc 67,166\nqc 68,196\nqc 69,198\nqc 70,228\nqc 71,230\n"
"qa 35,4\nqa 36,5\nqa 37,6\nqa 38,7\nv_mul_f32 v35, v29, v35\nv_mul_f32 v36, v29, v36\n"
"v_mul_f32 v37, v29, v37\nv_mul_f32 v38, v29, v38\nqb 4,v35\nqb 5,v36\nqb 6,v37\nqb 7,v38\n"
"qa 35,12\nqa 36,13\nqa 37,14\nqa 38,15\nv_mul_f32 v35, v29, v35\nv_mul_f32 v36, v29, v36\n"
"v_mul_f32 v37, v29, v37\nv_mul_f32 v38, v29, v38\nqb 12,v35\nqb 13,v36\nqb 14,v37\nqb 15,v38\n"
"qa 35,20\nqa 36,21\nqa 37,22\nqa 38,23\nv_mul_f32 v35, v29, v35\nv_mul_f32 v36, v29, v36\n"
"v_mul_f32 v37, v29, v37\nv_mul_f32 v38, v29, v38\nqb 20,v35\nqb 21,v36\nqb 22,v37\nqb 23,v38\n"
"qa 35,28\nqa 36,29\nqa 37,30\nqa 38,31\nv_mul_f32 v35, v29, v35\nv_mul_f32 v36, v29, v36\n"
"v_mul_f32 v37, v29, v37\nv_mul_f32 v38, v29, v38\nqb 28,v35\nqb 29,v36\nqb 30,v37\nqb 31,v38\n"
"qa 35,36\nqa 36,37\nqa 37,38\nqa 38,39\nv_mul_f32 v35, v29, v35\nv_mul_f32 v36, v29, v36\n"
"v_mul_f32 v37, v29, v37\nv_mul_f32 v38, v29, v38\nqb 36,v35\nqb 37,v36\nqb 38,v37\nqb 39,v38\n"
"qa 35,44\nqa 36,45\nqa 37,46\nqa 38,47\nv_mul_f32 v35, v29, v35\nv_mul_f32 v36, v29, v36\n"
"v_mul_f32 v37, v29, v37\nv_mul_f32 v38, v29, v38\nqb 44,v35\nqb 45,v36\nqb 46,v37\nqb 47,v38\n"
"qa 35,52\nqa 36,53\nqa 37,54\nqa 38,55\nv_mul_f32 v35, v29, v35\nv_mul_f32 v36, v29, v36\n"
"v_mul_f32 v37, v29, v37\nv_mul_f32 v38, v29, v38\nqb 52,v35\nqb 53,v36\nqb 54,v37\nqb 55,v38\n"
"qa 35,60\nqa 36,61\nqa 37,62\nqa 38,63\nv_mul_f32 v35, v29, v35\nv_mul_f32 v36, v29, v36\n"
"v_mul_f32 v37, v29, v37\nv_mul_f32 v38, v29, v38\nqb 60,v35\nqb 61,v36\nqb 62,v37\nqb 63,v38\n"
"qa 35,68\nqa 36,69\nqa 37,70\nqa 38,71\nv_mul_f32 v35, v29, v35\nv_mul_f32 v36, v29, v36\n"
"v_mul_f32 v37, v29, v37\nv_mul_f32 v38, v29, v38\nqb 68,v35\nqb 69,v36\nqb 70,v37\nqb 71,v38\n"
"v_cmp_gt_i32 vcc, 0, v22\ns_nop 1\nv_cndmask_b32 v140, v140, v32, vcc\nv_cmp_gt_i32 vcc, 1, v22\ns_nop 1\nv_cndmask_b32 v141, v141, v32, vcc\n"
"v_cmp_gt_i32 vcc, 2, v22\ns_nop 1\nv_cndmask_b32 v142, v142, v32, vcc\nv_cmp_gt_i32 vcc, 3, v22\ns_nop 1\nv_cndmask_b32 v143, v143, v32, vcc\n"
"v_cmp_gt_i32 vcc, 16, v22\ns_nop 1\nv_cndmask_b32 v172, v172, v32, vcc\nv_cmp_gt_i32 vcc, 17, v22\ns_nop 1\nv_cndmask_b32 v173, v173, v32, vcc\n"
"v_cmp_gt_i32 vcc, 18, v22\ns_nop 1\nv_cndmask_b32 v174, v174, v32, vcc\nv_cmp_gt_i32 vcc, 19, v22\ns_nop 1\nv_cndmask_b32 v175, v175, v32, vcc\n"
"v_cmp_gt_i32 vcc, 32, v22\ns_nop 1\nv_cndmask_b32 v204, v204, v32, vcc\nv_cmp_gt_i32 vcc, 33, v22\ns_nop 1\nv_cndmask_b32 v205, v205, v32, vcc\n"
"v_cmp_gt_i32 vcc, 34, v22\ns_nop 1\nv_cndmask_b32 v206, v206, v32, vcc\nv_cmp_gt_i32 vcc, 35, v22\ns_nop 1\nv_cndmask_b32 v207, v207, v32, vcc\n"
"v_cmp_gt_i32 vcc, 48, v22\ns_nop 1\nv_cndmask_b32 v236, v236, v32, vcc\nv_cmp_gt_i32 vcc, 49, v22\ns_nop 1\nv_cndmask_b32 v237, v237, v32, vcc\n"
"v_cmp_gt_i32 vcc, 50, v22\ns_nop 1\nv_cndmask_b32 v238, v238, v32, vcc\nv_cmp_gt_i32 vcc, 51, v22\ns_nop 1\nv_cndmask_b32 v239, v239, v32, vcc\n"
"v_subrev_u32 v22, 64, v22\nv_max3_f32 v33, v26, v140, v141\nv_max3_f32 v33, v33, v142, v143\nv_max3_f32 v33, v33, v172, v173\nv_max3_f32 v33, v33, v174, v175\nv_max3_f32 v33, v33, v204, v205\n"
"v_max3_f32 v33, v33, v206, v207\nv_max3_f32 v33, v33, v236, v237\nv_max3_f32 v33, v33, v238, v239\nv_mov_b32 v34, v33\ns_nop 1\nv_permlane16_swap_b32 v34, v33\n"
"v_max_f32 v33, v33, v34\nv_mov_b32 v34, v33\ns_nop 1\nv_permlane32_swap_b32 v34, v33\nv_max_f32 v33, v33, v34\nv_sub_f32 v30, v26, v33\n"
"qx 30\nv_mov_b32 v26, v33\nv_sub_f32 v140, v140, v33\nv_sub_f32 v141, v141, v33\nv_sub_f32 v142, v142, v33\nv_sub_f32 v143, v143, v33\n"
"v_sub_f32 v172, v172, v33\nv_sub_f32 v173, v173, v33\nv_sub_f32 v174, v174, v33\nv_sub_f32 v175, v175, v33\nv_sub_f32 v204, v204, v33\nv_sub_f32 v205, v205, v33\n"
"v_sub_f32 v206, v206, v33\nv_sub_f32 v207, v207, v33\nv_sub_f32 v236, v236, v33\nv_sub_f32 v237, v237, v33\nv_sub_f32 v238, v238, v33\nv_sub_f32 v239, v239, v33\n"
"qx 140\nqx 141\nqx 142\nqx 143\nqx 172\nqx 173\n"
"qx 174\nqx 175\nqx 204\nqx 205\nqx 206\nqx 207\n"
"qx 236\nqx 237\nqx 238\nqx 239\nqc 72,140\nqc 73,142\n"
"qc 74,172\nqc 75,174\nqc 76,204\nqc 77,206\nqc 78,236\nqc 79,238\n"
"qa 35,72\nqa 36,73\nqa 37,74\nqa 38,75\nv_mul_f32 v35, v30, v35\nv_mul_f32 v36, v30, v36\n"
"v_mul_f32 v37, v30, v37\nv_mul_f32 v38, v30, v38\nqb 72,v35\nqb 73,v36\nqb 74,v37\nqb 75,v38\n"
"qa 35,80\nqa 36,81\nqa 37,82\nqa 38,83\nv_mul_f32 v35, v30, v35\nv_mul_f32 v36, v30, v36\n"
"v_mul_f32 v37, v30, v37\nv_mul_f32 v38, v30, v38\nqb 80,v35\nqb 81,v36\nqb 82,v37\nqb 83,v38\n"
"qa 35,88\nqa 36,89\nqa 37,90\nqa 38,91\nv_mul_f32 v35, v30, v35\nv_mul_f32 v36, v30, v36\n"
"v_mul_f32 v37, v30, v37\nv_mul_f32 v38, v30, v38\nqb 88,v35\nqb 89,v36\nqb 90,v37\nqb 91,v38\n"
"qa 35,96\nqa 36,97\nqa 37,98\nqa 38,99\nv_mul_f32 v35, v30, v35\nv_mul_f32 v36, v30, v36\n"
"v_mul_f32 v37, v30, v37\nv_mul_f32 v38, v30, v38\nqb 96,v35\nqb 97,v36\nqb 98,v37\nqb 99,v38\n"
"qa 35,104\nqa 36,105\nqa 37,106\nqa 38,107\nv_mul_f32 v35, v30, v35\nv_mul_f32 v36, v30, v36\n"
"v_mul_f32 v37, v30, v37\nv_mul_f32 v38, v30, v38\nqb 104,v35\nqb 105,v36\nqb 106,v37\nqb 107,v38\n"
"qa 35,112\nqa 36,113\nqa 37,114\nqa 38,115\nv_mul_f32 v35, v30, v35\nv_mul_f32 v36, v30, v36\n"
"v_mul_f32 v37, v30, v37\nv_mul_f32 v38, v30, v38\nqb 112,v35\nqb 113,v36\nqb 114,v37\nqb 115,v38\n"
"qa 35,120\nqa 36,121\nqa 37,122\nqa 38,123\nv_mul_f32 v35, v30, v35\nv_mul_f32 v36, v30, v36\n"
"v_mul_f32 v37, v30, v37\nv_mul_f32 v38, v30, v38\nqb 120,v35\nqb 121,v36\nqb 122,v37\nqb 123,v38\n"
"qa 35,128\nqa 36,129\nqa 37,130\nqa 38,131\nv_mul_f32 v35, v30, v35\nv_mul_f32 v36, v30, v36\n"
"v_mul_f32 v37, v30, v37\nv_mul_f32 v38, v30, v38\nqb 128,v35\nqb 129,v36\nqb 130,v37\nqb 131,v38\n"
"qa 35,136\nqa 36,137\nqa 37,138\nqa 38,139\nv_mul_f32 v35, v30, v35\nv_mul_f32 v36, v30, v36\n"
"v_mul_f32 v37, v30, v37\nv_mul_f32 v38, v30, v38\nqb 136,v35\nqb 137,v36\nqb 138,v37\nqb 139,v38\n"
"v_cmp_gt_i32 vcc, 0, v23\ns_nop 1\nv_cndmask_b32 v148, v148, v32, vcc\nv_cmp_gt_i32 vcc, 1, v23\ns_nop 1\nv_cndmask_b32 v149, v149, v32, vcc\n"
"v_cmp_gt_i32 vcc, 2, v23\ns_nop 1\nv_cndmask_b32 v150, v150, v32, vcc\nv_cmp_gt_i32 vcc, 3, v23\ns_nop 1\nv_cndmask_b32 v151, v151, v32, vcc\n"
"v_cmp_gt_i32 vcc, 16, v23\ns_nop 1\nv_cndmask_b32 v180, v180, v32, vcc\nv_cmp_gt_i32 vcc, 17, v23\ns_nop 1\nv_cndmask_b32 v181, v181, v32, vcc\n"
"v_cmp_gt_i32 vcc, 18, v23\ns_nop 1\nv_cndmask_b32 v182, v182, v32, vcc\nv_cmp_gt_i32 vcc, 19, v23\ns_nop 1\nv_cndmask_b32 v183, v183, v32, vcc\n"
"v_cmp_gt_i32 vcc, 32, v23\ns_nop 1\nv_cndmask_b32 v212, v212, v32, vcc\nv_cmp_gt_i32 vcc, 33, v23\ns_nop 1\nv_cndmask_b32 v213, v213, v32, vcc\n"
"v_cmp_gt_i32 vcc, 34, v23\ns_nop 1\nv_cndmask_b32 v214, v214, v32, vcc\nv_cmp_gt_i32 vcc, 35, v23\ns_nop 1\nv_cndmask_b32 v215, v215, v32, vcc\n"
"v_cmp_gt_i32 vcc, 48, v23\ns_nop 1\nv_cndmask_b32 v244, v244, v32, vcc\nv_cmp_gt_i32 vcc, 49, v23\ns_nop 1\nv_cndmask_b32 v245, v245, v32, vcc\n"
"v_cmp_gt_i32 vcc, 50, v23\ns_nop 1\nv_cndmask_b32 v246, v246, v32, vcc\nv_cmp_gt_i32 vcc, 51, v23\ns_nop 1\nv_cndmask_b32 v247, v247, v32, vcc\n"
"v_subrev_u32 v23, 64, v23\nv_max3_f32 v33, v27, v148, v149\nv_max3_f32 v33, v33, v150, v151\nv_max3_f32 v33, v33, v180, v181\nv_max3_f32 v33, v33, v182, v183\nv_max3_f32 v33, v33, v212, v213\n"
"v_max3_f32 v33, v33, v214, v215\nv_max3_f32 v33, v33, v244, v245\nv_max3_f32 v33, v33, v246, v247\nv_mov_b32 v34, v33\ns_nop 1\nv_permlane16_swap_b32 v34, v33\n"
"v_max_f32 v33, v33, v34\nv_mov_b32 v34, v33\ns_nop 1\nv_permlane32_swap_b32 v34, v33\nv_max_f32 v33, v33, v34\nv_sub_f32 v31, v27, v33\n"
"qx 31\nv_mov_b32 v27, v33\nv_sub_f32 v148, v148, v33\nv_sub_f32 v149, v149, v33\nv_sub_f32 v150, v150, v33\nv_sub_f32 v151, v151, v33\n"
"v_sub_f32 v180, v180, v33\nv_sub_f32 v181, v181, v33\nv_sub_f32 v182, v182, v33\nv_sub_f32 v183, v183, v33\nv_sub_f32 v212, v212, v33\nv_sub_f32 v213, v213, v33\n"
"v_sub_f32 v214, v214, v33\nv_sub_f32 v215, v215, v33\nv_sub_f32 v244, v244, v33\nv_sub_f32 v245, v245, v33\nv_sub_f32 v246, v246, v33\nv_sub_f32 v247, v247, v33\n"
"qx 148\nqx 149\nqx 150\nqx 151\nqx 180\nqx 181\n"
"qx 182\nqx 183\nqx 212\nqx 213\nqx 214\nqx 215\n"
"qx 244\nqx 245\nqx 246\nqx 247\nqc 80,148\nqc 81,150\n"
"qc 82,180\nqc 83,182\nqc 84,212\nqc 85,214\nqc 86,244\nqc 87,246\n"
"qa 35,76\nqa 36,77\nqa 37,78\nqa 38,79\nv_mul_f32 v35, v31, v35\nv_mul_f32 v36, v31, v36\n"
"v_mul_f32 v37, v31, v37\nv_mul_f32 v38, v31, v38\nqb 76,v35\nqb 77,v36\nqb 78,v37\nqb 79,v38\n"
"qa 35,84\nqa 36,85\nqa 37,86\nqa 38,87\nv_mul_f32 v35, v31, v35\nv_mul_f32 v36, v31, v36\n"
"v_mul_f32 v37, v31, v37\nv_mul_f32 v38, v31, v38\nqb 84,v35\nqb 85,v36\nqb 86,v37\nqb 87,v38\n"
"qa 35,92\nqa 36,93\nqa 37,94\nqa 38,95\nv_mul_f32 v35, v31, v35\nv_mul_f32 v36, v31, v36\n"
"v_mul_f32 v37, v31, v37\nv_mul_f32 v38, v31, v38\nqb 92,v35\nqb 93,v36\nqb 94,v37\nqb 95,v38\n"
"qa 35,100\nqa 36,101\nqa 37,102\nqa 38,103\nv_mul_f32 v35, v31, v35\nv_mul_f32 v36, v31, v36\n"
"v_mul_f32 v37, v31, v37\nv_mul_f32 v38, v31, v38\nqb 100,v35\nqb 101,v36\nqb 102,v37\nqb 103,v38\n"
"qa 35,108\nqa 36,109\nqa 37,110\nqa 38,111\nv_mul_f32 v35, v31, v35\nv_mul_f32 v36, v31, v36\n"
"v_mul_f32 v37, v31, v37\nv_mul_f32 v38, v31, v38\nqb 108,v35\nqb 109,v36\nqb 110,v37\nqb 111,v38\n"
"qa 35,116\nqa 36,117\nqa 37,118\nqa 38,119\nv_mul_f32 v35, v31, v35\nv_mul_f32 v36, v31, v36\n"
"v_mul_f32 v37, v31, v37\nv_mul_f32 v38, v31, v38\nqb 116,v35\nqb 117,v36\nqb 118,v37\nqb 119,v38\n"
"qa 35,124\nqa 36,125\nqa 37,126\nqa 38,127\nv_mul_f32 v35, v31, v35\nv_mul_f32 v36, v31, v36\n"
"v_mul_f32 v37, v31, v37\nv_mul_f32 v38, v31, v38\nqb 124,v35\nqb 125,v36\nqb 126,v37\nqb 127,v38\n"
"qa 35,132\nqa 36,133\nqa 37,134\nqa 38,135\nv_mul_f32 v35, v31, v35\nv_mul_f32 v36, v31, v36\n"
"v_mul_f32 v37, v31, v37\nv_mul_f32 v38, v31, v38\nqb 132,v35\nqb 133,v36\nqb 134,v37\nqb 135,v38\n"
"qa 35,140\nqa 36,141\nqa 37,142\nqa 38,143\nv_mul_f32 v35, v31, v35\nv_mul_f32 v36, v31, v36\n"
"v_mul_f32 v37, v31, v37\nv_mul_f32 v38, v31, v38\nqb 140,v35\nqb 141,v36\nqb 142,v37\nqb 143,v38\n"
"qm a,v,v,64,252,56\nqm a,v,v,64,252,60\nqm a,v,v,68,252,64\nqm a,v,v,68,252,68\nqm a,v,v,136,252,72\nqm a,v,v,136,252,76\n"
"qm a,v,v,140,252,80\nqm a,v,v,140,252,84\nqm a,a,v,0,144,56\nqm a,a,v,0,148,60\nqm a,a,v,4,144,64\nqm a,a,v,4,148,68\n"
"qm a,a,v,72,144,72\nqm a,a,v,72,148,76\nqm a,a,v,76,144,80\nqm a,a,v,76,148,84\nqm a,a,v,8,152,56\nqm a,a,v,8,156,60\n"
"qm a,a,v,12,152,64\nqm a,a,v,12,156,68\nqm a,a,v,80,152,72\nqm a,a,v,80,156,76\nqm a,a,v,84,152,80\nqm a,a,v,84,156,84\n"
"qm a,a,v,16,160,56\nqm a,a,v,16,164,60\nqm a,a,v,20,160,64\nqm a,a,v,20,164,68\nqm a,a,v,88,160,72\nqm a,a,v,88,164,76\n"
"qm a,a,v,92,160,80\nqm a,a,v,92,164,84\nqm a,a,v,24,168,56\nqm a,a,v,24,172,60\nqm a,a,v,28,168,64\nqm a,a,v,28,172,68\n"
"qm a,a,v,96,168,72\nqm a,a,v,96,172,76\nqm a,a,v,100,168,80\nqm a,a,v,100,172,84\nqm a,a,v,32,176,56\nqm a,a,v,32,180,60\n"
"qm a,a,v,36,176,64\nqm a,a,v,36,180,68\nqm a,a,v,104,176,72\nqm a,a,v,104,180,76\nqm a,a,v,108,176,80\nqm a,a,v,108,180,84\n"
"qm a,a,v,40,184,56\nqm a,a,v,40,188,60\nqm a,a,v,44,184,64\nqm a,a,v,44,188,68\nqm a,a,v,112,184,72\nqm a,a,v,112,188,76\n"
"qm a,a,v,116,184,80\nqm a,a,v,116,188,84\nqm a,a,v,48,192,56\nqm a,a,v,48,196,60\nqm a,a,v,52,192,64\nqm a,a,v,52,196,68\n"
"qm a,a,v,120,192,72\nqm a,a,v,120,196,76\nqm a,a,v,124,192,80\nqm a,a,v,124,196,84\nqm a,a,v,56,200,56\nqm a,a,v,56,204,60\n"
"qm a,a,v,60,200,64\nqm a,a,v,60,204,68\nqm a,a,v,128,200,72\nqm a,a,v,128,204,76\nqm a,a,v,132,200,80\nqm a,a,v,132,204,84\n"
"s_sub_u32 s84, s84, 1\ns_cmp_lg_u32 s84, 0\ns_cbranch_scc1 SLOWL%=\nSLOWX%=:\ns_mov_b32 s72, s36\ns_and_b32 s73, s37, 0xffff\n"
"s_mov_b32 s74, 0x400000\ns_mov_b32 s75, 0x20000\nbuffer_load_dwordx4 v[20:23], v16, s[72:75], s3 offen offset:0\nbuffer_load_dwordx4 v[24:27], v16, s[72:75], s3 offen offset:64\nbuffer_load_dwordx4 v[28:31], v16, s[72:75], s4 offen offset:0\nbuffer_load_dwordx4 v[32:35], v16, s[72:75], s4 offen offset:64\n"
"v_mbcnt_lo_u32_b32 v8, -1, 0\nv_mbcnt_hi_u32_b32 v8, -1, v8\nv_lshrrev_b32 v9, 4, v8\nv_bfe_u32 v10, v8, 2, 2\nv_lshl_add_u32 v11, v9, 2, v10\nv_and_b32 v9, 7, v11\n"
"v_and_b32 v10, 3, v8\nv_lshlrev_b32 v10, 3, v10\nv_lshl_add_u32 v10, v11, 8, v10\nv_lshl_add_u32 v10, v9, 5, v10\nv_mov_b32 v36, v10\nv_xor_b32 v37, 32, v10\n"
"v_xor_b32 v38, 64, v10\nv_xor_b32 v39, 96, v10\nv_xor_b32 v40, 128, v10\nv_xor_b32 v41, 160, v10\nv_xor_b32 v42, 192, v10\nv_xor_b32 v43, 224, v10\n"
"qv 0\nv_permlane16_swap_b32 v20, v22\nv_permlane16_swap_b32 v21, v23\nv_permlane16_swap_b32 v24, v26\nv_permlane16_swap_b32 v25, v27\nv_permlane16_swap_b32 v28, v30\n"
"v_permlane16_swap_b32 v29, v31\nv_permlane16_swap_b32 v32, v34\nv_permlane16_swap_b32 v33, v35\nv_lshlrev_b32 v88, 16, v20\nv_and_b32 v89, 0xffff0000, v20\nv_lshlrev_b32 v90, 16, v21\n"
"v_and_b32 v91, 0xffff0000, v21\nv_lshlrev_b32 v92, 16, v22\nv_and_b32 v93, 0xffff0000, v22\nv_lshlrev_b32 v94, 16, v23\nv_and_b32 v95, 0xffff0000, v23\nv_lshlrev_b32 v96, 16, v24\n"
"v_and_b32 v97, 0xffff0000, v24\nv_lshlrev_b32 v98, 16, v25\nv_and_b32 v99, 0xffff0000, v25\nv_lshlrev_b32 v100, 16, v26\nv_and_b32 v101, 0xffff0000, v26\nv_lshlrev_b32 v102, 16, v27\n"
"v_and_b32 v103, 0xffff0000, v27\nv_lshlrev_b32 v104, 16, v28\nv_and_b32 v105, 0xffff0000, v28\nv_lshlrev_b32 v106, 16, v29\nv_and_b32 v107, 0xffff0000, v29\nv_lshlrev_b32 v108, 16, v30\n"
"v_and_b32 v109, 0xffff0000, v30\nv_lshlrev_b32 v110, 16, v31\nv_and_b32 v111, 0xffff0000, v31\nv_lshlrev_b32 v112, 16, v32\nv_and_b32 v113, 0xffff0000, v32\nv_lshlrev_b32 v114, 16, v33\n"
"v_and_b32 v115, 0xffff0000, v33\nv_lshlrev_b32 v116, 16, v34\nv_and_b32 v117, 0xffff0000, v34\nv_lshlrev_b32 v118, 16, v35\nv_and_b32 v119, 0xffff0000, v35\ns_lshl_b32 s6, s88, 17\n"
"s_lshl_b32 s7, s20, 12\ns_add_u32 s6, s6, s7\ns_add_u32 s6, s6, 0x2800000\ns_xor_b32 s7, s6, 0xe0000\nqa 6,64\nqa 7,68\n"
"qp 6\nqp 7\ns_lshr_b32 s8, s88, 3\ns_lshl2_add_u32 s8, s8, s15\ns_lshl_b32 s8, s8, 8\ns_lshl3_add_u32 s8, s20, s8\n"
"s_add_u32 s8, s8, 0x5000\nqa 184,0\nqa 185,1\nqa 186,2\nqa 187,3\nqa 188,8\n"
"qa 189,9\nqa 190,10\nqa 191,11\nv_pk_mul_f32 v[184:185], v[184:185], v[6:7] op_sel:[0,0] op_sel_hi:[1,0]\nv_pk_mul_f32 v[186:187], v[186:187], v[6:7] op_sel:[0,0] op_sel_hi:[1,0]\nv_pk_mul_f32 v[188:189], v[188:189], v[6:7] op_sel:[0,0] op_sel_hi:[1,0]\n"
"v_pk_mul_f32 v[190:191], v[190:191], v[6:7] op_sel:[0,0] op_sel_hi:[1,0]\nqc 184,184\nqc 185,186\nqc 186,188\nqc 187,190\nbuffer_store_dwordx4 v[184:187], v0, s[56:59], s6 offen offset:0 sc1\n"
"qa 192,4\nqa 193,5\nqa 194,6\nqa 195,7\nqa 196,12\nqa 197,13\n"
"qa 198,14\nqa 199,15\nv_pk_mul_f32 v[192:193], v[192:193], v[6:7] op_sel:[0,1] op_sel_hi:[1,1]\nv_pk_mul_f32 v[194:195], v[194:195], v[6:7] op_sel:[0,1] op_sel_hi:[1,1]\nv_pk_mul_f32 v[196:197], v[196:197], v[6:7] op_sel:[0,1] op_sel_hi:[1,1]\nv_pk_mul_f32 v[198:199], v[198:199], v[6:7] op_sel:[0,1] op_sel_hi:[1,1]\n"
"qc 192,192\nqc 193,194\nqc 194,196\nqc 195,198\nbuffer_store_dwordx4 v[192:195], v0, s[56:59], s7 offen offset:0 sc1\nqa 200,16\n"
"qa 201,17\nqa 202,18\nqa 203,19\nqa 204,24\nqa 205,25\nqa 206,26\n"
"qa 207,27\nv_pk_mul_f32 v[200:201], v[200:201], v[6:7] op_sel:[0,0] op_sel_hi:[1,0]\nv_pk_mul_f32 v[202:203], v[202:203], v[6:7] op_sel:[0,0] op_sel_hi:[1,0]\nv_pk_mul_f32 v[204:205], v[204:205], v[6:7] op_sel:[0,0] op_sel_hi:[1,0]\nv_pk_mul_f32 v[206:207], v[206:207], v[6:7] op_sel:[0,0] op_sel_hi:[1,0]\nqc 200,200\n"
"qc 201,202\nqc 202,204\nqc 203,206\nbuffer_store_dwordx4 v[200:203], v0, s[56:59], s6 offen offset:1024 sc1\nqa 208,20\nqa 209,21\n"
"qa 210,22\nqa 211,23\nqa 212,28\nqa 213,29\nqa 214,30\nqa 215,31\n"
"v_pk_mul_f32 v[208:209], v[208:209], v[6:7] op_sel:[0,1] op_sel_hi:[1,1]\nv_pk_mul_f32 v[210:211], v[210:211], v[6:7] op_sel:[0,1] op_sel_hi:[1,1]\nv_pk_mul_f32 v[212:213], v[212:213], v[6:7] op_sel:[0,1] op_sel_hi:[1,1]\nv_pk_mul_f32 v[214:215], v[214:215], v[6:7] op_sel:[0,1] op_sel_hi:[1,1]\nqc 208,208\nqc 209,210\n"
"qc 210,212\nqc 211,214\nbuffer_store_dwordx4 v[208:211], v0, s[56:59], s7 offen offset:1024 sc1\nqa 184,32\nqa 185,33\nqa 186,34\n"
"qa 187,35\nqa 188,40\nqa 189,41\nqa 190,42\nqa 191,43\nv_pk_mul_f32 v[184:185], v[184:185], v[6:7] op_sel:[0,0] op_sel_hi:[1,0]\n"
"v_pk_mul_f32 v[186:187], v[186:187], v[6:7] op_sel:[0,0] op_sel_hi:[1,0]\nv_pk_mul_f32 v[188:189], v[188:189], v[6:7] op_sel:[0,0] op_sel_hi:[1,0]\nv_pk_mul_f32 v[190:191], v[190:191], v[6:7] op_sel:[0,0] op_sel_hi:[1,0]\nqc 184,184\nqc 185,186\nqc 186,188\n"
"qc 187,190\nbuffer_store_dwordx4 v[184:187], v0, s[56:59], s6 offen offset:2048 sc1\nqa 192,36\nqa 193,37\nqa 194,38\nqa 195,39\n"
"qa 196,44\nqa 197,45\nqa 198,46\nqa 199,47\nv_pk_mul_f32 v[192:193], v[192:193], v[6:7] op_sel:[0,1] op_sel_hi:[1,1]\nv_pk_mul_f32 v[194:195], v[194:195], v[6:7] op_sel:[0,1] op_sel_hi:[1,1]\n"
"v_pk_mul_f32 v[196:197], v[196:197], v[6:7] op_sel:[0,1] op_sel_hi:[1,1]\nv_pk_mul_f32 v[198:199], v[198:199], v[6:7] op_sel:[0,1] op_sel_hi:[1,1]\nqc 192,192\nqc 193,194\nqc 194,196\nqc 195,198\n"
"buffer_store_dwordx4 v[192:195], v0, s[56:59], s7 offen offset:2048 sc1\nqa 200,48\nqa 201,49\nqa 202,50\nqa 203,51\nqa 204,56\n"
"qa 205,57\nqa 206,58\nqa 207,59\nv_pk_mul_f32 v[200:201], v[200:201], v[6:7] op_sel:[0,0] op_sel_hi:[1,0]\nv_pk_mul_f32 v[202:203], v[202:203], v[6:7] op_sel:[0,0] op_sel_hi:[1,0]\nv_pk_mul_f32 v[204:205], v[204:205], v[6:7] op_sel:[0,0] op_sel_hi:[1,0]\n"
"v_pk_mul_f32 v[206:207], v[206:207], v[6:7] op_sel:[0,0] op_sel_hi:[1,0]\nqc 200,200\nqc 201,202\nqc 202,204\nqc 203,206\nbuffer_store_dwordx4 v[200:203], v0, s[56:59], s6 offen offset:3072 sc1\n"
"qa 208,52\nqa 209,53\nqa 210,54\nqa 211,55\nqa 212,60\nqa 213,61\n"
"qa 214,62\nqa 215,63\nv_pk_mul_f32 v[208:209], v[208:209], v[6:7] op_sel:[0,1] op_sel_hi:[1,1]\nv_pk_mul_f32 v[210:211], v[210:211], v[6:7] op_sel:[0,1] op_sel_hi:[1,1]\nv_pk_mul_f32 v[212:213], v[212:213], v[6:7] op_sel:[0,1] op_sel_hi:[1,1]\nv_pk_mul_f32 v[214:215], v[214:215], v[6:7] op_sel:[0,1] op_sel_hi:[1,1]\n"
"qc 208,208\nqc 209,210\nqc 210,212\nqc 211,214\nbuffer_store_dwordx4 v[208:211], v0, s[56:59], s7 offen offset:3072 sc1\ns_barrier\n"
"v_readlane_b32 s2, v18, 0\ns_add_u32 m0, s0, 0x0\ns_nop 3\nqd 12,60,2,0\nqd 13,60,2,1024\nqd 14,60,2,2048\n"
"qd 15,60,2,3072\nv_readlane_b32 s2, v18, 1\ns_add_u32 m0, s0, 0x4000\ns_nop 3\nqd 12,60,2,0\nqd 13,60,2,1024\n"
"qd 14,60,2,2048\nqd 15,60,2,3072\nv_readlane_b32 s2, v18, 2\ns_add_u32 m0, s0, 0x8000\ns_nop 3\nqd 12,60,2,0\n"
"qd 13,60,2,1024\nqd 14,60,2,2048\nqd 15,60,2,3072\nqv 0\ns_mov_b64 exec, 1\nbuffer_store_dwordx2 v[250:251], off, s[56:59], s8 sc1\n"
"s_mov_b64 exec, -1\ns_barrier\nqt 144,36,0\nqt 146,36,4096\nqt 148,36,8192\nqt 150,36,12288\n"
"qt 152,37,0\nqt 154,37,4096\nqt 156,37,8192\nqt 158,37,12288\nqt 160,38,0\nqt 162,38,4096\n"
"qt 164,38,8192\nqt 166,38,12288\ns_mov_b32 s35, 200000\nPESLOW%=:\nbuffer_load_dwordx2 v[248:249], v19, s[56:59], s5 offen offset:0 sc1\nqv 0\n"
"v_cmp_eq_u64 vcc, s[16:17], v[248:249]\ns_nop 3\ns_cmp_eq_u64 vcc, exec\ns_cbranch_scc1 PESLOWx%=\ns_sleep 3\ns_sub_u32 s35, s35, 1\n"
"s_cmp_lg_u32 s35, 0\ns_cbranch_scc1 PESLOW%=\nPESLOWx%=:\nv_readlane_b32 s1, v17, 0\ns_nop 4\nbuffer_load_dwordx4 v[152:155], v0, s[64:67], s1 offen offset:0 sc1\n"
"buffer_load_dwordx4 v[156:159], v0, s[64:67], s1 offen offset:1024 sc1\nbuffer_load_dwordx4 v[160:163], v0, s[68:71], s1 offen offset:0 sc1\nbuffer_load_dwordx4 v[164:167], v0, s[68:71], s1 offen offset:1024 sc1\nv_readlane_b32 s1, v17, 1\ns_nop 4\nbuffer_load_dwordx4 v[168:171], v0, s[64:67], s1 offen offset:0 sc1\n"
"buffer_load_dwordx4 v[172:175], v0, s[64:67], s1 offen offset:1024 sc1\nbuffer_load_dwordx4 v[176:179], v0, s[68:71], s1 offen offset:0 sc1\nbuffer_load_dwordx4 v[180:183], v0, s[68:71], s1 offen offset:1024 sc1\ns_waitcnt vmcnt(0) lgkmcnt(0)\ns_branch G2GO%=\nPXG2_o%=:\n"
"s_mov_b32 s35, 200000\nPXG2_s%=:\nbuffer_load_dwordx2 v[248:249], v11, s[56:59], s87 offen offset:0 sc1\nqv 0\nv_cmp_eq_u64 vcc, s[16:17], v[248:249]\ns_nop 3\n"
"s_cmp_eq_u64 vcc, exec\ns_cbranch_scc1 PXG2_sx%=\ns_sleep 3\ns_sub_u32 s35, s35, 1\ns_cmp_lg_u32 s35, 0\ns_cbranch_scc1 PXG2_s%=\n"
"PXG2_sx%=:\ns_branch PXG2_r%=\nPLG2_o%=:\ns_mov_b32 s35, 200000\nPLG2_s%=:\nbuffer_load_dwordx2 v[248:249], v19, s[56:59], s5 offen offset:0 sc1\n"
"qv 0\nv_cmp_eq_u64 vcc, s[16:17], v[248:249]\ns_nop 3\ns_cmp_eq_u64 vcc, exec\ns_cbranch_scc1 PLG2_sx%=\ns_sleep 3\n"
"s_sub_u32 s35, s35, 1\ns_cmp_lg_u32 s35, 0\ns_cbranch_scc1 PLG2_s%=\nPLG2_sx%=:\ns_branch PLG2_r%=\nPCS_o%=:\n"
"s_mov_b32 s35, 200000\nPCS_s%=:\nbuffer_load_dwordx2 v[248:249], v53, s[56:59], s11 offen offset:0 sc1\nqv 0\nv_cmp_eq_u64 vcc, s[16:17], v[248:249]\ns_nop 3\n"
"s_cmp_eq_u64 vcc, exec\ns_cbranch_scc1 PCS_sx%=\ns_sleep 3\ns_sub_u32 s35, s35, 1\ns_cmp_lg_u32 s35, 0\ns_cbranch_scc1 PCS_s%=\n"
"PCS_sx%=:\ns_branch PCS_r%=\nPEG4_o%=:\ns_mov_b32 s35, 200000\nPEG4_s%=:\nbuffer_load_dwordx2 v[248:249], v19, s[56:59], s5 offen offset:0 sc1\n"
"qv 0\nv_cmp_eq_u64 vcc, s[16:17], v[248:249]\ns_nop 3\ns_cmp_eq_u64 vcc, exec\ns_cbranch_scc1 PEG4_sx%=\ns_sleep 3\n"
"s_sub_u32 s35, s35, 1\ns_cmp_lg_u32 s35, 0\ns_cbranch_scc1 PEG4_s%=\nPEG4_sx%=:\ns_branch PEG4_r%=\nPXG4_o%=:\n"
"s_mov_b32 s35, 200000\nPXG4_s%=:\nbuffer_load_dwordx2 v[248:249], v11, s[56:59], s87 offen offset:0 sc1\nqv 0\nv_cmp_eq_u64 vcc, s[16:17], v[248:249]\ns_nop 3\n"
"s_cmp_eq_u64 vcc, exec\ns_cbranch_scc1 PXG4_sx%=\ns_sleep 3\ns_sub_u32 s35, s35, 1\ns_cmp_lg_u32 s35, 0\ns_cbranch_scc1 PXG4_s%=\n"
"PXG4_sx%=:\ns_branch PXG4_r%=\nPLG4_o%=:\ns_mov_b32 s35, 200000\nPLG4_s%=:\nbuffer_load_dwordx2 v[248:249], v19, s[56:59], s5 offen offset:0 sc1\n"
"qv 0\nv_cmp_eq_u64 vcc, s[16:17], v[248:249]\ns_nop 3\ns_cmp_eq_u64 vcc, exec\ns_cbranch_scc1 PLG4_sx%=\ns_sleep 3\n"
"s_sub_u32 s35, s35, 1\ns_cmp_lg_u32 s35, 0\ns_cbranch_scc1 PLG4_s%=\nPLG4_sx%=:\ns_branch PLG4_r%=\nFINISH%=:\n"
".purgem qm\n.purgem qz\n.purgem qt\n.purgem qr\n.purgem qx\n.purgem qp\n"
".purgem qc\n.purgem qw\n.purgem qv\n.purgem qd\n.purgem qe\n.purgem ql\n"
".purgem qa\n.purgem qb\n" :: "s"(__builtin_amdgcn_kernarg_segment_ptr()), "s"(blockIdx.x), "s"(w), "s"((unsigned)(size_t)lds) : "v0","v1","v2","v3","v4","v5","v6","v7","v8","v9","v10","v11","v12","v13","v14","v15","v16","v17","v18","v19","v20","v21","v22","v23","v24","v25","v26","v27","v28","v29","v30","v31","v32","v33","v34","v35","v36","v37","v38","v39","v40","v41","v42","v43","v44","v45","v46","v47","v48","v49","v50","v51","v52","v53","v54","v55","v56","v57","v58","v59","v60","v61","v62","v63","v64","v65","v66","v67","v68","v69","v70","v71","v72","v73","v74","v75","v76","v77","v78","v79","v80","v81","v82","v83","v84","v85","v86","v87","v88","v89","v90","v91","v92","v93","v94","v95","v96","v97","v98","v99","v100","v101","v102","v103","v104","v105","v106","v107","v108","v109","v110","v111","v112","v113","v114","v115","v116","v117","v118","v119","v120","v121","v122","v123","v124","v125","v126","v127","v128","v129","v130","v131","v132","v133","v134","v135","v136","v137","v138","v139","v140","v141","v142","v143","v144","v145","v146","v147","v148","v149","v150","v151","v152","v153","v154","v155","v156","v157","v158","v159","v160","v161","v162","v163","v164","v165","v166","v167","v168","v169","v170","v171","v172","v173","v174","v175","v176","v177","v178","v179","v180","v181","v182","v183","v184","v185","v186","v187","v188","v189","v190","v191","v192","v193","v194","v195","v196","v197","v198","v199","v200","v201","v202","v203","v204","v205","v206","v207","v208","v209","v210","v211","v212","v213","v214","v215","v216","v217","v218","v219","v220","v221","v222","v223","v224","v225","v226","v227","v228","v229","v230","v231","v232","v233","v234","v235","v236","v237","v238","v239","v240","v241","v242","v243","v244","v245","v246","v247","v248","v249","v250","v251","v252","v253","v254","v255","a0","a1","a2","a3","a4","a5","a6","a7","a8","a9","a10","a11","a12","a13","a14","a15","a16","a17","a18","a19","a20","a21","a22","a23","a24","a25","a26","a27","a28","a29","a30","a31","a32","a33","a34","a35","a36","a37","a38","a39","a40","a41","a42","a43","a44","a45","a46","a47","a48","a49","a50","a51","a52","a53","a54","a55","a56","a57","a58","a59","a60","a61","a62","a63","a64","a65","a66","a67","a68","a69","a70","a71","a72","a73","a74","a75","a76","a77","a78","a79","a80","a81","a82","a83","a84","a85","a86","a87","a88","a89","a90","a91","a92","a93","a94","a95","a96","a97","a98","a99","a100","a101","a102","a103","a104","a105","a106","a107","a108","a109","a110","a111","a112","a113","a114","a115","a116","a117","a118","a119","a120","a121","a122","a123","a124","a125","a126","a127","a128","a129","a130","a131","a132","a133","a134","a135","a136","a137","a138","a139","a140","a141","a142","a143","a144","a145","a146","a147","a148","a149","a150","a151","a152","a153","a154","a155","a156","a157","a158","a159","a160","a161","a162","a163","a164","a165","a166","a167","a168","a169","a170","a171","a172","a173","a174","a175","a176","a177","a178","a179","a180","a181","a182","a183","a184","a185","a186","a187","a188","a189","a190","a191","a192","a193","a194","a195","a196","a197","a198","a199","a200","a201","a202","a203","a204","a205","a206","a207","a208","a209","a210","a211","a212","a213","a214","a215","a216","a217","a218","a219","a220","a221","a222","a223","a224","a225","a226","a227","a228","a229","a230","a231","a232","a233","a234","a235","a236","a237","a238","a239","a240","a241","a242","a243","a244","a245","a246","a247","a248","a249","a250","a251","a252","a253","a254","a255","s12","s13","s14","s15","s16","s17","s18","s19","s20","s21","s22","s23","s24","s25","s26","s27","s28","s29","s30","s31","s34","s35","s36","s37","s38","s39","s40","s41","s42","s43","s44","s45","s46","s47","s48","s49","s50","s51","s52","s53","s54","s55","s56","s57","s58","s59","s60","s61","s62","s63","s64","s65","s66","s67","s68","s69","s70","s71","s72","s73","s74","s75","s76","s77","s78","s79","s80","s81","s82","s83","s84","s85","s86","s87","s88","s89","s90","s91","s92","s93","s94","s95","s96","s97","s98","s99","vcc","scc","memory");
}
extern "C" void kernel_launch(void* const* d_in, const int* in_sizes, int n_in, void* d_out, int out_size, void* d_ws, size_t ws_size, hipStream_t stream) {
  k_fused<<<256, 256, 0, stream>>>(d_in[0], d_in[1], d_in[2], d_in[3], d_in[4], d_in[5], d_in[7], d_in[8], d_out, d_ws);
}
```
